```python
import torch

def _softmax(x, axis=-1):
    return torch.softmax(x, dim=axis)

def _gelu(x):
    return torch.nn.functional.gelu(x, approximate='tanh')

def _silu(x):
    return torch.nn.functional.silu(x)

def _sigmoid(x):
    return torch.sigmoid(x)

def _layernorm(x, axis=-1, eps=1e-05):
    m = x.mean(dim=axis, keepdim=True)
    v = x.var(dim=axis, keepdim=True, unbiased=False)
    return (x - m) / torch.sqrt(v + eps)

def _scatter_add(tgt, idx, src, axis=0):
    return tgt.clone().index_add_(axis, idx, src)

def _segment_sum(src, seg, n):
    out = torch.zeros((n,) + src.shape[1:], dtype=src.dtype)
    return out.index_add_(0, seg, src)

def _pad(x, pw):
    flat = tuple((v for pair in reversed(pw) for v in pair))
    return torch.nn.functional.pad(x, flat)
kernel_name = 'gated_mix__compressed_kv_cla__mixer_gmlp__prompt'

def setup_inputs():

    _w = lambda a: (lambda t: t.to(torch.bfloat16) if t.is_floating_point() else t)(torch.as_tensor(a))

    def _A_setup():
        B, H, Sq, Sk, D, M = (1, 32, 2048, 2048, 128, 512)
        Dc = 4 * D
        return {'x': _w((lambda *s: torch.randn(*s, dtype=torch.bfloat16) * 0.05)(B, Sq, M)), 'c_kv': _w((lambda *s: torch.randn(*s, dtype=torch.bfloat16) * 0.8)(B, Sk, Dc)), 'w_q': _w((lambda *s: torch.randn(*s, dtype=torch.bfloat16) * 6.4)(M, H, D) * M ** (-0.5)), 'w_uk': _w((lambda *s: torch.randn(*s, dtype=torch.bfloat16) * 6.4)(Dc, H, D) * Dc ** (-0.5)), 'w_uv': _w((lambda *s: torch.randn(*s, dtype=torch.bfloat16) * 0.8)(Dc, H, D) * Dc ** (-0.5)), 'w_o': _w((lambda *s: torch.randn(*s, dtype=torch.bfloat16) * 0.8)(H, D, M) * (H * D) ** (-0.5))}
    _A_in = _A_setup()

    def _B_setup():
        B, S, D, F = (1, 2048, 512, 1792)
        Fh = F // 2
        return {'x': _w((lambda *s: torch.randn(*s, dtype=torch.bfloat16) * 0.05)(B, S, D)), 'w_in': _w((lambda *s: torch.randn(*s, dtype=torch.bfloat16) * 0.4)(D, 2 * Fh)), 'w_sp': _w(0.01 * (lambda *s: torch.randn(*s, dtype=torch.bfloat16) * 0.4)(S, S)), 'b_sp': _w(1.0 + 0.01 * (lambda *s: torch.randn(*s, dtype=torch.bfloat16) * 0.05)(S)), 'w_out': _w((lambda *s: torch.randn(*s, dtype=torch.bfloat16) * 0.05)(Fh, D))}
    _B_in = _B_setup()
    return {'A_x': _A_in['x'], 'A_c_kv': _A_in['c_kv'], 'A_w_q': _A_in['w_q'], 'A_w_uk': _A_in['w_uk'], 'A_w_uv': _A_in['w_uv'], 'A_w_o': _A_in['w_o'], 'B_x': _B_in['x'], 'B_w_in': _B_in['w_in'], 'B_w_sp': _B_in['w_sp'], 'B_b_sp': _B_in['b_sp'], 'B_w_out': _B_in['w_out'], 'gate_w': _w((lambda *s: torch.randn(*s, dtype=torch.bfloat16) * 0.05)(512) * 16.0)}

def reference(A_x, A_c_kv, A_w_q, A_w_uk, A_w_uv, A_w_o, B_x, B_w_in, B_w_sp, B_b_sp, B_w_out, gate_w):

    def _A_ref(x, c_kv, w_q, w_uk, w_uv, w_o):
        B, Sq, M = (x.shape[0], x.shape[1], x.shape[2])
        Sk = c_kv.shape[1]
        H, D = (w_q.shape[1], w_q.shape[2])
        q = torch.einsum('bqm,mhd->bhqd', x, w_q)
        k = torch.einsum('bkc,chd->bhkd', c_kv, w_uk)
        v = torch.einsum('bkc,chd->bhkd', c_kv, w_uv)
        scores = torch.einsum('bhqd,bhkd->bhqk', q, k) * D ** (-0.5)
        off = Sk - Sq
        qi = torch.arange(Sq)[:, None]
        kj = torch.arange(Sk)[None, :]
        cmask = kj <= qi + off
        scores = torch.where(cmask[None, None], scores, -1000000000.0)
        p = _softmax(scores, axis=-1)
        ctx = torch.einsum('bhqk,bhkd->bhqd', p, v)
        return torch.einsum('bhqd,hdm->bqm', ctx, w_o)

    def _B_ref(x, w_in, w_sp, b_sp, w_out):
        B, S, D = x.shape
        F = w_in.shape[-1]
        Fh = F // 2
        z = _gelu(torch.einsum('bsd,df->bsf', x, w_in))
        z1 = z[..., :Fh].clone()
        z2 = _layernorm(z[..., Fh:], axis=-1)
        g = torch.einsum('ts,bsf->btf', w_sp, z2) + b_sp[None, :, None]
        return torch.einsum('bsf,fd->bsd', z1 * g, w_out)
    x = A_x
    g = _sigmoid((x * gate_w).sum(-1, keepdims=True))
    a = _A_ref(x, A_c_kv, A_w_q, A_w_uk, A_w_uv, A_w_o)
    b = _B_ref(x, B_w_in, B_w_sp, B_b_sp, B_w_out)
    return g * a + (1 - g) * b
```

```cpp
#include <hip/hip_runtime.h>
#include <hip/hip_bf16.h>
#include <stdint.h>
typedef unsigned short u16;
__device__ __forceinline__ float bf2f(u16 b){ return __uint_as_float(((unsigned)b)<<16); }
__device__ __forceinline__ u16 f2bf(float f){ unsigned u=__float_as_uint(f); u += 0x7fffu + ((u>>16)&1u); return (u16)(u>>16); }

template<int MODE>
__global__ void ngemm(const u16* A, int lda, const u16* B, int ldb, u16* C, int ldc, int K, float scale){
  __shared__ float As[16][65], Bs[16][65];
  int tx=threadIdx.x&15, ty=threadIdx.x>>4;
  int m0=blockIdx.y*64, n0=blockIdx.x*64;
  float acc[4][4]={};
  for(int k0=0;k0<K;k0+=16){
    for(int i=threadIdx.x;i<1024;i+=256){ int r=i>>4,c=i&15; As[c][r]=bf2f(A[(size_t)(m0+r)*lda+k0+c]); }
    for(int i=threadIdx.x;i<1024;i+=256){ int r=i>>6,c=i&63; Bs[r][c]=bf2f(B[(size_t)(k0+r)*ldb+n0+c]); }
    __syncthreads();
    for(int k=0;k<16;k++){
      float a[4],b[4];
      for(int i=0;i<4;i++){a[i]=As[k][ty*4+i]; b[i]=Bs[k][tx*4+i];}
      for(int i=0;i<4;i++)for(int j=0;j<4;j++)acc[i][j]+=a[i]*b[j];
    }
    __syncthreads();
  }
  for(int i=0;i<4;i++)for(int j=0;j<4;j++){
    float v=acc[i][j]*scale;
    if(MODE==1){ v=bf2f(f2bf(v)); float t=0.7978845608028654f*(v+0.044715f*v*v*v); v=0.5f*v*(1.f+tanhf(t)); }
    C[(size_t)(m0+ty*4+i)*ldc+n0+tx*4+j]=f2bf(v);
  }
}
__global__ void nattn(const u16* Q,const u16* K,const u16* V,u16* O){
  int q=blockIdx.x, h=blockIdx.y, t=threadIdx.x;
  __shared__ float s[2048]; __shared__ float qs[128]; __shared__ float red[128];
  qs[t]=bf2f(Q[(size_t)q*4096+h*128+t]); __syncthreads();
  float mx=-1e30f;
  for(int j=t;j<=q;j+=128){ float a=0; const u16* kp=K+(size_t)j*4096+h*128; for(int d=0;d<128;d++)a+=qs[d]*bf2f(kp[d]); a*=0.08838834764831845f; s[j]=a; mx=fmaxf(mx,a);}
  red[t]=mx; __syncthreads();
  for(int o=64;o>0;o>>=1){ if(t<o)red[t]=fmaxf(red[t],red[t+o]); __syncthreads(); }
  mx=red[0]; __syncthreads();
  float sum=0; for(int j=t;j<=q;j+=128){ float e=__expf(s[j]-mx); s[j]=e; sum+=e; }
  red[t]=sum; __syncthreads();
  for(int o=64;o>0;o>>=1){ if(t<o)red[t]+=red[t+o]; __syncthreads(); }
  sum=red[0];
  float a=0; for(int j=0;j<=q;j++) a+=s[j]*bf2f(V[(size_t)j*4096+h*128+t]);
  O[(size_t)q*4096+h*128+t]=f2bf(a/sum);
}
__global__ void nln(const u16* Z,u16* Z2){
  int r=blockIdx.x,t=threadIdx.x; __shared__ float red[128];
  float v[7]; float s=0; for(int i=0;i<7;i++){v[i]=bf2f(Z[(size_t)r*1792+896+t+128*i]); s+=v[i];}
  red[t]=s; __syncthreads(); for(int o=64;o>0;o>>=1){ if(t<o)red[t]+=red[t+o]; __syncthreads(); }
  float m=red[0]/896.f; __syncthreads();
  float q=0; for(int i=0;i<7;i++){float d=v[i]-m;q+=d*d;}
  red[t]=q; __syncthreads(); for(int o=64;o>0;o>>=1){ if(t<o)red[t]+=red[t+o]; __syncthreads(); }
  float rs=1.f/sqrtf(red[0]/896.f+1e-5f);
  for(int i=0;i<7;i++)Z2[(size_t)r*896+t+128*i]=f2bf((v[i]-m)*rs);
}
__global__ void nmul(const u16* Z,const u16* G,const u16* bsp,u16* U){
  int i=blockIdx.x*256+threadIdx.x; int r=i/896,c=i%896;
  float g=bf2f(f2bf(bf2f(G[i])+bf2f(bsp[r])));
  U[i]=f2bf(bf2f(Z[(size_t)r*1792+c])*g);
}
__global__ void nfinal(const u16* x,const u16* gw,const u16* a,const u16* b,u16* out){
  int r=blockIdx.x,t=threadIdx.x; __shared__ float red[128];
  float s=0; for(int i=0;i<4;i++)s+=bf2f(x[r*512+t+128*i])*bf2f(gw[t+128*i]);
  red[t]=s; __syncthreads(); for(int o=64;o>0;o>>=1){ if(t<o)red[t]+=red[t+o]; __syncthreads(); }
  float g=1.f/(1.f+__expf(-red[0]));
  for(int i=0;i<4;i++){int c=r*512+t+128*i; out[c]=f2bf(g*bf2f(a[c])+(1.f-g)*bf2f(b[c]));}
}
extern "C" void kernel_launch(void* const* d_in, const int* in_sizes, int n_in, void* d_out, int out_size, void* d_ws, size_t ws_size, hipStream_t stream){
  const u16* x=(const u16*)d_in[0]; const u16* ckv=(const u16*)d_in[1]; const u16* wq=(const u16*)d_in[2]; const u16* wuk=(const u16*)d_in[3];
  const u16* wuv=(const u16*)d_in[4]; const u16* wo=(const u16*)d_in[5]; const u16* win=(const u16*)d_in[7]; const u16* wsp=(const u16*)d_in[8];
  const u16* bsp=(const u16*)d_in[9]; const u16* wout=(const u16*)d_in[10]; const u16* gw=(const u16*)d_in[11];
  char* ws=(char*)d_ws; size_t MB=1<<20;
  u16* Q=(u16*)(ws); u16* K=(u16*)(ws+16*MB); u16* V=(u16*)(ws+32*MB); u16* O=(u16*)(ws+48*MB);
  u16* Z=(u16*)(ws+64*MB); u16* Z2=(u16*)(ws+72*MB); u16* G=(u16*)(ws+80*MB); u16* U=(u16*)(ws+88*MB); u16* A=(u16*)(ws+96*MB); u16* Bo=(u16*)(ws+100*MB);
  ngemm<0><<<dim3(64,32),256,0,stream>>>(x,512,wq,4096,Q,4096,512,1.f);
  ngemm<0><<<dim3(64,32),256,0,stream>>>(ckv,512,wuk,4096,K,4096,512,1.f);
  ngemm<0><<<dim3(64,32),256,0,stream>>>(ckv,512,wuv,4096,V,4096,512,1.f);
  nattn<<<dim3(2048,32),128,0,stream>>>(Q,K,V,O);
  ngemm<0><<<dim3(8,32),256,0,stream>>>(O,4096,wo,512,A,512,4096,1.f);
  ngemm<1><<<dim3(28,32),256,0,stream>>>(x,512,win,1792,Z,1792,512,1.f);
  nln<<<2048,128,0,stream>>>(Z,Z2);
  ngemm<0><<<dim3(14,32),256,0,stream>>>(wsp,2048,Z2,896,G,896,2048,1.f);
  nmul<<<2048*896/256,256,0,stream>>>(Z,G,bsp,U);
  ngemm<0><<<dim3(8,32),256,0,stream>>>(U,896,wout,512,Bo,512,896,1.f);
  nfinal<<<2048,128,0,stream>>>(x,gw,A,Bo,(u16*)d_out);
}
```

```cpp
#include <hip/hip_runtime.h>

extern "C" __global__ __launch_bounds__(256,1) void k_fused(const void* x,const void* ckv,const void* wq,const void* wuk,const void* wuv,const void* wo,const void* win,const void* wsp,const void* bsp,const void* wout,const void* gw,void* out,void* ws){
  unsigned tid=threadIdx.x;
  asm volatile(
    "s_load_dwordx16 s[72:87], %1, 0\n"
    "s_load_dwordx8 s[88:95], %1, 64\n"
    "s_load_dwordx2 s[96:97], %1, 96\n"
    "s_mov_b32 s18, %2\n"
    "v_and_b32 v1, 63, %0\n"
    "v_lshrrev_b32 v2, 6, %0\n"
    "s_nop 0\n"
    "v_readfirstlane_b32 s19, v2\n"
    "v_lshlrev_b32 v2, 4, v1\n"
    "s_and_b32 s22, s18, 7\n"
    "s_lshr_b32 s23, s18, 3\n"
    "s_and_b32 s21, s23, 7\n"
    "s_lshr_b32 s23, s23, 3\n"
    "s_lshl_b32 s20, s22, 2\n"
    "s_add_u32 s20, s20, s23\n"
    "s_lshl_b32 s16, s19, 12\n"
    "s_lshl_b32 s17, s19, 13\n"
    "s_getpc_b64 s[64:65]\n"
    "PCB_%=:\n"
    "s_mov_b32 s66, END_%= - PCB_%=\n"
    "s_mov_b32 s67, 0x20000\n"
    "s_lshr_b32 s22, s18, 3\n"
    "s_lshl_b32 s22, s22, 12\n"
    "s_cmp_eq_u32 s19, 3\n"
    "s_cselect_b32 s22, s22, 0x7ff00000\n"
    "v_lshlrev_b32 v60, 6, v1\n"
    "buffer_load_dword v61, v60, s[64:67], s22 offen\n"
    "s_and_b32 s22, s18, 7\n"
    "s_lshr_b32 s23, s18, 3\n"
    "s_mul_i32 s59, s23, 37\n"
    "s_lshr_b32 s59, s59, 8\n"
    "s_mul_i32 s34, s59, 7\n"
    "s_sub_u32 s57, s23, s34\n"
    "s_lshr_b32 s56, s22, 1\n"
    "s_lshl_b32 s56, s56, 2\n"
    "s_add_u32 s56, s56, s59\n"
    "s_and_b32 s58, s22, 1\n"
    "v_lshrrev_b32 v20, 3, v1\n"
    "v_lshlrev_b32 v20, 10, v20\n"
    "v_lshrrev_b32 v21, 4, v1\n"
    "v_and_b32 v22, 7, v1\n"
    "v_xor_b32 v21, v21, v22\n"
    "v_lshlrev_b32 v21, 4, v21\n"
    "v_xor_b32 v22, 0, v21\n"
    "v_add_u32 v22, v22, v20\n"
    "v_add_u32 v4, 0, v22\n"
    "v_xor_b32 v22, 64, v21\n"
    "v_add_u32 v22, v22, v20\n"
    "v_add_u32 v5, 0x1c00, v22\n"
    "v_xor_b32 v22, 0, v21\n"
    "v_add_u32 v22, v22, v20\n"
    "v_add_u32 v6, 0x3800, v22\n"
    "v_xor_b32 v22, 64, v21\n"
    "v_add_u32 v22, v22, v20\n"
    "v_add_u32 v7, 0x5400, v22\n"
    "v_bfe_u32 v20, v1, 2, 2\n"
    "v_mul_u32_u24 v20, 0xe00, v20\n"
    "v_lshrrev_b32 v21, 4, v1\n"
    "v_lshl_add_u32 v20, v21, 6, v20\n"
    "v_and_b32 v21, 3, v1\n"
    "v_lshlrev_b32 v21, 4, v21\n"
    "v_xor_b32 v22, 0, v21\n"
    "v_add_u32 v22, v22, v20\n"
    "v_add_u32 v12, 0, v22\n"
    "v_xor_b32 v22, 0, v21\n"
    "v_add_u32 v22, v22, v20\n"
    "v_add_u32 v13, 0x3400, v22\n"
    "v_xor_b32 v22, 32, v21\n"
    "v_add_u32 v22, v22, v20\n"
    "v_add_u32 v14, 0x6800, v22\n"
    "v_xor_b32 v22, 32, v21\n"
    "v_add_u32 v22, v22, v20\n"
    "v_add_u32 v15, 0x9c00, v22\n"
    "s_lshl_b32 s60, s56, 17\n"
    "s_lshl_b32 s22, s19, 15\n"
    "s_add_u32 s60, s60, s22\n"
    "s_mul_i32 s61, s19, 0xe000\n"
    "s_mul_i32 s22, s58, 0x700\n"
    "s_add_u32 s61, s61, s22\n"
    "s_lshl_b32 s22, s57, 8\n"
    "s_add_u32 s61, s61, s22\n"
    "s_lshl_b32 s6, s59, 1\n"
    "s_add_u32 s6, s6, s57\n"
    "s_and_b32 s6, s6, 7\n"
    "v_mov_b32 v50, 0\n"
    "s_waitcnt lgkmcnt(0)\n"
    "s_mov_b64 s[24:25], s[72:73]\n"
    "s_mov_b32 s26, 0x200000\n"
    "s_mov_b32 s27, 0x20000\n"
    "s_mov_b64 s[28:29], s[84:85]\n"
    "s_mov_b32 s30, 0x1c0000\n"
    "s_mov_b32 s31, 0x20000\n"
    "s_mov_b64 s[44:45], s[96:97]\n"
    "s_mov_b32 s46, 0x10000000\n"
    "s_mov_b32 s47, 0x20000\n"
    "s_cmp_eq_u32 s59, 4\n"
    "s_cbranch_scc1 GATE_%=\n"
    "s_mul_i32 s7, s6, 0x38000\n"
    "s_add_u32 s7, s7, s61\n"
    "s_add_u32 m0, s16, 0xc000\n"
    "s_nop 0\n"
    "buffer_load_dwordx4 v12, s[28:31], s7 offen lds\n"
    "buffer_load_dwordx4 v13, s[28:31], s7 offen offset:1024 lds\n"
    "buffer_load_dwordx4 v14, s[28:31], s7 offen offset:2048 lds\n"
    "buffer_load_dwordx4 v15, s[28:31], s7 offen offset:3072 lds\n"
    "s_lshl_b32 s7, s6, 7\n"
    "s_add_u32 s7, s7, s60\n"
    "s_add_u32 m0, s16, 0x8000\n"
    "s_nop 0\n"
    "buffer_load_dwordx4 v4, s[24:27], s7 offen lds\n"
    "buffer_load_dwordx4 v5, s[24:27], s7 offen offset:1024 lds\n"
    "buffer_load_dwordx4 v6, s[24:27], s7 offen offset:2048 lds\n"
    "buffer_load_dwordx4 v7, s[24:27], s7 offen offset:3072 lds\n"
    "s_add_u32 s6, s6, 1\n"
    "s_and_b32 s6, s6, 7\n"
    "s_mul_i32 s7, s6, 0x38000\n"
    "s_add_u32 s7, s7, s61\n"
    "s_add_u32 m0, s16, 0x14000\n"
    "s_nop 0\n"
    "buffer_load_dwordx4 v12, s[28:31], s7 offen lds\n"
    "buffer_load_dwordx4 v13, s[28:31], s7 offen offset:1024 lds\n"
    "buffer_load_dwordx4 v14, s[28:31], s7 offen offset:2048 lds\n"
    "buffer_load_dwordx4 v15, s[28:31], s7 offen offset:3072 lds\n"
    "s_lshl_b32 s7, s6, 7\n"
    "s_add_u32 s7, s7, s60\n"
    "s_add_u32 m0, s16, 0x10000\n"
    "s_nop 0\n"
    "buffer_load_dwordx4 v4, s[24:27], s7 offen lds\n"
    "buffer_load_dwordx4 v5, s[24:27], s7 offen offset:1024 lds\n"
    "buffer_load_dwordx4 v6, s[24:27], s7 offen offset:2048 lds\n"
    "buffer_load_dwordx4 v7, s[24:27], s7 offen offset:3072 lds\n"
    "s_add_u32 s6, s6, 1\n"
    "s_and_b32 s6, s6, 7\n"
    "s_and_b32 s22, s19, 1\n"
    "s_lshl_b32 s22, s22, 13\n"
    "s_add_u32 s22, s22, 0x8000\n"
    "v_and_b32 v20, 15, v1\n"
    "v_lshrrev_b32 v21, 1, v20\n"
    "v_lshrrev_b32 v22, 4, v1\n"
    "v_xor_b32 v21, v21, v22\n"
    "v_lshlrev_b32 v21, 4, v21\n"
    "v_lshlrev_b32 v20, 7, v20\n"
    "v_add_u32 v20, v20, v21\n"
    "v_add_u32 v16, s22, v20\n"
    "v_xor_b32 v17, 64, v16\n"
    "s_lshr_b32 s22, s19, 1\n"
    "s_lshl_b32 s22, s22, 9\n"
    "s_add_u32 s22, s22, 0xc000\n"
    "v_lshrrev_b32 v20, 4, v1\n"
    "v_lshlrev_b32 v21, 11, v20\n"
    "v_and_b32 v20, 1, v20\n"
    "v_lshl_add_u32 v21, v20, 5, v21\n"
    "v_bfe_u32 v20, v1, 2, 2\n"
    "v_lshl_add_u32 v21, v20, 6, v21\n"
    "v_and_b32 v20, 3, v1\n"
    "v_lshl_add_u32 v21, v20, 3, v21\n"
    "v_add_u32 v18, s22, v21\n"
    "v_xor_b32 v19, 32, v18\n"
    "buffer_load_dwordx2 v[48:49], v50, s[44:47], 0 offen sc1\n"
    "v_bfe_u32 v20, v1, 2, 2\n"
    "v_mul_u32_u24 v20, 0x2000, v20\n"
    "v_lshrrev_b32 v21, 4, v1\n"
    "v_lshl_add_u32 v20, v21, 6, v20\n"
    "v_and_b32 v21, 3, v1\n"
    "v_lshlrev_b32 v21, 4, v21\n"
    "v_xor_b32 v22, 0, v21\n"
    "v_add_u32 v22, v22, v20\n"
    "v_add_u32 v8, 0, v22\n"
    "v_xor_b32 v22, 0, v21\n"
    "v_add_u32 v22, v22, v20\n"
    "v_add_u32 v9, 0x7c00, v22\n"
    "v_xor_b32 v22, 32, v21\n"
    "v_add_u32 v22, v22, v20\n"
    "v_add_u32 v10, 0xf800, v22\n"
    "v_xor_b32 v22, 32, v21\n"
    "v_add_u32 v22, v22, v20\n"
    "v_add_u32 v11, 0x17400, v22\n"
    "s_mov_b64 s[36:37], s[74:75]\n"
    "s_mov_b32 s38, 0x200000\n"
    "s_mov_b32 s39, 0x20000\n"
    "s_mov_b64 s[40:41], s[78:79]\n"
    "s_mov_b32 s42, 0x400000\n"
    "s_mov_b32 s43, 0x20000\n"
    "s_mov_b64 s[64:65], s[80:81]\n"
    "s_mov_b32 s66, 0x400000\n"
    "s_mov_b32 s67, 0x20000\n"
    "s_lshl_b32 s53, s21, 18\n"
    "s_lshl_b32 s22, s19, 16\n"
    "s_add_u32 s53, s53, s22\n"
    "s_lshl_b32 s54, s19, 17\n"
    "s_lshl_b32 s22, s20, 8\n"
    "s_add_u32 s54, s54, s22\n"
    "s_lshl_b32 s22, s57, 3\n"
    "s_lshr_b32 s23, s19, 1\n"
    "s_lshl_b32 s23, s23, 2\n"
    "s_add_u32 s22, s22, s23\n"
    "s_lshl_b32 s62, s56, 3\n"
    "s_and_b32 s23, s19, 1\n"
    "s_lshl_b32 s23, s23, 2\n"
    "s_add_u32 s62, s62, s23\n"
    "s_lshl_b32 s12, s62, 7\n"
    "s_add_u32 s12, s12, 0x5000000\n"
    "s_mul_i32 s62, s62, 0x7000\n"
    "s_add_u32 s23, s58, 9\n"
    "s_lshl_b32 s22, s22, s23\n"
    "s_add_u32 s62, s62, s22\n"
    "s_mul_i32 s22, s58, 0x800000\n"
    "s_add_u32 s62, s62, s22\n"
    "s_add_u32 s8, s62, 0x4000000\n"
    "s_add_u32 s10, s8, 0xe000\n"
    "s_mov_b32 s22, 0x7000\n"
    "s_cmp_eq_u32 s58, 1\n"
    "s_cselect_b32 s22, 0x100, s22\n"
    "s_cselect_b64 vcc, -1, 0\n"
    "s_add_u32 s9, s8, s22\n"
    "s_add_u32 s11, s10, s22\n"
    "v_bfe_u32 v20, v1, 2, 1\n"
    "v_lshlrev_b32 v20, 9, v20\n"
    "v_bfe_u32 v21, v1, 3, 1\n"
    "v_lshl_add_u32 v20, v21, 7, v20\n"
    "v_and_b32 v21, 3, v1\n"
    "v_lshl_add_u32 v20, v21, 5, v20\n"
    "v_bfe_u32 v22, v1, 4, 1\n"
    "v_lshl_add_u32 v20, v22, 10, v20\n"
    "v_and_b32 v21, 15, v1\n"
    "v_lshlrev_b32 v21, 5, v21\n"
    "v_lshl_add_u32 v21, v22, 9, v21\n"
    "v_cndmask_b32 v20, v21, v20, vcc\n"
    "v_lshrrev_b32 v21, 5, v1\n"
    "v_lshl_add_u32 v56, v21, 4, v20\n"
    "s_lshl_b32 s22, 0x400, s58\n"
    "v_add_u32 v57, s22, v56\n"
    "v_mov_b32 v24, 0xc0135761\n"
    "v_mov_b32 v25, 0xc0135761\n"
    "v_mov_b32 v26, 0xbdd2d3e8\n"
    "v_mov_b32 v27, 0xbdd2d3e8\n"
    "v_mov_b32 v28, 1.0\n"
    "v_mov_b32 v29, 1.0\n"
    "s_waitcnt vmcnt(9)\n"
    "s_barrier\n"
    "ds_read_b128 a[16:19], v16\n"
    "ds_read_b128 a[20:23], v16 offset:2048\n"
    "ds_read_b128 a[24:27], v16 offset:4096\n"
    "ds_read_b128 a[28:31], v16 offset:6144\n"
    "ds_read_b64_tr_b16 a[0:1], v18\n"
    "ds_read_b64_tr_b16 a[2:3], v18 offset:1024\n"
    "ds_read_b64_tr_b16 a[4:5], v19\n"
    "ds_read_b64_tr_b16 a[6:7], v19 offset:1024\n"
    "ds_read_b64_tr_b16 a[8:9], v18 offset:256\n"
    "ds_read_b64_tr_b16 a[10:11], v18 offset:1280\n"
    "ds_read_b64_tr_b16 a[12:13], v19 offset:256\n"
    "ds_read_b64_tr_b16 a[14:15], v19 offset:1280\n"
    "s_waitcnt lgkmcnt(4)\n"
    "v_mfma_f32_16x16x32_bf16 v[192:195], a[0:3], a[16:19], 0\n"
    "ds_read_b128 a[48:51], v17\n"
    "s_mul_i32 s7, s6, 0x38000\n"
    "v_mfma_f32_16x16x32_bf16 v[196:199], a[0:3], a[20:23], 0\n"
    "ds_read_b128 a[52:55], v17 offset:2048\n"
    "s_add_u32 s7, s7, s61\n"
    "v_mfma_f32_16x16x32_bf16 v[200:203], a[0:3], a[24:27], 0\n"
    "ds_read_b128 a[56:59], v17 offset:4096\n"
    "s_add_u32 m0, s16, 0x1c000\n"
    "v_mfma_f32_16x16x32_bf16 v[204:207], a[0:3], a[28:31], 0\n"
    "buffer_load_dwordx4 v12, s[28:31], s7 offen lds\n"
    "v_mfma_f32_16x16x32_bf16 v[208:211], a[4:7], a[16:19], 0\n"
    "ds_read_b128 a[60:63], v17 offset:6144\n"
    "v_mfma_f32_16x16x32_bf16 v[212:215], a[4:7], a[20:23], 0\n"
    "ds_read_b64_tr_b16 a[32:33], v18 offset:8192\n"
    "v_mfma_f32_16x16x32_bf16 v[216:219], a[4:7], a[24:27], 0\n"
    "ds_read_b64_tr_b16 a[34:35], v18 offset:9216\n"
    "v_mfma_f32_16x16x32_bf16 v[220:223], a[4:7], a[28:31], 0\n"
    "buffer_load_dwordx4 v13, s[28:31], s7 offen offset:1024 lds\n"
    "s_waitcnt lgkmcnt(6)\n"
    "v_mfma_f32_16x16x32_bf16 v[224:227], a[8:11], a[16:19], 0\n"
    "ds_read_b64_tr_b16 a[36:37], v19 offset:8192\n"
    "v_mfma_f32_16x16x32_bf16 v[228:231], a[8:11], a[20:23], 0\n"
    "ds_read_b64_tr_b16 a[38:39], v19 offset:9216\n"
    "v_mfma_f32_16x16x32_bf16 v[232:235], a[8:11], a[24:27], 0\n"
    "ds_read_b64_tr_b16 a[40:41], v18 offset:8448\n"
    "v_mfma_f32_16x16x32_bf16 v[236:239], a[8:11], a[28:31], 0\n"
    "buffer_load_dwordx4 v14, s[28:31], s7 offen offset:2048 lds\n"
    "v_mfma_f32_16x16x32_bf16 v[240:243], a[12:15], a[16:19], 0\n"
    "ds_read_b64_tr_b16 a[42:43], v18 offset:9472\n"
    "v_mfma_f32_16x16x32_bf16 v[244:247], a[12:15], a[20:23], 0\n"
    "ds_read_b64_tr_b16 a[44:45], v19 offset:8448\n"
    "v_mfma_f32_16x16x32_bf16 v[248:251], a[12:15], a[24:27], 0\n"
    "ds_read_b64_tr_b16 a[46:47], v19 offset:9472\n"
    "v_mfma_f32_16x16x32_bf16 v[252:255], a[12:15], a[28:31], 0\n"
    "buffer_load_dwordx4 v15, s[28:31], s7 offen offset:3072 lds\n"
    "s_lshl_b32 s7, s6, 7\n"
    "s_add_u32 s7, s7, s60\n"
    "s_add_u32 m0, s16, 0x18000\n"
    "s_nop 0\n"
    "buffer_load_dwordx4 v4, s[24:27], s7 offen lds\n"
    "buffer_load_dwordx4 v5, s[24:27], s7 offen offset:1024 lds\n"
    "buffer_load_dwordx4 v6, s[24:27], s7 offen offset:2048 lds\n"
    "buffer_load_dwordx4 v7, s[24:27], s7 offen offset:3072 lds\n"
    "s_add_u32 s6, s6, 1\n"
    "s_and_b32 s6, s6, 7\n"
    "s_waitcnt vmcnt(9)\n"
    "s_barrier\n"
    "s_waitcnt lgkmcnt(4)\n"
    "v_mfma_f32_16x16x32_bf16 v[192:195], a[32:35], a[48:51], v[192:195]\n"
    "ds_read_b128 a[16:19], v16 offset:32768\n"
    "s_mul_i32 s7, s6, 0x38000\n"
    "v_mfma_f32_16x16x32_bf16 v[196:199], a[32:35], a[52:55], v[196:199]\n"
    "ds_read_b128 a[20:23], v16 offset:34816\n"
    "s_add_u32 s7, s7, s61\n"
    "v_mfma_f32_16x16x32_bf16 v[200:203], a[32:35], a[56:59], v[200:203]\n"
    "ds_read_b128 a[24:27], v16 offset:36864\n"
    "s_add_u32 m0, s16, 0x24000\n"
    "v_mfma_f32_16x16x32_bf16 v[204:207], a[32:35], a[60:63], v[204:207]\n"
    "buffer_load_dwordx4 v12, s[28:31], s7 offen lds\n"
    "v_mfma_f32_16x16x32_bf16 v[208:211], a[36:39], a[48:51], v[208:211]\n"
    "ds_read_b128 a[28:31], v16 offset:38912\n"
    "v_mfma_f32_16x16x32_bf16 v[212:215], a[36:39], a[52:55], v[212:215]\n"
    "ds_read_b64_tr_b16 a[0:1], v18 offset:32768\n"
    "v_mfma_f32_16x16x32_bf16 v[216:219], a[36:39], a[56:59], v[216:219]\n"
    "ds_read_b64_tr_b16 a[2:3], v18 offset:33792\n"
    "v_mfma_f32_16x16x32_bf16 v[220:223], a[36:39], a[60:63], v[220:223]\n"
    "buffer_load_dwordx4 v13, s[28:31], s7 offen offset:1024 lds\n"
    "s_waitcnt lgkmcnt(6)\n"
    "v_mfma_f32_16x16x32_bf16 v[224:227], a[40:43], a[48:51], v[224:227]\n"
    "ds_read_b64_tr_b16 a[4:5], v19 offset:32768\n"
    "v_mfma_f32_16x16x32_bf16 v[228:231], a[40:43], a[52:55], v[228:231]\n"
    "ds_read_b64_tr_b16 a[6:7], v19 offset:33792\n"
    "v_mfma_f32_16x16x32_bf16 v[232:235], a[40:43], a[56:59], v[232:235]\n"
    "ds_read_b64_tr_b16 a[8:9], v18 offset:33024\n"
    "v_mfma_f32_16x16x32_bf16 v[236:239], a[40:43], a[60:63], v[236:239]\n"
    "buffer_load_dwordx4 v14, s[28:31], s7 offen offset:2048 lds\n"
    "v_mfma_f32_16x16x32_bf16 v[240:243], a[44:47], a[48:51], v[240:243]\n"
    "ds_read_b64_tr_b16 a[10:11], v18 offset:34048\n"
    "v_mfma_f32_16x16x32_bf16 v[244:247], a[44:47], a[52:55], v[244:247]\n"
    "ds_read_b64_tr_b16 a[12:13], v19 offset:33024\n"
    "v_mfma_f32_16x16x32_bf16 v[248:251], a[44:47], a[56:59], v[248:251]\n"
    "ds_read_b64_tr_b16 a[14:15], v19 offset:34048\n"
    "v_mfma_f32_16x16x32_bf16 v[252:255], a[44:47], a[60:63], v[252:255]\n"
    "buffer_load_dwordx4 v15, s[28:31], s7 offen offset:3072 lds\n"
    "s_waitcnt lgkmcnt(4)\n"
    "v_mfma_f32_16x16x32_bf16 v[192:195], a[0:3], a[16:19], v[192:195]\n"
    "ds_read_b128 a[48:51], v17 offset:32768\n"
    "s_lshl_b32 s7, s6, 7\n"
    "v_mfma_f32_16x16x32_bf16 v[196:199], a[0:3], a[20:23], v[196:199]\n"
    "ds_read_b128 a[52:55], v17 offset:34816\n"
    "s_add_u32 s7, s7, s60\n"
    "v_mfma_f32_16x16x32_bf16 v[200:203], a[0:3], a[24:27], v[200:203]\n"
    "ds_read_b128 a[56:59], v17 offset:36864\n"
    "s_add_u32 m0, s16, 0x20000\n"
    "v_mfma_f32_16x16x32_bf16 v[204:207], a[0:3], a[28:31], v[204:207]\n"
    "buffer_load_dwordx4 v4, s[24:27], s7 offen lds\n"
    "v_mfma_f32_16x16x32_bf16 v[208:211], a[4:7], a[16:19], v[208:211]\n"
    "ds_read_b128 a[60:63], v17 offset:38912\n"
    "v_mfma_f32_16x16x32_bf16 v[212:215], a[4:7], a[20:23], v[212:215]\n"
    "ds_read_b64_tr_b16 a[32:33], v18 offset:40960\n"
    "v_mfma_f32_16x16x32_bf16 v[216:219], a[4:7], a[24:27], v[216:219]\n"
    "ds_read_b64_tr_b16 a[34:35], v18 offset:41984\n"
    "v_mfma_f32_16x16x32_bf16 v[220:223], a[4:7], a[28:31], v[220:223]\n"
    "buffer_load_dwordx4 v5, s[24:27], s7 offen offset:1024 lds\n"
    "s_waitcnt lgkmcnt(6)\n"
    "v_mfma_f32_16x16x32_bf16 v[224:227], a[8:11], a[16:19], v[224:227]\n"
    "ds_read_b64_tr_b16 a[36:37], v19 offset:40960\n"
    "v_mfma_f32_16x16x32_bf16 v[228:231], a[8:11], a[20:23], v[228:231]\n"
    "ds_read_b64_tr_b16 a[38:39], v19 offset:41984\n"
    "v_mfma_f32_16x16x32_bf16 v[232:235], a[8:11], a[24:27], v[232:235]\n"
    "ds_read_b64_tr_b16 a[40:41], v18 offset:41216\n"
    "v_mfma_f32_16x16x32_bf16 v[236:239], a[8:11], a[28:31], v[236:239]\n"
    "buffer_load_dwordx4 v6, s[24:27], s7 offen offset:2048 lds\n"
    "v_mfma_f32_16x16x32_bf16 v[240:243], a[12:15], a[16:19], v[240:243]\n"
    "ds_read_b64_tr_b16 a[42:43], v18 offset:42240\n"
    "v_mfma_f32_16x16x32_bf16 v[244:247], a[12:15], a[20:23], v[244:247]\n"
    "ds_read_b64_tr_b16 a[44:45], v19 offset:41216\n"
    "v_mfma_f32_16x16x32_bf16 v[248:251], a[12:15], a[24:27], v[248:251]\n"
    "ds_read_b64_tr_b16 a[46:47], v19 offset:42240\n"
    "v_add_u32 v16, 0x10000, v16\n"
    "v_add_u32 v17, 0x10000, v17\n"
    "v_add_u32 v18, 0x10000, v18\n"
    "v_add_u32 v19, 0x10000, v19\n"
    "v_mfma_f32_16x16x32_bf16 v[252:255], a[12:15], a[28:31], v[252:255]\n"
    "buffer_load_dwordx4 v7, s[24:27], s7 offen offset:3072 lds\n"
    "s_add_u32 s6, s6, 1\n"
    "s_and_b32 s6, s6, 7\n"
    "s_waitcnt vmcnt(8)\n"
    "s_barrier\n"
    "s_waitcnt lgkmcnt(4)\n"
    "v_mfma_f32_16x16x32_bf16 v[192:195], a[32:35], a[48:51], v[192:195]\n"
    "ds_read_b128 a[16:19], v16\n"
    "s_mul_i32 s7, s6, 0x38000\n"
    "v_mfma_f32_16x16x32_bf16 v[196:199], a[32:35], a[52:55], v[196:199]\n"
    "ds_read_b128 a[20:23], v16 offset:2048\n"
    "s_add_u32 s7, s7, s61\n"
    "v_mfma_f32_16x16x32_bf16 v[200:203], a[32:35], a[56:59], v[200:203]\n"
    "ds_read_b128 a[24:27], v16 offset:4096\n"
    "s_add_u32 m0, s16, 0xc000\n"
    "v_mfma_f32_16x16x32_bf16 v[204:207], a[32:35], a[60:63], v[204:207]\n"
    "buffer_load_dwordx4 v12, s[28:31], s7 offen lds\n"
    "v_mfma_f32_16x16x32_bf16 v[208:211], a[36:39], a[48:51], v[208:211]\n"
    "ds_read_b128 a[28:31], v16 offset:6144\n"
    "v_mfma_f32_16x16x32_bf16 v[212:215], a[36:39], a[52:55], v[212:215]\n"
    "ds_read_b64_tr_b16 a[0:1], v18\n"
    "v_mfma_f32_16x16x32_bf16 v[216:219], a[36:39], a[56:59], v[216:219]\n"
    "ds_read_b64_tr_b16 a[2:3], v18 offset:1024\n"
    "v_mfma_f32_16x16x32_bf16 v[220:223], a[36:39], a[60:63], v[220:223]\n"
    "buffer_load_dwordx4 v13, s[28:31], s7 offen offset:1024 lds\n"
    "s_waitcnt lgkmcnt(6)\n"
    "v_mfma_f32_16x16x32_bf16 v[224:227], a[40:43], a[48:51], v[224:227]\n"
    "ds_read_b64_tr_b16 a[4:5], v19\n"
    "v_mfma_f32_16x16x32_bf16 v[228:231], a[40:43], a[52:55], v[228:231]\n"
    "ds_read_b64_tr_b16 a[6:7], v19 offset:1024\n"
    "v_mfma_f32_16x16x32_bf16 v[232:235], a[40:43], a[56:59], v[232:235]\n"
    "ds_read_b64_tr_b16 a[8:9], v18 offset:256\n"
    "v_mfma_f32_16x16x32_bf16 v[236:239], a[40:43], a[60:63], v[236:239]\n"
    "buffer_load_dwordx4 v14, s[28:31], s7 offen offset:2048 lds\n"
    "v_mfma_f32_16x16x32_bf16 v[240:243], a[44:47], a[48:51], v[240:243]\n"
    "ds_read_b64_tr_b16 a[10:11], v18 offset:1280\n"
    "v_mfma_f32_16x16x32_bf16 v[244:247], a[44:47], a[52:55], v[244:247]\n"
    "ds_read_b64_tr_b16 a[12:13], v19 offset:256\n"
    "v_mfma_f32_16x16x32_bf16 v[248:251], a[44:47], a[56:59], v[248:251]\n"
    "ds_read_b64_tr_b16 a[14:15], v19 offset:1280\n"
    "v_mfma_f32_16x16x32_bf16 v[252:255], a[44:47], a[60:63], v[252:255]\n"
    "buffer_load_dwordx4 v15, s[28:31], s7 offen offset:3072 lds\n"
    "s_waitcnt lgkmcnt(4)\n"
    "v_mfma_f32_16x16x32_bf16 v[192:195], a[0:3], a[16:19], v[192:195]\n"
    "ds_read_b128 a[48:51], v17\n"
    "s_lshl_b32 s7, s6, 7\n"
    "v_mfma_f32_16x16x32_bf16 v[196:199], a[0:3], a[20:23], v[196:199]\n"
    "ds_read_b128 a[52:55], v17 offset:2048\n"
    "s_add_u32 s7, s7, s60\n"
    "v_mfma_f32_16x16x32_bf16 v[200:203], a[0:3], a[24:27], v[200:203]\n"
    "ds_read_b128 a[56:59], v17 offset:4096\n"
    "s_add_u32 m0, s16, 0x8000\n"
    "v_mfma_f32_16x16x32_bf16 v[204:207], a[0:3], a[28:31], v[204:207]\n"
    "buffer_load_dwordx4 v4, s[24:27], s7 offen lds\n"
    "v_mfma_f32_16x16x32_bf16 v[208:211], a[4:7], a[16:19], v[208:211]\n"
    "ds_read_b128 a[60:63], v17 offset:6144\n"
    "v_mfma_f32_16x16x32_bf16 v[212:215], a[4:7], a[20:23], v[212:215]\n"
    "ds_read_b64_tr_b16 a[32:33], v18 offset:8192\n"
    "v_mfma_f32_16x16x32_bf16 v[216:219], a[4:7], a[24:27], v[216:219]\n"
    "ds_read_b64_tr_b16 a[34:35], v18 offset:9216\n"
    "v_mfma_f32_16x16x32_bf16 v[220:223], a[4:7], a[28:31], v[220:223]\n"
    "buffer_load_dwordx4 v5, s[24:27], s7 offen offset:1024 lds\n"
    "s_waitcnt lgkmcnt(6)\n"
    "v_mfma_f32_16x16x32_bf16 v[224:227], a[8:11], a[16:19], v[224:227]\n"
    "ds_read_b64_tr_b16 a[36:37], v19 offset:8192\n"
    "v_mfma_f32_16x16x32_bf16 v[228:231], a[8:11], a[20:23], v[228:231]\n"
    "ds_read_b64_tr_b16 a[38:39], v19 offset:9216\n"
    "v_mfma_f32_16x16x32_bf16 v[232:235], a[8:11], a[24:27], v[232:235]\n"
    "ds_read_b64_tr_b16 a[40:41], v18 offset:8448\n"
    "v_mfma_f32_16x16x32_bf16 v[236:239], a[8:11], a[28:31], v[236:239]\n"
    "buffer_load_dwordx4 v6, s[24:27], s7 offen offset:2048 lds\n"
    "v_mfma_f32_16x16x32_bf16 v[240:243], a[12:15], a[16:19], v[240:243]\n"
    "ds_read_b64_tr_b16 a[42:43], v18 offset:9472\n"
    "v_mfma_f32_16x16x32_bf16 v[244:247], a[12:15], a[20:23], v[244:247]\n"
    "ds_read_b64_tr_b16 a[44:45], v19 offset:8448\n"
    "v_mfma_f32_16x16x32_bf16 v[248:251], a[12:15], a[24:27], v[248:251]\n"
    "ds_read_b64_tr_b16 a[46:47], v19 offset:9472\n"
    "v_mfma_f32_16x16x32_bf16 v[252:255], a[12:15], a[28:31], v[252:255]\n"
    "buffer_load_dwordx4 v7, s[24:27], s7 offen offset:3072 lds\n"
    "s_add_u32 s6, s6, 1\n"
    "s_and_b32 s6, s6, 7\n"
    "s_waitcnt vmcnt(8)\n"
    "s_barrier\n"
    "s_waitcnt lgkmcnt(4)\n"
    "v_mfma_f32_16x16x32_bf16 v[192:195], a[32:35], a[48:51], v[192:195]\n"
    "ds_read_b128 a[16:19], v16 offset:32768\n"
    "s_mul_i32 s7, s6, 0x38000\n"
    "v_mfma_f32_16x16x32_bf16 v[196:199], a[32:35], a[52:55], v[196:199]\n"
    "ds_read_b128 a[20:23], v16 offset:34816\n"
    "s_add_u32 s7, s7, s61\n"
    "v_mfma_f32_16x16x32_bf16 v[200:203], a[32:35], a[56:59], v[200:203]\n"
    "ds_read_b128 a[24:27], v16 offset:36864\n"
    "s_add_u32 m0, s16, 0x14000\n"
    "v_mfma_f32_16x16x32_bf16 v[204:207], a[32:35], a[60:63], v[204:207]\n"
    "buffer_load_dwordx4 v12, s[28:31], s7 offen lds\n"
    "v_mfma_f32_16x16x32_bf16 v[208:211], a[36:39], a[48:51], v[208:211]\n"
    "ds_read_b128 a[28:31], v16 offset:38912\n"
    "v_mfma_f32_16x16x32_bf16 v[212:215], a[36:39], a[52:55], v[212:215]\n"
    "ds_read_b64_tr_b16 a[0:1], v18 offset:32768\n"
    "v_mfma_f32_16x16x32_bf16 v[216:219], a[36:39], a[56:59], v[216:219]\n"
    "ds_read_b64_tr_b16 a[2:3], v18 offset:33792\n"
    "v_mfma_f32_16x16x32_bf16 v[220:223], a[36:39], a[60:63], v[220:223]\n"
    "buffer_load_dwordx4 v13, s[28:31], s7 offen offset:1024 lds\n"
    "s_waitcnt lgkmcnt(6)\n"
    "v_mfma_f32_16x16x32_bf16 v[224:227], a[40:43], a[48:51], v[224:227]\n"
    "ds_read_b64_tr_b16 a[4:5], v19 offset:32768\n"
    "v_mfma_f32_16x16x32_bf16 v[228:231], a[40:43], a[52:55], v[228:231]\n"
    "ds_read_b64_tr_b16 a[6:7], v19 offset:33792\n"
    "v_mfma_f32_16x16x32_bf16 v[232:235], a[40:43], a[56:59], v[232:235]\n"
    "ds_read_b64_tr_b16 a[8:9], v18 offset:33024\n"
    "v_mfma_f32_16x16x32_bf16 v[236:239], a[40:43], a[60:63], v[236:239]\n"
    "buffer_load_dwordx4 v14, s[28:31], s7 offen offset:2048 lds\n"
    "v_mfma_f32_16x16x32_bf16 v[240:243], a[44:47], a[48:51], v[240:243]\n"
    "ds_read_b64_tr_b16 a[10:11], v18 offset:34048\n"
    "v_mfma_f32_16x16x32_bf16 v[244:247], a[44:47], a[52:55], v[244:247]\n"
    "ds_read_b64_tr_b16 a[12:13], v19 offset:33024\n"
    "v_mfma_f32_16x16x32_bf16 v[248:251], a[44:47], a[56:59], v[248:251]\n"
    "ds_read_b64_tr_b16 a[14:15], v19 offset:34048\n"
    "v_mfma_f32_16x16x32_bf16 v[252:255], a[44:47], a[60:63], v[252:255]\n"
    "buffer_load_dwordx4 v15, s[28:31], s7 offen offset:3072 lds\n"
    "s_waitcnt lgkmcnt(4)\n"
    "v_mfma_f32_16x16x32_bf16 v[192:195], a[0:3], a[16:19], v[192:195]\n"
    "ds_read_b128 a[48:51], v17 offset:32768\n"
    "s_lshl_b32 s7, s6, 7\n"
    "v_mfma_f32_16x16x32_bf16 v[196:199], a[0:3], a[20:23], v[196:199]\n"
    "ds_read_b128 a[52:55], v17 offset:34816\n"
    "s_add_u32 s7, s7, s60\n"
    "v_mfma_f32_16x16x32_bf16 v[200:203], a[0:3], a[24:27], v[200:203]\n"
    "ds_read_b128 a[56:59], v17 offset:36864\n"
    "s_add_u32 m0, s16, 0x10000\n"
    "v_mfma_f32_16x16x32_bf16 v[204:207], a[0:3], a[28:31], v[204:207]\n"
    "buffer_load_dwordx4 v4, s[24:27], s7 offen lds\n"
    "v_mfma_f32_16x16x32_bf16 v[208:211], a[4:7], a[16:19], v[208:211]\n"
    "ds_read_b128 a[60:63], v17 offset:38912\n"
    "v_mfma_f32_16x16x32_bf16 v[212:215], a[4:7], a[20:23], v[212:215]\n"
    "ds_read_b64_tr_b16 a[32:33], v18 offset:40960\n"
    "v_mfma_f32_16x16x32_bf16 v[216:219], a[4:7], a[24:27], v[216:219]\n"
    "ds_read_b64_tr_b16 a[34:35], v18 offset:41984\n"
    "v_mfma_f32_16x16x32_bf16 v[220:223], a[4:7], a[28:31], v[220:223]\n"
    "buffer_load_dwordx4 v5, s[24:27], s7 offen offset:1024 lds\n"
    "s_waitcnt lgkmcnt(6)\n"
    "v_mfma_f32_16x16x32_bf16 v[224:227], a[8:11], a[16:19], v[224:227]\n"
    "ds_read_b64_tr_b16 a[36:37], v19 offset:40960\n"
    "v_mfma_f32_16x16x32_bf16 v[228:231], a[8:11], a[20:23], v[228:231]\n"
    "ds_read_b64_tr_b16 a[38:39], v19 offset:41984\n"
    "v_mfma_f32_16x16x32_bf16 v[232:235], a[8:11], a[24:27], v[232:235]\n"
    "ds_read_b64_tr_b16 a[40:41], v18 offset:41216\n"
    "v_mfma_f32_16x16x32_bf16 v[236:239], a[8:11], a[28:31], v[236:239]\n"
    "buffer_load_dwordx4 v6, s[24:27], s7 offen offset:2048 lds\n"
    "v_mfma_f32_16x16x32_bf16 v[240:243], a[12:15], a[16:19], v[240:243]\n"
    "ds_read_b64_tr_b16 a[42:43], v18 offset:42240\n"
    "v_mfma_f32_16x16x32_bf16 v[244:247], a[12:15], a[20:23], v[244:247]\n"
    "ds_read_b64_tr_b16 a[44:45], v19 offset:41216\n"
    "v_mfma_f32_16x16x32_bf16 v[248:251], a[12:15], a[24:27], v[248:251]\n"
    "ds_read_b64_tr_b16 a[46:47], v19 offset:42240\n"
    "v_subrev_u32 v16, 0x10000, v16\n"
    "v_subrev_u32 v17, 0x10000, v17\n"
    "v_subrev_u32 v18, 0x10000, v18\n"
    "v_subrev_u32 v19, 0x10000, v19\n"
    "v_mfma_f32_16x16x32_bf16 v[252:255], a[12:15], a[28:31], v[252:255]\n"
    "buffer_load_dwordx4 v7, s[24:27], s7 offen offset:3072 lds\n"
    "s_add_u32 s6, s6, 1\n"
    "s_and_b32 s6, s6, 7\n"
    "s_waitcnt vmcnt(8)\n"
    "s_barrier\n"
    "s_waitcnt lgkmcnt(4)\n"
    "v_mfma_f32_16x16x32_bf16 v[192:195], a[32:35], a[48:51], v[192:195]\n"
    "ds_read_b128 a[16:19], v16\n"
    "s_mul_i32 s7, s6, 0x38000\n"
    "v_mfma_f32_16x16x32_bf16 v[196:199], a[32:35], a[52:55], v[196:199]\n"
    "ds_read_b128 a[20:23], v16 offset:2048\n"
    "s_add_u32 s7, s7, s61\n"
    "v_mfma_f32_16x16x32_bf16 v[200:203], a[32:35], a[56:59], v[200:203]\n"
    "ds_read_b128 a[24:27], v16 offset:4096\n"
    "s_add_u32 m0, s16, 0x1c000\n"
    "v_mfma_f32_16x16x32_bf16 v[204:207], a[32:35], a[60:63], v[204:207]\n"
    "buffer_load_dwordx4 v12, s[28:31], s7 offen lds\n"
    "v_mfma_f32_16x16x32_bf16 v[208:211], a[36:39], a[48:51], v[208:211]\n"
    "ds_read_b128 a[28:31], v16 offset:6144\n"
    "v_mfma_f32_16x16x32_bf16 v[212:215], a[36:39], a[52:55], v[212:215]\n"
    "ds_read_b64_tr_b16 a[0:1], v18\n"
    "v_mfma_f32_16x16x32_bf16 v[216:219], a[36:39], a[56:59], v[216:219]\n"
    "ds_read_b64_tr_b16 a[2:3], v18 offset:1024\n"
    "v_mfma_f32_16x16x32_bf16 v[220:223], a[36:39], a[60:63], v[220:223]\n"
    "buffer_load_dwordx4 v13, s[28:31], s7 offen offset:1024 lds\n"
    "s_waitcnt lgkmcnt(6)\n"
    "v_mfma_f32_16x16x32_bf16 v[224:227], a[40:43], a[48:51], v[224:227]\n"
    "ds_read_b64_tr_b16 a[4:5], v19\n"
    "v_mfma_f32_16x16x32_bf16 v[228:231], a[40:43], a[52:55], v[228:231]\n"
    "ds_read_b64_tr_b16 a[6:7], v19 offset:1024\n"
    "v_mfma_f32_16x16x32_bf16 v[232:235], a[40:43], a[56:59], v[232:235]\n"
    "ds_read_b64_tr_b16 a[8:9], v18 offset:256\n"
    "v_mfma_f32_16x16x32_bf16 v[236:239], a[40:43], a[60:63], v[236:239]\n"
    "buffer_load_dwordx4 v14, s[28:31], s7 offen offset:2048 lds\n"
    "v_mfma_f32_16x16x32_bf16 v[240:243], a[44:47], a[48:51], v[240:243]\n"
    "ds_read_b64_tr_b16 a[10:11], v18 offset:1280\n"
    "v_mfma_f32_16x16x32_bf16 v[244:247], a[44:47], a[52:55], v[244:247]\n"
    "ds_read_b64_tr_b16 a[12:13], v19 offset:256\n"
    "v_mfma_f32_16x16x32_bf16 v[248:251], a[44:47], a[56:59], v[248:251]\n"
    "ds_read_b64_tr_b16 a[14:15], v19 offset:1280\n"
    "v_mfma_f32_16x16x32_bf16 v[252:255], a[44:47], a[60:63], v[252:255]\n"
    "buffer_load_dwordx4 v15, s[28:31], s7 offen offset:3072 lds\n"
    "s_waitcnt lgkmcnt(4)\n"
    "v_mfma_f32_16x16x32_bf16 v[192:195], a[0:3], a[16:19], v[192:195]\n"
    "ds_read_b128 a[48:51], v17\n"
    "s_lshl_b32 s7, s6, 7\n"
    "v_mfma_f32_16x16x32_bf16 v[196:199], a[0:3], a[20:23], v[196:199]\n"
    "ds_read_b128 a[52:55], v17 offset:2048\n"
    "s_add_u32 s7, s7, s60\n"
    "v_mfma_f32_16x16x32_bf16 v[200:203], a[0:3], a[24:27], v[200:203]\n"
    "ds_read_b128 a[56:59], v17 offset:4096\n"
    "s_add_u32 m0, s16, 0x18000\n"
    "v_mfma_f32_16x16x32_bf16 v[204:207], a[0:3], a[28:31], v[204:207]\n"
    "buffer_load_dwordx4 v4, s[24:27], s7 offen lds\n"
    "v_mfma_f32_16x16x32_bf16 v[208:211], a[4:7], a[16:19], v[208:211]\n"
    "ds_read_b128 a[60:63], v17 offset:6144\n"
    "v_mfma_f32_16x16x32_bf16 v[212:215], a[4:7], a[20:23], v[212:215]\n"
    "ds_read_b64_tr_b16 a[32:33], v18 offset:8192\n"
    "v_mfma_f32_16x16x32_bf16 v[216:219], a[4:7], a[24:27], v[216:219]\n"
    "ds_read_b64_tr_b16 a[34:35], v18 offset:9216\n"
    "v_mfma_f32_16x16x32_bf16 v[220:223], a[4:7], a[28:31], v[220:223]\n"
    "buffer_load_dwordx4 v5, s[24:27], s7 offen offset:1024 lds\n"
    "s_waitcnt lgkmcnt(6)\n"
    "v_mfma_f32_16x16x32_bf16 v[224:227], a[8:11], a[16:19], v[224:227]\n"
    "ds_read_b64_tr_b16 a[36:37], v19 offset:8192\n"
    "v_mfma_f32_16x16x32_bf16 v[228:231], a[8:11], a[20:23], v[228:231]\n"
    "ds_read_b64_tr_b16 a[38:39], v19 offset:9216\n"
    "v_mfma_f32_16x16x32_bf16 v[232:235], a[8:11], a[24:27], v[232:235]\n"
    "ds_read_b64_tr_b16 a[40:41], v18 offset:8448\n"
    "v_mfma_f32_16x16x32_bf16 v[236:239], a[8:11], a[28:31], v[236:239]\n"
    "buffer_load_dwordx4 v6, s[24:27], s7 offen offset:2048 lds\n"
    "v_mfma_f32_16x16x32_bf16 v[240:243], a[12:15], a[16:19], v[240:243]\n"
    "ds_read_b64_tr_b16 a[42:43], v18 offset:9472\n"
    "v_mfma_f32_16x16x32_bf16 v[244:247], a[12:15], a[20:23], v[244:247]\n"
    "ds_read_b64_tr_b16 a[44:45], v19 offset:8448\n"
    "v_mfma_f32_16x16x32_bf16 v[248:251], a[12:15], a[24:27], v[248:251]\n"
    "ds_read_b64_tr_b16 a[46:47], v19 offset:9472\n"
    "v_mfma_f32_16x16x32_bf16 v[252:255], a[12:15], a[28:31], v[252:255]\n"
    "buffer_load_dwordx4 v7, s[24:27], s7 offen offset:3072 lds\n"
    "s_add_u32 s6, s6, 1\n"
    "s_and_b32 s6, s6, 7\n"
    "s_waitcnt vmcnt(8)\n"
    "s_barrier\n"
    "s_waitcnt lgkmcnt(4)\n"
    "v_mfma_f32_16x16x32_bf16 v[192:195], a[32:35], a[48:51], v[192:195]\n"
    "ds_read_b128 a[16:19], v16 offset:32768\n"
    "s_mul_i32 s7, s6, 0x38000\n"
    "v_mfma_f32_16x16x32_bf16 v[196:199], a[32:35], a[52:55], v[196:199]\n"
    "ds_read_b128 a[20:23], v16 offset:34816\n"
    "s_add_u32 s7, s7, s61\n"
    "v_mfma_f32_16x16x32_bf16 v[200:203], a[32:35], a[56:59], v[200:203]\n"
    "ds_read_b128 a[24:27], v16 offset:36864\n"
    "s_add_u32 m0, s16, 0x24000\n"
    "v_mfma_f32_16x16x32_bf16 v[204:207], a[32:35], a[60:63], v[204:207]\n"
    "buffer_load_dwordx4 v12, s[28:31], s7 offen lds\n"
    "v_mfma_f32_16x16x32_bf16 v[208:211], a[36:39], a[48:51], v[208:211]\n"
    "ds_read_b128 a[28:31], v16 offset:38912\n"
    "v_mfma_f32_16x16x32_bf16 v[212:215], a[36:39], a[52:55], v[212:215]\n"
    "ds_read_b64_tr_b16 a[0:1], v18 offset:32768\n"
    "v_mfma_f32_16x16x32_bf16 v[216:219], a[36:39], a[56:59], v[216:219]\n"
    "ds_read_b64_tr_b16 a[2:3], v18 offset:33792\n"
    "v_mfma_f32_16x16x32_bf16 v[220:223], a[36:39], a[60:63], v[220:223]\n"
    "buffer_load_dwordx4 v13, s[28:31], s7 offen offset:1024 lds\n"
    "s_waitcnt lgkmcnt(6)\n"
    "v_mfma_f32_16x16x32_bf16 v[224:227], a[40:43], a[48:51], v[224:227]\n"
    "ds_read_b64_tr_b16 a[4:5], v19 offset:32768\n"
    "v_mfma_f32_16x16x32_bf16 v[228:231], a[40:43], a[52:55], v[228:231]\n"
    "ds_read_b64_tr_b16 a[6:7], v19 offset:33792\n"
    "v_mfma_f32_16x16x32_bf16 v[232:235], a[40:43], a[56:59], v[232:235]\n"
    "ds_read_b64_tr_b16 a[8:9], v18 offset:33024\n"
    "v_mfma_f32_16x16x32_bf16 v[236:239], a[40:43], a[60:63], v[236:239]\n"
    "buffer_load_dwordx4 v14, s[28:31], s7 offen offset:2048 lds\n"
    "v_mfma_f32_16x16x32_bf16 v[240:243], a[44:47], a[48:51], v[240:243]\n"
    "ds_read_b64_tr_b16 a[10:11], v18 offset:34048\n"
    "v_mfma_f32_16x16x32_bf16 v[244:247], a[44:47], a[52:55], v[244:247]\n"
    "ds_read_b64_tr_b16 a[12:13], v19 offset:33024\n"
    "v_mfma_f32_16x16x32_bf16 v[248:251], a[44:47], a[56:59], v[248:251]\n"
    "ds_read_b64_tr_b16 a[14:15], v19 offset:34048\n"
    "v_mfma_f32_16x16x32_bf16 v[252:255], a[44:47], a[60:63], v[252:255]\n"
    "buffer_load_dwordx4 v15, s[28:31], s7 offen offset:3072 lds\n"
    "s_waitcnt lgkmcnt(4)\n"
    "v_mfma_f32_16x16x32_bf16 v[192:195], a[0:3], a[16:19], v[192:195]\n"
    "ds_read_b128 a[48:51], v17 offset:32768\n"
    "s_lshl_b32 s7, s6, 7\n"
    "v_mfma_f32_16x16x32_bf16 v[196:199], a[0:3], a[20:23], v[196:199]\n"
    "ds_read_b128 a[52:55], v17 offset:34816\n"
    "s_add_u32 s7, s7, s60\n"
    "v_mfma_f32_16x16x32_bf16 v[200:203], a[0:3], a[24:27], v[200:203]\n"
    "ds_read_b128 a[56:59], v17 offset:36864\n"
    "s_add_u32 m0, s16, 0x20000\n"
    "v_mfma_f32_16x16x32_bf16 v[204:207], a[0:3], a[28:31], v[204:207]\n"
    "buffer_load_dwordx4 v4, s[24:27], s7 offen lds\n"
    "v_mfma_f32_16x16x32_bf16 v[208:211], a[4:7], a[16:19], v[208:211]\n"
    "ds_read_b128 a[60:63], v17 offset:38912\n"
    "v_mfma_f32_16x16x32_bf16 v[212:215], a[4:7], a[20:23], v[212:215]\n"
    "ds_read_b64_tr_b16 a[32:33], v18 offset:40960\n"
    "v_mfma_f32_16x16x32_bf16 v[216:219], a[4:7], a[24:27], v[216:219]\n"
    "ds_read_b64_tr_b16 a[34:35], v18 offset:41984\n"
    "v_mfma_f32_16x16x32_bf16 v[220:223], a[4:7], a[28:31], v[220:223]\n"
    "buffer_load_dwordx4 v5, s[24:27], s7 offen offset:1024 lds\n"
    "s_waitcnt lgkmcnt(6)\n"
    "v_mfma_f32_16x16x32_bf16 v[224:227], a[8:11], a[16:19], v[224:227]\n"
    "ds_read_b64_tr_b16 a[36:37], v19 offset:40960\n"
    "v_mfma_f32_16x16x32_bf16 v[228:231], a[8:11], a[20:23], v[228:231]\n"
    "ds_read_b64_tr_b16 a[38:39], v19 offset:41984\n"
    "v_mfma_f32_16x16x32_bf16 v[232:235], a[8:11], a[24:27], v[232:235]\n"
    "ds_read_b64_tr_b16 a[40:41], v18 offset:41216\n"
    "v_mfma_f32_16x16x32_bf16 v[236:239], a[8:11], a[28:31], v[236:239]\n"
    "buffer_load_dwordx4 v6, s[24:27], s7 offen offset:2048 lds\n"
    "v_mfma_f32_16x16x32_bf16 v[240:243], a[12:15], a[16:19], v[240:243]\n"
    "ds_read_b64_tr_b16 a[42:43], v18 offset:42240\n"
    "v_mfma_f32_16x16x32_bf16 v[244:247], a[12:15], a[20:23], v[244:247]\n"
    "ds_read_b64_tr_b16 a[44:45], v19 offset:41216\n"
    "v_mfma_f32_16x16x32_bf16 v[248:251], a[12:15], a[24:27], v[248:251]\n"
    "ds_read_b64_tr_b16 a[46:47], v19 offset:42240\n"
    "v_add_u32 v16, 0x10000, v16\n"
    "v_add_u32 v17, 0x10000, v17\n"
    "v_add_u32 v18, 0x10000, v18\n"
    "v_add_u32 v19, 0x10000, v19\n"
    "v_mfma_f32_16x16x32_bf16 v[252:255], a[12:15], a[28:31], v[252:255]\n"
    "buffer_load_dwordx4 v7, s[24:27], s7 offen offset:3072 lds\n"
    "s_add_u32 s6, s6, 1\n"
    "s_and_b32 s6, s6, 7\n"
    "s_waitcnt vmcnt(8)\n"
    "s_barrier\n"
    "s_waitcnt lgkmcnt(4)\n"
    "v_mfma_f32_16x16x32_bf16 v[192:195], a[32:35], a[48:51], v[192:195]\n"
    "ds_read_b128 a[16:19], v16\n"
    "v_mfma_f32_16x16x32_bf16 v[196:199], a[32:35], a[52:55], v[196:199]\n"
    "ds_read_b128 a[20:23], v16 offset:2048\n"
    "s_add_u32 s52, s53, 0\n"
    "v_mfma_f32_16x16x32_bf16 v[200:203], a[32:35], a[56:59], v[200:203]\n"
    "ds_read_b128 a[24:27], v16 offset:4096\n"
    "s_add_u32 m0, s17, 0\n"
    "v_mfma_f32_16x16x32_bf16 v[204:207], a[32:35], a[60:63], v[204:207]\n"
    "buffer_load_dwordx4 v4, s[36:39], s52 offen lds\n"
    "v_readfirstlane_b32 s4, v48\n"
    "v_mfma_f32_16x16x32_bf16 v[208:211], a[36:39], a[48:51], v[208:211]\n"
    "ds_read_b128 a[28:31], v16 offset:6144\n"
    "v_readfirstlane_b32 s5, v49\n"
    "s_nop 0\n"
    "v_mfma_f32_16x16x32_bf16 v[212:215], a[36:39], a[52:55], v[212:215]\n"
    "ds_read_b64_tr_b16 a[0:1], v18\n"
    "s_mul_i32 s4, s4, 0x19660d\n"
    "s_mul_i32 s5, s5, 0x15a4e35\n"
    "v_mfma_f32_16x16x32_bf16 v[216:219], a[36:39], a[56:59], v[216:219]\n"
    "ds_read_b64_tr_b16 a[2:3], v18 offset:1024\n"
    "s_add_u32 s4, s4, 0x3c6ef35f\n"
    "s_add_u32 s5, s5, 0x3039\n"
    "v_mfma_f32_16x16x32_bf16 v[220:223], a[36:39], a[60:63], v[220:223]\n"
    "buffer_load_dwordx4 v5, s[36:39], s52 offen offset:1024 lds\n"
    "s_lshr_b32 s22, s19, 1\n"
    "s_waitcnt lgkmcnt(6)\n"
    "v_mfma_f32_16x16x32_bf16 v[224:227], a[40:43], a[48:51], v[224:227]\n"
    "ds_read_b64_tr_b16 a[4:5], v19\n"
    "s_lshl_b32 s22, s22, 14\n"
    "v_and_b32 v20, 15, v1\n"
    "v_mfma_f32_16x16x32_bf16 v[228:231], a[40:43], a[52:55], v[228:231]\n"
    "ds_read_b64_tr_b16 a[6:7], v19 offset:1024\n"
    "v_lshrrev_b32 v21, 1, v20\n"
    "v_lshrrev_b32 v22, 4, v1\n"
    "v_mfma_f32_16x16x32_bf16 v[232:235], a[40:43], a[56:59], v[232:235]\n"
    "ds_read_b64_tr_b16 a[8:9], v18 offset:256\n"
    "v_xor_b32 v21, v21, v22\n"
    "v_lshlrev_b32 v21, 4, v21\n"
    "v_mfma_f32_16x16x32_bf16 v[236:239], a[40:43], a[60:63], v[236:239]\n"
    "buffer_load_dwordx4 v6, s[36:39], s52 offen offset:2048 lds\n"
    "v_lshlrev_b32 v20, 7, v20\n"
    "v_mfma_f32_16x16x32_bf16 v[240:243], a[44:47], a[48:51], v[240:243]\n"
    "ds_read_b64_tr_b16 a[10:11], v18 offset:1280\n"
    "v_add_u32 v20, v20, v21\n"
    "v_add_u32 v12, s22, v20\n"
    "v_mfma_f32_16x16x32_bf16 v[244:247], a[44:47], a[52:55], v[244:247]\n"
    "ds_read_b64_tr_b16 a[12:13], v19 offset:256\n"
    "v_xor_b32 v13, 64, v12\n"
    "s_and_b32 s22, s19, 1\n"
    "v_mfma_f32_16x16x32_bf16 v[248:251], a[44:47], a[56:59], v[248:251]\n"
    "ds_read_b64_tr_b16 a[14:15], v19 offset:1280\n"
    "s_lshl_b32 s22, s22, 9\n"
    "s_add_u32 s22, s22, 0x8000\n"
    "v_mfma_f32_16x16x32_bf16 v[252:255], a[44:47], a[60:63], v[252:255]\n"
    "buffer_load_dwordx4 v7, s[36:39], s52 offen offset:3072 lds\n"
    "v_lshrrev_b32 v20, 4, v1\n"
    "s_waitcnt lgkmcnt(4)\n"
    "v_mfma_f32_16x16x32_bf16 v[192:195], a[0:3], a[16:19], v[192:195]\n"
    "ds_read_b128 a[48:51], v17\n"
    "v_mfma_f32_16x16x32_bf16 v[196:199], a[0:3], a[20:23], v[196:199]\n"
    "ds_read_b128 a[52:55], v17 offset:2048\n"
    "s_add_u32 s52, s52, 0x8000\n"
    "v_mfma_f32_16x16x32_bf16 v[200:203], a[0:3], a[24:27], v[200:203]\n"
    "ds_read_b128 a[56:59], v17 offset:4096\n"
    "s_add_u32 m0, m0, 0x1000\n"
    "v_mfma_f32_16x16x32_bf16 v[204:207], a[0:3], a[28:31], v[204:207]\n"
    "buffer_load_dwordx4 v4, s[36:39], s52 offen lds\n"
    "v_lshlrev_b32 v21, 11, v20\n"
    "v_mfma_f32_16x16x32_bf16 v[208:211], a[4:7], a[16:19], v[208:211]\n"
    "ds_read_b128 a[60:63], v17 offset:6144\n"
    "v_and_b32 v20, 1, v20\n"
    "v_lshl_add_u32 v21, v20, 5, v21\n"
    "v_mfma_f32_16x16x32_bf16 v[212:215], a[4:7], a[20:23], v[212:215]\n"
    "ds_read_b64_tr_b16 a[32:33], v18 offset:8192\n"
    "v_bfe_u32 v20, v1, 2, 2\n"
    "v_lshl_add_u32 v21, v20, 6, v21\n"
    "v_mfma_f32_16x16x32_bf16 v[216:219], a[4:7], a[24:27], v[216:219]\n"
    "ds_read_b64_tr_b16 a[34:35], v18 offset:9216\n"
    "v_and_b32 v20, 3, v1\n"
    "v_lshl_add_u32 v21, v20, 3, v21\n"
    "v_mfma_f32_16x16x32_bf16 v[220:223], a[4:7], a[28:31], v[220:223]\n"
    "buffer_load_dwordx4 v5, s[36:39], s52 offen offset:1024 lds\n"
    "v_add_u32 v14, s22, v21\n"
    "s_waitcnt lgkmcnt(6)\n"
    "v_mfma_f32_16x16x32_bf16 v[224:227], a[8:11], a[16:19], v[224:227]\n"
    "ds_read_b64_tr_b16 a[36:37], v19 offset:8192\n"
    "v_xor_b32 v15, 32, v14\n"
    "s_mov_b64 s[28:29], s[76:77]\n"
    "v_mfma_f32_16x16x32_bf16 v[228:231], a[8:11], a[20:23], v[228:231]\n"
    "ds_read_b64_tr_b16 a[38:39], v19 offset:9216\n"
    "s_mov_b32 s30, 0x400000\n"
    "s_mov_b32 s31, 0x20000\n"
    "v_mfma_f32_16x16x32_bf16 v[232:235], a[8:11], a[24:27], v[232:235]\n"
    "ds_read_b64_tr_b16 a[40:41], v18 offset:8448\n"
    "s_cmp_eq_u32 s19, 0\n"
    "s_cselect_b64 s[48:49], s[40:41], s[64:65]\n"
    "v_mfma_f32_16x16x32_bf16 v[236:239], a[8:11], a[28:31], v[236:239]\n"
    "buffer_load_dwordx4 v6, s[36:39], s52 offen offset:2048 lds\n"
    "s_mov_b64 s[50:51], s[42:43]\n"
    "v_mfma_f32_16x16x32_bf16 v[240:243], a[12:15], a[16:19], v[240:243]\n"
    "ds_read_b64_tr_b16 a[42:43], v18 offset:9472\n"
    "s_lshl_b32 s6, s21, 16\n"
    "s_lshl_b32 s22, s20, 8\n"
    "v_mfma_f32_16x16x32_bf16 v[244:247], a[12:15], a[20:23], v[244:247]\n"
    "ds_read_b64_tr_b16 a[44:45], v19 offset:8448\n"
    "s_add_u32 s6, s6, s22\n"
    "s_add_u32 s6, s6, 0x100000\n"
    "v_mfma_f32_16x16x32_bf16 v[248:251], a[12:15], a[24:27], v[248:251]\n"
    "ds_read_b64_tr_b16 a[46:47], v19 offset:9472\n"
    "s_lshl_b32 s22, s21, 2\n"
    "s_and_b32 s23, s20, 3\n"
    "v_mfma_f32_16x16x32_bf16 v[252:255], a[12:15], a[28:31], v[252:255]\n"
    "buffer_load_dwordx4 v7, s[36:39], s52 offen offset:3072 lds\n"
    "s_add_u32 s22, s22, s23\n"
    "s_waitcnt vmcnt(8)\n"
    "s_barrier\n"
    "s_waitcnt lgkmcnt(4)\n"
    "v_mfma_f32_16x16x32_bf16 v[192:195], a[32:35], a[48:51], v[192:195]\n"
    "ds_read_b128 a[16:19], v16 offset:32768\n"
    "v_mfma_f32_16x16x32_bf16 v[196:199], a[32:35], a[52:55], v[196:199]\n"
    "ds_read_b128 a[20:23], v16 offset:34816\n"
    "s_add_u32 s13, s54, 0\n"
    "v_mfma_f32_16x16x32_bf16 v[200:203], a[32:35], a[56:59], v[200:203]\n"
    "ds_read_b128 a[24:27], v16 offset:36864\n"
    "s_add_u32 m0, s16, 0x8000\n"
    "v_mfma_f32_16x16x32_bf16 v[204:207], a[32:35], a[60:63], v[204:207]\n"
    "buffer_load_dwordx4 v8, s[40:43], s13 offen lds\n"
    "s_lshl_b32 s22, s22, 1\n"
    "v_mfma_f32_16x16x32_bf16 v[208:211], a[36:39], a[48:51], v[208:211]\n"
    "ds_read_b128 a[28:31], v16 offset:38912\n"
    "s_and_b32 s23, s19, 1\n"
    "s_add_u32 s22, s22, s23\n"
    "v_mfma_f32_16x16x32_bf16 v[212:215], a[36:39], a[52:55], v[212:215]\n"
    "ds_read_b64_tr_b16 a[0:1], v18 offset:32768\n"
    "s_lshl_b32 s22, s22, 15\n"
    "s_add_u32 s22, s22, 0x100\n"
    "v_mfma_f32_16x16x32_bf16 v[216:219], a[36:39], a[56:59], v[216:219]\n"
    "ds_read_b64_tr_b16 a[2:3], v18 offset:33792\n"
    "s_mov_b32 s7, 0x80000\n"
    "v_bfe_u32 v20, v1, 1, 3\n"
    "v_mfma_f32_16x16x32_bf16 v[220:223], a[36:39], a[60:63], v[220:223]\n"
    "buffer_load_dwordx4 v9, s[40:43], s13 offen offset:1024 lds\n"
    "v_lshlrev_b32 v20, 13, v20\n"
    "s_waitcnt lgkmcnt(6)\n"
    "v_mfma_f32_16x16x32_bf16 v[224:227], a[40:43], a[48:51], v[224:227]\n"
    "ds_read_b64_tr_b16 a[4:5], v19 offset:32768\n"
    "v_and_b32 v21, 1, v1\n"
    "v_lshl_add_u32 v20, v21, 7, v20\n"
    "v_mfma_f32_16x16x32_bf16 v[228:231], a[40:43], a[52:55], v[228:231]\n"
    "ds_read_b64_tr_b16 a[6:7], v19 offset:33792\n"
    "v_and_b32 v21, 31, v1\n"
    "v_lshlrev_b32 v21, 10, v21\n"
    "v_mfma_f32_16x16x32_bf16 v[232:235], a[40:43], a[56:59], v[232:235]\n"
    "ds_read_b64_tr_b16 a[8:9], v18 offset:33024\n"
    "s_cmp_ge_u32 s19, 2\n"
    "s_cselect_b64 s[48:49], s[36:37], s[48:49]\n"
    "s_cselect_b32 s50, s38, s50\n"
    "s_cselect_b32 s6, s22, s6\n"
    "s_cselect_b32 s7, 0x80, s7\n"
    "s_cselect_b64 vcc, -1, 0\n"
    "v_mfma_f32_16x16x32_bf16 v[236:239], a[40:43], a[60:63], v[236:239]\n"
    "buffer_load_dwordx4 v10, s[40:43], s13 offen offset:2048 lds\n"
    "v_cndmask_b32 v54, v20, v21, vcc\n"
    "v_mfma_f32_16x16x32_bf16 v[240:243], a[44:47], a[48:51], v[240:243]\n"
    "ds_read_b64_tr_b16 a[10:11], v18 offset:34048\n"
    "s_lshl_b32 s70, s21, 1\n"
    "s_and_b32 s22, s19, 1\n"
    "v_mfma_f32_16x16x32_bf16 v[244:247], a[44:47], a[52:55], v[244:247]\n"
    "ds_read_b64_tr_b16 a[12:13], v19 offset:33024\n"
    "s_add_u32 s22, s22, s70\n"
    "s_sub_u32 s69, 31, s22\n"
    "v_mfma_f32_16x16x32_bf16 v[248:251], a[44:47], a[56:59], v[248:251]\n"
    "ds_read_b64_tr_b16 a[14:15], v19 offset:34048\n"
    "s_cmp_lt_u32 s19, 2\n"
    "s_cselect_b32 s68, s69, s22\n"
    "v_mfma_f32_16x16x32_bf16 v[252:255], a[44:47], a[60:63], v[252:255]\n"
    "buffer_load_dwordx4 v11, s[40:43], s13 offen offset:3072 lds\n"
    "s_lshl_b32 s63, s68, 16\n"
    "s_waitcnt lgkmcnt(4)\n"
    "v_mfma_f32_16x16x32_bf16 v[192:195], a[0:3], a[16:19], v[192:195]\n"
    "ds_read_b128 a[48:51], v17 offset:32768\n"
    "v_mfma_f32_16x16x32_bf16 v[196:199], a[0:3], a[20:23], v[196:199]\n"
    "ds_read_b128 a[52:55], v17 offset:34816\n"
    "v_mfma_f32_16x16x32_bf16 v[200:203], a[0:3], a[24:27], v[200:203]\n"
    "ds_read_b128 a[56:59], v17 offset:36864\n"
    "s_add_u32 m0, m0, 0x4000\n"
    "v_mfma_f32_16x16x32_bf16 v[204:207], a[0:3], a[28:31], v[204:207]\n"
    "buffer_load_dwordx4 v8, s[64:67], s13 offen lds\n"
    "s_lshl_b32 s55, s56, 1\n"
    "v_mfma_f32_16x16x32_bf16 v[208:211], a[4:7], a[16:19], v[208:211]\n"
    "ds_read_b128 a[60:63], v17 offset:38912\n"
    "s_and_b32 s22, s19, 1\n"
    "s_add_u32 s55, s55, s22\n"
    "v_mfma_f32_16x16x32_bf16 v[212:215], a[4:7], a[20:23], v[212:215]\n"
    "ds_read_b64_tr_b16 a[32:33], v18 offset:40960\n"
    "s_lshl_b32 s55, s55, 7\n"
    "s_add_u32 s55, s55, 0x2000\n"
    "v_mfma_f32_16x16x32_bf16 v[216:219], a[4:7], a[24:27], v[216:219]\n"
    "ds_read_b64_tr_b16 a[34:35], v18 offset:41984\n"
    "v_mfma_f32_16x16x32_bf16 v[220:223], a[4:7], a[28:31], v[220:223]\n"
    "buffer_load_dwordx4 v9, s[64:67], s13 offen offset:1024 lds\n"
    "s_waitcnt lgkmcnt(6)\n"
    "v_mfma_f32_16x16x32_bf16 v[224:227], a[8:11], a[16:19], v[224:227]\n"
    "ds_read_b64_tr_b16 a[36:37], v19 offset:40960\n"
    "v_mfma_f32_16x16x32_bf16 v[228:231], a[8:11], a[20:23], v[228:231]\n"
    "ds_read_b64_tr_b16 a[38:39], v19 offset:41984\n"
    "v_mfma_f32_16x16x32_bf16 v[232:235], a[8:11], a[24:27], v[232:235]\n"
    "ds_read_b64_tr_b16 a[40:41], v18 offset:41216\n"
    "v_mfma_f32_16x16x32_bf16 v[236:239], a[8:11], a[28:31], v[236:239]\n"
    "buffer_load_dwordx4 v10, s[64:67], s13 offen offset:2048 lds\n"
    "v_mfma_f32_16x16x32_bf16 v[240:243], a[12:15], a[16:19], v[240:243]\n"
    "ds_read_b64_tr_b16 a[42:43], v18 offset:42240\n"
    "v_mfma_f32_16x16x32_bf16 v[244:247], a[12:15], a[20:23], v[244:247]\n"
    "ds_read_b64_tr_b16 a[44:45], v19 offset:41216\n"
    "v_mfma_f32_16x16x32_bf16 v[248:251], a[12:15], a[24:27], v[248:251]\n"
    "ds_read_b64_tr_b16 a[46:47], v19 offset:42240\n"
    "v_subrev_u32 v16, 0x10000, v16\n"
    "v_subrev_u32 v17, 0x10000, v17\n"
    "v_subrev_u32 v18, 0x10000, v18\n"
    "v_subrev_u32 v19, 0x10000, v19\n"
    "v_mfma_f32_16x16x32_bf16 v[252:255], a[12:15], a[28:31], v[252:255]\n"
    "buffer_load_dwordx4 v11, s[64:67], s13 offen offset:3072 lds\n"
    "s_barrier\n"
    "s_waitcnt lgkmcnt(4)\n"
    "v_mfma_f32_16x16x32_bf16 v[192:195], a[32:35], a[48:51], v[192:195]\n"
    "v_mfma_f32_16x16x32_bf16 v[196:199], a[32:35], a[52:55], v[196:199]\n"
    "s_add_u32 s52, s53, 0x80\n"
    "v_mfma_f32_16x16x32_bf16 v[200:203], a[32:35], a[56:59], v[200:203]\n"
    "s_add_u32 m0, s17, 0x10000\n"
    "v_mfma_f32_16x16x32_bf16 v[204:207], a[32:35], a[60:63], v[204:207]\n"
    "buffer_load_dwordx4 v4, s[36:39], s52 offen lds\n"
    "v_mfma_f32_16x16x32_bf16 v[208:211], a[36:39], a[48:51], v[208:211]\n"
    "v_mfma_f32_16x16x32_bf16 v[212:215], a[36:39], a[52:55], v[212:215]\n"
    "v_mfma_f32_16x16x32_bf16 v[216:219], a[36:39], a[56:59], v[216:219]\n"
    "v_mfma_f32_16x16x32_bf16 v[220:223], a[36:39], a[60:63], v[220:223]\n"
    "buffer_load_dwordx4 v5, s[36:39], s52 offen offset:1024 lds\n"
    "s_waitcnt lgkmcnt(0)\n"
    "v_mfma_f32_16x16x32_bf16 v[224:227], a[40:43], a[48:51], v[224:227]\n"
    "v_mfma_f32_16x16x32_bf16 v[228:231], a[40:43], a[52:55], v[228:231]\n"
    "v_mfma_f32_16x16x32_bf16 v[232:235], a[40:43], a[56:59], v[232:235]\n"
    "v_mfma_f32_16x16x32_bf16 v[236:239], a[40:43], a[60:63], v[236:239]\n"
    "buffer_load_dwordx4 v6, s[36:39], s52 offen offset:2048 lds\n"
    "v_mfma_f32_16x16x32_bf16 v[240:243], a[44:47], a[48:51], v[240:243]\n"
    "v_mfma_f32_16x16x32_bf16 v[244:247], a[44:47], a[52:55], v[244:247]\n"
    "v_mfma_f32_16x16x32_bf16 v[248:251], a[44:47], a[56:59], v[248:251]\n"
    "v_mfma_f32_16x16x32_bf16 v[252:255], a[44:47], a[60:63], v[252:255]\n"
    "buffer_load_dwordx4 v7, s[36:39], s52 offen offset:3072 lds\n"
    "s_nop 7\n"
    "s_add_u32 s52, s52, 0x8000\n"
    "s_add_u32 m0, m0, 0x1000\n"
    "s_nop 0\n"
    "buffer_load_dwordx4 v4, s[36:39], s52 offen lds\n"
    "v_pk_mul_f32 v[32:33], v[192:193], v[192:193]\n"
    "v_pk_mul_f32 v[34:35], v[194:195], v[194:195]\n"
    "v_pk_mul_f32 v[36:37], v[196:197], v[196:197]\n"
    "v_pk_mul_f32 v[38:39], v[198:199], v[198:199]\n"
    "v_pk_fma_f32 v[32:33], v[32:33], v[26:27], v[24:25]\n"
    "v_pk_fma_f32 v[34:35], v[34:35], v[26:27], v[24:25]\n"
    "v_pk_fma_f32 v[36:37], v[36:37], v[26:27], v[24:25]\n"
    "v_pk_fma_f32 v[38:39], v[38:39], v[26:27], v[24:25]\n"
    "v_pk_mul_f32 v[32:33], v[32:33], v[192:193]\n"
    "v_pk_mul_f32 v[34:35], v[34:35], v[194:195]\n"
    "v_pk_mul_f32 v[36:37], v[36:37], v[196:197]\n"
    "v_pk_mul_f32 v[38:39], v[38:39], v[198:199]\n"
    "v_exp_f32 v32, v32\n"
    "v_exp_f32 v33, v33\n"
    "buffer_load_dwordx4 v5, s[36:39], s52 offen offset:1024 lds\n"
    "v_exp_f32 v34, v34\n"
    "v_exp_f32 v35, v35\n"
    "v_exp_f32 v36, v36\n"
    "v_exp_f32 v37, v37\n"
    "v_exp_f32 v38, v38\n"
    "v_exp_f32 v39, v39\n"
    "v_pk_add_f32 v[32:33], v[32:33], v[28:29]\n"
    "v_pk_add_f32 v[34:35], v[34:35], v[28:29]\n"
    "v_pk_add_f32 v[36:37], v[36:37], v[28:29]\n"
    "v_pk_add_f32 v[38:39], v[38:39], v[28:29]\n"
    "v_rcp_f32 v32, v32\n"
    "v_rcp_f32 v33, v33\n"
    "v_rcp_f32 v34, v34\n"
    "v_rcp_f32 v35, v35\n"
    "buffer_load_dwordx4 v6, s[36:39], s52 offen offset:2048 lds\n"
    "v_rcp_f32 v36, v36\n"
    "v_rcp_f32 v37, v37\n"
    "v_rcp_f32 v38, v38\n"
    "v_rcp_f32 v39, v39\n"
    "v_pk_mul_f32 v[192:193], v[192:193], v[32:33]\n"
    "v_pk_mul_f32 v[194:195], v[194:195], v[34:35]\n"
    "v_pk_mul_f32 v[196:197], v[196:197], v[36:37]\n"
    "v_pk_mul_f32 v[198:199], v[198:199], v[38:39]\n"
    "v_pk_mul_f32 v[40:41], v[200:201], v[200:201]\n"
    "v_pk_mul_f32 v[42:43], v[202:203], v[202:203]\n"
    "v_pk_mul_f32 v[44:45], v[204:205], v[204:205]\n"
    "v_pk_mul_f32 v[46:47], v[206:207], v[206:207]\n"
    "v_pk_fma_f32 v[40:41], v[40:41], v[26:27], v[24:25]\n"
    "v_pk_fma_f32 v[42:43], v[42:43], v[26:27], v[24:25]\n"
    "buffer_load_dwordx4 v7, s[36:39], s52 offen offset:3072 lds\n"
    "v_pk_fma_f32 v[44:45], v[44:45], v[26:27], v[24:25]\n"
    "v_pk_fma_f32 v[46:47], v[46:47], v[26:27], v[24:25]\n"
    "v_pk_mul_f32 v[40:41], v[40:41], v[200:201]\n"
    "v_pk_mul_f32 v[42:43], v[42:43], v[202:203]\n"
    "v_pk_mul_f32 v[44:45], v[44:45], v[204:205]\n"
    "v_pk_mul_f32 v[46:47], v[46:47], v[206:207]\n"
    "v_exp_f32 v40, v40\n"
    "v_exp_f32 v41, v41\n"
    "v_exp_f32 v42, v42\n"
    "v_exp_f32 v43, v43\n"
    "v_exp_f32 v44, v44\n"
    "v_exp_f32 v45, v45\n"
    "v_exp_f32 v46, v46\n"
    "v_exp_f32 v47, v47\n"
    "s_add_u32 s13, s54, 0x80000\n"
    "s_add_u32 m0, s16, 0x18000\n"
    "s_nop 0\n"
    "buffer_load_dwordx4 v8, s[40:43], s13 offen lds\n"
    "v_pk_add_f32 v[40:41], v[40:41], v[28:29]\n"
    "v_pk_add_f32 v[42:43], v[42:43], v[28:29]\n"
    "v_pk_add_f32 v[44:45], v[44:45], v[28:29]\n"
    "v_pk_add_f32 v[46:47], v[46:47], v[28:29]\n"
    "v_rcp_f32 v40, v40\n"
    "v_rcp_f32 v41, v41\n"
    "v_rcp_f32 v42, v42\n"
    "v_rcp_f32 v43, v43\n"
    "v_rcp_f32 v44, v44\n"
    "v_rcp_f32 v45, v45\n"
    "v_rcp_f32 v46, v46\n"
    "v_rcp_f32 v47, v47\n"
    "v_pk_mul_f32 v[200:201], v[200:201], v[40:41]\n"
    "v_pk_mul_f32 v[202:203], v[202:203], v[42:43]\n"
    "buffer_load_dwordx4 v9, s[40:43], s13 offen offset:1024 lds\n"
    "v_pk_mul_f32 v[204:205], v[204:205], v[44:45]\n"
    "v_pk_mul_f32 v[206:207], v[206:207], v[46:47]\n"
    "v_pk_mul_f32 v[32:33], v[208:209], v[208:209]\n"
    "v_pk_mul_f32 v[34:35], v[210:211], v[210:211]\n"
    "v_pk_mul_f32 v[36:37], v[212:213], v[212:213]\n"
    "v_pk_mul_f32 v[38:39], v[214:215], v[214:215]\n"
    "v_pk_fma_f32 v[32:33], v[32:33], v[26:27], v[24:25]\n"
    "v_pk_fma_f32 v[34:35], v[34:35], v[26:27], v[24:25]\n"
    "v_pk_fma_f32 v[36:37], v[36:37], v[26:27], v[24:25]\n"
    "v_pk_fma_f32 v[38:39], v[38:39], v[26:27], v[24:25]\n"
    "v_pk_mul_f32 v[32:33], v[32:33], v[208:209]\n"
    "v_pk_mul_f32 v[34:35], v[34:35], v[210:211]\n"
    "v_pk_mul_f32 v[36:37], v[36:37], v[212:213]\n"
    "v_pk_mul_f32 v[38:39], v[38:39], v[214:215]\n"
    "buffer_load_dwordx4 v10, s[40:43], s13 offen offset:2048 lds\n"
    "v_exp_f32 v32, v32\n"
    "v_exp_f32 v33, v33\n"
    "v_exp_f32 v34, v34\n"
    "v_exp_f32 v35, v35\n"
    "v_exp_f32 v36, v36\n"
    "v_exp_f32 v37, v37\n"
    "v_exp_f32 v38, v38\n"
    "v_exp_f32 v39, v39\n"
    "v_pk_add_f32 v[32:33], v[32:33], v[28:29]\n"
    "v_pk_add_f32 v[34:35], v[34:35], v[28:29]\n"
    "v_pk_add_f32 v[36:37], v[36:37], v[28:29]\n"
    "v_pk_add_f32 v[38:39], v[38:39], v[28:29]\n"
    "v_rcp_f32 v32, v32\n"
    "v_rcp_f32 v33, v33\n"
    "buffer_load_dwordx4 v11, s[40:43], s13 offen offset:3072 lds\n"
    "v_rcp_f32 v34, v34\n"
    "v_rcp_f32 v35, v35\n"
    "v_rcp_f32 v36, v36\n"
    "v_rcp_f32 v37, v37\n"
    "v_rcp_f32 v38, v38\n"
    "v_rcp_f32 v39, v39\n"
    "v_pk_mul_f32 v[208:209], v[208:209], v[32:33]\n"
    "v_pk_mul_f32 v[210:211], v[210:211], v[34:35]\n"
    "v_pk_mul_f32 v[212:213], v[212:213], v[36:37]\n"
    "v_pk_mul_f32 v[214:215], v[214:215], v[38:39]\n"
    "v_pk_mul_f32 v[40:41], v[216:217], v[216:217]\n"
    "v_pk_mul_f32 v[42:43], v[218:219], v[218:219]\n"
    "v_pk_mul_f32 v[44:45], v[220:221], v[220:221]\n"
    "v_pk_mul_f32 v[46:47], v[222:223], v[222:223]\n"
    "s_add_u32 m0, m0, 0x4000\n"
    "s_nop 0\n"
    "buffer_load_dwordx4 v8, s[64:67], s13 offen lds\n"
    "v_pk_fma_f32 v[40:41], v[40:41], v[26:27], v[24:25]\n"
    "v_pk_fma_f32 v[42:43], v[42:43], v[26:27], v[24:25]\n"
    "v_pk_fma_f32 v[44:45], v[44:45], v[26:27], v[24:25]\n"
    "v_pk_fma_f32 v[46:47], v[46:47], v[26:27], v[24:25]\n"
    "v_pk_mul_f32 v[40:41], v[40:41], v[216:217]\n"
    "v_pk_mul_f32 v[42:43], v[42:43], v[218:219]\n"
    "v_pk_mul_f32 v[44:45], v[44:45], v[220:221]\n"
    "v_pk_mul_f32 v[46:47], v[46:47], v[222:223]\n"
    "v_exp_f32 v40, v40\n"
    "v_exp_f32 v41, v41\n"
    "v_exp_f32 v42, v42\n"
    "v_exp_f32 v43, v43\n"
    "v_exp_f32 v44, v44\n"
    "v_exp_f32 v45, v45\n"
    "buffer_load_dwordx4 v9, s[64:67], s13 offen offset:1024 lds\n"
    "v_exp_f32 v46, v46\n"
    "v_exp_f32 v47, v47\n"
    "v_pk_add_f32 v[40:41], v[40:41], v[28:29]\n"
    "v_pk_add_f32 v[42:43], v[42:43], v[28:29]\n"
    "v_pk_add_f32 v[44:45], v[44:45], v[28:29]\n"
    "v_pk_add_f32 v[46:47], v[46:47], v[28:29]\n"
    "v_rcp_f32 v40, v40\n"
    "v_rcp_f32 v41, v41\n"
    "v_rcp_f32 v42, v42\n"
    "v_rcp_f32 v43, v43\n"
    "v_rcp_f32 v44, v44\n"
    "v_rcp_f32 v45, v45\n"
    "v_rcp_f32 v46, v46\n"
    "v_rcp_f32 v47, v47\n"
    "buffer_load_dwordx4 v10, s[64:67], s13 offen offset:2048 lds\n"
    "v_pk_mul_f32 v[216:217], v[216:217], v[40:41]\n"
    "v_pk_mul_f32 v[218:219], v[218:219], v[42:43]\n"
    "v_pk_mul_f32 v[220:221], v[220:221], v[44:45]\n"
    "v_pk_mul_f32 v[222:223], v[222:223], v[46:47]\n"
    "v_pk_mul_f32 v[32:33], v[224:225], v[224:225]\n"
    "v_pk_mul_f32 v[34:35], v[226:227], v[226:227]\n"
    "v_pk_mul_f32 v[36:37], v[228:229], v[228:229]\n"
    "v_pk_mul_f32 v[38:39], v[230:231], v[230:231]\n"
    "v_pk_fma_f32 v[32:33], v[32:33], v[26:27], v[24:25]\n"
    "v_pk_fma_f32 v[34:35], v[34:35], v[26:27], v[24:25]\n"
    "v_pk_fma_f32 v[36:37], v[36:37], v[26:27], v[24:25]\n"
    "v_pk_fma_f32 v[38:39], v[38:39], v[26:27], v[24:25]\n"
    "v_pk_mul_f32 v[32:33], v[32:33], v[224:225]\n"
    "v_pk_mul_f32 v[34:35], v[34:35], v[226:227]\n"
    "buffer_load_dwordx4 v11, s[64:67], s13 offen offset:3072 lds\n"
    "v_pk_mul_f32 v[36:37], v[36:37], v[228:229]\n"
    "v_pk_mul_f32 v[38:39], v[38:39], v[230:231]\n"
    "v_exp_f32 v32, v32\n"
    "v_exp_f32 v33, v33\n"
    "v_exp_f32 v34, v34\n"
    "v_exp_f32 v35, v35\n"
    "v_exp_f32 v36, v36\n"
    "v_exp_f32 v37, v37\n"
    "v_exp_f32 v38, v38\n"
    "v_exp_f32 v39, v39\n"
    "v_pk_add_f32 v[32:33], v[32:33], v[28:29]\n"
    "v_pk_add_f32 v[34:35], v[34:35], v[28:29]\n"
    "v_pk_add_f32 v[36:37], v[36:37], v[28:29]\n"
    "v_pk_add_f32 v[38:39], v[38:39], v[28:29]\n"
    "v_rcp_f32 v32, v32\n"
    "v_rcp_f32 v33, v33\n"
    "v_rcp_f32 v34, v34\n"
    "v_rcp_f32 v35, v35\n"
    "v_rcp_f32 v36, v36\n"
    "v_rcp_f32 v37, v37\n"
    "v_rcp_f32 v38, v38\n"
    "v_rcp_f32 v39, v39\n"
    "v_pk_mul_f32 v[224:225], v[224:225], v[32:33]\n"
    "v_pk_mul_f32 v[226:227], v[226:227], v[34:35]\n"
    "v_pk_mul_f32 v[228:229], v[228:229], v[36:37]\n"
    "v_pk_mul_f32 v[230:231], v[230:231], v[38:39]\n"
    "v_pk_mul_f32 v[40:41], v[232:233], v[232:233]\n"
    "v_pk_mul_f32 v[42:43], v[234:235], v[234:235]\n"
    "v_pk_mul_f32 v[44:45], v[236:237], v[236:237]\n"
    "v_pk_mul_f32 v[46:47], v[238:239], v[238:239]\n"
    "v_pk_fma_f32 v[40:41], v[40:41], v[26:27], v[24:25]\n"
    "v_pk_fma_f32 v[42:43], v[42:43], v[26:27], v[24:25]\n"
    "v_pk_fma_f32 v[44:45], v[44:45], v[26:27], v[24:25]\n"
    "v_pk_fma_f32 v[46:47], v[46:47], v[26:27], v[24:25]\n"
    "v_pk_mul_f32 v[40:41], v[40:41], v[232:233]\n"
    "v_pk_mul_f32 v[42:43], v[42:43], v[234:235]\n"
    "v_pk_mul_f32 v[44:45], v[44:45], v[236:237]\n"
    "v_pk_mul_f32 v[46:47], v[46:47], v[238:239]\n"
    "v_exp_f32 v40, v40\n"
    "v_exp_f32 v41, v41\n"
    "v_exp_f32 v42, v42\n"
    "v_exp_f32 v43, v43\n"
    "v_exp_f32 v44, v44\n"
    "v_exp_f32 v45, v45\n"
    "v_exp_f32 v46, v46\n"
    "v_exp_f32 v47, v47\n"
    "v_pk_add_f32 v[40:41], v[40:41], v[28:29]\n"
    "v_pk_add_f32 v[42:43], v[42:43], v[28:29]\n"
    "v_pk_add_f32 v[44:45], v[44:45], v[28:29]\n"
    "v_pk_add_f32 v[46:47], v[46:47], v[28:29]\n"
    "v_rcp_f32 v40, v40\n"
    "v_rcp_f32 v41, v41\n"
    "v_rcp_f32 v42, v42\n"
    "v_rcp_f32 v43, v43\n"
    "v_rcp_f32 v44, v44\n"
    "v_rcp_f32 v45, v45\n"
    "v_rcp_f32 v46, v46\n"
    "v_rcp_f32 v47, v47\n"
    "v_pk_mul_f32 v[232:233], v[232:233], v[40:41]\n"
    "v_pk_mul_f32 v[234:235], v[234:235], v[42:43]\n"
    "v_pk_mul_f32 v[236:237], v[236:237], v[44:45]\n"
    "v_pk_mul_f32 v[238:239], v[238:239], v[46:47]\n"
    "v_pk_mul_f32 v[32:33], v[240:241], v[240:241]\n"
    "v_pk_mul_f32 v[34:35], v[242:243], v[242:243]\n"
    "v_pk_mul_f32 v[36:37], v[244:245], v[244:245]\n"
    "v_pk_mul_f32 v[38:39], v[246:247], v[246:247]\n"
    "v_pk_fma_f32 v[32:33], v[32:33], v[26:27], v[24:25]\n"
    "v_pk_fma_f32 v[34:35], v[34:35], v[26:27], v[24:25]\n"
    "v_pk_fma_f32 v[36:37], v[36:37], v[26:27], v[24:25]\n"
    "v_pk_fma_f32 v[38:39], v[38:39], v[26:27], v[24:25]\n"
    "v_pk_mul_f32 v[32:33], v[32:33], v[240:241]\n"
    "v_pk_mul_f32 v[34:35], v[34:35], v[242:243]\n"
    "v_pk_mul_f32 v[36:37], v[36:37], v[244:245]\n"
    "v_pk_mul_f32 v[38:39], v[38:39], v[246:247]\n"
    "v_exp_f32 v32, v32\n"
    "v_exp_f32 v33, v33\n"
    "v_exp_f32 v34, v34\n"
    "v_exp_f32 v35, v35\n"
    "v_exp_f32 v36, v36\n"
    "v_exp_f32 v37, v37\n"
    "v_exp_f32 v38, v38\n"
    "v_exp_f32 v39, v39\n"
    "v_pk_add_f32 v[32:33], v[32:33], v[28:29]\n"
    "v_pk_add_f32 v[34:35], v[34:35], v[28:29]\n"
    "v_pk_add_f32 v[36:37], v[36:37], v[28:29]\n"
    "v_pk_add_f32 v[38:39], v[38:39], v[28:29]\n"
    "v_rcp_f32 v32, v32\n"
    "v_rcp_f32 v33, v33\n"
    "v_rcp_f32 v34, v34\n"
    "v_rcp_f32 v35, v35\n"
    "v_rcp_f32 v36, v36\n"
    "v_rcp_f32 v37, v37\n"
    "v_rcp_f32 v38, v38\n"
    "v_rcp_f32 v39, v39\n"
    "v_pk_mul_f32 v[240:241], v[240:241], v[32:33]\n"
    "v_pk_mul_f32 v[242:243], v[242:243], v[34:35]\n"
    "v_pk_mul_f32 v[244:245], v[244:245], v[36:37]\n"
    "v_pk_mul_f32 v[246:247], v[246:247], v[38:39]\n"
    "v_pk_mul_f32 v[40:41], v[248:249], v[248:249]\n"
    "v_pk_mul_f32 v[42:43], v[250:251], v[250:251]\n"
    "v_pk_mul_f32 v[44:45], v[252:253], v[252:253]\n"
    "v_pk_mul_f32 v[46:47], v[254:255], v[254:255]\n"
    "v_pk_fma_f32 v[40:41], v[40:41], v[26:27], v[24:25]\n"
    "v_pk_fma_f32 v[42:43], v[42:43], v[26:27], v[24:25]\n"
    "v_pk_fma_f32 v[44:45], v[44:45], v[26:27], v[24:25]\n"
    "v_pk_fma_f32 v[46:47], v[46:47], v[26:27], v[24:25]\n"
    "v_pk_mul_f32 v[40:41], v[40:41], v[248:249]\n"
    "v_pk_mul_f32 v[42:43], v[42:43], v[250:251]\n"
    "v_pk_mul_f32 v[44:45], v[44:45], v[252:253]\n"
    "v_pk_mul_f32 v[46:47], v[46:47], v[254:255]\n"
    "v_exp_f32 v40, v40\n"
    "v_exp_f32 v41, v41\n"
    "v_exp_f32 v42, v42\n"
    "v_exp_f32 v43, v43\n"
    "v_exp_f32 v44, v44\n"
    "v_exp_f32 v45, v45\n"
    "v_exp_f32 v46, v46\n"
    "v_exp_f32 v47, v47\n"
    "v_pk_add_f32 v[40:41], v[40:41], v[28:29]\n"
    "v_pk_add_f32 v[42:43], v[42:43], v[28:29]\n"
    "v_pk_add_f32 v[44:45], v[44:45], v[28:29]\n"
    "v_pk_add_f32 v[46:47], v[46:47], v[28:29]\n"
    "v_rcp_f32 v40, v40\n"
    "v_rcp_f32 v41, v41\n"
    "v_rcp_f32 v42, v42\n"
    "v_rcp_f32 v43, v43\n"
    "v_rcp_f32 v44, v44\n"
    "v_rcp_f32 v45, v45\n"
    "v_rcp_f32 v46, v46\n"
    "v_rcp_f32 v47, v47\n"
    "v_pk_mul_f32 v[248:249], v[248:249], v[40:41]\n"
    "v_pk_mul_f32 v[250:251], v[250:251], v[42:43]\n"
    "v_pk_mul_f32 v[252:253], v[252:253], v[44:45]\n"
    "v_pk_mul_f32 v[254:255], v[254:255], v[46:47]\n"
    "buffer_load_dword v55, v54, s[48:51], s6 offen\n"
    "s_add_u32 s6, s6, s7\n"
    "buffer_load_dword v55, v54, s[48:51], s6 offen\n"
    "s_add_u32 s6, s6, s7\n"
    "s_waitcnt vmcnt(18)\n"
    "s_branch P1S_%=\n"
    "GATE_%=:\n"
    "buffer_load_dwordx2 v[48:49], v50, s[44:47], 0 offen sc1\n"
    "v_bfe_u32 v20, v1, 2, 2\n"
    "v_mul_u32_u24 v20, 0x2000, v20\n"
    "v_lshrrev_b32 v21, 4, v1\n"
    "v_lshl_add_u32 v20, v21, 6, v20\n"
    "v_and_b32 v21, 3, v1\n"
    "v_lshlrev_b32 v21, 4, v21\n"
    "v_xor_b32 v22, 0, v21\n"
    "v_add_u32 v22, v22, v20\n"
    "v_add_u32 v8, 0, v22\n"
    "v_xor_b32 v22, 0, v21\n"
    "v_add_u32 v22, v22, v20\n"
    "v_add_u32 v9, 0x7c00, v22\n"
    "v_xor_b32 v22, 32, v21\n"
    "v_add_u32 v22, v22, v20\n"
    "v_add_u32 v10, 0xf800, v22\n"
    "v_xor_b32 v22, 32, v21\n"
    "v_add_u32 v22, v22, v20\n"
    "v_add_u32 v11, 0x17400, v22\n"
    "s_mov_b64 s[36:37], s[74:75]\n"
    "s_mov_b32 s38, 0x200000\n"
    "s_mov_b32 s39, 0x20000\n"
    "s_mov_b64 s[40:41], s[78:79]\n"
    "s_mov_b32 s42, 0x400000\n"
    "s_mov_b32 s43, 0x20000\n"
    "s_mov_b64 s[64:65], s[80:81]\n"
    "s_mov_b32 s66, 0x400000\n"
    "s_mov_b32 s67, 0x20000\n"
    "s_lshl_b32 s53, s21, 18\n"
    "s_lshl_b32 s22, s19, 16\n"
    "s_add_u32 s53, s53, s22\n"
    "s_lshl_b32 s54, s19, 17\n"
    "s_lshl_b32 s22, s20, 8\n"
    "s_add_u32 s54, s54, s22\n"
    "s_mov_b64 s[28:29], s[92:93]\n"
    "s_mov_b32 s30, 0x400\n"
    "s_mov_b32 s31, 0x20000\n"
    "s_mov_b32 s8, 0x7ff00000\n"
    "s_mov_b32 s9, 0x7ff00000\n"
    "s_mov_b32 s10, 0x7ff00000\n"
    "s_mov_b32 s11, 0x7ff00000\n"
    "s_mov_b32 s12, 0x7ff00000\n"
    "s_mov_b32 s58, 0\n"
    "s_mov_b32 s56, 0\n"
    "v_mov_b32 v56, 0\n"
    "v_mov_b32 v57, 0\n"
    "s_and_b32 s22, s18, 7\n"
    "s_lshl_b32 s22, s22, 2\n"
    "s_add_u32 s22, s22, s57\n"
    "s_lshl_b32 s22, s22, 2\n"
    "s_add_u32 s60, s22, s19\n"
    "s_lshl_b32 s23, s60, 14\n"
    "v_and_b32 v20, 15, v1\n"
    "v_lshlrev_b32 v20, 10, v20\n"
    "v_lshrrev_b32 v21, 4, v1\n"
    "v_lshlrev_b32 v21, 4, v21\n"
    "v_add_u32 v20, v20, v21\n"
    "buffer_load_dwordx4 v[64:67], v20, s[24:27], s23 offen\n"
    "buffer_load_dwordx4 v[128:131], v21, s[28:31], 0 offen\n"
    "buffer_load_dwordx4 v[68:71], v20, s[24:27], s23 offen offset:64\n"
    "buffer_load_dwordx4 v[132:135], v21, s[28:31], 0 offen offset:64\n"
    "buffer_load_dwordx4 v[72:75], v20, s[24:27], s23 offen offset:128\n"
    "buffer_load_dwordx4 v[136:139], v21, s[28:31], 0 offen offset:128\n"
    "buffer_load_dwordx4 v[76:79], v20, s[24:27], s23 offen offset:192\n"
    "buffer_load_dwordx4 v[140:143], v21, s[28:31], 0 offen offset:192\n"
    "buffer_load_dwordx4 v[80:83], v20, s[24:27], s23 offen offset:256\n"
    "buffer_load_dwordx4 v[144:147], v21, s[28:31], 0 offen offset:256\n"
    "buffer_load_dwordx4 v[84:87], v20, s[24:27], s23 offen offset:320\n"
    "buffer_load_dwordx4 v[148:151], v21, s[28:31], 0 offen offset:320\n"
    "buffer_load_dwordx4 v[88:91], v20, s[24:27], s23 offen offset:384\n"
    "buffer_load_dwordx4 v[152:155], v21, s[28:31], 0 offen offset:384\n"
    "buffer_load_dwordx4 v[92:95], v20, s[24:27], s23 offen offset:448\n"
    "buffer_load_dwordx4 v[156:159], v21, s[28:31], 0 offen offset:448\n"
    "buffer_load_dwordx4 v[96:99], v20, s[24:27], s23 offen offset:512\n"
    "buffer_load_dwordx4 v[160:163], v21, s[28:31], 0 offen offset:512\n"
    "buffer_load_dwordx4 v[100:103], v20, s[24:27], s23 offen offset:576\n"
    "buffer_load_dwordx4 v[164:167], v21, s[28:31], 0 offen offset:576\n"
    "buffer_load_dwordx4 v[104:107], v20, s[24:27], s23 offen offset:640\n"
    "buffer_load_dwordx4 v[168:171], v21, s[28:31], 0 offen offset:640\n"
    "buffer_load_dwordx4 v[108:111], v20, s[24:27], s23 offen offset:704\n"
    "buffer_load_dwordx4 v[172:175], v21, s[28:31], 0 offen offset:704\n"
    "buffer_load_dwordx4 v[112:115], v20, s[24:27], s23 offen offset:768\n"
    "buffer_load_dwordx4 v[176:179], v21, s[28:31], 0 offen offset:768\n"
    "buffer_load_dwordx4 v[116:119], v20, s[24:27], s23 offen offset:832\n"
    "buffer_load_dwordx4 v[180:183], v21, s[28:31], 0 offen offset:832\n"
    "buffer_load_dwordx4 v[120:123], v20, s[24:27], s23 offen offset:896\n"
    "buffer_load_dwordx4 v[184:187], v21, s[28:31], 0 offen offset:896\n"
    "buffer_load_dwordx4 v[124:127], v20, s[24:27], s23 offen offset:960\n"
    "buffer_load_dwordx4 v[188:191], v21, s[28:31], 0 offen offset:960\n"
    "s_mov_b32 s6, s60\n"
    "s_add_u32 s52, s53, 0\n"
    "s_add_u32 m0, s17, 0\n"
    "s_nop 0\n"
    "buffer_load_dwordx4 v4, s[36:39], s52 offen lds\n"
    "buffer_load_dwordx4 v5, s[36:39], s52 offen offset:1024 lds\n"
    "buffer_load_dwordx4 v6, s[36:39], s52 offen offset:2048 lds\n"
    "buffer_load_dwordx4 v7, s[36:39], s52 offen offset:3072 lds\n"
    "s_add_u32 s52, s52, 0x8000\n"
    "s_add_u32 m0, m0, 0x1000\n"
    "s_nop 0\n"
    "buffer_load_dwordx4 v4, s[36:39], s52 offen lds\n"
    "buffer_load_dwordx4 v5, s[36:39], s52 offen offset:1024 lds\n"
    "buffer_load_dwordx4 v6, s[36:39], s52 offen offset:2048 lds\n"
    "buffer_load_dwordx4 v7, s[36:39], s52 offen offset:3072 lds\n"
    "s_add_u32 s13, s54, 0\n"
    "s_add_u32 m0, s16, 0x8000\n"
    "s_nop 0\n"
    "buffer_load_dwordx4 v8, s[40:43], s13 offen lds\n"
    "buffer_load_dwordx4 v9, s[40:43], s13 offen offset:1024 lds\n"
    "buffer_load_dwordx4 v10, s[40:43], s13 offen offset:2048 lds\n"
    "buffer_load_dwordx4 v11, s[40:43], s13 offen offset:3072 lds\n"
    "s_add_u32 m0, m0, 0x4000\n"
    "s_nop 0\n"
    "buffer_load_dwordx4 v8, s[64:67], s13 offen lds\n"
    "buffer_load_dwordx4 v9, s[64:67], s13 offen offset:1024 lds\n"
    "buffer_load_dwordx4 v10, s[64:67], s13 offen offset:2048 lds\n"
    "buffer_load_dwordx4 v11, s[64:67], s13 offen offset:3072 lds\n"
    "s_waitcnt vmcnt(16)\n"
    "s_add_u32 s52, s53, 0x80\n"
    "s_add_u32 m0, s17, 0x10000\n"
    "s_nop 0\n"
    "buffer_load_dwordx4 v4, s[36:39], s52 offen lds\n"
    "buffer_load_dwordx4 v5, s[36:39], s52 offen offset:1024 lds\n"
    "buffer_load_dwordx4 v6, s[36:39], s52 offen offset:2048 lds\n"
    "buffer_load_dwordx4 v7, s[36:39], s52 offen offset:3072 lds\n"
    "s_add_u32 s52, s52, 0x8000\n"
    "s_add_u32 m0, m0, 0x1000\n"
    "s_nop 0\n"
    "buffer_load_dwordx4 v4, s[36:39], s52 offen lds\n"
    "buffer_load_dwordx4 v5, s[36:39], s52 offen offset:1024 lds\n"
    "buffer_load_dwordx4 v6, s[36:39], s52 offen offset:2048 lds\n"
    "buffer_load_dwordx4 v7, s[36:39], s52 offen offset:3072 lds\n"
    "s_add_u32 s13, s54, 0x80000\n"
    "s_add_u32 m0, s16, 0x18000\n"
    "s_nop 0\n"
    "buffer_load_dwordx4 v8, s[40:43], s13 offen lds\n"
    "buffer_load_dwordx4 v9, s[40:43], s13 offen offset:1024 lds\n"
    "buffer_load_dwordx4 v10, s[40:43], s13 offen offset:2048 lds\n"
    "buffer_load_dwordx4 v11, s[40:43], s13 offen offset:3072 lds\n"
    "s_add_u32 m0, m0, 0x4000\n"
    "s_nop 0\n"
    "buffer_load_dwordx4 v8, s[64:67], s13 offen lds\n"
    "buffer_load_dwordx4 v9, s[64:67], s13 offen offset:1024 lds\n"
    "buffer_load_dwordx4 v10, s[64:67], s13 offen offset:2048 lds\n"
    "buffer_load_dwordx4 v11, s[64:67], s13 offen offset:3072 lds\n"
    "v_mfma_f32_16x16x32_bf16 v[32:35], v[128:131], v[64:67], 0\n"
    "v_mfma_f32_16x16x32_bf16 v[32:35], v[132:135], v[68:71], v[32:35]\n"
    "v_mfma_f32_16x16x32_bf16 v[32:35], v[136:139], v[72:75], v[32:35]\n"
    "v_mfma_f32_16x16x32_bf16 v[32:35], v[140:143], v[76:79], v[32:35]\n"
    "v_mfma_f32_16x16x32_bf16 v[32:35], v[144:147], v[80:83], v[32:35]\n"
    "v_mfma_f32_16x16x32_bf16 v[32:35], v[148:151], v[84:87], v[32:35]\n"
    "v_mfma_f32_16x16x32_bf16 v[32:35], v[152:155], v[88:91], v[32:35]\n"
    "v_mfma_f32_16x16x32_bf16 v[32:35], v[156:159], v[92:95], v[32:35]\n"
    "v_mfma_f32_16x16x32_bf16 v[32:35], v[160:163], v[96:99], v[32:35]\n"
    "v_mfma_f32_16x16x32_bf16 v[32:35], v[164:167], v[100:103], v[32:35]\n"
    "v_mfma_f32_16x16x32_bf16 v[32:35], v[168:171], v[104:107], v[32:35]\n"
    "v_mfma_f32_16x16x32_bf16 v[32:35], v[172:175], v[108:111], v[32:35]\n"
    "v_mfma_f32_16x16x32_bf16 v[32:35], v[176:179], v[112:115], v[32:35]\n"
    "v_mfma_f32_16x16x32_bf16 v[32:35], v[180:183], v[116:119], v[32:35]\n"
    "v_mfma_f32_16x16x32_bf16 v[32:35], v[184:187], v[120:123], v[32:35]\n"
    "v_mfma_f32_16x16x32_bf16 v[32:35], v[188:191], v[124:127], v[32:35]\n"
    "v_mov_b32 v192, 0\n"
    "v_mov_b32 v193, 0\n"
    "v_mov_b32 v194, 0\n"
    "v_mov_b32 v195, 0\n"
    "v_mov_b32 v196, 0\n"
    "v_mov_b32 v197, 0\n"
    "v_mov_b32 v198, 0\n"
    "v_mov_b32 v199, 0\n"
    "v_mov_b32 v200, 0\n"
    "v_mov_b32 v201, 0\n"
    "v_mov_b32 v202, 0\n"
    "v_mov_b32 v203, 0\n"
    "v_mov_b32 v204, 0\n"
    "v_mov_b32 v205, 0\n"
    "v_mov_b32 v206, 0\n"
    "v_mov_b32 v207, 0\n"
    "v_mov_b32 v208, 0\n"
    "v_mov_b32 v209, 0\n"
    "v_mov_b32 v210, 0\n"
    "v_mov_b32 v211, 0\n"
    "v_mov_b32 v212, 0\n"
    "v_mov_b32 v213, 0\n"
    "v_mov_b32 v214, 0\n"
    "v_mov_b32 v215, 0\n"
    "v_mov_b32 v216, 0\n"
    "v_mov_b32 v217, 0\n"
    "v_mov_b32 v218, 0\n"
    "v_mov_b32 v219, 0\n"
    "v_mov_b32 v220, 0\n"
    "v_mov_b32 v221, 0\n"
    "v_mov_b32 v222, 0\n"
    "v_mov_b32 v223, 0\n"
    "v_mov_b32 v224, 0\n"
    "v_mov_b32 v225, 0\n"
    "v_mov_b32 v226, 0\n"
    "v_mov_b32 v227, 0\n"
    "v_mov_b32 v228, 0\n"
    "v_mov_b32 v229, 0\n"
    "v_mov_b32 v230, 0\n"
    "v_mov_b32 v231, 0\n"
    "v_mov_b32 v232, 0\n"
    "v_mov_b32 v233, 0\n"
    "v_mov_b32 v234, 0\n"
    "v_mov_b32 v235, 0\n"
    "v_mov_b32 v236, 0\n"
    "v_mov_b32 v237, 0\n"
    "v_mov_b32 v238, 0\n"
    "v_mov_b32 v239, 0\n"
    "v_mov_b32 v240, 0\n"
    "v_mov_b32 v241, 0\n"
    "v_mov_b32 v242, 0\n"
    "v_mov_b32 v243, 0\n"
    "v_mov_b32 v244, 0\n"
    "v_mov_b32 v245, 0\n"
    "v_mov_b32 v246, 0\n"
    "v_mov_b32 v247, 0\n"
    "v_mov_b32 v248, 0\n"
    "v_mov_b32 v249, 0\n"
    "v_mov_b32 v250, 0\n"
    "v_mov_b32 v251, 0\n"
    "v_mov_b32 v252, 0\n"
    "v_mov_b32 v253, 0\n"
    "v_mov_b32 v254, 0\n"
    "v_mov_b32 v255, 0\n"
    "v_mul_f32 v32, 0xbfb8aa3b, v32\n"
    "v_exp_f32 v32, v32\n"
    "s_nop 0\n"
    "v_add_f32 v32, 1.0, v32\n"
    "v_rcp_f32 v32, v32\n"
    "v_lshlrev_b32 v20, 2, v1\n"
    "s_lshl_b32 s22, s6, 6\n"
    "s_add_u32 s22, s22, 0x3c00000\n"
    "s_mov_b64 exec, 0xffff\n"
    "buffer_store_dword v32, v20, s[44:47], s22 offen sc1\n"
    "s_mov_b64 exec, -1\n"
    "v_readfirstlane_b32 s4, v48\n"
    "v_readfirstlane_b32 s5, v49\n"
    "s_nop 0\n"
    "s_mul_i32 s4, s4, 0x19660d\n"
    "s_mul_i32 s5, s5, 0x15a4e35\n"
    "s_add_u32 s4, s4, 0x3c6ef35f\n"
    "s_add_u32 s5, s5, 0x3039\n"
    "s_lshr_b32 s22, s19, 1\n"
    "s_lshl_b32 s22, s22, 14\n"
    "v_and_b32 v20, 15, v1\n"
    "v_lshrrev_b32 v21, 1, v20\n"
    "v_lshrrev_b32 v22, 4, v1\n"
    "v_xor_b32 v21, v21, v22\n"
    "v_lshlrev_b32 v21, 4, v21\n"
    "v_lshlrev_b32 v20, 7, v20\n"
    "v_add_u32 v20, v20, v21\n"
    "v_add_u32 v12, s22, v20\n"
    "v_xor_b32 v13, 64, v12\n"
    "s_and_b32 s22, s19, 1\n"
    "s_lshl_b32 s22, s22, 9\n"
    "s_add_u32 s22, s22, 0x8000\n"
    "v_lshrrev_b32 v20, 4, v1\n"
    "v_lshlrev_b32 v21, 11, v20\n"
    "v_and_b32 v20, 1, v20\n"
    "v_lshl_add_u32 v21, v20, 5, v21\n"
    "v_bfe_u32 v20, v1, 2, 2\n"
    "v_lshl_add_u32 v21, v20, 6, v21\n"
    "v_and_b32 v20, 3, v1\n"
    "v_lshl_add_u32 v21, v20, 3, v21\n"
    "v_add_u32 v14, s22, v21\n"
    "v_xor_b32 v15, 32, v14\n"
    "s_mov_b64 s[28:29], s[76:77]\n"
    "s_mov_b32 s30, 0x400000\n"
    "s_mov_b32 s31, 0x20000\n"
    "s_cmp_eq_u32 s19, 0\n"
    "s_cselect_b64 s[48:49], s[40:41], s[64:65]\n"
    "s_mov_b64 s[50:51], s[42:43]\n"
    "s_lshl_b32 s6, s21, 16\n"
    "s_lshl_b32 s22, s20, 8\n"
    "s_add_u32 s6, s6, s22\n"
    "s_add_u32 s6, s6, 0x100000\n"
    "s_lshl_b32 s22, s21, 2\n"
    "s_and_b32 s23, s20, 3\n"
    "s_add_u32 s22, s22, s23\n"
    "s_lshl_b32 s22, s22, 1\n"
    "s_and_b32 s23, s19, 1\n"
    "s_add_u32 s22, s22, s23\n"
    "s_lshl_b32 s22, s22, 15\n"
    "s_add_u32 s22, s22, 0x100\n"
    "s_mov_b32 s7, 0x80000\n"
    "v_bfe_u32 v20, v1, 1, 3\n"
    "v_lshlrev_b32 v20, 13, v20\n"
    "v_and_b32 v21, 1, v1\n"
    "v_lshl_add_u32 v20, v21, 7, v20\n"
    "v_and_b32 v21, 31, v1\n"
    "v_lshlrev_b32 v21, 10, v21\n"
    "s_cmp_ge_u32 s19, 2\n"
    "s_cselect_b64 s[48:49], s[36:37], s[48:49]\n"
    "s_cselect_b32 s50, s38, s50\n"
    "s_cselect_b32 s6, s22, s6\n"
    "s_cselect_b32 s7, 0x80, s7\n"
    "s_cselect_b64 vcc, -1, 0\n"
    "v_cndmask_b32 v54, v20, v21, vcc\n"
    "s_lshl_b32 s70, s21, 1\n"
    "s_and_b32 s22, s19, 1\n"
    "s_add_u32 s22, s22, s70\n"
    "s_sub_u32 s69, 31, s22\n"
    "s_cmp_lt_u32 s19, 2\n"
    "s_cselect_b32 s68, s69, s22\n"
    "s_lshl_b32 s63, s68, 16\n"
    "s_lshl_b32 s55, s56, 1\n"
    "s_and_b32 s22, s19, 1\n"
    "s_add_u32 s55, s55, s22\n"
    "s_lshl_b32 s55, s55, 7\n"
    "s_add_u32 s55, s55, 0x2000\n"
    "buffer_load_dword v55, v54, s[48:51], s6 offen\n"
    "s_add_u32 s6, s6, s7\n"
    "buffer_load_dword v55, v54, s[48:51], s6 offen\n"
    "s_add_u32 s6, s6, s7\n"
    "s_waitcnt vmcnt(19)\n"
    "P1S_%=:\n"
    "s_barrier\n"
    "ds_read_b64_tr_b16 v[96:97], v14\n"
    "ds_read_b64_tr_b16 v[98:99], v14 offset:1024\n"
    "ds_read_b64_tr_b16 v[112:113], v14 offset:16384\n"
    "ds_read_b64_tr_b16 v[114:115], v14 offset:17408\n"
    "ds_read_b64_tr_b16 v[100:101], v15\n"
    "ds_read_b64_tr_b16 v[102:103], v15 offset:1024\n"
    "ds_read_b64_tr_b16 v[116:117], v15 offset:16384\n"
    "ds_read_b64_tr_b16 v[118:119], v15 offset:17408\n"
    "ds_read_b64_tr_b16 v[104:105], v14 offset:256\n"
    "ds_read_b64_tr_b16 v[106:107], v14 offset:1280\n"
    "ds_read_b64_tr_b16 v[120:121], v14 offset:16640\n"
    "ds_read_b64_tr_b16 v[122:123], v14 offset:17664\n"
    "s_waitcnt lgkmcnt(4)\n"
    "ds_read_b64_tr_b16 v[108:109], v15 offset:256\n"
    "ds_read_b64_tr_b16 v[110:111], v15 offset:1280\n"
    "ds_read_b64_tr_b16 v[124:125], v15 offset:16640\n"
    "ds_read_b64_tr_b16 v[126:127], v15 offset:17664\n"
    "ds_read_b128 v[64:67], v12\n"
    "ds_read_b128 v[68:71], v12 offset:2048\n"
    "ds_read_b128 v[72:75], v12 offset:4096\n"
    "ds_read_b128 v[76:79], v12 offset:6144\n"
    "ds_read_b128 v[80:83], v12 offset:8192\n"
    "ds_read_b128 v[84:87], v12 offset:10240\n"
    "ds_read_b128 v[88:91], v12 offset:12288\n"
    "ds_read_b128 v[92:95], v12 offset:14336\n"
    "s_waitcnt lgkmcnt(0)\n"
    "v_mfma_f32_16x16x32_bf16 a[0:3], v[96:99], v[64:67], 0\n"
    "ds_read_b64_tr_b16 v[160:161], v14 offset:8192\n"
    "v_add_f32 v32, v192, v193\n"
    "v_mfma_f32_16x16x32_bf16 a[128:131], v[64:67], v[112:115], 0\n"
    "s_add_u32 s13, s54, 0x100000\n"
    "s_add_u32 m0, s16, 0x20000\n"
    "v_mfma_f32_16x16x32_bf16 a[32:35], v[100:103], v[64:67], 0\n"
    "ds_read_b64_tr_b16 v[162:163], v14 offset:9216\n"
    "v_add_f32 v33, v196, v197\n"
    "v_mfma_f32_16x16x32_bf16 a[160:163], v[64:67], v[116:119], 0\n"
    "buffer_load_dwordx4 v8, s[40:43], s13 offen lds\n"
    "v_mfma_f32_16x16x32_bf16 a[64:67], v[104:107], v[64:67], 0\n"
    "ds_read_b64_tr_b16 v[176:177], v14 offset:24576\n"
    "v_add_f32 v34, v200, v201\n"
    "v_mfma_f32_16x16x32_bf16 a[192:195], v[64:67], v[120:123], 0\n"
    "v_add_f32 v35, v204, v205\n"
    "v_add_f32 v32, v32, v194\n"
    "v_mfma_f32_16x16x32_bf16 a[96:99], v[108:111], v[64:67], 0\n"
    "ds_read_b64_tr_b16 v[178:179], v14 offset:25600\n"
    "v_add_f32 v33, v33, v198\n"
    "v_mfma_f32_16x16x32_bf16 a[224:227], v[64:67], v[124:127], 0\n"
    "buffer_load_dwordx4 v9, s[40:43], s13 offen offset:1024 lds\n"
    "v_mfma_f32_16x16x32_bf16 a[4:7], v[96:99], v[68:71], 0\n"
    "ds_read_b64_tr_b16 v[164:165], v15 offset:8192\n"
    "v_add_f32 v34, v34, v202\n"
    "v_mfma_f32_16x16x32_bf16 a[132:135], v[68:71], v[112:115], 0\n"
    "v_add_f32 v35, v35, v206\n"
    "v_add_f32 v32, v32, v195\n"
    "v_mfma_f32_16x16x32_bf16 a[36:39], v[100:103], v[68:71], 0\n"
    "ds_read_b64_tr_b16 v[166:167], v15 offset:9216\n"
    "v_add_f32 v33, v33, v199\n"
    "v_mfma_f32_16x16x32_bf16 a[164:167], v[68:71], v[116:119], 0\n"
    "buffer_load_dwordx4 v10, s[40:43], s13 offen offset:2048 lds\n"
    "v_mfma_f32_16x16x32_bf16 a[68:71], v[104:107], v[68:71], 0\n"
    "ds_read_b64_tr_b16 v[180:181], v15 offset:24576\n"
    "v_add_f32 v34, v34, v203\n"
    "v_mfma_f32_16x16x32_bf16 a[196:199], v[68:71], v[120:123], 0\n"
    "v_add_f32 v35, v35, v207\n"
    "v_add_f32 v32, v32, v208\n"
    "v_mfma_f32_16x16x32_bf16 a[100:103], v[108:111], v[68:71], 0\n"
    "ds_read_b64_tr_b16 v[182:183], v15 offset:25600\n"
    "v_add_f32 v33, v33, v212\n"
    "v_mfma_f32_16x16x32_bf16 a[228:231], v[68:71], v[124:127], 0\n"
    "buffer_load_dwordx4 v11, s[40:43], s13 offen offset:3072 lds\n"
    "v_mfma_f32_16x16x32_bf16 a[8:11], v[96:99], v[72:75], 0\n"
    "ds_read_b64_tr_b16 v[168:169], v14 offset:8448\n"
    "v_add_f32 v34, v34, v216\n"
    "v_mfma_f32_16x16x32_bf16 a[136:139], v[72:75], v[112:115], 0\n"
    "s_add_u32 m0, m0, 0x4000\n"
    "v_add_f32 v35, v35, v220\n"
    "v_mfma_f32_16x16x32_bf16 a[40:43], v[100:103], v[72:75], 0\n"
    "ds_read_b64_tr_b16 v[170:171], v14 offset:9472\n"
    "v_add_f32 v32, v32, v209\n"
    "v_mfma_f32_16x16x32_bf16 a[168:171], v[72:75], v[116:119], 0\n"
    "buffer_load_dwordx4 v8, s[64:67], s13 offen lds\n"
    "v_mfma_f32_16x16x32_bf16 a[72:75], v[104:107], v[72:75], 0\n"
    "ds_read_b64_tr_b16 v[184:185], v14 offset:24832\n"
    "v_add_f32 v33, v33, v213\n"
    "v_mfma_f32_16x16x32_bf16 a[200:203], v[72:75], v[120:123], 0\n"
    "v_add_f32 v34, v34, v217\n"
    "v_add_f32 v35, v35, v221\n"
    "v_mfma_f32_16x16x32_bf16 a[104:107], v[108:111], v[72:75], 0\n"
    "ds_read_b64_tr_b16 v[186:187], v14 offset:25856\n"
    "v_add_f32 v32, v32, v210\n"
    "v_mfma_f32_16x16x32_bf16 a[232:235], v[72:75], v[124:127], 0\n"
    "buffer_load_dwordx4 v9, s[64:67], s13 offen offset:1024 lds\n"
    "v_mfma_f32_16x16x32_bf16 a[12:15], v[96:99], v[76:79], 0\n"
    "ds_read_b64_tr_b16 v[172:173], v15 offset:8448\n"
    "v_add_f32 v33, v33, v214\n"
    "v_mfma_f32_16x16x32_bf16 a[140:143], v[76:79], v[112:115], 0\n"
    "v_add_f32 v34, v34, v218\n"
    "v_add_f32 v35, v35, v222\n"
    "v_mfma_f32_16x16x32_bf16 a[44:47], v[100:103], v[76:79], 0\n"
    "ds_read_b64_tr_b16 v[174:175], v15 offset:9472\n"
    "v_add_f32 v32, v32, v211\n"
    "v_mfma_f32_16x16x32_bf16 a[172:175], v[76:79], v[116:119], 0\n"
    "buffer_load_dwordx4 v10, s[64:67], s13 offen offset:2048 lds\n"
    "v_mfma_f32_16x16x32_bf16 a[76:79], v[104:107], v[76:79], 0\n"
    "ds_read_b64_tr_b16 v[188:189], v15 offset:24832\n"
    "v_add_f32 v33, v33, v215\n"
    "v_mfma_f32_16x16x32_bf16 a[204:207], v[76:79], v[120:123], 0\n"
    "v_add_f32 v34, v34, v219\n"
    "v_add_f32 v35, v35, v223\n"
    "v_mfma_f32_16x16x32_bf16 a[108:111], v[108:111], v[76:79], 0\n"
    "ds_read_b64_tr_b16 v[190:191], v15 offset:25856\n"
    "v_add_f32 v32, v32, v224\n"
    "v_mfma_f32_16x16x32_bf16 a[236:239], v[76:79], v[124:127], 0\n"
    "buffer_load_dwordx4 v11, s[64:67], s13 offen offset:3072 lds\n"
    "v_mfma_f32_16x16x32_bf16 a[16:19], v[96:99], v[80:83], 0\n"
    "ds_read_b128 v[128:131], v13\n"
    "v_add_f32 v33, v33, v228\n"
    "v_mfma_f32_16x16x32_bf16 a[144:147], v[80:83], v[112:115], 0\n"
    "v_add_f32 v34, v34, v232\n"
    "v_add_f32 v35, v35, v236\n"
    "v_mfma_f32_16x16x32_bf16 a[48:51], v[100:103], v[80:83], 0\n"
    "ds_read_b128 v[132:135], v13 offset:2048\n"
    "v_add_f32 v32, v32, v225\n"
    "v_mfma_f32_16x16x32_bf16 a[176:179], v[80:83], v[116:119], 0\n"
    "v_add_f32 v33, v33, v229\n"
    "v_add_f32 v34, v34, v233\n"
    "v_mfma_f32_16x16x32_bf16 a[80:83], v[104:107], v[80:83], 0\n"
    "ds_read_b128 v[136:139], v13 offset:4096\n"
    "v_add_f32 v35, v35, v237\n"
    "v_mfma_f32_16x16x32_bf16 a[208:211], v[80:83], v[120:123], 0\n"
    "v_add_f32 v32, v32, v226\n"
    "v_add_f32 v33, v33, v230\n"
    "v_mfma_f32_16x16x32_bf16 a[112:115], v[108:111], v[80:83], 0\n"
    "ds_read_b128 v[140:143], v13 offset:6144\n"
    "v_add_f32 v34, v34, v234\n"
    "v_mfma_f32_16x16x32_bf16 a[240:243], v[80:83], v[124:127], 0\n"
    "v_add_f32 v35, v35, v238\n"
    "v_add_f32 v32, v32, v227\n"
    "v_mfma_f32_16x16x32_bf16 a[20:23], v[96:99], v[84:87], 0\n"
    "ds_read_b128 v[144:147], v13 offset:8192\n"
    "v_add_f32 v33, v33, v231\n"
    "v_mfma_f32_16x16x32_bf16 a[148:151], v[84:87], v[112:115], 0\n"
    "v_add_f32 v34, v34, v235\n"
    "v_add_f32 v35, v35, v239\n"
    "v_mfma_f32_16x16x32_bf16 a[52:55], v[100:103], v[84:87], 0\n"
    "ds_read_b128 v[148:151], v13 offset:10240\n"
    "v_add_f32 v32, v32, v240\n"
    "v_mfma_f32_16x16x32_bf16 a[180:183], v[84:87], v[116:119], 0\n"
    "v_add_f32 v33, v33, v244\n"
    "v_add_f32 v34, v34, v248\n"
    "v_mfma_f32_16x16x32_bf16 a[84:87], v[104:107], v[84:87], 0\n"
    "ds_read_b128 v[152:155], v13 offset:12288\n"
    "v_add_f32 v35, v35, v252\n"
    "v_mfma_f32_16x16x32_bf16 a[212:215], v[84:87], v[120:123], 0\n"
    "v_add_f32 v32, v32, v241\n"
    "v_add_f32 v33, v33, v245\n"
    "v_mfma_f32_16x16x32_bf16 a[116:119], v[108:111], v[84:87], 0\n"
    "ds_read_b128 v[156:159], v13 offset:14336\n"
    "v_add_f32 v34, v34, v249\n"
    "v_mfma_f32_16x16x32_bf16 a[244:247], v[84:87], v[124:127], 0\n"
    "v_add_f32 v35, v35, v253\n"
    "v_add_f32 v32, v32, v242\n"
    "v_mfma_f32_16x16x32_bf16 a[24:27], v[96:99], v[88:91], 0\n"
    "v_add_f32 v33, v33, v246\n"
    "v_add_f32 v34, v34, v250\n"
    "v_mfma_f32_16x16x32_bf16 a[152:155], v[88:91], v[112:115], 0\n"
    "v_add_f32 v35, v35, v254\n"
    "v_add_f32 v32, v32, v243\n"
    "v_mfma_f32_16x16x32_bf16 a[56:59], v[100:103], v[88:91], 0\n"
    "v_add_f32 v33, v33, v247\n"
    "v_add_f32 v34, v34, v251\n"
    "v_mfma_f32_16x16x32_bf16 a[184:187], v[88:91], v[116:119], 0\n"
    "v_add_f32 v35, v35, v255\n"
    "v_mul_f32 v36, v192, v192\n"
    "v_mfma_f32_16x16x32_bf16 a[88:91], v[104:107], v[88:91], 0\n"
    "v_xor_b32 v12, 0x10000, v12\n"
    "v_xor_b32 v13, 0x10000, v13\n"
    "v_mfma_f32_16x16x32_bf16 a[216:219], v[88:91], v[120:123], 0\n"
    "v_add_u32 v14, 0x10000, v14\n"
    "v_add_u32 v15, 0x10000, v15\n"
    "v_mfma_f32_16x16x32_bf16 a[120:123], v[108:111], v[88:91], 0\n"
    "v_mul_f32 v37, v196, v196\n"
    "v_mul_f32 v38, v200, v200\n"
    "v_mfma_f32_16x16x32_bf16 a[248:251], v[88:91], v[124:127], 0\n"
    "v_mul_f32 v39, v204, v204\n"
    "v_fmac_f32 v36, v193, v193\n"
    "v_mfma_f32_16x16x32_bf16 a[28:31], v[96:99], v[92:95], 0\n"
    "v_fmac_f32 v37, v197, v197\n"
    "v_fmac_f32 v38, v201, v201\n"
    "v_mfma_f32_16x16x32_bf16 a[156:159], v[92:95], v[112:115], 0\n"
    "v_fmac_f32 v39, v205, v205\n"
    "v_fmac_f32 v36, v194, v194\n"
    "v_mfma_f32_16x16x32_bf16 a[60:63], v[100:103], v[92:95], 0\n"
    "v_fmac_f32 v37, v198, v198\n"
    "v_fmac_f32 v38, v202, v202\n"
    "v_mfma_f32_16x16x32_bf16 a[188:191], v[92:95], v[116:119], 0\n"
    "v_fmac_f32 v39, v206, v206\n"
    "v_fmac_f32 v36, v195, v195\n"
    "v_mfma_f32_16x16x32_bf16 a[92:95], v[104:107], v[92:95], 0\n"
    "v_fmac_f32 v37, v199, v199\n"
    "v_fmac_f32 v38, v203, v203\n"
    "v_mfma_f32_16x16x32_bf16 a[220:223], v[92:95], v[120:123], 0\n"
    "v_fmac_f32 v39, v207, v207\n"
    "v_fmac_f32 v36, v208, v208\n"
    "v_mfma_f32_16x16x32_bf16 a[124:127], v[108:111], v[92:95], 0\n"
    "v_fmac_f32 v37, v212, v212\n"
    "v_fmac_f32 v38, v216, v216\n"
    "v_mfma_f32_16x16x32_bf16 a[252:255], v[92:95], v[124:127], 0\n"
    "v_fmac_f32 v39, v220, v220\n"
    "v_fmac_f32 v36, v209, v209\n"
    "s_waitcnt vmcnt(10) lgkmcnt(0)\n"
    "s_barrier\n"
    "v_mfma_f32_16x16x32_bf16 a[0:3], v[160:163], v[128:131], a[0:3]\n"
    "ds_read_b64_tr_b16 v[96:97], v14\n"
    "v_fmac_f32 v37, v213, v213\n"
    "v_mfma_f32_16x16x32_bf16 a[128:131], v[128:131], v[176:179], a[128:131]\n"
    "s_add_u32 s52, s53, 0x100\n"
    "s_add_u32 m0, s17, 0\n"
    "v_mfma_f32_16x16x32_bf16 a[32:35], v[164:167], v[128:131], a[32:35]\n"
    "ds_read_b64_tr_b16 v[98:99], v14 offset:1024\n"
    "v_fmac_f32 v38, v217, v217\n"
    "v_mfma_f32_16x16x32_bf16 a[160:163], v[128:131], v[180:183], a[160:163]\n"
    "buffer_load_dwordx4 v4, s[36:39], s52 offen lds\n"
    "v_mfma_f32_16x16x32_bf16 a[64:67], v[168:171], v[128:131], a[64:67]\n"
    "ds_read_b64_tr_b16 v[112:113], v14 offset:16384\n"
    "v_fmac_f32 v39, v221, v221\n"
    "v_mfma_f32_16x16x32_bf16 a[192:195], v[128:131], v[184:187], a[192:195]\n"
    "v_fmac_f32 v36, v210, v210\n"
    "v_fmac_f32 v37, v214, v214\n"
    "v_mfma_f32_16x16x32_bf16 a[96:99], v[172:175], v[128:131], a[96:99]\n"
    "ds_read_b64_tr_b16 v[114:115], v14 offset:17408\n"
    "v_fmac_f32 v38, v218, v218\n"
    "v_mfma_f32_16x16x32_bf16 a[224:227], v[128:131], v[188:191], a[224:227]\n"
    "buffer_load_dwordx4 v5, s[36:39], s52 offen offset:1024 lds\n"
    "v_mfma_f32_16x16x32_bf16 a[4:7], v[160:163], v[132:135], a[4:7]\n"
    "ds_read_b64_tr_b16 v[100:101], v15\n"
    "v_fmac_f32 v39, v222, v222\n"
    "v_mfma_f32_16x16x32_bf16 a[132:135], v[132:135], v[176:179], a[132:135]\n"
    "v_fmac_f32 v36, v211, v211\n"
    "v_fmac_f32 v37, v215, v215\n"
    "v_mfma_f32_16x16x32_bf16 a[36:39], v[164:167], v[132:135], a[36:39]\n"
    "ds_read_b64_tr_b16 v[102:103], v15 offset:1024\n"
    "v_fmac_f32 v38, v219, v219\n"
    "v_mfma_f32_16x16x32_bf16 a[164:167], v[132:135], v[180:183], a[164:167]\n"
    "buffer_load_dwordx4 v6, s[36:39], s52 offen offset:2048 lds\n"
    "v_mfma_f32_16x16x32_bf16 a[68:71], v[168:171], v[132:135], a[68:71]\n"
    "ds_read_b64_tr_b16 v[116:117], v15 offset:16384\n"
    "v_fmac_f32 v39, v223, v223\n"
    "v_mfma_f32_16x16x32_bf16 a[196:199], v[132:135], v[184:187], a[196:199]\n"
    "v_fmac_f32 v36, v224, v224\n"
    "v_fmac_f32 v37, v228, v228\n"
    "v_mfma_f32_16x16x32_bf16 a[100:103], v[172:175], v[132:135], a[100:103]\n"
    "ds_read_b64_tr_b16 v[118:119], v15 offset:17408\n"
    "v_fmac_f32 v38, v232, v232\n"
    "v_mfma_f32_16x16x32_bf16 a[228:231], v[132:135], v[188:191], a[228:231]\n"
    "buffer_load_dwordx4 v7, s[36:39], s52 offen offset:3072 lds\n"
    "v_mfma_f32_16x16x32_bf16 a[8:11], v[160:163], v[136:139], a[8:11]\n"
    "ds_read_b64_tr_b16 v[104:105], v14 offset:256\n"
    "s_add_u32 s52, s52, 0x8000\n"
    "v_mfma_f32_16x16x32_bf16 a[136:139], v[136:139], v[176:179], a[136:139]\n"
    "s_add_u32 m0, m0, 0x1000\n"
    "v_fmac_f32 v39, v236, v236\n"
    "v_mfma_f32_16x16x32_bf16 a[40:43], v[164:167], v[136:139], a[40:43]\n"
    "ds_read_b64_tr_b16 v[106:107], v14 offset:1280\n"
    "v_fmac_f32 v36, v225, v225\n"
    "v_mfma_f32_16x16x32_bf16 a[168:171], v[136:139], v[180:183], a[168:171]\n"
    "buffer_load_dwordx4 v4, s[36:39], s52 offen lds\n"
    "v_mfma_f32_16x16x32_bf16 a[72:75], v[168:171], v[136:139], a[72:75]\n"
    "ds_read_b64_tr_b16 v[120:121], v14 offset:16640\n"
    "v_fmac_f32 v37, v229, v229\n"
    "v_mfma_f32_16x16x32_bf16 a[200:203], v[136:139], v[184:187], a[200:203]\n"
    "v_fmac_f32 v38, v233, v233\n"
    "v_fmac_f32 v39, v237, v237\n"
    "v_mfma_f32_16x16x32_bf16 a[104:107], v[172:175], v[136:139], a[104:107]\n"
    "ds_read_b64_tr_b16 v[122:123], v14 offset:17664\n"
    "v_fmac_f32 v36, v226, v226\n"
    "v_mfma_f32_16x16x32_bf16 a[232:235], v[136:139], v[188:191], a[232:235]\n"
    "buffer_load_dwordx4 v5, s[36:39], s52 offen offset:1024 lds\n"
    "v_mfma_f32_16x16x32_bf16 a[12:15], v[160:163], v[140:143], a[12:15]\n"
    "ds_read_b64_tr_b16 v[108:109], v15 offset:256\n"
    "v_fmac_f32 v37, v230, v230\n"
    "v_mfma_f32_16x16x32_bf16 a[140:143], v[140:143], v[176:179], a[140:143]\n"
    "v_fmac_f32 v38, v234, v234\n"
    "v_fmac_f32 v39, v238, v238\n"
    "v_mfma_f32_16x16x32_bf16 a[44:47], v[164:167], v[140:143], a[44:47]\n"
    "ds_read_b64_tr_b16 v[110:111], v15 offset:1280\n"
    "v_fmac_f32 v36, v227, v227\n"
    "v_mfma_f32_16x16x32_bf16 a[172:175], v[140:143], v[180:183], a[172:175]\n"
    "buffer_load_dwordx4 v6, s[36:39], s52 offen offset:2048 lds\n"
    "v_mfma_f32_16x16x32_bf16 a[76:79], v[168:171], v[140:143], a[76:79]\n"
    "ds_read_b64_tr_b16 v[124:125], v15 offset:16640\n"
    "v_fmac_f32 v37, v231, v231\n"
    "v_mfma_f32_16x16x32_bf16 a[204:207], v[140:143], v[184:187], a[204:207]\n"
    "v_fmac_f32 v38, v235, v235\n"
    "v_fmac_f32 v39, v239, v239\n"
    "v_mfma_f32_16x16x32_bf16 a[108:111], v[172:175], v[140:143], a[108:111]\n"
    "ds_read_b64_tr_b16 v[126:127], v15 offset:17664\n"
    "v_fmac_f32 v36, v240, v240\n"
    "v_mfma_f32_16x16x32_bf16 a[236:239], v[140:143], v[188:191], a[236:239]\n"
    "buffer_load_dwordx4 v7, s[36:39], s52 offen offset:3072 lds\n"
    "v_mfma_f32_16x16x32_bf16 a[16:19], v[160:163], v[144:147], a[16:19]\n"
    "ds_read_b128 v[64:67], v12\n"
    "s_add_u32 s13, s54, 0x180000\n"
    "v_mfma_f32_16x16x32_bf16 a[144:147], v[144:147], v[176:179], a[144:147]\n"
    "s_add_u32 m0, s16, 0x8000\n"
    "v_fmac_f32 v37, v244, v244\n"
    "v_mfma_f32_16x16x32_bf16 a[48:51], v[164:167], v[144:147], a[48:51]\n"
    "ds_read_b128 v[68:71], v12 offset:2048\n"
    "v_fmac_f32 v38, v248, v248\n"
    "v_mfma_f32_16x16x32_bf16 a[176:179], v[144:147], v[180:183], a[176:179]\n"
    "buffer_load_dwordx4 v8, s[40:43], s13 offen lds\n"
    "v_mfma_f32_16x16x32_bf16 a[80:83], v[168:171], v[144:147], a[80:83]\n"
    "ds_read_b128 v[72:75], v12 offset:4096\n"
    "v_fmac_f32 v39, v252, v252\n"
    "v_mfma_f32_16x16x32_bf16 a[208:211], v[144:147], v[184:187], a[208:211]\n"
    "v_fmac_f32 v36, v241, v241\n"
    "v_fmac_f32 v37, v245, v245\n"
    "v_mfma_f32_16x16x32_bf16 a[112:115], v[172:175], v[144:147], a[112:115]\n"
    "ds_read_b128 v[76:79], v12 offset:6144\n"
    "v_fmac_f32 v38, v249, v249\n"
    "v_mfma_f32_16x16x32_bf16 a[240:243], v[144:147], v[188:191], a[240:243]\n"
    "buffer_load_dwordx4 v9, s[40:43], s13 offen offset:1024 lds\n"
    "v_mfma_f32_16x16x32_bf16 a[20:23], v[160:163], v[148:151], a[20:23]\n"
    "ds_read_b128 v[80:83], v12 offset:8192\n"
    "v_fmac_f32 v39, v253, v253\n"
    "v_mfma_f32_16x16x32_bf16 a[148:151], v[148:151], v[176:179], a[148:151]\n"
    "v_fmac_f32 v36, v242, v242\n"
    "v_fmac_f32 v37, v246, v246\n"
    "v_mfma_f32_16x16x32_bf16 a[52:55], v[164:167], v[148:151], a[52:55]\n"
    "ds_read_b128 v[84:87], v12 offset:10240\n"
    "v_fmac_f32 v38, v250, v250\n"
    "v_mfma_f32_16x16x32_bf16 a[180:183], v[148:151], v[180:183], a[180:183]\n"
    "buffer_load_dwordx4 v10, s[40:43], s13 offen offset:2048 lds\n"
    "v_mfma_f32_16x16x32_bf16 a[84:87], v[168:171], v[148:151], a[84:87]\n"
    "ds_read_b128 v[88:91], v12 offset:12288\n"
    "v_fmac_f32 v39, v254, v254\n"
    "v_mfma_f32_16x16x32_bf16 a[212:215], v[148:151], v[184:187], a[212:215]\n"
    "v_fmac_f32 v36, v243, v243\n"
    "v_fmac_f32 v37, v247, v247\n"
    "v_mfma_f32_16x16x32_bf16 a[116:119], v[172:175], v[148:151], a[116:119]\n"
    "ds_read_b128 v[92:95], v12 offset:14336\n"
    "v_fmac_f32 v38, v251, v251\n"
    "v_mfma_f32_16x16x32_bf16 a[244:247], v[148:151], v[188:191], a[244:247]\n"
    "buffer_load_dwordx4 v11, s[40:43], s13 offen offset:3072 lds\n"
    "v_mfma_f32_16x16x32_bf16 a[24:27], v[160:163], v[152:155], a[24:27]\n"
    "s_add_u32 m0, m0, 0x4000\n"
    "v_fmac_f32 v39, v255, v255\n"
    "v_mfma_f32_16x16x32_bf16 a[152:155], v[152:155], v[176:179], a[152:155]\n"
    "v_mov_b32 v40, v32\n"
    "v_mov_b32 v41, v33\n"
    "v_mfma_f32_16x16x32_bf16 a[56:59], v[164:167], v[152:155], a[56:59]\n"
    "buffer_load_dwordx4 v8, s[64:67], s13 offen lds\n"
    "v_mfma_f32_16x16x32_bf16 a[184:187], v[152:155], v[180:183], a[184:187]\n"
    "v_mov_b32 v42, v34\n"
    "v_mov_b32 v43, v35\n"
    "v_mfma_f32_16x16x32_bf16 a[88:91], v[168:171], v[152:155], a[88:91]\n"
    "v_mov_b32 v16, v36\n"
    "v_mov_b32 v17, v37\n"
    "v_mfma_f32_16x16x32_bf16 a[216:219], v[152:155], v[184:187], a[216:219]\n"
    "buffer_load_dwordx4 v9, s[64:67], s13 offen offset:1024 lds\n"
    "v_mfma_f32_16x16x32_bf16 a[120:123], v[172:175], v[152:155], a[120:123]\n"
    "v_mov_b32 v18, v38\n"
    "v_mov_b32 v19, v39\n"
    "v_mfma_f32_16x16x32_bf16 a[248:251], v[152:155], v[188:191], a[248:251]\n"
    "v_permlane32_swap_b32 v32, v40\n"
    "v_permlane32_swap_b32 v33, v41\n"
    "v_mfma_f32_16x16x32_bf16 a[28:31], v[160:163], v[156:159], a[28:31]\n"
    "buffer_load_dwordx4 v10, s[64:67], s13 offen offset:2048 lds\n"
    "v_mfma_f32_16x16x32_bf16 a[156:159], v[156:159], v[176:179], a[156:159]\n"
    "v_permlane32_swap_b32 v34, v42\n"
    "v_permlane32_swap_b32 v35, v43\n"
    "v_mfma_f32_16x16x32_bf16 a[60:63], v[164:167], v[156:159], a[60:63]\n"
    "v_permlane32_swap_b32 v36, v16\n"
    "v_permlane32_swap_b32 v37, v17\n"
    "v_mfma_f32_16x16x32_bf16 a[188:191], v[156:159], v[180:183], a[188:191]\n"
    "buffer_load_dwordx4 v11, s[64:67], s13 offen offset:3072 lds\n"
    "v_mfma_f32_16x16x32_bf16 a[92:95], v[168:171], v[156:159], a[92:95]\n"
    "v_permlane32_swap_b32 v38, v18\n"
    "v_permlane32_swap_b32 v39, v19\n"
    "v_mfma_f32_16x16x32_bf16 a[220:223], v[156:159], v[184:187], a[220:223]\n"
    "v_add_f32 v32, v32, v40\n"
    "v_add_f32 v33, v33, v41\n"
    "v_mfma_f32_16x16x32_bf16 a[124:127], v[172:175], v[156:159], a[124:127]\n"
    "v_add_f32 v34, v34, v42\n"
    "v_add_f32 v35, v35, v43\n"
    "v_mfma_f32_16x16x32_bf16 a[252:255], v[156:159], v[188:191], a[252:255]\n"
    "v_add_f32 v36, v36, v16\n"
    "v_add_f32 v37, v37, v17\n"
    "buffer_load_dword v55, v54, s[48:51], s6 offen\n"
    "s_add_u32 s6, s6, s7\n"
    "s_waitcnt lgkmcnt(0)\n"
    "v_mfma_f32_16x16x32_bf16 a[0:3], v[96:99], v[64:67], a[0:3]\n"
    "ds_read_b64_tr_b16 v[160:161], v14 offset:8192\n"
    "v_add_f32 v38, v38, v18\n"
    "v_mfma_f32_16x16x32_bf16 a[128:131], v[64:67], v[112:115], a[128:131]\n"
    "v_add_f32 v39, v39, v19\n"
    "v_mov_b32 v40, v32\n"
    "v_mfma_f32_16x16x32_bf16 a[32:35], v[100:103], v[64:67], a[32:35]\n"
    "ds_read_b64_tr_b16 v[162:163], v14 offset:9216\n"
    "v_mov_b32 v41, v33\n"
    "v_mfma_f32_16x16x32_bf16 a[160:163], v[64:67], v[116:119], a[160:163]\n"
    "v_mov_b32 v42, v34\n"
    "v_mov_b32 v43, v35\n"
    "v_mfma_f32_16x16x32_bf16 a[64:67], v[104:107], v[64:67], a[64:67]\n"
    "ds_read_b64_tr_b16 v[176:177], v14 offset:24576\n"
    "v_mov_b32 v16, v36\n"
    "v_mfma_f32_16x16x32_bf16 a[192:195], v[64:67], v[120:123], a[192:195]\n"
    "v_mov_b32 v17, v37\n"
    "v_mov_b32 v18, v38\n"
    "v_mfma_f32_16x16x32_bf16 a[96:99], v[108:111], v[64:67], a[96:99]\n"
    "ds_read_b64_tr_b16 v[178:179], v14 offset:25600\n"
    "v_mov_b32 v19, v39\n"
    "v_mfma_f32_16x16x32_bf16 a[224:227], v[64:67], v[124:127], a[224:227]\n"
    "v_permlane16_swap_b32 v32, v40\n"
    "v_permlane16_swap_b32 v33, v41\n"
    "v_mfma_f32_16x16x32_bf16 a[4:7], v[96:99], v[68:71], a[4:7]\n"
    "ds_read_b64_tr_b16 v[164:165], v15 offset:8192\n"
    "v_permlane16_swap_b32 v34, v42\n"
    "v_mfma_f32_16x16x32_bf16 a[132:135], v[68:71], v[112:115], a[132:135]\n"
    "v_permlane16_swap_b32 v35, v43\n"
    "v_permlane16_swap_b32 v36, v16\n"
    "v_mfma_f32_16x16x32_bf16 a[36:39], v[100:103], v[68:71], a[36:39]\n"
    "ds_read_b64_tr_b16 v[166:167], v15 offset:9216\n"
    "v_permlane16_swap_b32 v37, v17\n"
    "v_mfma_f32_16x16x32_bf16 a[164:167], v[68:71], v[116:119], a[164:167]\n"
    "v_permlane16_swap_b32 v38, v18\n"
    "v_permlane16_swap_b32 v39, v19\n"
    "v_mfma_f32_16x16x32_bf16 a[68:71], v[104:107], v[68:71], a[68:71]\n"
    "ds_read_b64_tr_b16 v[180:181], v15 offset:24576\n"
    "v_add_f32 v32, v32, v40\n"
    "v_mfma_f32_16x16x32_bf16 a[196:199], v[68:71], v[120:123], a[196:199]\n"
    "v_add_f32 v33, v33, v41\n"
    "v_add_f32 v34, v34, v42\n"
    "v_mfma_f32_16x16x32_bf16 a[100:103], v[108:111], v[68:71], a[100:103]\n"
    "ds_read_b64_tr_b16 v[182:183], v15 offset:25600\n"
    "v_add_f32 v35, v35, v43\n"
    "v_mfma_f32_16x16x32_bf16 a[228:231], v[68:71], v[124:127], a[228:231]\n"
    "v_add_f32 v36, v36, v16\n"
    "v_add_f32 v37, v37, v17\n"
    "v_mfma_f32_16x16x32_bf16 a[8:11], v[96:99], v[72:75], a[8:11]\n"
    "ds_read_b64_tr_b16 v[168:169], v14 offset:8448\n"
    "v_add_f32 v38, v38, v18\n"
    "v_mfma_f32_16x16x32_bf16 a[136:139], v[72:75], v[112:115], a[136:139]\n"
    "v_add_f32 v39, v39, v19\n"
    "v_mov_b32 v44, v32\n"
    "v_mfma_f32_16x16x32_bf16 a[40:43], v[100:103], v[72:75], a[40:43]\n"
    "ds_read_b64_tr_b16 v[170:171], v14 offset:9472\n"
    "v_mov_b32 v45, v36\n"
    "v_mfma_f32_16x16x32_bf16 a[168:171], v[72:75], v[116:119], a[168:171]\n"
    "v_lshlrev_b32 v46, 3, v1\n"
    "v_cmp_le_u32 vcc, 16, v1\n"
    "v_mfma_f32_16x16x32_bf16 a[72:75], v[104:107], v[72:75], a[72:75]\n"
    "ds_read_b64_tr_b16 v[184:185], v14 offset:24832\n"
    "s_nop 0\n"
    "v_mfma_f32_16x16x32_bf16 a[200:203], v[72:75], v[120:123], a[200:203]\n"
    "s_nop 0\n"
    "v_cndmask_b32 v44, v44, v33, vcc\n"
    "v_mfma_f32_16x16x32_bf16 a[104:107], v[108:111], v[72:75], a[104:107]\n"
    "ds_read_b64_tr_b16 v[186:187], v14 offset:25856\n"
    "v_cndmask_b32 v45, v45, v37, vcc\n"
    "v_mfma_f32_16x16x32_bf16 a[232:235], v[72:75], v[124:127], a[232:235]\n"
    "v_cmp_le_u32 vcc, 32, v1\n"
    "s_nop 0\n"
    "v_mfma_f32_16x16x32_bf16 a[12:15], v[96:99], v[76:79], a[12:15]\n"
    "ds_read_b64_tr_b16 v[172:173], v15 offset:8448\n"
    "s_nop 0\n"
    "v_mfma_f32_16x16x32_bf16 a[140:143], v[76:79], v[112:115], a[140:143]\n"
    "v_cndmask_b32 v44, v44, v34, vcc\n"
    "v_cndmask_b32 v45, v45, v38, vcc\n"
    "v_mfma_f32_16x16x32_bf16 a[44:47], v[100:103], v[76:79], a[44:47]\n"
    "ds_read_b64_tr_b16 v[174:175], v15 offset:9472\n"
    "v_cmp_le_u32 vcc, 48, v1\n"
    "v_mfma_f32_16x16x32_bf16 a[172:175], v[76:79], v[116:119], a[172:175]\n"
    "s_nop 0\n"
    "s_nop 0\n"
    "v_mfma_f32_16x16x32_bf16 a[76:79], v[104:107], v[76:79], a[76:79]\n"
    "ds_read_b64_tr_b16 v[188:189], v15 offset:24832\n"
    "v_cndmask_b32 v44, v44, v35, vcc\n"
    "v_mfma_f32_16x16x32_bf16 a[204:207], v[76:79], v[120:123], a[204:207]\n"
    "v_cndmask_b32 v45, v45, v39, vcc\n"
    "v_mul_f32 v40, v44, v44\n"
    "v_mfma_f32_16x16x32_bf16 a[108:111], v[108:111], v[76:79], a[108:111]\n"
    "ds_read_b64_tr_b16 v[190:191], v15 offset:25856\n"
    "v_mul_f32 v44, 0x3c800000, v44\n"
    "v_mfma_f32_16x16x32_bf16 a[236:239], v[76:79], v[124:127], a[236:239]\n"
    "v_mul_f32 v40, 0x3c800000, v40\n"
    "v_sub_f32 v45, v45, v40\n"
    "v_mfma_f32_16x16x32_bf16 a[16:19], v[96:99], v[80:83], a[16:19]\n"
    "ds_read_b128 v[128:131], v13\n"
    "v_max_f32 v45, 0, v45\n"
    "v_mfma_f32_16x16x32_bf16 a[144:147], v[80:83], v[112:115], a[144:147]\n"
    "s_lshl_b32 s22, s57, 1\n"
    "s_lshr_b32 s23, s19, 1\n"
    "v_mfma_f32_16x16x32_bf16 a[48:51], v[100:103], v[80:83], a[48:51]\n"
    "ds_read_b128 v[132:135], v13 offset:2048\n"
    "s_add_u32 s22, s22, s23\n"
    "v_mfma_f32_16x16x32_bf16 a[176:179], v[80:83], v[116:119], a[176:179]\n"
    "s_lshl_b32 s22, s22, 14\n"
    "s_add_u32 s22, s22, s12\n"
    "v_mfma_f32_16x16x32_bf16 a[80:83], v[104:107], v[80:83], a[80:83]\n"
    "ds_read_b128 v[136:139], v13 offset:4096\n"
    "v_mfma_f32_16x16x32_bf16 a[208:211], v[80:83], v[120:123], a[208:211]\n"
    "s_cmp_eq_u32 s58, 1\n"
    "s_cselect_b32 s22, s22, 0x7ff00000\n"
    "v_mfma_f32_16x16x32_bf16 a[112:115], v[108:111], v[80:83], a[112:115]\n"
    "ds_read_b128 v[140:143], v13 offset:6144\n"
    "v_mfma_f32_16x16x32_bf16 a[240:243], v[80:83], v[124:127], a[240:243]\n"
    "buffer_store_dwordx2 v[44:45], v46, s[44:47], s22 offen sc1\n"
    "v_mfma_f32_16x16x32_bf16 a[20:23], v[96:99], v[84:87], a[20:23]\n"
    "ds_read_b128 v[144:147], v13 offset:8192\n"
    "v_mfma_f32_16x16x32_bf16 a[148:151], v[84:87], v[112:115], a[148:151]\n"
    "v_mfma_f32_16x16x32_bf16 a[52:55], v[100:103], v[84:87], a[52:55]\n"
    "ds_read_b128 v[148:151], v13 offset:10240\n"
    "v_mfma_f32_16x16x32_bf16 a[180:183], v[84:87], v[116:119], a[180:183]\n"
    "v_mfma_f32_16x16x32_bf16 a[84:87], v[104:107], v[84:87], a[84:87]\n"
    "ds_read_b128 v[152:155], v13 offset:12288\n"
    "v_mfma_f32_16x16x32_bf16 a[212:215], v[84:87], v[120:123], a[212:215]\n"
    "v_mfma_f32_16x16x32_bf16 a[116:119], v[108:111], v[84:87], a[116:119]\n"
    "ds_read_b128 v[156:159], v13 offset:14336\n"
    "v_mfma_f32_16x16x32_bf16 a[244:247], v[84:87], v[124:127], a[244:247]\n"
    "v_mfma_f32_16x16x32_bf16 a[24:27], v[96:99], v[88:91], a[24:27]\n"
    "v_mfma_f32_16x16x32_bf16 a[152:155], v[88:91], v[112:115], a[152:155]\n"
    "v_mfma_f32_16x16x32_bf16 a[56:59], v[100:103], v[88:91], a[56:59]\n"
    "v_mfma_f32_16x16x32_bf16 a[184:187], v[88:91], v[116:119], a[184:187]\n"
    "v_mfma_f32_16x16x32_bf16 a[88:91], v[104:107], v[88:91], a[88:91]\n"
    "v_xor_b32 v12, 0x10000, v12\n"
    "v_xor_b32 v13, 0x10000, v13\n"
    "v_mfma_f32_16x16x32_bf16 a[216:219], v[88:91], v[120:123], a[216:219]\n"
    "v_add_u32 v14, 0x8000, v14\n"
    "v_add_u32 v15, 0x8000, v15\n"
    "v_mfma_f32_16x16x32_bf16 a[120:123], v[108:111], v[88:91], a[120:123]\n"
    "v_mfma_f32_16x16x32_bf16 a[248:251], v[88:91], v[124:127], a[248:251]\n"
    "v_mfma_f32_16x16x32_bf16 a[28:31], v[96:99], v[92:95], a[28:31]\n"
    "v_mfma_f32_16x16x32_bf16 a[156:159], v[92:95], v[112:115], a[156:159]\n"
    "v_mfma_f32_16x16x32_bf16 a[60:63], v[100:103], v[92:95], a[60:63]\n"
    "v_mfma_f32_16x16x32_bf16 a[188:191], v[92:95], v[116:119], a[188:191]\n"
    "v_mfma_f32_16x16x32_bf16 a[92:95], v[104:107], v[92:95], a[92:95]\n"
    "v_mfma_f32_16x16x32_bf16 a[220:223], v[92:95], v[120:123], a[220:223]\n"
    "v_mfma_f32_16x16x32_bf16 a[124:127], v[108:111], v[92:95], a[124:127]\n"
    "v_mfma_f32_16x16x32_bf16 a[252:255], v[92:95], v[124:127], a[252:255]\n"
    "s_waitcnt vmcnt(10) lgkmcnt(0)\n"
    "s_barrier\n"
    "v_mfma_f32_16x16x32_bf16 a[0:3], v[160:163], v[128:131], a[0:3]\n"
    "ds_read_b64_tr_b16 v[96:97], v14\n"
    "v_mfma_f32_16x16x32_bf16 a[128:131], v[128:131], v[176:179], a[128:131]\n"
    "s_add_u32 s52, s53, 0x180\n"
    "s_add_u32 m0, s17, 0x10000\n"
    "v_mfma_f32_16x16x32_bf16 a[32:35], v[164:167], v[128:131], a[32:35]\n"
    "ds_read_b64_tr_b16 v[98:99], v14 offset:1024\n"
    "v_mfma_f32_16x16x32_bf16 a[160:163], v[128:131], v[180:183], a[160:163]\n"
    "buffer_load_dwordx4 v4, s[36:39], s52 offen lds\n"
    "v_mfma_f32_16x16x32_bf16 a[64:67], v[168:171], v[128:131], a[64:67]\n"
    "ds_read_b64_tr_b16 v[112:113], v14 offset:16384\n"
    "v_mfma_f32_16x16x32_bf16 a[192:195], v[128:131], v[184:187], a[192:195]\n"
    "v_mfma_f32_16x16x32_bf16 a[96:99], v[172:175], v[128:131], a[96:99]\n"
    "ds_read_b64_tr_b16 v[114:115], v14 offset:17408\n"
    "v_mfma_f32_16x16x32_bf16 a[224:227], v[128:131], v[188:191], a[224:227]\n"
    "buffer_load_dwordx4 v5, s[36:39], s52 offen offset:1024 lds\n"
    "v_mfma_f32_16x16x32_bf16 a[4:7], v[160:163], v[132:135], a[4:7]\n"
    "ds_read_b64_tr_b16 v[100:101], v15\n"
    "v_mfma_f32_16x16x32_bf16 a[132:135], v[132:135], v[176:179], a[132:135]\n"
    "v_mfma_f32_16x16x32_bf16 a[36:39], v[164:167], v[132:135], a[36:39]\n"
    "ds_read_b64_tr_b16 v[102:103], v15 offset:1024\n"
    "v_mfma_f32_16x16x32_bf16 a[164:167], v[132:135], v[180:183], a[164:167]\n"
    "buffer_load_dwordx4 v6, s[36:39], s52 offen offset:2048 lds\n"
    "v_mfma_f32_16x16x32_bf16 a[68:71], v[168:171], v[132:135], a[68:71]\n"
    "ds_read_b64_tr_b16 v[116:117], v15 offset:16384\n"
    "v_mfma_f32_16x16x32_bf16 a[196:199], v[132:135], v[184:187], a[196:199]\n"
    "v_mfma_f32_16x16x32_bf16 a[100:103], v[172:175], v[132:135], a[100:103]\n"
    "ds_read_b64_tr_b16 v[118:119], v15 offset:17408\n"
    "v_mfma_f32_16x16x32_bf16 a[228:231], v[132:135], v[188:191], a[228:231]\n"
    "buffer_load_dwordx4 v7, s[36:39], s52 offen offset:3072 lds\n"
    "v_mfma_f32_16x16x32_bf16 a[8:11], v[160:163], v[136:139], a[8:11]\n"
    "ds_read_b64_tr_b16 v[104:105], v14 offset:256\n"
    "s_add_u32 s52, s52, 0x8000\n"
    "v_mfma_f32_16x16x32_bf16 a[136:139], v[136:139], v[176:179], a[136:139]\n"
    "s_add_u32 m0, m0, 0x1000\n"
    "v_mfma_f32_16x16x32_bf16 a[40:43], v[164:167], v[136:139], a[40:43]\n"
    "ds_read_b64_tr_b16 v[106:107], v14 offset:1280\n"
    "v_mfma_f32_16x16x32_bf16 a[168:171], v[136:139], v[180:183], a[168:171]\n"
    "buffer_load_dwordx4 v4, s[36:39], s52 offen lds\n"
    "v_mfma_f32_16x16x32_bf16 a[72:75], v[168:171], v[136:139], a[72:75]\n"
    "ds_read_b64_tr_b16 v[120:121], v14 offset:16640\n"
    "v_mfma_f32_16x16x32_bf16 a[200:203], v[136:139], v[184:187], a[200:203]\n"
    "v_mfma_f32_16x16x32_bf16 a[104:107], v[172:175], v[136:139], a[104:107]\n"
    "ds_read_b64_tr_b16 v[122:123], v14 offset:17664\n"
    "v_mfma_f32_16x16x32_bf16 a[232:235], v[136:139], v[188:191], a[232:235]\n"
    "buffer_load_dwordx4 v5, s[36:39], s52 offen offset:1024 lds\n"
    "v_mfma_f32_16x16x32_bf16 a[12:15], v[160:163], v[140:143], a[12:15]\n"
    "ds_read_b64_tr_b16 v[108:109], v15 offset:256\n"
    "v_mfma_f32_16x16x32_bf16 a[140:143], v[140:143], v[176:179], a[140:143]\n"
    "v_mfma_f32_16x16x32_bf16 a[44:47], v[164:167], v[140:143], a[44:47]\n"
    "ds_read_b64_tr_b16 v[110:111], v15 offset:1280\n"
    "v_mfma_f32_16x16x32_bf16 a[172:175], v[140:143], v[180:183], a[172:175]\n"
    "buffer_load_dwordx4 v6, s[36:39], s52 offen offset:2048 lds\n"
    "v_mfma_f32_16x16x32_bf16 a[76:79], v[168:171], v[140:143], a[76:79]\n"
    "ds_read_b64_tr_b16 v[124:125], v15 offset:16640\n"
    "v_mfma_f32_16x16x32_bf16 a[204:207], v[140:143], v[184:187], a[204:207]\n"
    "v_mfma_f32_16x16x32_bf16 a[108:111], v[172:175], v[140:143], a[108:111]\n"
    "ds_read_b64_tr_b16 v[126:127], v15 offset:17664\n"
    "v_mfma_f32_16x16x32_bf16 a[236:239], v[140:143], v[188:191], a[236:239]\n"
    "buffer_load_dwordx4 v7, s[36:39], s52 offen offset:3072 lds\n"
    "v_mfma_f32_16x16x32_bf16 a[16:19], v[160:163], v[144:147], a[16:19]\n"
    "ds_read_b128 v[64:67], v12\n"
    "s_add_u32 s13, s54, 0x200000\n"
    "v_mfma_f32_16x16x32_bf16 a[144:147], v[144:147], v[176:179], a[144:147]\n"
    "s_add_u32 m0, s16, 0x18000\n"
    "v_mfma_f32_16x16x32_bf16 a[48:51], v[164:167], v[144:147], a[48:51]\n"
    "ds_read_b128 v[68:71], v12 offset:2048\n"
    "v_mfma_f32_16x16x32_bf16 a[176:179], v[144:147], v[180:183], a[176:179]\n"
    "buffer_load_dwordx4 v8, s[40:43], s13 offen lds\n"
    "v_mfma_f32_16x16x32_bf16 a[80:83], v[168:171], v[144:147], a[80:83]\n"
    "ds_read_b128 v[72:75], v12 offset:4096\n"
    "v_mfma_f32_16x16x32_bf16 a[208:211], v[144:147], v[184:187], a[208:211]\n"
    "v_mfma_f32_16x16x32_bf16 a[112:115], v[172:175], v[144:147], a[112:115]\n"
    "ds_read_b128 v[76:79], v12 offset:6144\n"
    "v_mfma_f32_16x16x32_bf16 a[240:243], v[144:147], v[188:191], a[240:243]\n"
    "buffer_load_dwordx4 v9, s[40:43], s13 offen offset:1024 lds\n"
    "v_mfma_f32_16x16x32_bf16 a[20:23], v[160:163], v[148:151], a[20:23]\n"
    "ds_read_b128 v[80:83], v12 offset:8192\n"
    "v_mfma_f32_16x16x32_bf16 a[148:151], v[148:151], v[176:179], a[148:151]\n"
    "v_mfma_f32_16x16x32_bf16 a[52:55], v[164:167], v[148:151], a[52:55]\n"
    "ds_read_b128 v[84:87], v12 offset:10240\n"
    "v_mfma_f32_16x16x32_bf16 a[180:183], v[148:151], v[180:183], a[180:183]\n"
    "buffer_load_dwordx4 v10, s[40:43], s13 offen offset:2048 lds\n"
    "v_mfma_f32_16x16x32_bf16 a[84:87], v[168:171], v[148:151], a[84:87]\n"
    "ds_read_b128 v[88:91], v12 offset:12288\n"
    "v_mfma_f32_16x16x32_bf16 a[212:215], v[148:151], v[184:187], a[212:215]\n"
    "v_mfma_f32_16x16x32_bf16 a[116:119], v[172:175], v[148:151], a[116:119]\n"
    "ds_read_b128 v[92:95], v12 offset:14336\n"
    "v_mfma_f32_16x16x32_bf16 a[244:247], v[148:151], v[188:191], a[244:247]\n"
    "buffer_load_dwordx4 v11, s[40:43], s13 offen offset:3072 lds\n"
    "v_mfma_f32_16x16x32_bf16 a[24:27], v[160:163], v[152:155], a[24:27]\n"
    "s_add_u32 m0, m0, 0x4000\n"
    "v_mfma_f32_16x16x32_bf16 a[152:155], v[152:155], v[176:179], a[152:155]\n"
    "v_mfma_f32_16x16x32_bf16 a[56:59], v[164:167], v[152:155], a[56:59]\n"
    "buffer_load_dwordx4 v8, s[64:67], s13 offen lds\n"
    "v_mfma_f32_16x16x32_bf16 a[184:187], v[152:155], v[180:183], a[184:187]\n"
    "v_mfma_f32_16x16x32_bf16 a[88:91], v[168:171], v[152:155], a[88:91]\n"
    "v_mfma_f32_16x16x32_bf16 a[216:219], v[152:155], v[184:187], a[216:219]\n"
    "buffer_load_dwordx4 v9, s[64:67], s13 offen offset:1024 lds\n"
    "v_mfma_f32_16x16x32_bf16 a[120:123], v[172:175], v[152:155], a[120:123]\n"
    "v_mfma_f32_16x16x32_bf16 a[248:251], v[152:155], v[188:191], a[248:251]\n"
    "v_mfma_f32_16x16x32_bf16 a[28:31], v[160:163], v[156:159], a[28:31]\n"
    "buffer_load_dwordx4 v10, s[64:67], s13 offen offset:2048 lds\n"
    "v_mfma_f32_16x16x32_bf16 a[156:159], v[156:159], v[176:179], a[156:159]\n"
    "v_mfma_f32_16x16x32_bf16 a[60:63], v[164:167], v[156:159], a[60:63]\n"
    "v_mfma_f32_16x16x32_bf16 a[188:191], v[156:159], v[180:183], a[188:191]\n"
    "buffer_load_dwordx4 v11, s[64:67], s13 offen offset:3072 lds\n"
    "v_mfma_f32_16x16x32_bf16 a[92:95], v[168:171], v[156:159], a[92:95]\n"
    "v_mfma_f32_16x16x32_bf16 a[220:223], v[156:159], v[184:187], a[220:223]\n"
    "v_mfma_f32_16x16x32_bf16 a[124:127], v[172:175], v[156:159], a[124:127]\n"
    "v_mfma_f32_16x16x32_bf16 a[252:255], v[156:159], v[188:191], a[252:255]\n"
    "buffer_load_dword v55, v54, s[48:51], s6 offen\n"
    "s_add_u32 s6, s6, s7\n"
    "s_waitcnt lgkmcnt(0)\n"
    "s_lshl_b32 s22, s57, 1\n"
    "s_lshr_b32 s23, s19, 1\n"
    "s_add_u32 s22, s22, s23\n"
    "s_lshl_b32 s22, s22, 3\n"
    "s_add_u32 s22, s22, s55\n"
    "s_cmp_eq_u32 s58, 1\n"
    "s_cselect_b32 s22, s22, 0x7ff00000\n"
    "s_waitcnt vmcnt(17)\n"
    "v_mov_b32 v60, s4\n"
    "v_mov_b32 v61, s5\n"
    "s_mov_b64 exec, 1\n"
    "buffer_store_dwordx2 v[60:61], v2, s[44:47], s22 offen sc1\n"
    "s_mov_b64 exec, -1\n"
    "v_mfma_f32_16x16x32_bf16 a[0:3], v[96:99], v[64:67], a[0:3]\n"
    "ds_read_b64_tr_b16 v[160:161], v14 offset:8192\n"
    "v_mfma_f32_16x16x32_bf16 a[128:131], v[64:67], v[112:115], a[128:131]\n"
    "v_mfma_f32_16x16x32_bf16 a[32:35], v[100:103], v[64:67], a[32:35]\n"
    "ds_read_b64_tr_b16 v[162:163], v14 offset:9216\n"
    "v_mfma_f32_16x16x32_bf16 a[160:163], v[64:67], v[116:119], a[160:163]\n"
    "v_mfma_f32_16x16x32_bf16 a[64:67], v[104:107], v[64:67], a[64:67]\n"
    "ds_read_b64_tr_b16 v[176:177], v14 offset:24576\n"
    "v_mfma_f32_16x16x32_bf16 a[192:195], v[64:67], v[120:123], a[192:195]\n"
    "v_mfma_f32_16x16x32_bf16 a[96:99], v[108:111], v[64:67], a[96:99]\n"
    "ds_read_b64_tr_b16 v[178:179], v14 offset:25600\n"
    "v_mfma_f32_16x16x32_bf16 a[224:227], v[64:67], v[124:127], a[224:227]\n"
    "v_mfma_f32_16x16x32_bf16 a[4:7], v[96:99], v[68:71], a[4:7]\n"
    "ds_read_b64_tr_b16 v[164:165], v15 offset:8192\n"
    "v_mfma_f32_16x16x32_bf16 a[132:135], v[68:71], v[112:115], a[132:135]\n"
    "v_mfma_f32_16x16x32_bf16 a[36:39], v[100:103], v[68:71], a[36:39]\n"
    "ds_read_b64_tr_b16 v[166:167], v15 offset:9216\n"
    "v_mfma_f32_16x16x32_bf16 a[164:167], v[68:71], v[116:119], a[164:167]\n"
    "v_mfma_f32_16x16x32_bf16 a[68:71], v[104:107], v[68:71], a[68:71]\n"
    "ds_read_b64_tr_b16 v[180:181], v15 offset:24576\n"
    "v_mfma_f32_16x16x32_bf16 a[196:199], v[68:71], v[120:123], a[196:199]\n"
    "v_mfma_f32_16x16x32_bf16 a[100:103], v[108:111], v[68:71], a[100:103]\n"
    "ds_read_b64_tr_b16 v[182:183], v15 offset:25600\n"
    "v_mfma_f32_16x16x32_bf16 a[228:231], v[68:71], v[124:127], a[228:231]\n"
    "v_mfma_f32_16x16x32_bf16 a[8:11], v[96:99], v[72:75], a[8:11]\n"
    "ds_read_b64_tr_b16 v[168:169], v14 offset:8448\n"
    "v_mfma_f32_16x16x32_bf16 a[136:139], v[72:75], v[112:115], a[136:139]\n"
    "v_mfma_f32_16x16x32_bf16 a[40:43], v[100:103], v[72:75], a[40:43]\n"
    "ds_read_b64_tr_b16 v[170:171], v14 offset:9472\n"
    "v_mfma_f32_16x16x32_bf16 a[168:171], v[72:75], v[116:119], a[168:171]\n"
    "v_mfma_f32_16x16x32_bf16 a[72:75], v[104:107], v[72:75], a[72:75]\n"
    "ds_read_b64_tr_b16 v[184:185], v14 offset:24832\n"
    "v_mfma_f32_16x16x32_bf16 a[200:203], v[72:75], v[120:123], a[200:203]\n"
    "v_mfma_f32_16x16x32_bf16 a[104:107], v[108:111], v[72:75], a[104:107]\n"
    "ds_read_b64_tr_b16 v[186:187], v14 offset:25856\n"
    "v_mfma_f32_16x16x32_bf16 a[232:235], v[72:75], v[124:127], a[232:235]\n"
    "v_mfma_f32_16x16x32_bf16 a[12:15], v[96:99], v[76:79], a[12:15]\n"
    "ds_read_b64_tr_b16 v[172:173], v15 offset:8448\n"
    "v_mfma_f32_16x16x32_bf16 a[140:143], v[76:79], v[112:115], a[140:143]\n"
    "v_mfma_f32_16x16x32_bf16 a[44:47], v[100:103], v[76:79], a[44:47]\n"
    "ds_read_b64_tr_b16 v[174:175], v15 offset:9472\n"
    "v_mfma_f32_16x16x32_bf16 a[172:175], v[76:79], v[116:119], a[172:175]\n"
    "v_mfma_f32_16x16x32_bf16 a[76:79], v[104:107], v[76:79], a[76:79]\n"
    "ds_read_b64_tr_b16 v[188:189], v15 offset:24832\n"
    "v_mfma_f32_16x16x32_bf16 a[204:207], v[76:79], v[120:123], a[204:207]\n"
    "v_mfma_f32_16x16x32_bf16 a[108:111], v[108:111], v[76:79], a[108:111]\n"
    "ds_read_b64_tr_b16 v[190:191], v15 offset:25856\n"
    "v_mfma_f32_16x16x32_bf16 a[236:239], v[76:79], v[124:127], a[236:239]\n"
    "v_mfma_f32_16x16x32_bf16 a[16:19], v[96:99], v[80:83], a[16:19]\n"
    "ds_read_b128 v[128:131], v13\n"
    "v_mfma_f32_16x16x32_bf16 a[144:147], v[80:83], v[112:115], a[144:147]\n"
    "v_mfma_f32_16x16x32_bf16 a[48:51], v[100:103], v[80:83], a[48:51]\n"
    "ds_read_b128 v[132:135], v13 offset:2048\n"
    "v_mfma_f32_16x16x32_bf16 a[176:179], v[80:83], v[116:119], a[176:179]\n"
    "v_mfma_f32_16x16x32_bf16 a[80:83], v[104:107], v[80:83], a[80:83]\n"
    "ds_read_b128 v[136:139], v13 offset:4096\n"
    "v_mfma_f32_16x16x32_bf16 a[208:211], v[80:83], v[120:123], a[208:211]\n"
    "v_mfma_f32_16x16x32_bf16 a[112:115], v[108:111], v[80:83], a[112:115]\n"
    "ds_read_b128 v[140:143], v13 offset:6144\n"
    "v_mfma_f32_16x16x32_bf16 a[240:243], v[80:83], v[124:127], a[240:243]\n"
    "v_mfma_f32_16x16x32_bf16 a[20:23], v[96:99], v[84:87], a[20:23]\n"
    "ds_read_b128 v[144:147], v13 offset:8192\n"
    "v_mfma_f32_16x16x32_bf16 a[148:151], v[84:87], v[112:115], a[148:151]\n"
    "v_mfma_f32_16x16x32_bf16 a[52:55], v[100:103], v[84:87], a[52:55]\n"
    "ds_read_b128 v[148:151], v13 offset:10240\n"
    "v_mfma_f32_16x16x32_bf16 a[180:183], v[84:87], v[116:119], a[180:183]\n"
    "v_mfma_f32_16x16x32_bf16 a[84:87], v[104:107], v[84:87], a[84:87]\n"
    "ds_read_b128 v[152:155], v13 offset:12288\n"
    "v_mfma_f32_16x16x32_bf16 a[212:215], v[84:87], v[120:123], a[212:215]\n"
    "v_mfma_f32_16x16x32_bf16 a[116:119], v[108:111], v[84:87], a[116:119]\n"
    "ds_read_b128 v[156:159], v13 offset:14336\n"
    "v_mfma_f32_16x16x32_bf16 a[244:247], v[84:87], v[124:127], a[244:247]\n"
    "v_mfma_f32_16x16x32_bf16 a[24:27], v[96:99], v[88:91], a[24:27]\n"
    "v_mfma_f32_16x16x32_bf16 a[152:155], v[88:91], v[112:115], a[152:155]\n"
    "v_mfma_f32_16x16x32_bf16 a[56:59], v[100:103], v[88:91], a[56:59]\n"
    "v_mfma_f32_16x16x32_bf16 a[184:187], v[88:91], v[116:119], a[184:187]\n"
    "v_mfma_f32_16x16x32_bf16 a[88:91], v[104:107], v[88:91], a[88:91]\n"
    "v_xor_b32 v12, 0x10000, v12\n"
    "v_xor_b32 v13, 0x10000, v13\n"
    "v_mfma_f32_16x16x32_bf16 a[216:219], v[88:91], v[120:123], a[216:219]\n"
    "v_subrev_u32 v14, 0x18000, v14\n"
    "v_subrev_u32 v15, 0x18000, v15\n"
    "v_mfma_f32_16x16x32_bf16 a[120:123], v[108:111], v[88:91], a[120:123]\n"
    "v_mfma_f32_16x16x32_bf16 a[248:251], v[88:91], v[124:127], a[248:251]\n"
    "v_mfma_f32_16x16x32_bf16 a[28:31], v[96:99], v[92:95], a[28:31]\n"
    "v_mfma_f32_16x16x32_bf16 a[156:159], v[92:95], v[112:115], a[156:159]\n"
    "v_mfma_f32_16x16x32_bf16 a[60:63], v[100:103], v[92:95], a[60:63]\n"
    "v_mfma_f32_16x16x32_bf16 a[188:191], v[92:95], v[116:119], a[188:191]\n"
    "v_mfma_f32_16x16x32_bf16 a[92:95], v[104:107], v[92:95], a[92:95]\n"
    "v_mfma_f32_16x16x32_bf16 a[220:223], v[92:95], v[120:123], a[220:223]\n"
    "v_mfma_f32_16x16x32_bf16 a[124:127], v[108:111], v[92:95], a[124:127]\n"
    "v_mfma_f32_16x16x32_bf16 a[252:255], v[92:95], v[124:127], a[252:255]\n"
    "s_waitcnt vmcnt(10) lgkmcnt(0)\n"
    "s_barrier\n"
    "v_min_u32 v62, 13, v1\n"
    "v_mul_u32_u24 v62, 8, v62\n"
    "buffer_load_dwordx2 v[60:61], v62, s[44:47], s55 offen sc1\n"
    "v_mfma_f32_16x16x32_bf16 a[0:3], v[160:163], v[128:131], a[0:3]\n"
    "ds_read_b64_tr_b16 v[96:97], v14\n"
    "v_mfma_f32_16x16x32_bf16 a[128:131], v[128:131], v[176:179], a[128:131]\n"
    "s_add_u32 s52, s53, 0x200\n"
    "s_add_u32 m0, s17, 0\n"
    "v_mfma_f32_16x16x32_bf16 a[32:35], v[164:167], v[128:131], a[32:35]\n"
    "ds_read_b64_tr_b16 v[98:99], v14 offset:1024\n"
    "v_mfma_f32_16x16x32_bf16 a[160:163], v[128:131], v[180:183], a[160:163]\n"
    "buffer_load_dwordx4 v4, s[36:39], s52 offen lds\n"
    "v_mfma_f32_16x16x32_bf16 a[64:67], v[168:171], v[128:131], a[64:67]\n"
    "ds_read_b64_tr_b16 v[112:113], v14 offset:16384\n"
    "v_mfma_f32_16x16x32_bf16 a[192:195], v[128:131], v[184:187], a[192:195]\n"
    "v_mfma_f32_16x16x32_bf16 a[96:99], v[172:175], v[128:131], a[96:99]\n"
    "ds_read_b64_tr_b16 v[114:115], v14 offset:17408\n"
    "v_mfma_f32_16x16x32_bf16 a[224:227], v[128:131], v[188:191], a[224:227]\n"
    "buffer_load_dwordx4 v5, s[36:39], s52 offen offset:1024 lds\n"
    "v_mfma_f32_16x16x32_bf16 a[4:7], v[160:163], v[132:135], a[4:7]\n"
    "ds_read_b64_tr_b16 v[100:101], v15\n"
    "v_mfma_f32_16x16x32_bf16 a[132:135], v[132:135], v[176:179], a[132:135]\n"
    "v_mfma_f32_16x16x32_bf16 a[36:39], v[164:167], v[132:135], a[36:39]\n"
    "ds_read_b64_tr_b16 v[102:103], v15 offset:1024\n"
    "v_mfma_f32_16x16x32_bf16 a[164:167], v[132:135], v[180:183], a[164:167]\n"
    "buffer_load_dwordx4 v6, s[36:39], s52 offen offset:2048 lds\n"
    "v_mfma_f32_16x16x32_bf16 a[68:71], v[168:171], v[132:135], a[68:71]\n"
    "ds_read_b64_tr_b16 v[116:117], v15 offset:16384\n"
    "v_mfma_f32_16x16x32_bf16 a[196:199], v[132:135], v[184:187], a[196:199]\n"
    "v_mfma_f32_16x16x32_bf16 a[100:103], v[172:175], v[132:135], a[100:103]\n"
    "ds_read_b64_tr_b16 v[118:119], v15 offset:17408\n"
    "v_mfma_f32_16x16x32_bf16 a[228:231], v[132:135], v[188:191], a[228:231]\n"
    "buffer_load_dwordx4 v7, s[36:39], s52 offen offset:3072 lds\n"
    "v_mfma_f32_16x16x32_bf16 a[8:11], v[160:163], v[136:139], a[8:11]\n"
    "ds_read_b64_tr_b16 v[104:105], v14 offset:256\n"
    "s_add_u32 s52, s52, 0x8000\n"
    "v_mfma_f32_16x16x32_bf16 a[136:139], v[136:139], v[176:179], a[136:139]\n"
    "s_add_u32 m0, m0, 0x1000\n"
    "v_mfma_f32_16x16x32_bf16 a[40:43], v[164:167], v[136:139], a[40:43]\n"
    "ds_read_b64_tr_b16 v[106:107], v14 offset:1280\n"
    "v_mfma_f32_16x16x32_bf16 a[168:171], v[136:139], v[180:183], a[168:171]\n"
    "buffer_load_dwordx4 v4, s[36:39], s52 offen lds\n"
    "v_mfma_f32_16x16x32_bf16 a[72:75], v[168:171], v[136:139], a[72:75]\n"
    "ds_read_b64_tr_b16 v[120:121], v14 offset:16640\n"
    "v_mfma_f32_16x16x32_bf16 a[200:203], v[136:139], v[184:187], a[200:203]\n"
    "v_mfma_f32_16x16x32_bf16 a[104:107], v[172:175], v[136:139], a[104:107]\n"
    "ds_read_b64_tr_b16 v[122:123], v14 offset:17664\n"
    "v_mfma_f32_16x16x32_bf16 a[232:235], v[136:139], v[188:191], a[232:235]\n"
    "buffer_load_dwordx4 v5, s[36:39], s52 offen offset:1024 lds\n"
    "v_mfma_f32_16x16x32_bf16 a[12:15], v[160:163], v[140:143], a[12:15]\n"
    "ds_read_b64_tr_b16 v[108:109], v15 offset:256\n"
    "v_mfma_f32_16x16x32_bf16 a[140:143], v[140:143], v[176:179], a[140:143]\n"
    "v_mfma_f32_16x16x32_bf16 a[44:47], v[164:167], v[140:143], a[44:47]\n"
    "ds_read_b64_tr_b16 v[110:111], v15 offset:1280\n"
    "v_mfma_f32_16x16x32_bf16 a[172:175], v[140:143], v[180:183], a[172:175]\n"
    "buffer_load_dwordx4 v6, s[36:39], s52 offen offset:2048 lds\n"
    "v_mfma_f32_16x16x32_bf16 a[76:79], v[168:171], v[140:143], a[76:79]\n"
    "ds_read_b64_tr_b16 v[124:125], v15 offset:16640\n"
    "v_mfma_f32_16x16x32_bf16 a[204:207], v[140:143], v[184:187], a[204:207]\n"
    "v_mfma_f32_16x16x32_bf16 a[108:111], v[172:175], v[140:143], a[108:111]\n"
    "ds_read_b64_tr_b16 v[126:127], v15 offset:17664\n"
    "v_mfma_f32_16x16x32_bf16 a[236:239], v[140:143], v[188:191], a[236:239]\n"
    "buffer_load_dwordx4 v7, s[36:39], s52 offen offset:3072 lds\n"
    "v_mfma_f32_16x16x32_bf16 a[16:19], v[160:163], v[144:147], a[16:19]\n"
    "ds_read_b128 v[64:67], v12\n"
    "s_add_u32 s13, s54, 0x280000\n"
    "v_mfma_f32_16x16x32_bf16 a[144:147], v[144:147], v[176:179], a[144:147]\n"
    "s_add_u32 m0, s16, 0x20000\n"
    "v_mfma_f32_16x16x32_bf16 a[48:51], v[164:167], v[144:147], a[48:51]\n"
    "ds_read_b128 v[68:71], v12 offset:2048\n"
    "v_mfma_f32_16x16x32_bf16 a[176:179], v[144:147], v[180:183], a[176:179]\n"
    "buffer_load_dwordx4 v8, s[40:43], s13 offen lds\n"
    "v_mfma_f32_16x16x32_bf16 a[80:83], v[168:171], v[144:147], a[80:83]\n"
    "ds_read_b128 v[72:75], v12 offset:4096\n"
    "v_mfma_f32_16x16x32_bf16 a[208:211], v[144:147], v[184:187], a[208:211]\n"
    "v_mfma_f32_16x16x32_bf16 a[112:115], v[172:175], v[144:147], a[112:115]\n"
    "ds_read_b128 v[76:79], v12 offset:6144\n"
    "v_mfma_f32_16x16x32_bf16 a[240:243], v[144:147], v[188:191], a[240:243]\n"
    "buffer_load_dwordx4 v9, s[40:43], s13 offen offset:1024 lds\n"
    "v_mfma_f32_16x16x32_bf16 a[20:23], v[160:163], v[148:151], a[20:23]\n"
    "ds_read_b128 v[80:83], v12 offset:8192\n"
    "v_mfma_f32_16x16x32_bf16 a[148:151], v[148:151], v[176:179], a[148:151]\n"
    "v_mfma_f32_16x16x32_bf16 a[52:55], v[164:167], v[148:151], a[52:55]\n"
    "ds_read_b128 v[84:87], v12 offset:10240\n"
    "v_mfma_f32_16x16x32_bf16 a[180:183], v[148:151], v[180:183], a[180:183]\n"
    "buffer_load_dwordx4 v10, s[40:43], s13 offen offset:2048 lds\n"
    "v_mfma_f32_16x16x32_bf16 a[84:87], v[168:171], v[148:151], a[84:87]\n"
    "ds_read_b128 v[88:91], v12 offset:12288\n"
    "v_mfma_f32_16x16x32_bf16 a[212:215], v[148:151], v[184:187], a[212:215]\n"
    "v_mfma_f32_16x16x32_bf16 a[116:119], v[172:175], v[148:151], a[116:119]\n"
    "ds_read_b128 v[92:95], v12 offset:14336\n"
    "v_mfma_f32_16x16x32_bf16 a[244:247], v[148:151], v[188:191], a[244:247]\n"
    "buffer_load_dwordx4 v11, s[40:43], s13 offen offset:3072 lds\n"
    "v_mfma_f32_16x16x32_bf16 a[24:27], v[160:163], v[152:155], a[24:27]\n"
    "s_add_u32 m0, m0, 0x4000\n"
    "v_mfma_f32_16x16x32_bf16 a[152:155], v[152:155], v[176:179], a[152:155]\n"
    "v_mfma_f32_16x16x32_bf16 a[56:59], v[164:167], v[152:155], a[56:59]\n"
    "buffer_load_dwordx4 v8, s[64:67], s13 offen lds\n"
    "v_mfma_f32_16x16x32_bf16 a[184:187], v[152:155], v[180:183], a[184:187]\n"
    "v_mfma_f32_16x16x32_bf16 a[88:91], v[168:171], v[152:155], a[88:91]\n"
    "v_mfma_f32_16x16x32_bf16 a[216:219], v[152:155], v[184:187], a[216:219]\n"
    "buffer_load_dwordx4 v9, s[64:67], s13 offen offset:1024 lds\n"
    "v_mfma_f32_16x16x32_bf16 a[120:123], v[172:175], v[152:155], a[120:123]\n"
    "v_mfma_f32_16x16x32_bf16 a[248:251], v[152:155], v[188:191], a[248:251]\n"
    "v_mfma_f32_16x16x32_bf16 a[28:31], v[160:163], v[156:159], a[28:31]\n"
    "buffer_load_dwordx4 v10, s[64:67], s13 offen offset:2048 lds\n"
    "v_mfma_f32_16x16x32_bf16 a[156:159], v[156:159], v[176:179], a[156:159]\n"
    "v_mfma_f32_16x16x32_bf16 a[60:63], v[164:167], v[156:159], a[60:63]\n"
    "v_mfma_f32_16x16x32_bf16 a[188:191], v[156:159], v[180:183], a[188:191]\n"
    "buffer_load_dwordx4 v11, s[64:67], s13 offen offset:3072 lds\n"
    "v_mfma_f32_16x16x32_bf16 a[92:95], v[168:171], v[156:159], a[92:95]\n"
    "v_mfma_f32_16x16x32_bf16 a[220:223], v[156:159], v[184:187], a[220:223]\n"
    "v_mfma_f32_16x16x32_bf16 a[124:127], v[172:175], v[156:159], a[124:127]\n"
    "v_mfma_f32_16x16x32_bf16 a[252:255], v[156:159], v[188:191], a[252:255]\n"
    "buffer_load_dword v55, v54, s[48:51], s6 offen\n"
    "s_add_u32 s6, s6, s7\n"
    "s_waitcnt lgkmcnt(0)\n"
    "s_waitcnt vmcnt(17)\n"
    "v_cmp_ne_u64 vcc, s[4:5], v[60:61]\n"
    "s_nop 0\n"
    "s_cbranch_vccnz PS0_%=\n"
    "PR0_%=:\n"
    "v_mfma_f32_16x16x32_bf16 a[0:3], v[96:99], v[64:67], a[0:3]\n"
    "ds_read_b64_tr_b16 v[160:161], v14 offset:8192\n"
    "s_add_u32 s22, s12, 0\n"
    "v_mfma_f32_16x16x32_bf16 a[128:131], v[64:67], v[112:115], a[128:131]\n"
    "buffer_load_dwordx2 v[16:17], v46, s[44:47], s22 offen sc1\n"
    "v_mfma_f32_16x16x32_bf16 a[32:35], v[100:103], v[64:67], a[32:35]\n"
    "ds_read_b64_tr_b16 v[162:163], v14 offset:9216\n"
    "s_add_u32 s22, s12, 0x4000\n"
    "v_mfma_f32_16x16x32_bf16 a[160:163], v[64:67], v[116:119], a[160:163]\n"
    "buffer_load_dwordx2 v[18:19], v46, s[44:47], s22 offen sc1\n"
    "v_mfma_f32_16x16x32_bf16 a[64:67], v[104:107], v[64:67], a[64:67]\n"
    "ds_read_b64_tr_b16 v[176:177], v14 offset:24576\n"
    "s_add_u32 s22, s12, 0x8000\n"
    "v_mfma_f32_16x16x32_bf16 a[192:195], v[64:67], v[120:123], a[192:195]\n"
    "buffer_load_dwordx2 v[20:21], v46, s[44:47], s22 offen sc1\n"
    "v_mfma_f32_16x16x32_bf16 a[96:99], v[108:111], v[64:67], a[96:99]\n"
    "ds_read_b64_tr_b16 v[178:179], v14 offset:25600\n"
    "s_add_u32 s22, s12, 0xc000\n"
    "v_mfma_f32_16x16x32_bf16 a[224:227], v[64:67], v[124:127], a[224:227]\n"
    "buffer_load_dwordx2 v[22:23], v46, s[44:47], s22 offen sc1\n"
    "v_mfma_f32_16x16x32_bf16 a[4:7], v[96:99], v[68:71], a[4:7]\n"
    "ds_read_b64_tr_b16 v[164:165], v15 offset:8192\n"
    "s_add_u32 s22, s12, 0x10000\n"
    "v_mfma_f32_16x16x32_bf16 a[132:135], v[68:71], v[112:115], a[132:135]\n"
    "buffer_load_dwordx2 v[24:25], v46, s[44:47], s22 offen sc1\n"
    "v_mfma_f32_16x16x32_bf16 a[36:39], v[100:103], v[68:71], a[36:39]\n"
    "ds_read_b64_tr_b16 v[166:167], v15 offset:9216\n"
    "s_add_u32 s22, s12, 0x14000\n"
    "v_mfma_f32_16x16x32_bf16 a[164:167], v[68:71], v[116:119], a[164:167]\n"
    "buffer_load_dwordx2 v[26:27], v46, s[44:47], s22 offen sc1\n"
    "v_mfma_f32_16x16x32_bf16 a[68:71], v[104:107], v[68:71], a[68:71]\n"
    "ds_read_b64_tr_b16 v[180:181], v15 offset:24576\n"
    "s_add_u32 s22, s12, 0x18000\n"
    "v_mfma_f32_16x16x32_bf16 a[196:199], v[68:71], v[120:123], a[196:199]\n"
    "buffer_load_dwordx2 v[36:37], v46, s[44:47], s22 offen sc1\n"
    "v_mfma_f32_16x16x32_bf16 a[100:103], v[108:111], v[68:71], a[100:103]\n"
    "ds_read_b64_tr_b16 v[182:183], v15 offset:25600\n"
    "s_add_u32 s22, s12, 0x1c000\n"
    "v_mfma_f32_16x16x32_bf16 a[228:231], v[68:71], v[124:127], a[228:231]\n"
    "buffer_load_dwordx2 v[38:39], v46, s[44:47], s22 offen sc1\n"
    "v_mfma_f32_16x16x32_bf16 a[8:11], v[96:99], v[72:75], a[8:11]\n"
    "ds_read_b64_tr_b16 v[168:169], v14 offset:8448\n"
    "s_add_u32 s22, s12, 0x20000\n"
    "v_mfma_f32_16x16x32_bf16 a[136:139], v[72:75], v[112:115], a[136:139]\n"
    "buffer_load_dwordx2 v[40:41], v46, s[44:47], s22 offen sc1\n"
    "v_mfma_f32_16x16x32_bf16 a[40:43], v[100:103], v[72:75], a[40:43]\n"
    "ds_read_b64_tr_b16 v[170:171], v14 offset:9472\n"
    "s_add_u32 s22, s12, 0x24000\n"
    "v_mfma_f32_16x16x32_bf16 a[168:171], v[72:75], v[116:119], a[168:171]\n"
    "buffer_load_dwordx2 v[42:43], v46, s[44:47], s22 offen sc1\n"
    "v_mfma_f32_16x16x32_bf16 a[72:75], v[104:107], v[72:75], a[72:75]\n"
    "ds_read_b64_tr_b16 v[184:185], v14 offset:24832\n"
    "s_add_u32 s22, s12, 0x28000\n"
    "v_mfma_f32_16x16x32_bf16 a[200:203], v[72:75], v[120:123], a[200:203]\n"
    "buffer_load_dwordx2 v[44:45], v46, s[44:47], s22 offen sc1\n"
    "v_mfma_f32_16x16x32_bf16 a[104:107], v[108:111], v[72:75], a[104:107]\n"
    "ds_read_b64_tr_b16 v[186:187], v14 offset:25856\n"
    "s_add_u32 s22, s12, 0x2c000\n"
    "v_mfma_f32_16x16x32_bf16 a[232:235], v[72:75], v[124:127], a[232:235]\n"
    "buffer_load_dwordx2 v[48:49], v46, s[44:47], s22 offen sc1\n"
    "v_mfma_f32_16x16x32_bf16 a[12:15], v[96:99], v[76:79], a[12:15]\n"
    "ds_read_b64_tr_b16 v[172:173], v15 offset:8448\n"
    "s_add_u32 s22, s12, 0x30000\n"
    "v_mfma_f32_16x16x32_bf16 a[140:143], v[76:79], v[112:115], a[140:143]\n"
    "buffer_load_dwordx2 v[50:51], v46, s[44:47], s22 offen sc1\n"
    "v_mfma_f32_16x16x32_bf16 a[44:47], v[100:103], v[76:79], a[44:47]\n"
    "ds_read_b64_tr_b16 v[174:175], v15 offset:9472\n"
    "s_add_u32 s22, s12, 0x34000\n"
    "v_mfma_f32_16x16x32_bf16 a[172:175], v[76:79], v[116:119], a[172:175]\n"
    "buffer_load_dwordx2 v[52:53], v46, s[44:47], s22 offen sc1\n"
    "v_mfma_f32_16x16x32_bf16 a[76:79], v[104:107], v[76:79], a[76:79]\n"
    "ds_read_b64_tr_b16 v[188:189], v15 offset:24832\n"
    "v_mfma_f32_16x16x32_bf16 a[204:207], v[76:79], v[120:123], a[204:207]\n"
    "v_mfma_f32_16x16x32_bf16 a[108:111], v[108:111], v[76:79], a[108:111]\n"
    "ds_read_b64_tr_b16 v[190:191], v15 offset:25856\n"
    "v_mfma_f32_16x16x32_bf16 a[236:239], v[76:79], v[124:127], a[236:239]\n"
    "v_mfma_f32_16x16x32_bf16 a[16:19], v[96:99], v[80:83], a[16:19]\n"
    "ds_read_b128 v[128:131], v13\n"
    "v_mfma_f32_16x16x32_bf16 a[144:147], v[80:83], v[112:115], a[144:147]\n"
    "v_mfma_f32_16x16x32_bf16 a[48:51], v[100:103], v[80:83], a[48:51]\n"
    "ds_read_b128 v[132:135], v13 offset:2048\n"
    "v_mfma_f32_16x16x32_bf16 a[176:179], v[80:83], v[116:119], a[176:179]\n"
    "v_mfma_f32_16x16x32_bf16 a[80:83], v[104:107], v[80:83], a[80:83]\n"
    "ds_read_b128 v[136:139], v13 offset:4096\n"
    "v_mfma_f32_16x16x32_bf16 a[208:211], v[80:83], v[120:123], a[208:211]\n"
    "v_mfma_f32_16x16x32_bf16 a[112:115], v[108:111], v[80:83], a[112:115]\n"
    "ds_read_b128 v[140:143], v13 offset:6144\n"
    "v_mfma_f32_16x16x32_bf16 a[240:243], v[80:83], v[124:127], a[240:243]\n"
    "v_mfma_f32_16x16x32_bf16 a[20:23], v[96:99], v[84:87], a[20:23]\n"
    "ds_read_b128 v[144:147], v13 offset:8192\n"
    "v_mfma_f32_16x16x32_bf16 a[148:151], v[84:87], v[112:115], a[148:151]\n"
    "v_mfma_f32_16x16x32_bf16 a[52:55], v[100:103], v[84:87], a[52:55]\n"
    "ds_read_b128 v[148:151], v13 offset:10240\n"
    "v_mfma_f32_16x16x32_bf16 a[180:183], v[84:87], v[116:119], a[180:183]\n"
    "v_mfma_f32_16x16x32_bf16 a[84:87], v[104:107], v[84:87], a[84:87]\n"
    "ds_read_b128 v[152:155], v13 offset:12288\n"
    "v_mfma_f32_16x16x32_bf16 a[212:215], v[84:87], v[120:123], a[212:215]\n"
    "v_mfma_f32_16x16x32_bf16 a[116:119], v[108:111], v[84:87], a[116:119]\n"
    "ds_read_b128 v[156:159], v13 offset:14336\n"
    "v_mfma_f32_16x16x32_bf16 a[244:247], v[84:87], v[124:127], a[244:247]\n"
    "v_mfma_f32_16x16x32_bf16 a[24:27], v[96:99], v[88:91], a[24:27]\n"
    "v_mfma_f32_16x16x32_bf16 a[152:155], v[88:91], v[112:115], a[152:155]\n"
    "v_mfma_f32_16x16x32_bf16 a[56:59], v[100:103], v[88:91], a[56:59]\n"
    "v_mfma_f32_16x16x32_bf16 a[184:187], v[88:91], v[116:119], a[184:187]\n"
    "v_mfma_f32_16x16x32_bf16 a[88:91], v[104:107], v[88:91], a[88:91]\n"
    "v_xor_b32 v12, 0x10000, v12\n"
    "v_xor_b32 v13, 0x10000, v13\n"
    "v_mfma_f32_16x16x32_bf16 a[216:219], v[88:91], v[120:123], a[216:219]\n"
    "v_add_u32 v14, 0x10000, v14\n"
    "v_add_u32 v15, 0x10000, v15\n"
    "v_mfma_f32_16x16x32_bf16 a[120:123], v[108:111], v[88:91], a[120:123]\n"
    "v_mfma_f32_16x16x32_bf16 a[248:251], v[88:91], v[124:127], a[248:251]\n"
    "v_mfma_f32_16x16x32_bf16 a[28:31], v[96:99], v[92:95], a[28:31]\n"
    "v_mfma_f32_16x16x32_bf16 a[156:159], v[92:95], v[112:115], a[156:159]\n"
    "v_mfma_f32_16x16x32_bf16 a[60:63], v[100:103], v[92:95], a[60:63]\n"
    "v_mfma_f32_16x16x32_bf16 a[188:191], v[92:95], v[116:119], a[188:191]\n"
    "v_mfma_f32_16x16x32_bf16 a[92:95], v[104:107], v[92:95], a[92:95]\n"
    "v_mfma_f32_16x16x32_bf16 a[220:223], v[92:95], v[120:123], a[220:223]\n"
    "v_mfma_f32_16x16x32_bf16 a[124:127], v[108:111], v[92:95], a[124:127]\n"
    "v_mfma_f32_16x16x32_bf16 a[252:255], v[92:95], v[124:127], a[252:255]\n"
    "s_waitcnt vmcnt(23) lgkmcnt(0)\n"
    "s_barrier\n"
    "s_waitcnt vmcnt(0)\n"
    "v_mfma_f32_16x16x32_bf16 a[0:3], v[160:163], v[128:131], a[0:3]\n"
    "ds_read_b64_tr_b16 v[96:97], v14\n"
    "v_add_f32 v60, v16, v18\n"
    "v_mfma_f32_16x16x32_bf16 a[128:131], v[128:131], v[176:179], a[128:131]\n"
    "s_add_u32 s52, s53, 0x280\n"
    "s_add_u32 m0, s17, 0x10000\n"
    "v_mfma_f32_16x16x32_bf16 a[32:35], v[164:167], v[128:131], a[32:35]\n"
    "ds_read_b64_tr_b16 v[98:99], v14 offset:1024\n"
    "v_add_f32 v61, v17, v19\n"
    "v_mfma_f32_16x16x32_bf16 a[160:163], v[128:131], v[180:183], a[160:163]\n"
    "buffer_load_dwordx4 v4, s[36:39], s52 offen lds\n"
    "v_mfma_f32_16x16x32_bf16 a[64:67], v[168:171], v[128:131], a[64:67]\n"
    "ds_read_b64_tr_b16 v[112:113], v14 offset:16384\n"
    "v_add_f32 v60, v60, v20\n"
    "v_mfma_f32_16x16x32_bf16 a[192:195], v[128:131], v[184:187], a[192:195]\n"
    "v_add_f32 v61, v61, v21\n"
    "v_add_f32 v60, v60, v22\n"
    "v_mfma_f32_16x16x32_bf16 a[96:99], v[172:175], v[128:131], a[96:99]\n"
    "ds_read_b64_tr_b16 v[114:115], v14 offset:17408\n"
    "v_add_f32 v61, v61, v23\n"
    "v_mfma_f32_16x16x32_bf16 a[224:227], v[128:131], v[188:191], a[224:227]\n"
    "buffer_load_dwordx4 v5, s[36:39], s52 offen offset:1024 lds\n"
    "v_mfma_f32_16x16x32_bf16 a[4:7], v[160:163], v[132:135], a[4:7]\n"
    "ds_read_b64_tr_b16 v[100:101], v15\n"
    "v_add_f32 v60, v60, v24\n"
    "v_mfma_f32_16x16x32_bf16 a[132:135], v[132:135], v[176:179], a[132:135]\n"
    "v_add_f32 v61, v61, v25\n"
    "v_add_f32 v60, v60, v26\n"
    "v_mfma_f32_16x16x32_bf16 a[36:39], v[164:167], v[132:135], a[36:39]\n"
    "ds_read_b64_tr_b16 v[102:103], v15 offset:1024\n"
    "v_add_f32 v61, v61, v27\n"
    "v_mfma_f32_16x16x32_bf16 a[164:167], v[132:135], v[180:183], a[164:167]\n"
    "buffer_load_dwordx4 v6, s[36:39], s52 offen offset:2048 lds\n"
    "v_mfma_f32_16x16x32_bf16 a[68:71], v[168:171], v[132:135], a[68:71]\n"
    "ds_read_b64_tr_b16 v[116:117], v15 offset:16384\n"
    "v_add_f32 v60, v60, v36\n"
    "v_mfma_f32_16x16x32_bf16 a[196:199], v[132:135], v[184:187], a[196:199]\n"
    "v_add_f32 v61, v61, v37\n"
    "v_add_f32 v60, v60, v38\n"
    "v_mfma_f32_16x16x32_bf16 a[100:103], v[172:175], v[132:135], a[100:103]\n"
    "ds_read_b64_tr_b16 v[118:119], v15 offset:17408\n"
    "v_add_f32 v61, v61, v39\n"
    "v_mfma_f32_16x16x32_bf16 a[228:231], v[132:135], v[188:191], a[228:231]\n"
    "buffer_load_dwordx4 v7, s[36:39], s52 offen offset:3072 lds\n"
    "v_mfma_f32_16x16x32_bf16 a[8:11], v[160:163], v[136:139], a[8:11]\n"
    "ds_read_b64_tr_b16 v[104:105], v14 offset:256\n"
    "s_add_u32 s52, s52, 0x8000\n"
    "v_mfma_f32_16x16x32_bf16 a[136:139], v[136:139], v[176:179], a[136:139]\n"
    "s_add_u32 m0, m0, 0x1000\n"
    "v_add_f32 v60, v60, v40\n"
    "v_mfma_f32_16x16x32_bf16 a[40:43], v[164:167], v[136:139], a[40:43]\n"
    "ds_read_b64_tr_b16 v[106:107], v14 offset:1280\n"
    "v_add_f32 v61, v61, v41\n"
    "v_mfma_f32_16x16x32_bf16 a[168:171], v[136:139], v[180:183], a[168:171]\n"
    "buffer_load_dwordx4 v4, s[36:39], s52 offen lds\n"
    "v_mfma_f32_16x16x32_bf16 a[72:75], v[168:171], v[136:139], a[72:75]\n"
    "ds_read_b64_tr_b16 v[120:121], v14 offset:16640\n"
    "v_add_f32 v60, v60, v42\n"
    "v_mfma_f32_16x16x32_bf16 a[200:203], v[136:139], v[184:187], a[200:203]\n"
    "v_add_f32 v61, v61, v43\n"
    "v_add_f32 v60, v60, v44\n"
    "v_mfma_f32_16x16x32_bf16 a[104:107], v[172:175], v[136:139], a[104:107]\n"
    "ds_read_b64_tr_b16 v[122:123], v14 offset:17664\n"
    "v_add_f32 v61, v61, v45\n"
    "v_mfma_f32_16x16x32_bf16 a[232:235], v[136:139], v[188:191], a[232:235]\n"
    "buffer_load_dwordx4 v5, s[36:39], s52 offen offset:1024 lds\n"
    "v_mfma_f32_16x16x32_bf16 a[12:15], v[160:163], v[140:143], a[12:15]\n"
    "ds_read_b64_tr_b16 v[108:109], v15 offset:256\n"
    "v_add_f32 v60, v60, v48\n"
    "v_mfma_f32_16x16x32_bf16 a[140:143], v[140:143], v[176:179], a[140:143]\n"
    "v_add_f32 v61, v61, v49\n"
    "v_add_f32 v60, v60, v50\n"
    "v_mfma_f32_16x16x32_bf16 a[44:47], v[164:167], v[140:143], a[44:47]\n"
    "ds_read_b64_tr_b16 v[110:111], v15 offset:1280\n"
    "v_add_f32 v61, v61, v51\n"
    "v_mfma_f32_16x16x32_bf16 a[172:175], v[140:143], v[180:183], a[172:175]\n"
    "buffer_load_dwordx4 v6, s[36:39], s52 offen offset:2048 lds\n"
    "v_mfma_f32_16x16x32_bf16 a[76:79], v[168:171], v[140:143], a[76:79]\n"
    "ds_read_b64_tr_b16 v[124:125], v15 offset:16640\n"
    "v_add_f32 v60, v60, v52\n"
    "v_mfma_f32_16x16x32_bf16 a[204:207], v[140:143], v[184:187], a[204:207]\n"
    "v_add_f32 v61, v61, v53\n"
    "v_mul_f32 v60, 0x3d924925, v60\n"
    "v_mfma_f32_16x16x32_bf16 a[108:111], v[172:175], v[140:143], a[108:111]\n"
    "ds_read_b64_tr_b16 v[126:127], v15 offset:17664\n"
    "v_mov_b32 v63, 0\n"
    "v_mfma_f32_16x16x32_bf16 a[236:239], v[140:143], v[188:191], a[236:239]\n"
    "buffer_load_dwordx4 v7, s[36:39], s52 offen offset:3072 lds\n"
    "v_mfma_f32_16x16x32_bf16 a[16:19], v[160:163], v[144:147], a[16:19]\n"
    "ds_read_b128 v[64:67], v12\n"
    "s_add_u32 s13, s54, 0x300000\n"
    "v_mfma_f32_16x16x32_bf16 a[144:147], v[144:147], v[176:179], a[144:147]\n"
    "s_add_u32 m0, s16, 0x8000\n"
    "v_sub_f32 v62, v16, v60\n"
    "v_mfma_f32_16x16x32_bf16 a[48:51], v[164:167], v[144:147], a[48:51]\n"
    "ds_read_b128 v[68:71], v12 offset:2048\n"
    "v_fmac_f32 v63, v62, v62\n"
    "v_mfma_f32_16x16x32_bf16 a[176:179], v[144:147], v[180:183], a[176:179]\n"
    "buffer_load_dwordx4 v8, s[40:43], s13 offen lds\n"
    "v_mfma_f32_16x16x32_bf16 a[80:83], v[168:171], v[144:147], a[80:83]\n"
    "ds_read_b128 v[72:75], v12 offset:4096\n"
    "v_sub_f32 v62, v18, v60\n"
    "v_mfma_f32_16x16x32_bf16 a[208:211], v[144:147], v[184:187], a[208:211]\n"
    "v_fmac_f32 v63, v62, v62\n"
    "v_sub_f32 v62, v20, v60\n"
    "v_mfma_f32_16x16x32_bf16 a[112:115], v[172:175], v[144:147], a[112:115]\n"
    "ds_read_b128 v[76:79], v12 offset:6144\n"
    "v_fmac_f32 v63, v62, v62\n"
    "v_mfma_f32_16x16x32_bf16 a[240:243], v[144:147], v[188:191], a[240:243]\n"
    "buffer_load_dwordx4 v9, s[40:43], s13 offen offset:1024 lds\n"
    "v_mfma_f32_16x16x32_bf16 a[20:23], v[160:163], v[148:151], a[20:23]\n"
    "ds_read_b128 v[80:83], v12 offset:8192\n"
    "v_sub_f32 v62, v22, v60\n"
    "v_mfma_f32_16x16x32_bf16 a[148:151], v[148:151], v[176:179], a[148:151]\n"
    "v_fmac_f32 v63, v62, v62\n"
    "v_sub_f32 v62, v24, v60\n"
    "v_mfma_f32_16x16x32_bf16 a[52:55], v[164:167], v[148:151], a[52:55]\n"
    "ds_read_b128 v[84:87], v12 offset:10240\n"
    "v_fmac_f32 v63, v62, v62\n"
    "v_mfma_f32_16x16x32_bf16 a[180:183], v[148:151], v[180:183], a[180:183]\n"
    "buffer_load_dwordx4 v10, s[40:43], s13 offen offset:2048 lds\n"
    "v_mfma_f32_16x16x32_bf16 a[84:87], v[168:171], v[148:151], a[84:87]\n"
    "ds_read_b128 v[88:91], v12 offset:12288\n"
    "v_sub_f32 v62, v26, v60\n"
    "v_mfma_f32_16x16x32_bf16 a[212:215], v[148:151], v[184:187], a[212:215]\n"
    "v_fmac_f32 v63, v62, v62\n"
    "v_sub_f32 v62, v36, v60\n"
    "v_mfma_f32_16x16x32_bf16 a[116:119], v[172:175], v[148:151], a[116:119]\n"
    "ds_read_b128 v[92:95], v12 offset:14336\n"
    "v_fmac_f32 v63, v62, v62\n"
    "v_mfma_f32_16x16x32_bf16 a[244:247], v[148:151], v[188:191], a[244:247]\n"
    "buffer_load_dwordx4 v11, s[40:43], s13 offen offset:3072 lds\n"
    "v_mfma_f32_16x16x32_bf16 a[24:27], v[160:163], v[152:155], a[24:27]\n"
    "s_add_u32 m0, m0, 0x4000\n"
    "v_sub_f32 v62, v38, v60\n"
    "v_mfma_f32_16x16x32_bf16 a[152:155], v[152:155], v[176:179], a[152:155]\n"
    "v_fmac_f32 v63, v62, v62\n"
    "v_sub_f32 v62, v40, v60\n"
    "v_mfma_f32_16x16x32_bf16 a[56:59], v[164:167], v[152:155], a[56:59]\n"
    "buffer_load_dwordx4 v8, s[64:67], s13 offen lds\n"
    "v_mfma_f32_16x16x32_bf16 a[184:187], v[152:155], v[180:183], a[184:187]\n"
    "v_fmac_f32 v63, v62, v62\n"
    "v_sub_f32 v62, v42, v60\n"
    "v_mfma_f32_16x16x32_bf16 a[88:91], v[168:171], v[152:155], a[88:91]\n"
    "v_fmac_f32 v63, v62, v62\n"
    "v_sub_f32 v62, v44, v60\n"
    "v_mfma_f32_16x16x32_bf16 a[216:219], v[152:155], v[184:187], a[216:219]\n"
    "buffer_load_dwordx4 v9, s[64:67], s13 offen offset:1024 lds\n"
    "v_mfma_f32_16x16x32_bf16 a[120:123], v[172:175], v[152:155], a[120:123]\n"
    "v_fmac_f32 v63, v62, v62\n"
    "v_sub_f32 v62, v48, v60\n"
    "v_mfma_f32_16x16x32_bf16 a[248:251], v[152:155], v[188:191], a[248:251]\n"
    "v_fmac_f32 v63, v62, v62\n"
    "v_sub_f32 v62, v50, v60\n"
    "v_mfma_f32_16x16x32_bf16 a[28:31], v[160:163], v[156:159], a[28:31]\n"
    "buffer_load_dwordx4 v10, s[64:67], s13 offen offset:2048 lds\n"
    "v_mfma_f32_16x16x32_bf16 a[156:159], v[156:159], v[176:179], a[156:159]\n"
    "v_fmac_f32 v63, v62, v62\n"
    "v_sub_f32 v62, v52, v60\n"
    "v_mfma_f32_16x16x32_bf16 a[60:63], v[164:167], v[156:159], a[60:63]\n"
    "v_fmac_f32 v63, v62, v62\n"
    "v_mov_b32 v62, 0x42800000\n"
    "v_mfma_f32_16x16x32_bf16 a[188:191], v[156:159], v[180:183], a[188:191]\n"
    "buffer_load_dwordx4 v11, s[64:67], s13 offen offset:3072 lds\n"
    "v_mfma_f32_16x16x32_bf16 a[92:95], v[168:171], v[156:159], a[92:95]\n"
    "v_fmac_f32 v61, v62, v63\n"
    "v_mov_b32 v62, 0x3727c5ac\n"
    "v_mfma_f32_16x16x32_bf16 a[220:223], v[156:159], v[184:187], a[220:223]\n"
    "v_mov_b32 v63, 0x3a924925\n"
    "v_fmac_f32 v62, v61, v63\n"
    "v_mfma_f32_16x16x32_bf16 a[124:127], v[172:175], v[156:159], a[124:127]\n"
    "v_rsq_f32 v62, v62\n"
    "v_mfma_f32_16x16x32_bf16 a[252:255], v[156:159], v[188:191], a[252:255]\n"
    "s_cmp_eq_u32 s58, 1\n"
    "s_cselect_b64 vcc, -1, 0\n"
    "buffer_load_dword v55, v54, s[48:51], s6 offen\n"
    "s_add_u32 s6, s6, s7\n"
    "s_mov_b64 s[48:49], s[28:29]\n"
    "s_lshl_b32 s6, s21, 16\n"
    "s_lshl_b32 s22, s20, 8\n"
    "s_add_u32 s6, s6, s22\n"
    "s_lshl_b32 s22, s19, 19\n"
    "s_add_u32 s6, s6, s22\n"
    "s_mov_b32 s7, 0x100000\n"
    "s_cmp_ge_u32 s19, 2\n"
    "s_cselect_b32 s50, 0, s50\n"
    "s_waitcnt lgkmcnt(0)\n"
    "v_mfma_f32_16x16x32_bf16 a[0:3], v[96:99], v[64:67], a[0:3]\n"
    "ds_read_b64_tr_b16 v[160:161], v14 offset:8192\n"
    "v_mul_f32 v60, v60, v62\n"
    "v_mfma_f32_16x16x32_bf16 a[128:131], v[64:67], v[112:115], a[128:131]\n"
    "v_sub_f32 v60, 0, v60\n"
    "v_cndmask_b32 v36, 1.0, v62, vcc\n"
    "v_mfma_f32_16x16x32_bf16 a[32:35], v[100:103], v[64:67], a[32:35]\n"
    "ds_read_b64_tr_b16 v[162:163], v14 offset:9216\n"
    "v_cndmask_b32 v40, 0, v60, vcc\n"
    "v_mfma_f32_16x16x32_bf16 a[160:163], v[64:67], v[116:119], a[160:163]\n"
    "v_mov_b32 v38, v36\n"
    "v_mov_b32 v42, v40\n"
    "v_mfma_f32_16x16x32_bf16 a[64:67], v[104:107], v[64:67], a[64:67]\n"
    "ds_read_b64_tr_b16 v[176:177], v14 offset:24576\n"
    "s_nop 0\n"
    "v_mfma_f32_16x16x32_bf16 a[192:195], v[64:67], v[120:123], a[192:195]\n"
    "v_permlane32_swap_b32 v36, v38\n"
    "v_permlane32_swap_b32 v40, v42\n"
    "v_mfma_f32_16x16x32_bf16 a[96:99], v[108:111], v[64:67], a[96:99]\n"
    "ds_read_b64_tr_b16 v[178:179], v14 offset:25600\n"
    "v_mov_b32 v37, v36\n"
    "v_mfma_f32_16x16x32_bf16 a[224:227], v[64:67], v[124:127], a[224:227]\n"
    "v_mov_b32 v41, v40\n"
    "v_mov_b32 v39, v38\n"
    "v_mfma_f32_16x16x32_bf16 a[4:7], v[96:99], v[68:71], a[4:7]\n"
    "ds_read_b64_tr_b16 v[164:165], v15 offset:8192\n"
    "v_mov_b32 v43, v42\n"
    "v_mfma_f32_16x16x32_bf16 a[132:135], v[68:71], v[112:115], a[132:135]\n"
    "s_nop 0\n"
    "v_permlane16_swap_b32 v36, v37\n"
    "v_mfma_f32_16x16x32_bf16 a[36:39], v[100:103], v[68:71], a[36:39]\n"
    "ds_read_b64_tr_b16 v[166:167], v15 offset:9216\n"
    "v_permlane16_swap_b32 v40, v41\n"
    "v_mfma_f32_16x16x32_bf16 a[164:167], v[68:71], v[116:119], a[164:167]\n"
    "v_permlane16_swap_b32 v38, v39\n"
    "v_permlane16_swap_b32 v42, v43\n"
    "v_mfma_f32_16x16x32_bf16 a[68:71], v[104:107], v[68:71], a[68:71]\n"
    "ds_read_b64_tr_b16 v[180:181], v15 offset:24576\n"
    "v_fma_f32 v192, v192, v36, v40\n"
    "v_mfma_f32_16x16x32_bf16 a[196:199], v[68:71], v[120:123], a[196:199]\n"
    "v_fma_f32 v193, v193, v36, v40\n"
    "v_fma_f32 v194, v194, v36, v40\n"
    "v_mfma_f32_16x16x32_bf16 a[100:103], v[108:111], v[68:71], a[100:103]\n"
    "ds_read_b64_tr_b16 v[182:183], v15 offset:25600\n"
    "v_fma_f32 v195, v195, v36, v40\n"
    "v_mfma_f32_16x16x32_bf16 a[228:231], v[68:71], v[124:127], a[228:231]\n"
    "v_cvt_pk_bf16_f32 v192, v192, v193\n"
    "v_cvt_pk_bf16_f32 v193, v194, v195\n"
    "v_mfma_f32_16x16x32_bf16 a[8:11], v[96:99], v[72:75], a[8:11]\n"
    "ds_read_b64_tr_b16 v[168:169], v14 offset:8448\n"
    "v_fma_f32 v208, v208, v36, v40\n"
    "v_mfma_f32_16x16x32_bf16 a[136:139], v[72:75], v[112:115], a[136:139]\n"
    "v_fma_f32 v209, v209, v36, v40\n"
    "v_fma_f32 v210, v210, v36, v40\n"
    "v_mfma_f32_16x16x32_bf16 a[40:43], v[100:103], v[72:75], a[40:43]\n"
    "ds_read_b64_tr_b16 v[170:171], v14 offset:9472\n"
    "v_fma_f32 v211, v211, v36, v40\n"
    "v_mfma_f32_16x16x32_bf16 a[168:171], v[72:75], v[116:119], a[168:171]\n"
    "v_cvt_pk_bf16_f32 v194, v208, v209\n"
    "v_cvt_pk_bf16_f32 v195, v210, v211\n"
    "v_mfma_f32_16x16x32_bf16 a[72:75], v[104:107], v[72:75], a[72:75]\n"
    "ds_read_b64_tr_b16 v[184:185], v14 offset:24832\n"
    "s_nop 0\n"
    "v_mfma_f32_16x16x32_bf16 a[200:203], v[72:75], v[120:123], a[200:203]\n"
    "v_permlane16_swap_b32 v192, v194\n"
    "v_permlane16_swap_b32 v193, v195\n"
    "v_mfma_f32_16x16x32_bf16 a[104:107], v[108:111], v[72:75], a[104:107]\n"
    "ds_read_b64_tr_b16 v[186:187], v14 offset:25856\n"
    "v_mfma_f32_16x16x32_bf16 a[232:235], v[72:75], v[124:127], a[232:235]\n"
    "buffer_store_dwordx4 v[192:195], v56, s[44:47], s8 offen sc1\n"
    "v_mfma_f32_16x16x32_bf16 a[12:15], v[96:99], v[76:79], a[12:15]\n"
    "ds_read_b64_tr_b16 v[172:173], v15 offset:8448\n"
    "v_fma_f32 v224, v224, v36, v40\n"
    "v_mfma_f32_16x16x32_bf16 a[140:143], v[76:79], v[112:115], a[140:143]\n"
    "v_fma_f32 v225, v225, v36, v40\n"
    "v_fma_f32 v226, v226, v36, v40\n"
    "v_mfma_f32_16x16x32_bf16 a[44:47], v[100:103], v[76:79], a[44:47]\n"
    "ds_read_b64_tr_b16 v[174:175], v15 offset:9472\n"
    "v_fma_f32 v227, v227, v36, v40\n"
    "v_mfma_f32_16x16x32_bf16 a[172:175], v[76:79], v[116:119], a[172:175]\n"
    "v_cvt_pk_bf16_f32 v224, v224, v225\n"
    "v_cvt_pk_bf16_f32 v225, v226, v227\n"
    "v_mfma_f32_16x16x32_bf16 a[76:79], v[104:107], v[76:79], a[76:79]\n"
    "ds_read_b64_tr_b16 v[188:189], v15 offset:24832\n"
    "v_fma_f32 v240, v240, v36, v40\n"
    "v_mfma_f32_16x16x32_bf16 a[204:207], v[76:79], v[120:123], a[204:207]\n"
    "v_fma_f32 v241, v241, v36, v40\n"
    "v_fma_f32 v242, v242, v36, v40\n"
    "v_mfma_f32_16x16x32_bf16 a[108:111], v[108:111], v[76:79], a[108:111]\n"
    "ds_read_b64_tr_b16 v[190:191], v15 offset:25856\n"
    "v_fma_f32 v243, v243, v36, v40\n"
    "v_mfma_f32_16x16x32_bf16 a[236:239], v[76:79], v[124:127], a[236:239]\n"
    "v_cvt_pk_bf16_f32 v226, v240, v241\n"
    "v_cvt_pk_bf16_f32 v227, v242, v243\n"
    "v_mfma_f32_16x16x32_bf16 a[16:19], v[96:99], v[80:83], a[16:19]\n"
    "ds_read_b128 v[128:131], v13\n"
    "s_nop 0\n"
    "v_mfma_f32_16x16x32_bf16 a[144:147], v[80:83], v[112:115], a[144:147]\n"
    "v_permlane16_swap_b32 v224, v226\n"
    "v_permlane16_swap_b32 v225, v227\n"
    "v_mfma_f32_16x16x32_bf16 a[48:51], v[100:103], v[80:83], a[48:51]\n"
    "ds_read_b128 v[132:135], v13 offset:2048\n"
    "v_mfma_f32_16x16x32_bf16 a[176:179], v[80:83], v[116:119], a[176:179]\n"
    "buffer_store_dwordx4 v[224:227], v57, s[44:47], s8 offen sc1\n"
    "v_mfma_f32_16x16x32_bf16 a[80:83], v[104:107], v[80:83], a[80:83]\n"
    "ds_read_b128 v[136:139], v13 offset:4096\n"
    "v_fma_f32 v196, v196, v37, v41\n"
    "v_mfma_f32_16x16x32_bf16 a[208:211], v[80:83], v[120:123], a[208:211]\n"
    "v_fma_f32 v197, v197, v37, v41\n"
    "v_fma_f32 v198, v198, v37, v41\n"
    "v_mfma_f32_16x16x32_bf16 a[112:115], v[108:111], v[80:83], a[112:115]\n"
    "ds_read_b128 v[140:143], v13 offset:6144\n"
    "v_fma_f32 v199, v199, v37, v41\n"
    "v_mfma_f32_16x16x32_bf16 a[240:243], v[80:83], v[124:127], a[240:243]\n"
    "v_cvt_pk_bf16_f32 v196, v196, v197\n"
    "v_cvt_pk_bf16_f32 v197, v198, v199\n"
    "v_mfma_f32_16x16x32_bf16 a[20:23], v[96:99], v[84:87], a[20:23]\n"
    "ds_read_b128 v[144:147], v13 offset:8192\n"
    "v_fma_f32 v212, v212, v37, v41\n"
    "v_mfma_f32_16x16x32_bf16 a[148:151], v[84:87], v[112:115], a[148:151]\n"
    "v_fma_f32 v213, v213, v37, v41\n"
    "v_fma_f32 v214, v214, v37, v41\n"
    "v_mfma_f32_16x16x32_bf16 a[52:55], v[100:103], v[84:87], a[52:55]\n"
    "ds_read_b128 v[148:151], v13 offset:10240\n"
    "v_fma_f32 v215, v215, v37, v41\n"
    "v_mfma_f32_16x16x32_bf16 a[180:183], v[84:87], v[116:119], a[180:183]\n"
    "v_cvt_pk_bf16_f32 v198, v212, v213\n"
    "v_cvt_pk_bf16_f32 v199, v214, v215\n"
    "v_mfma_f32_16x16x32_bf16 a[84:87], v[104:107], v[84:87], a[84:87]\n"
    "ds_read_b128 v[152:155], v13 offset:12288\n"
    "s_nop 0\n"
    "v_mfma_f32_16x16x32_bf16 a[212:215], v[84:87], v[120:123], a[212:215]\n"
    "v_permlane16_swap_b32 v196, v198\n"
    "v_permlane16_swap_b32 v197, v199\n"
    "v_mfma_f32_16x16x32_bf16 a[116:119], v[108:111], v[84:87], a[116:119]\n"
    "ds_read_b128 v[156:159], v13 offset:14336\n"
    "v_mfma_f32_16x16x32_bf16 a[244:247], v[84:87], v[124:127], a[244:247]\n"
    "buffer_store_dwordx4 v[196:199], v56, s[44:47], s9 offen sc1\n"
    "v_mfma_f32_16x16x32_bf16 a[24:27], v[96:99], v[88:91], a[24:27]\n"
    "v_fma_f32 v228, v228, v37, v41\n"
    "v_fma_f32 v229, v229, v37, v41\n"
    "v_mfma_f32_16x16x32_bf16 a[152:155], v[88:91], v[112:115], a[152:155]\n"
    "v_fma_f32 v230, v230, v37, v41\n"
    "v_fma_f32 v231, v231, v37, v41\n"
    "v_mfma_f32_16x16x32_bf16 a[56:59], v[100:103], v[88:91], a[56:59]\n"
    "v_cvt_pk_bf16_f32 v228, v228, v229\n"
    "v_cvt_pk_bf16_f32 v229, v230, v231\n"
    "v_mfma_f32_16x16x32_bf16 a[184:187], v[88:91], v[116:119], a[184:187]\n"
    "v_fma_f32 v244, v244, v37, v41\n"
    "v_fma_f32 v245, v245, v37, v41\n"
    "v_mfma_f32_16x16x32_bf16 a[88:91], v[104:107], v[88:91], a[88:91]\n"
    "v_xor_b32 v12, 0x10000, v12\n"
    "v_xor_b32 v13, 0x10000, v13\n"
    "v_mfma_f32_16x16x32_bf16 a[216:219], v[88:91], v[120:123], a[216:219]\n"
    "v_add_u32 v14, 0x8000, v14\n"
    "v_add_u32 v15, 0x8000, v15\n"
    "v_mfma_f32_16x16x32_bf16 a[120:123], v[108:111], v[88:91], a[120:123]\n"
    "v_fma_f32 v246, v246, v37, v41\n"
    "v_fma_f32 v247, v247, v37, v41\n"
    "v_mfma_f32_16x16x32_bf16 a[248:251], v[88:91], v[124:127], a[248:251]\n"
    "v_cvt_pk_bf16_f32 v230, v244, v245\n"
    "v_cvt_pk_bf16_f32 v231, v246, v247\n"
    "v_mfma_f32_16x16x32_bf16 a[28:31], v[96:99], v[92:95], a[28:31]\n"
    "s_nop 0\n"
    "v_permlane16_swap_b32 v228, v230\n"
    "v_mfma_f32_16x16x32_bf16 a[156:159], v[92:95], v[112:115], a[156:159]\n"
    "v_permlane16_swap_b32 v229, v231\n"
    "v_mfma_f32_16x16x32_bf16 a[60:63], v[100:103], v[92:95], a[60:63]\n"
    "buffer_store_dwordx4 v[228:231], v57, s[44:47], s9 offen sc1\n"
    "v_mfma_f32_16x16x32_bf16 a[188:191], v[92:95], v[116:119], a[188:191]\n"
    "v_fma_f32 v200, v200, v38, v42\n"
    "v_fma_f32 v201, v201, v38, v42\n"
    "v_mfma_f32_16x16x32_bf16 a[92:95], v[104:107], v[92:95], a[92:95]\n"
    "v_fma_f32 v202, v202, v38, v42\n"
    "v_fma_f32 v203, v203, v38, v42\n"
    "v_mfma_f32_16x16x32_bf16 a[220:223], v[92:95], v[120:123], a[220:223]\n"
    "v_cvt_pk_bf16_f32 v200, v200, v201\n"
    "v_cvt_pk_bf16_f32 v201, v202, v203\n"
    "v_mfma_f32_16x16x32_bf16 a[124:127], v[108:111], v[92:95], a[124:127]\n"
    "v_fma_f32 v216, v216, v38, v42\n"
    "v_fma_f32 v217, v217, v38, v42\n"
    "v_mfma_f32_16x16x32_bf16 a[252:255], v[92:95], v[124:127], a[252:255]\n"
    "v_fma_f32 v218, v218, v38, v42\n"
    "v_fma_f32 v219, v219, v38, v42\n"
    "s_waitcnt vmcnt(13) lgkmcnt(0)\n"
    "s_barrier\n"
    "v_mfma_f32_16x16x32_bf16 a[0:3], v[160:163], v[128:131], a[0:3]\n"
    "ds_read_b64_tr_b16 v[96:97], v14\n"
    "v_cvt_pk_bf16_f32 v202, v216, v217\n"
    "v_mfma_f32_16x16x32_bf16 a[128:131], v[128:131], v[176:179], a[128:131]\n"
    "s_add_u32 s52, s53, 0x300\n"
    "s_add_u32 m0, s17, 0\n"
    "v_mfma_f32_16x16x32_bf16 a[32:35], v[164:167], v[128:131], a[32:35]\n"
    "ds_read_b64_tr_b16 v[98:99], v14 offset:1024\n"
    "v_cvt_pk_bf16_f32 v203, v218, v219\n"
    "v_mfma_f32_16x16x32_bf16 a[160:163], v[128:131], v[180:183], a[160:163]\n"
    "buffer_load_dwordx4 v4, s[36:39], s52 offen lds\n"
    "v_mfma_f32_16x16x32_bf16 a[64:67], v[168:171], v[128:131], a[64:67]\n"
    "ds_read_b64_tr_b16 v[112:113], v14 offset:16384\n"
    "s_nop 0\n"
    "v_mfma_f32_16x16x32_bf16 a[192:195], v[128:131], v[184:187], a[192:195]\n"
    "v_permlane16_swap_b32 v200, v202\n"
    "v_permlane16_swap_b32 v201, v203\n"
    "v_mfma_f32_16x16x32_bf16 a[96:99], v[172:175], v[128:131], a[96:99]\n"
    "ds_read_b64_tr_b16 v[114:115], v14 offset:17408\n"
    "v_mfma_f32_16x16x32_bf16 a[224:227], v[128:131], v[188:191], a[224:227]\n"
    "buffer_load_dwordx4 v5, s[36:39], s52 offen offset:1024 lds\n"
    "v_mfma_f32_16x16x32_bf16 a[4:7], v[160:163], v[132:135], a[4:7]\n"
    "ds_read_b64_tr_b16 v[100:101], v15\n"
    "v_mfma_f32_16x16x32_bf16 a[132:135], v[132:135], v[176:179], a[132:135]\n"
    "buffer_store_dwordx4 v[200:203], v56, s[44:47], s10 offen sc1\n"
    "v_mfma_f32_16x16x32_bf16 a[36:39], v[164:167], v[132:135], a[36:39]\n"
    "ds_read_b64_tr_b16 v[102:103], v15 offset:1024\n"
    "v_fma_f32 v232, v232, v38, v42\n"
    "v_mfma_f32_16x16x32_bf16 a[164:167], v[132:135], v[180:183], a[164:167]\n"
    "buffer_load_dwordx4 v6, s[36:39], s52 offen offset:2048 lds\n"
    "v_mfma_f32_16x16x32_bf16 a[68:71], v[168:171], v[132:135], a[68:71]\n"
    "ds_read_b64_tr_b16 v[116:117], v15 offset:16384\n"
    "v_fma_f32 v233, v233, v38, v42\n"
    "v_mfma_f32_16x16x32_bf16 a[196:199], v[132:135], v[184:187], a[196:199]\n"
    "v_fma_f32 v234, v234, v38, v42\n"
    "v_fma_f32 v235, v235, v38, v42\n"
    "v_mfma_f32_16x16x32_bf16 a[100:103], v[172:175], v[132:135], a[100:103]\n"
    "ds_read_b64_tr_b16 v[118:119], v15 offset:17408\n"
    "v_cvt_pk_bf16_f32 v232, v232, v233\n"
    "v_mfma_f32_16x16x32_bf16 a[228:231], v[132:135], v[188:191], a[228:231]\n"
    "buffer_load_dwordx4 v7, s[36:39], s52 offen offset:3072 lds\n"
    "v_mfma_f32_16x16x32_bf16 a[8:11], v[160:163], v[136:139], a[8:11]\n"
    "ds_read_b64_tr_b16 v[104:105], v14 offset:256\n"
    "s_add_u32 s52, s52, 0x8000\n"
    "v_mfma_f32_16x16x32_bf16 a[136:139], v[136:139], v[176:179], a[136:139]\n"
    "s_add_u32 m0, m0, 0x1000\n"
    "v_cvt_pk_bf16_f32 v233, v234, v235\n"
    "v_mfma_f32_16x16x32_bf16 a[40:43], v[164:167], v[136:139], a[40:43]\n"
    "ds_read_b64_tr_b16 v[106:107], v14 offset:1280\n"
    "v_fma_f32 v248, v248, v38, v42\n"
    "v_mfma_f32_16x16x32_bf16 a[168:171], v[136:139], v[180:183], a[168:171]\n"
    "buffer_load_dwordx4 v4, s[36:39], s52 offen lds\n"
    "v_mfma_f32_16x16x32_bf16 a[72:75], v[168:171], v[136:139], a[72:75]\n"
    "ds_read_b64_tr_b16 v[120:121], v14 offset:16640\n"
    "v_fma_f32 v249, v249, v38, v42\n"
    "v_mfma_f32_16x16x32_bf16 a[200:203], v[136:139], v[184:187], a[200:203]\n"
    "v_fma_f32 v250, v250, v38, v42\n"
    "v_fma_f32 v251, v251, v38, v42\n"
    "v_mfma_f32_16x16x32_bf16 a[104:107], v[172:175], v[136:139], a[104:107]\n"
    "ds_read_b64_tr_b16 v[122:123], v14 offset:17664\n"
    "v_cvt_pk_bf16_f32 v234, v248, v249\n"
    "v_mfma_f32_16x16x32_bf16 a[232:235], v[136:139], v[188:191], a[232:235]\n"
    "buffer_load_dwordx4 v5, s[36:39], s52 offen offset:1024 lds\n"
    "v_mfma_f32_16x16x32_bf16 a[12:15], v[160:163], v[140:143], a[12:15]\n"
    "ds_read_b64_tr_b16 v[108:109], v15 offset:256\n"
    "v_cvt_pk_bf16_f32 v235, v250, v251\n"
    "v_mfma_f32_16x16x32_bf16 a[140:143], v[140:143], v[176:179], a[140:143]\n"
    "s_nop 0\n"
    "v_permlane16_swap_b32 v232, v234\n"
    "v_mfma_f32_16x16x32_bf16 a[44:47], v[164:167], v[140:143], a[44:47]\n"
    "ds_read_b64_tr_b16 v[110:111], v15 offset:1280\n"
    "v_permlane16_swap_b32 v233, v235\n"
    "v_mfma_f32_16x16x32_bf16 a[172:175], v[140:143], v[180:183], a[172:175]\n"
    "buffer_load_dwordx4 v6, s[36:39], s52 offen offset:2048 lds\n"
    "v_mfma_f32_16x16x32_bf16 a[76:79], v[168:171], v[140:143], a[76:79]\n"
    "ds_read_b64_tr_b16 v[124:125], v15 offset:16640\n"
    "v_mfma_f32_16x16x32_bf16 a[204:207], v[140:143], v[184:187], a[204:207]\n"
    "buffer_store_dwordx4 v[232:235], v57, s[44:47], s10 offen sc1\n"
    "v_mfma_f32_16x16x32_bf16 a[108:111], v[172:175], v[140:143], a[108:111]\n"
    "ds_read_b64_tr_b16 v[126:127], v15 offset:17664\n"
    "v_fma_f32 v204, v204, v39, v43\n"
    "v_mfma_f32_16x16x32_bf16 a[236:239], v[140:143], v[188:191], a[236:239]\n"
    "buffer_load_dwordx4 v7, s[36:39], s52 offen offset:3072 lds\n"
    "v_mfma_f32_16x16x32_bf16 a[16:19], v[160:163], v[144:147], a[16:19]\n"
    "ds_read_b128 v[64:67], v12\n"
    "s_add_u32 s13, s54, 0x380000\n"
    "v_mfma_f32_16x16x32_bf16 a[144:147], v[144:147], v[176:179], a[144:147]\n"
    "s_add_u32 m0, s16, 0x18000\n"
    "v_fma_f32 v205, v205, v39, v43\n"
    "v_mfma_f32_16x16x32_bf16 a[48:51], v[164:167], v[144:147], a[48:51]\n"
    "ds_read_b128 v[68:71], v12 offset:2048\n"
    "v_fma_f32 v206, v206, v39, v43\n"
    "v_mfma_f32_16x16x32_bf16 a[176:179], v[144:147], v[180:183], a[176:179]\n"
    "buffer_load_dwordx4 v8, s[40:43], s13 offen lds\n"
    "v_mfma_f32_16x16x32_bf16 a[80:83], v[168:171], v[144:147], a[80:83]\n"
    "ds_read_b128 v[72:75], v12 offset:4096\n"
    "v_fma_f32 v207, v207, v39, v43\n"
    "v_mfma_f32_16x16x32_bf16 a[208:211], v[144:147], v[184:187], a[208:211]\n"
    "v_cvt_pk_bf16_f32 v204, v204, v205\n"
    "v_cvt_pk_bf16_f32 v205, v206, v207\n"
    "v_mfma_f32_16x16x32_bf16 a[112:115], v[172:175], v[144:147], a[112:115]\n"
    "ds_read_b128 v[76:79], v12 offset:6144\n"
    "v_fma_f32 v220, v220, v39, v43\n"
    "v_mfma_f32_16x16x32_bf16 a[240:243], v[144:147], v[188:191], a[240:243]\n"
    "buffer_load_dwordx4 v9, s[40:43], s13 offen offset:1024 lds\n"
    "v_mfma_f32_16x16x32_bf16 a[20:23], v[160:163], v[148:151], a[20:23]\n"
    "ds_read_b128 v[80:83], v12 offset:8192\n"
    "v_fma_f32 v221, v221, v39, v43\n"
    "v_mfma_f32_16x16x32_bf16 a[148:151], v[148:151], v[176:179], a[148:151]\n"
    "v_fma_f32 v222, v222, v39, v43\n"
    "v_fma_f32 v223, v223, v39, v43\n"
    "v_mfma_f32_16x16x32_bf16 a[52:55], v[164:167], v[148:151], a[52:55]\n"
    "ds_read_b128 v[84:87], v12 offset:10240\n"
    "v_cvt_pk_bf16_f32 v206, v220, v221\n"
    "v_mfma_f32_16x16x32_bf16 a[180:183], v[148:151], v[180:183], a[180:183]\n"
    "buffer_load_dwordx4 v10, s[40:43], s13 offen offset:2048 lds\n"
    "v_mfma_f32_16x16x32_bf16 a[84:87], v[168:171], v[148:151], a[84:87]\n"
    "ds_read_b128 v[88:91], v12 offset:12288\n"
    "v_cvt_pk_bf16_f32 v207, v222, v223\n"
    "v_mfma_f32_16x16x32_bf16 a[212:215], v[148:151], v[184:187], a[212:215]\n"
    "s_nop 0\n"
    "v_permlane16_swap_b32 v204, v206\n"
    "v_mfma_f32_16x16x32_bf16 a[116:119], v[172:175], v[148:151], a[116:119]\n"
    "ds_read_b128 v[92:95], v12 offset:14336\n"
    "v_permlane16_swap_b32 v205, v207\n"
    "v_mfma_f32_16x16x32_bf16 a[244:247], v[148:151], v[188:191], a[244:247]\n"
    "buffer_load_dwordx4 v11, s[40:43], s13 offen offset:3072 lds\n"
    "v_mfma_f32_16x16x32_bf16 a[24:27], v[160:163], v[152:155], a[24:27]\n"
    "s_add_u32 m0, m0, 0x4000\n"
    "v_mfma_f32_16x16x32_bf16 a[152:155], v[152:155], v[176:179], a[152:155]\n"
    "buffer_store_dwordx4 v[204:207], v56, s[44:47], s11 offen sc1\n"
    "v_mfma_f32_16x16x32_bf16 a[56:59], v[164:167], v[152:155], a[56:59]\n"
    "buffer_load_dwordx4 v8, s[64:67], s13 offen lds\n"
    "v_mfma_f32_16x16x32_bf16 a[184:187], v[152:155], v[180:183], a[184:187]\n"
    "v_fma_f32 v236, v236, v39, v43\n"
    "v_fma_f32 v237, v237, v39, v43\n"
    "v_mfma_f32_16x16x32_bf16 a[88:91], v[168:171], v[152:155], a[88:91]\n"
    "v_fma_f32 v238, v238, v39, v43\n"
    "v_fma_f32 v239, v239, v39, v43\n"
    "v_mfma_f32_16x16x32_bf16 a[216:219], v[152:155], v[184:187], a[216:219]\n"
    "buffer_load_dwordx4 v9, s[64:67], s13 offen offset:1024 lds\n"
    "v_mfma_f32_16x16x32_bf16 a[120:123], v[172:175], v[152:155], a[120:123]\n"
    "v_cvt_pk_bf16_f32 v236, v236, v237\n"
    "v_cvt_pk_bf16_f32 v237, v238, v239\n"
    "v_mfma_f32_16x16x32_bf16 a[248:251], v[152:155], v[188:191], a[248:251]\n"
    "v_fma_f32 v252, v252, v39, v43\n"
    "v_fma_f32 v253, v253, v39, v43\n"
    "v_mfma_f32_16x16x32_bf16 a[28:31], v[160:163], v[156:159], a[28:31]\n"
    "buffer_load_dwordx4 v10, s[64:67], s13 offen offset:2048 lds\n"
    "v_mfma_f32_16x16x32_bf16 a[156:159], v[156:159], v[176:179], a[156:159]\n"
    "v_fma_f32 v254, v254, v39, v43\n"
    "v_fma_f32 v255, v255, v39, v43\n"
    "v_mfma_f32_16x16x32_bf16 a[60:63], v[164:167], v[156:159], a[60:63]\n"
    "v_cvt_pk_bf16_f32 v238, v252, v253\n"
    "v_cvt_pk_bf16_f32 v239, v254, v255\n"
    "v_mfma_f32_16x16x32_bf16 a[188:191], v[156:159], v[180:183], a[188:191]\n"
    "buffer_load_dwordx4 v11, s[64:67], s13 offen offset:3072 lds\n"
    "v_mfma_f32_16x16x32_bf16 a[92:95], v[168:171], v[156:159], a[92:95]\n"
    "s_nop 0\n"
    "v_permlane16_swap_b32 v236, v238\n"
    "v_mfma_f32_16x16x32_bf16 a[220:223], v[156:159], v[184:187], a[220:223]\n"
    "v_permlane16_swap_b32 v237, v239\n"
    "v_mfma_f32_16x16x32_bf16 a[124:127], v[172:175], v[156:159], a[124:127]\n"
    "buffer_store_dwordx4 v[236:239], v57, s[44:47], s11 offen sc1\n"
    "v_mfma_f32_16x16x32_bf16 a[252:255], v[156:159], v[188:191], a[252:255]\n"
    "buffer_load_dword v55, v54, s[48:51], s6 offen\n"
    "s_add_u32 s6, s6, s7\n"
    "buffer_load_dword v55, v54, s[48:51], s6 offen\n"
    "s_add_u32 s6, s6, s7\n"
    "s_waitcnt lgkmcnt(0)\n"
    "v_mfma_f32_16x16x32_bf16 a[0:3], v[96:99], v[64:67], a[0:3]\n"
    "ds_read_b64_tr_b16 v[160:161], v14 offset:8192\n"
    "v_mfma_f32_16x16x32_bf16 a[128:131], v[64:67], v[112:115], a[128:131]\n"
    "v_mfma_f32_16x16x32_bf16 a[32:35], v[100:103], v[64:67], a[32:35]\n"
    "ds_read_b64_tr_b16 v[162:163], v14 offset:9216\n"
    "v_mfma_f32_16x16x32_bf16 a[160:163], v[64:67], v[116:119], a[160:163]\n"
    "v_mfma_f32_16x16x32_bf16 a[64:67], v[104:107], v[64:67], a[64:67]\n"
    "ds_read_b64_tr_b16 v[176:177], v14 offset:24576\n"
    "v_mfma_f32_16x16x32_bf16 a[192:195], v[64:67], v[120:123], a[192:195]\n"
    "v_mfma_f32_16x16x32_bf16 a[96:99], v[108:111], v[64:67], a[96:99]\n"
    "ds_read_b64_tr_b16 v[178:179], v14 offset:25600\n"
    "v_mfma_f32_16x16x32_bf16 a[224:227], v[64:67], v[124:127], a[224:227]\n"
    "v_mfma_f32_16x16x32_bf16 a[4:7], v[96:99], v[68:71], a[4:7]\n"
    "ds_read_b64_tr_b16 v[164:165], v15 offset:8192\n"
    "v_mfma_f32_16x16x32_bf16 a[132:135], v[68:71], v[112:115], a[132:135]\n"
    "v_mfma_f32_16x16x32_bf16 a[36:39], v[100:103], v[68:71], a[36:39]\n"
    "ds_read_b64_tr_b16 v[166:167], v15 offset:9216\n"
    "v_mfma_f32_16x16x32_bf16 a[164:167], v[68:71], v[116:119], a[164:167]\n"
    "v_mfma_f32_16x16x32_bf16 a[68:71], v[104:107], v[68:71], a[68:71]\n"
    "ds_read_b64_tr_b16 v[180:181], v15 offset:24576\n"
    "v_mfma_f32_16x16x32_bf16 a[196:199], v[68:71], v[120:123], a[196:199]\n"
    "v_mfma_f32_16x16x32_bf16 a[100:103], v[108:111], v[68:71], a[100:103]\n"
    "ds_read_b64_tr_b16 v[182:183], v15 offset:25600\n"
    "v_mfma_f32_16x16x32_bf16 a[228:231], v[68:71], v[124:127], a[228:231]\n"
    "v_mfma_f32_16x16x32_bf16 a[8:11], v[96:99], v[72:75], a[8:11]\n"
    "ds_read_b64_tr_b16 v[168:169], v14 offset:8448\n"
    "v_mfma_f32_16x16x32_bf16 a[136:139], v[72:75], v[112:115], a[136:139]\n"
    "v_mfma_f32_16x16x32_bf16 a[40:43], v[100:103], v[72:75], a[40:43]\n"
    "ds_read_b64_tr_b16 v[170:171], v14 offset:9472\n"
    "v_mfma_f32_16x16x32_bf16 a[168:171], v[72:75], v[116:119], a[168:171]\n"
    "v_mfma_f32_16x16x32_bf16 a[72:75], v[104:107], v[72:75], a[72:75]\n"
    "ds_read_b64_tr_b16 v[184:185], v14 offset:24832\n"
    "v_mfma_f32_16x16x32_bf16 a[200:203], v[72:75], v[120:123], a[200:203]\n"
    "v_mfma_f32_16x16x32_bf16 a[104:107], v[108:111], v[72:75], a[104:107]\n"
    "ds_read_b64_tr_b16 v[186:187], v14 offset:25856\n"
    "v_mfma_f32_16x16x32_bf16 a[232:235], v[72:75], v[124:127], a[232:235]\n"
    "v_mfma_f32_16x16x32_bf16 a[12:15], v[96:99], v[76:79], a[12:15]\n"
    "ds_read_b64_tr_b16 v[172:173], v15 offset:8448\n"
    "v_mfma_f32_16x16x32_bf16 a[140:143], v[76:79], v[112:115], a[140:143]\n"
    "v_mfma_f32_16x16x32_bf16 a[44:47], v[100:103], v[76:79], a[44:47]\n"
    "ds_read_b64_tr_b16 v[174:175], v15 offset:9472\n"
    "v_mfma_f32_16x16x32_bf16 a[172:175], v[76:79], v[116:119], a[172:175]\n"
    "v_mfma_f32_16x16x32_bf16 a[76:79], v[104:107], v[76:79], a[76:79]\n"
    "ds_read_b64_tr_b16 v[188:189], v15 offset:24832\n"
    "v_mfma_f32_16x16x32_bf16 a[204:207], v[76:79], v[120:123], a[204:207]\n"
    "v_mfma_f32_16x16x32_bf16 a[108:111], v[108:111], v[76:79], a[108:111]\n"
    "ds_read_b64_tr_b16 v[190:191], v15 offset:25856\n"
    "v_mfma_f32_16x16x32_bf16 a[236:239], v[76:79], v[124:127], a[236:239]\n"
    "v_mfma_f32_16x16x32_bf16 a[16:19], v[96:99], v[80:83], a[16:19]\n"
    "ds_read_b128 v[128:131], v13\n"
    "v_mfma_f32_16x16x32_bf16 a[144:147], v[80:83], v[112:115], a[144:147]\n"
    "v_mfma_f32_16x16x32_bf16 a[48:51], v[100:103], v[80:83], a[48:51]\n"
    "ds_read_b128 v[132:135], v13 offset:2048\n"
    "v_mfma_f32_16x16x32_bf16 a[176:179], v[80:83], v[116:119], a[176:179]\n"
    "v_mfma_f32_16x16x32_bf16 a[80:83], v[104:107], v[80:83], a[80:83]\n"
    "ds_read_b128 v[136:139], v13 offset:4096\n"
    "v_mfma_f32_16x16x32_bf16 a[208:211], v[80:83], v[120:123], a[208:211]\n"
    "v_mfma_f32_16x16x32_bf16 a[112:115], v[108:111], v[80:83], a[112:115]\n"
    "ds_read_b128 v[140:143], v13 offset:6144\n"
    "v_mfma_f32_16x16x32_bf16 a[240:243], v[80:83], v[124:127], a[240:243]\n"
    "v_mfma_f32_16x16x32_bf16 a[20:23], v[96:99], v[84:87], a[20:23]\n"
    "ds_read_b128 v[144:147], v13 offset:8192\n"
    "v_mfma_f32_16x16x32_bf16 a[148:151], v[84:87], v[112:115], a[148:151]\n"
    "v_mfma_f32_16x16x32_bf16 a[52:55], v[100:103], v[84:87], a[52:55]\n"
    "ds_read_b128 v[148:151], v13 offset:10240\n"
    "v_mfma_f32_16x16x32_bf16 a[180:183], v[84:87], v[116:119], a[180:183]\n"
    "v_mfma_f32_16x16x32_bf16 a[84:87], v[104:107], v[84:87], a[84:87]\n"
    "ds_read_b128 v[152:155], v13 offset:12288\n"
    "v_mfma_f32_16x16x32_bf16 a[212:215], v[84:87], v[120:123], a[212:215]\n"
    "v_mfma_f32_16x16x32_bf16 a[116:119], v[108:111], v[84:87], a[116:119]\n"
    "ds_read_b128 v[156:159], v13 offset:14336\n"
    "v_mfma_f32_16x16x32_bf16 a[244:247], v[84:87], v[124:127], a[244:247]\n"
    "v_mfma_f32_16x16x32_bf16 a[24:27], v[96:99], v[88:91], a[24:27]\n"
    "v_mfma_f32_16x16x32_bf16 a[152:155], v[88:91], v[112:115], a[152:155]\n"
    "v_mfma_f32_16x16x32_bf16 a[56:59], v[100:103], v[88:91], a[56:59]\n"
    "v_mfma_f32_16x16x32_bf16 a[184:187], v[88:91], v[116:119], a[184:187]\n"
    "v_mfma_f32_16x16x32_bf16 a[88:91], v[104:107], v[88:91], a[88:91]\n"
    "v_xor_b32 v12, 0x10000, v12\n"
    "v_xor_b32 v13, 0x10000, v13\n"
    "v_mfma_f32_16x16x32_bf16 a[216:219], v[88:91], v[120:123], a[216:219]\n"
    "v_subrev_u32 v14, 0x18000, v14\n"
    "v_subrev_u32 v15, 0x18000, v15\n"
    "v_mfma_f32_16x16x32_bf16 a[120:123], v[108:111], v[88:91], a[120:123]\n"
    "v_mfma_f32_16x16x32_bf16 a[248:251], v[88:91], v[124:127], a[248:251]\n"
    "v_mfma_f32_16x16x32_bf16 a[28:31], v[96:99], v[92:95], a[28:31]\n"
    "v_mfma_f32_16x16x32_bf16 a[156:159], v[92:95], v[112:115], a[156:159]\n"
    "v_mfma_f32_16x16x32_bf16 a[60:63], v[100:103], v[92:95], a[60:63]\n"
    "v_mfma_f32_16x16x32_bf16 a[188:191], v[92:95], v[116:119], a[188:191]\n"
    "v_mfma_f32_16x16x32_bf16 a[92:95], v[104:107], v[92:95], a[92:95]\n"
    "v_mfma_f32_16x16x32_bf16 a[220:223], v[92:95], v[120:123], a[220:223]\n"
    "v_mfma_f32_16x16x32_bf16 a[124:127], v[108:111], v[92:95], a[124:127]\n"
    "v_mfma_f32_16x16x32_bf16 a[252:255], v[92:95], v[124:127], a[252:255]\n"
    "s_waitcnt vmcnt(12) lgkmcnt(0)\n"
    "s_barrier\n"
    "v_mfma_f32_16x16x32_bf16 a[0:3], v[160:163], v[128:131], a[0:3]\n"
    "ds_read_b64_tr_b16 v[96:97], v14\n"
    "v_mfma_f32_16x16x32_bf16 a[128:131], v[128:131], v[176:179], a[128:131]\n"
    "s_add_u32 s52, s53, 0x380\n"
    "s_add_u32 m0, s17, 0x10000\n"
    "v_mfma_f32_16x16x32_bf16 a[32:35], v[164:167], v[128:131], a[32:35]\n"
    "ds_read_b64_tr_b16 v[98:99], v14 offset:1024\n"
    "v_mfma_f32_16x16x32_bf16 a[160:163], v[128:131], v[180:183], a[160:163]\n"
    "buffer_load_dwordx4 v4, s[36:39], s52 offen lds\n"
    "v_mfma_f32_16x16x32_bf16 a[64:67], v[168:171], v[128:131], a[64:67]\n"
    "ds_read_b64_tr_b16 v[112:113], v14 offset:16384\n"
    "v_mfma_f32_16x16x32_bf16 a[192:195], v[128:131], v[184:187], a[192:195]\n"
    "v_mfma_f32_16x16x32_bf16 a[96:99], v[172:175], v[128:131], a[96:99]\n"
    "ds_read_b64_tr_b16 v[114:115], v14 offset:17408\n"
    "v_mfma_f32_16x16x32_bf16 a[224:227], v[128:131], v[188:191], a[224:227]\n"
    "buffer_load_dwordx4 v5, s[36:39], s52 offen offset:1024 lds\n"
    "v_mfma_f32_16x16x32_bf16 a[4:7], v[160:163], v[132:135], a[4:7]\n"
    "ds_read_b64_tr_b16 v[100:101], v15\n"
    "v_mfma_f32_16x16x32_bf16 a[132:135], v[132:135], v[176:179], a[132:135]\n"
    "v_mfma_f32_16x16x32_bf16 a[36:39], v[164:167], v[132:135], a[36:39]\n"
    "ds_read_b64_tr_b16 v[102:103], v15 offset:1024\n"
    "v_mfma_f32_16x16x32_bf16 a[164:167], v[132:135], v[180:183], a[164:167]\n"
    "buffer_load_dwordx4 v6, s[36:39], s52 offen offset:2048 lds\n"
    "v_mfma_f32_16x16x32_bf16 a[68:71], v[168:171], v[132:135], a[68:71]\n"
    "ds_read_b64_tr_b16 v[116:117], v15 offset:16384\n"
    "v_mfma_f32_16x16x32_bf16 a[196:199], v[132:135], v[184:187], a[196:199]\n"
    "v_mfma_f32_16x16x32_bf16 a[100:103], v[172:175], v[132:135], a[100:103]\n"
    "ds_read_b64_tr_b16 v[118:119], v15 offset:17408\n"
    "v_mfma_f32_16x16x32_bf16 a[228:231], v[132:135], v[188:191], a[228:231]\n"
    "buffer_load_dwordx4 v7, s[36:39], s52 offen offset:3072 lds\n"
    "v_mfma_f32_16x16x32_bf16 a[8:11], v[160:163], v[136:139], a[8:11]\n"
    "ds_read_b64_tr_b16 v[104:105], v14 offset:256\n"
    "s_add_u32 s52, s52, 0x8000\n"
    "v_mfma_f32_16x16x32_bf16 a[136:139], v[136:139], v[176:179], a[136:139]\n"
    "s_add_u32 m0, m0, 0x1000\n"
    "v_mfma_f32_16x16x32_bf16 a[40:43], v[164:167], v[136:139], a[40:43]\n"
    "ds_read_b64_tr_b16 v[106:107], v14 offset:1280\n"
    "v_mfma_f32_16x16x32_bf16 a[168:171], v[136:139], v[180:183], a[168:171]\n"
    "buffer_load_dwordx4 v4, s[36:39], s52 offen lds\n"
    "v_mfma_f32_16x16x32_bf16 a[72:75], v[168:171], v[136:139], a[72:75]\n"
    "ds_read_b64_tr_b16 v[120:121], v14 offset:16640\n"
    "v_mfma_f32_16x16x32_bf16 a[200:203], v[136:139], v[184:187], a[200:203]\n"
    "v_mfma_f32_16x16x32_bf16 a[104:107], v[172:175], v[136:139], a[104:107]\n"
    "ds_read_b64_tr_b16 v[122:123], v14 offset:17664\n"
    "v_mfma_f32_16x16x32_bf16 a[232:235], v[136:139], v[188:191], a[232:235]\n"
    "buffer_load_dwordx4 v5, s[36:39], s52 offen offset:1024 lds\n"
    "v_mfma_f32_16x16x32_bf16 a[12:15], v[160:163], v[140:143], a[12:15]\n"
    "ds_read_b64_tr_b16 v[108:109], v15 offset:256\n"
    "v_mfma_f32_16x16x32_bf16 a[140:143], v[140:143], v[176:179], a[140:143]\n"
    "v_mfma_f32_16x16x32_bf16 a[44:47], v[164:167], v[140:143], a[44:47]\n"
    "ds_read_b64_tr_b16 v[110:111], v15 offset:1280\n"
    "v_mfma_f32_16x16x32_bf16 a[172:175], v[140:143], v[180:183], a[172:175]\n"
    "buffer_load_dwordx4 v6, s[36:39], s52 offen offset:2048 lds\n"
    "v_mfma_f32_16x16x32_bf16 a[76:79], v[168:171], v[140:143], a[76:79]\n"
    "ds_read_b64_tr_b16 v[124:125], v15 offset:16640\n"
    "v_mfma_f32_16x16x32_bf16 a[204:207], v[140:143], v[184:187], a[204:207]\n"
    "v_mfma_f32_16x16x32_bf16 a[108:111], v[172:175], v[140:143], a[108:111]\n"
    "ds_read_b64_tr_b16 v[126:127], v15 offset:17664\n"
    "v_mfma_f32_16x16x32_bf16 a[236:239], v[140:143], v[188:191], a[236:239]\n"
    "buffer_load_dwordx4 v7, s[36:39], s52 offen offset:3072 lds\n"
    "v_mfma_f32_16x16x32_bf16 a[16:19], v[160:163], v[144:147], a[16:19]\n"
    "ds_read_b128 v[64:67], v12\n"
    "s_add_u32 s60, s63, 0x80\n"
    "v_mfma_f32_16x16x32_bf16 a[144:147], v[144:147], v[176:179], a[144:147]\n"
    "s_add_u32 m0, s17, 0x20000\n"
    "v_mfma_f32_16x16x32_bf16 a[48:51], v[164:167], v[144:147], a[48:51]\n"
    "ds_read_b128 v[68:71], v12 offset:2048\n"
    "v_mfma_f32_16x16x32_bf16 a[176:179], v[144:147], v[180:183], a[176:179]\n"
    "buffer_load_dwordx4 v4, s[24:27], s60 offen lds\n"
    "v_mfma_f32_16x16x32_bf16 a[80:83], v[168:171], v[144:147], a[80:83]\n"
    "ds_read_b128 v[72:75], v12 offset:4096\n"
    "v_mfma_f32_16x16x32_bf16 a[208:211], v[144:147], v[184:187], a[208:211]\n"
    "v_mfma_f32_16x16x32_bf16 a[112:115], v[172:175], v[144:147], a[112:115]\n"
    "ds_read_b128 v[76:79], v12 offset:6144\n"
    "v_mfma_f32_16x16x32_bf16 a[240:243], v[144:147], v[188:191], a[240:243]\n"
    "buffer_load_dwordx4 v5, s[24:27], s60 offen offset:1024 lds\n"
    "v_mfma_f32_16x16x32_bf16 a[20:23], v[160:163], v[148:151], a[20:23]\n"
    "ds_read_b128 v[80:83], v12 offset:8192\n"
    "v_mfma_f32_16x16x32_bf16 a[148:151], v[148:151], v[176:179], a[148:151]\n"
    "v_mfma_f32_16x16x32_bf16 a[52:55], v[164:167], v[148:151], a[52:55]\n"
    "ds_read_b128 v[84:87], v12 offset:10240\n"
    "v_mfma_f32_16x16x32_bf16 a[180:183], v[148:151], v[180:183], a[180:183]\n"
    "buffer_load_dwordx4 v6, s[24:27], s60 offen offset:2048 lds\n"
    "v_mfma_f32_16x16x32_bf16 a[84:87], v[168:171], v[148:151], a[84:87]\n"
    "ds_read_b128 v[88:91], v12 offset:12288\n"
    "v_mfma_f32_16x16x32_bf16 a[212:215], v[148:151], v[184:187], a[212:215]\n"
    "v_mfma_f32_16x16x32_bf16 a[116:119], v[172:175], v[148:151], a[116:119]\n"
    "ds_read_b128 v[92:95], v12 offset:14336\n"
    "v_mfma_f32_16x16x32_bf16 a[244:247], v[148:151], v[188:191], a[244:247]\n"
    "buffer_load_dwordx4 v7, s[24:27], s60 offen offset:3072 lds\n"
    "v_mfma_f32_16x16x32_bf16 a[24:27], v[160:163], v[152:155], a[24:27]\n"
    "s_add_u32 s60, s60, 0x8000\n"
    "s_add_u32 m0, m0, 0x1000\n"
    "v_mfma_f32_16x16x32_bf16 a[152:155], v[152:155], v[176:179], a[152:155]\n"
    "v_mfma_f32_16x16x32_bf16 a[56:59], v[164:167], v[152:155], a[56:59]\n"
    "buffer_load_dwordx4 v4, s[24:27], s60 offen lds\n"
    "v_mfma_f32_16x16x32_bf16 a[184:187], v[152:155], v[180:183], a[184:187]\n"
    "v_mfma_f32_16x16x32_bf16 a[88:91], v[168:171], v[152:155], a[88:91]\n"
    "v_mfma_f32_16x16x32_bf16 a[216:219], v[152:155], v[184:187], a[216:219]\n"
    "buffer_load_dwordx4 v5, s[24:27], s60 offen offset:1024 lds\n"
    "v_mfma_f32_16x16x32_bf16 a[120:123], v[172:175], v[152:155], a[120:123]\n"
    "v_mfma_f32_16x16x32_bf16 a[248:251], v[152:155], v[188:191], a[248:251]\n"
    "v_mfma_f32_16x16x32_bf16 a[28:31], v[160:163], v[156:159], a[28:31]\n"
    "buffer_load_dwordx4 v6, s[24:27], s60 offen offset:2048 lds\n"
    "v_mfma_f32_16x16x32_bf16 a[156:159], v[156:159], v[176:179], a[156:159]\n"
    "v_mfma_f32_16x16x32_bf16 a[60:63], v[164:167], v[156:159], a[60:63]\n"
    "v_mfma_f32_16x16x32_bf16 a[188:191], v[156:159], v[180:183], a[188:191]\n"
    "buffer_load_dwordx4 v7, s[24:27], s60 offen offset:3072 lds\n"
    "v_mfma_f32_16x16x32_bf16 a[92:95], v[168:171], v[156:159], a[92:95]\n"
    "v_mfma_f32_16x16x32_bf16 a[220:223], v[156:159], v[184:187], a[220:223]\n"
    "v_mfma_f32_16x16x32_bf16 a[124:127], v[172:175], v[156:159], a[124:127]\n"
    "v_mfma_f32_16x16x32_bf16 a[252:255], v[156:159], v[188:191], a[252:255]\n"
    "buffer_load_dword v55, v54, s[48:51], s6 offen\n"
    "s_add_u32 s6, s6, s7\n"
    "buffer_load_dword v55, v54, s[48:51], s6 offen\n"
    "s_add_u32 s6, s6, s7\n"
    "s_waitcnt lgkmcnt(0)\n"
    "v_mfma_f32_16x16x32_bf16 a[0:3], v[96:99], v[64:67], a[0:3]\n"
    "ds_read_b64_tr_b16 v[160:161], v14 offset:8192\n"
    "v_mfma_f32_16x16x32_bf16 a[128:131], v[64:67], v[112:115], a[128:131]\n"
    "v_mfma_f32_16x16x32_bf16 a[32:35], v[100:103], v[64:67], a[32:35]\n"
    "ds_read_b64_tr_b16 v[162:163], v14 offset:9216\n"
    "v_mfma_f32_16x16x32_bf16 a[160:163], v[64:67], v[116:119], a[160:163]\n"
    "v_mfma_f32_16x16x32_bf16 a[64:67], v[104:107], v[64:67], a[64:67]\n"
    "ds_read_b64_tr_b16 v[176:177], v14 offset:24576\n"
    "v_mfma_f32_16x16x32_bf16 a[192:195], v[64:67], v[120:123], a[192:195]\n"
    "v_mfma_f32_16x16x32_bf16 a[96:99], v[108:111], v[64:67], a[96:99]\n"
    "ds_read_b64_tr_b16 v[178:179], v14 offset:25600\n"
    "v_mfma_f32_16x16x32_bf16 a[224:227], v[64:67], v[124:127], a[224:227]\n"
    "v_mfma_f32_16x16x32_bf16 a[4:7], v[96:99], v[68:71], a[4:7]\n"
    "ds_read_b64_tr_b16 v[164:165], v15 offset:8192\n"
    "v_mfma_f32_16x16x32_bf16 a[132:135], v[68:71], v[112:115], a[132:135]\n"
    "v_mfma_f32_16x16x32_bf16 a[36:39], v[100:103], v[68:71], a[36:39]\n"
    "ds_read_b64_tr_b16 v[166:167], v15 offset:9216\n"
    "v_mfma_f32_16x16x32_bf16 a[164:167], v[68:71], v[116:119], a[164:167]\n"
    "v_mfma_f32_16x16x32_bf16 a[68:71], v[104:107], v[68:71], a[68:71]\n"
    "ds_read_b64_tr_b16 v[180:181], v15 offset:24576\n"
    "v_mfma_f32_16x16x32_bf16 a[196:199], v[68:71], v[120:123], a[196:199]\n"
    "v_mfma_f32_16x16x32_bf16 a[100:103], v[108:111], v[68:71], a[100:103]\n"
    "ds_read_b64_tr_b16 v[182:183], v15 offset:25600\n"
    "v_mfma_f32_16x16x32_bf16 a[228:231], v[68:71], v[124:127], a[228:231]\n"
    "v_mfma_f32_16x16x32_bf16 a[8:11], v[96:99], v[72:75], a[8:11]\n"
    "ds_read_b64_tr_b16 v[168:169], v14 offset:8448\n"
    "v_mfma_f32_16x16x32_bf16 a[136:139], v[72:75], v[112:115], a[136:139]\n"
    "v_mfma_f32_16x16x32_bf16 a[40:43], v[100:103], v[72:75], a[40:43]\n"
    "ds_read_b64_tr_b16 v[170:171], v14 offset:9472\n"
    "v_mfma_f32_16x16x32_bf16 a[168:171], v[72:75], v[116:119], a[168:171]\n"
    "v_mfma_f32_16x16x32_bf16 a[72:75], v[104:107], v[72:75], a[72:75]\n"
    "ds_read_b64_tr_b16 v[184:185], v14 offset:24832\n"
    "v_mfma_f32_16x16x32_bf16 a[200:203], v[72:75], v[120:123], a[200:203]\n"
    "v_mfma_f32_16x16x32_bf16 a[104:107], v[108:111], v[72:75], a[104:107]\n"
    "ds_read_b64_tr_b16 v[186:187], v14 offset:25856\n"
    "v_mfma_f32_16x16x32_bf16 a[232:235], v[72:75], v[124:127], a[232:235]\n"
    "v_mfma_f32_16x16x32_bf16 a[12:15], v[96:99], v[76:79], a[12:15]\n"
    "ds_read_b64_tr_b16 v[172:173], v15 offset:8448\n"
    "v_mfma_f32_16x16x32_bf16 a[140:143], v[76:79], v[112:115], a[140:143]\n"
    "v_mfma_f32_16x16x32_bf16 a[44:47], v[100:103], v[76:79], a[44:47]\n"
    "ds_read_b64_tr_b16 v[174:175], v15 offset:9472\n"
    "v_mfma_f32_16x16x32_bf16 a[172:175], v[76:79], v[116:119], a[172:175]\n"
    "v_mfma_f32_16x16x32_bf16 a[76:79], v[104:107], v[76:79], a[76:79]\n"
    "ds_read_b64_tr_b16 v[188:189], v15 offset:24832\n"
    "v_mfma_f32_16x16x32_bf16 a[204:207], v[76:79], v[120:123], a[204:207]\n"
    "v_mfma_f32_16x16x32_bf16 a[108:111], v[108:111], v[76:79], a[108:111]\n"
    "ds_read_b64_tr_b16 v[190:191], v15 offset:25856\n"
    "v_mfma_f32_16x16x32_bf16 a[236:239], v[76:79], v[124:127], a[236:239]\n"
    "v_mfma_f32_16x16x32_bf16 a[16:19], v[96:99], v[80:83], a[16:19]\n"
    "ds_read_b128 v[128:131], v13\n"
    "v_mfma_f32_16x16x32_bf16 a[144:147], v[80:83], v[112:115], a[144:147]\n"
    "v_mfma_f32_16x16x32_bf16 a[48:51], v[100:103], v[80:83], a[48:51]\n"
    "ds_read_b128 v[132:135], v13 offset:2048\n"
    "v_mfma_f32_16x16x32_bf16 a[176:179], v[80:83], v[116:119], a[176:179]\n"
    "v_mfma_f32_16x16x32_bf16 a[80:83], v[104:107], v[80:83], a[80:83]\n"
    "ds_read_b128 v[136:139], v13 offset:4096\n"
    "v_mfma_f32_16x16x32_bf16 a[208:211], v[80:83], v[120:123], a[208:211]\n"
    "v_mfma_f32_16x16x32_bf16 a[112:115], v[108:111], v[80:83], a[112:115]\n"
    "ds_read_b128 v[140:143], v13 offset:6144\n"
    "v_mfma_f32_16x16x32_bf16 a[240:243], v[80:83], v[124:127], a[240:243]\n"
    "v_mfma_f32_16x16x32_bf16 a[20:23], v[96:99], v[84:87], a[20:23]\n"
    "ds_read_b128 v[144:147], v13 offset:8192\n"
    "v_mfma_f32_16x16x32_bf16 a[148:151], v[84:87], v[112:115], a[148:151]\n"
    "v_mfma_f32_16x16x32_bf16 a[52:55], v[100:103], v[84:87], a[52:55]\n"
    "ds_read_b128 v[148:151], v13 offset:10240\n"
    "v_mfma_f32_16x16x32_bf16 a[180:183], v[84:87], v[116:119], a[180:183]\n"
    "v_mfma_f32_16x16x32_bf16 a[84:87], v[104:107], v[84:87], a[84:87]\n"
    "ds_read_b128 v[152:155], v13 offset:12288\n"
    "v_mfma_f32_16x16x32_bf16 a[212:215], v[84:87], v[120:123], a[212:215]\n"
    "v_mfma_f32_16x16x32_bf16 a[116:119], v[108:111], v[84:87], a[116:119]\n"
    "ds_read_b128 v[156:159], v13 offset:14336\n"
    "v_mfma_f32_16x16x32_bf16 a[244:247], v[84:87], v[124:127], a[244:247]\n"
    "v_mfma_f32_16x16x32_bf16 a[24:27], v[96:99], v[88:91], a[24:27]\n"
    "v_mfma_f32_16x16x32_bf16 a[152:155], v[88:91], v[112:115], a[152:155]\n"
    "v_mfma_f32_16x16x32_bf16 a[56:59], v[100:103], v[88:91], a[56:59]\n"
    "v_mfma_f32_16x16x32_bf16 a[184:187], v[88:91], v[116:119], a[184:187]\n"
    "v_mfma_f32_16x16x32_bf16 a[88:91], v[104:107], v[88:91], a[88:91]\n"
    "v_xor_b32 v12, 0x10000, v12\n"
    "v_xor_b32 v13, 0x10000, v13\n"
    "v_mfma_f32_16x16x32_bf16 a[216:219], v[88:91], v[120:123], a[216:219]\n"
    "v_add_u32 v14, 0x10000, v14\n"
    "v_add_u32 v15, 0x10000, v15\n"
    "v_mfma_f32_16x16x32_bf16 a[120:123], v[108:111], v[88:91], a[120:123]\n"
    "v_mfma_f32_16x16x32_bf16 a[248:251], v[88:91], v[124:127], a[248:251]\n"
    "v_mfma_f32_16x16x32_bf16 a[28:31], v[96:99], v[92:95], a[28:31]\n"
    "v_mfma_f32_16x16x32_bf16 a[156:159], v[92:95], v[112:115], a[156:159]\n"
    "v_mfma_f32_16x16x32_bf16 a[60:63], v[100:103], v[92:95], a[60:63]\n"
    "v_mfma_f32_16x16x32_bf16 a[188:191], v[92:95], v[116:119], a[188:191]\n"
    "v_mfma_f32_16x16x32_bf16 a[92:95], v[104:107], v[92:95], a[92:95]\n"
    "v_mfma_f32_16x16x32_bf16 a[220:223], v[92:95], v[120:123], a[220:223]\n"
    "v_mfma_f32_16x16x32_bf16 a[124:127], v[108:111], v[92:95], a[124:127]\n"
    "v_mfma_f32_16x16x32_bf16 a[252:255], v[92:95], v[124:127], a[252:255]\n"
    "s_waitcnt vmcnt(10) lgkmcnt(0)\n"
    "s_barrier\n"
    "v_mfma_f32_16x16x32_bf16 a[0:3], v[160:163], v[128:131], a[0:3]\n"
    "ds_read_b64_tr_b16 v[96:97], v14\n"
    "s_lshl_b32 s48, s20, 5\n"
    "v_mfma_f32_16x16x32_bf16 a[128:131], v[128:131], v[176:179], a[128:131]\n"
    "s_add_u32 s61, s54, 0\n"
    "s_add_u32 m0, s16, 0x8000\n"
    "v_mfma_f32_16x16x32_bf16 a[32:35], v[164:167], v[128:131], a[32:35]\n"
    "ds_read_b64_tr_b16 v[98:99], v14 offset:1024\n"
    "s_lshl_b32 s22, s21, 2\n"
    "v_mfma_f32_16x16x32_bf16 a[160:163], v[128:131], v[180:183], a[160:163]\n"
    "buffer_load_dwordx4 v8, s[28:31], s61 offen lds\n"
    "v_mfma_f32_16x16x32_bf16 a[64:67], v[168:171], v[128:131], a[64:67]\n"
    "ds_read_b64_tr_b16 v[112:113], v14 offset:16384\n"
    "s_add_u32 s48, s48, s22\n"
    "v_mfma_f32_16x16x32_bf16 a[192:195], v[128:131], v[184:187], a[192:195]\n"
    "s_and_b32 s22, s19, 2\n"
    "s_add_u32 s48, s48, s22\n"
    "v_mfma_f32_16x16x32_bf16 a[96:99], v[172:175], v[128:131], a[96:99]\n"
    "ds_read_b64_tr_b16 v[114:115], v14 offset:17408\n"
    "s_lshl_b32 s48, s48, 15\n"
    "v_mfma_f32_16x16x32_bf16 a[224:227], v[128:131], v[188:191], a[224:227]\n"
    "buffer_load_dwordx4 v9, s[28:31], s61 offen offset:1024 lds\n"
    "v_mfma_f32_16x16x32_bf16 a[4:7], v[160:163], v[132:135], a[4:7]\n"
    "ds_read_b64_tr_b16 v[100:101], v15\n"
    "s_add_u32 s48, s48, 0x100000\n"
    "v_mfma_f32_16x16x32_bf16 a[132:135], v[132:135], v[176:179], a[132:135]\n"
    "s_and_b32 s22, s19, 1\n"
    "s_lshl_b32 s49, s22, 12\n"
    "v_mfma_f32_16x16x32_bf16 a[36:39], v[164:167], v[132:135], a[36:39]\n"
    "ds_read_b64_tr_b16 v[102:103], v15 offset:1024\n"
    "s_add_u32 s49, s49, s48\n"
    "v_mfma_f32_16x16x32_bf16 a[164:167], v[132:135], v[180:183], a[164:167]\n"
    "buffer_load_dwordx4 v10, s[28:31], s61 offen offset:2048 lds\n"
    "v_mfma_f32_16x16x32_bf16 a[68:71], v[168:171], v[132:135], a[68:71]\n"
    "ds_read_b64_tr_b16 v[116:117], v15 offset:16384\n"
    "s_add_u32 s49, s49, 0x4000\n"
    "v_mfma_f32_16x16x32_bf16 a[196:199], v[132:135], v[184:187], a[196:199]\n"
    "s_lshl_b32 s22, s22, 11\n"
    "s_add_u32 s48, s48, s22\n"
    "v_mfma_f32_16x16x32_bf16 a[100:103], v[172:175], v[132:135], a[100:103]\n"
    "ds_read_b64_tr_b16 v[118:119], v15 offset:17408\n"
    "v_mfma_f32_16x16x32_bf16 a[228:231], v[132:135], v[188:191], a[228:231]\n"
    "buffer_load_dwordx4 v11, s[28:31], s61 offen offset:3072 lds\n"
    "v_mfma_f32_16x16x32_bf16 a[8:11], v[160:163], v[136:139], a[8:11]\n"
    "ds_read_b64_tr_b16 v[104:105], v14 offset:256\n"
    "s_add_u32 s60, s63, 0\n"
    "v_mfma_f32_16x16x32_bf16 a[136:139], v[136:139], v[176:179], a[136:139]\n"
    "s_add_u32 m0, s17, 0\n"
    "v_mfma_f32_16x16x32_bf16 a[40:43], v[164:167], v[136:139], a[40:43]\n"
    "ds_read_b64_tr_b16 v[106:107], v14 offset:1280\n"
    "v_mfma_f32_16x16x32_bf16 a[168:171], v[136:139], v[180:183], a[168:171]\n"
    "buffer_load_dwordx4 v4, s[24:27], s60 offen lds\n"
    "v_mfma_f32_16x16x32_bf16 a[72:75], v[168:171], v[136:139], a[72:75]\n"
    "ds_read_b64_tr_b16 v[120:121], v14 offset:16640\n"
    "v_mfma_f32_16x16x32_bf16 a[200:203], v[136:139], v[184:187], a[200:203]\n"
    "v_mfma_f32_16x16x32_bf16 a[104:107], v[172:175], v[136:139], a[104:107]\n"
    "ds_read_b64_tr_b16 v[122:123], v14 offset:17664\n"
    "v_mfma_f32_16x16x32_bf16 a[232:235], v[136:139], v[188:191], a[232:235]\n"
    "buffer_load_dwordx4 v5, s[24:27], s60 offen offset:1024 lds\n"
    "v_mfma_f32_16x16x32_bf16 a[12:15], v[160:163], v[140:143], a[12:15]\n"
    "ds_read_b64_tr_b16 v[108:109], v15 offset:256\n"
    "v_mfma_f32_16x16x32_bf16 a[140:143], v[140:143], v[176:179], a[140:143]\n"
    "v_mfma_f32_16x16x32_bf16 a[44:47], v[164:167], v[140:143], a[44:47]\n"
    "ds_read_b64_tr_b16 v[110:111], v15 offset:1280\n"
    "v_mfma_f32_16x16x32_bf16 a[172:175], v[140:143], v[180:183], a[172:175]\n"
    "buffer_load_dwordx4 v6, s[24:27], s60 offen offset:2048 lds\n"
    "v_mfma_f32_16x16x32_bf16 a[76:79], v[168:171], v[140:143], a[76:79]\n"
    "ds_read_b64_tr_b16 v[124:125], v15 offset:16640\n"
    "v_mfma_f32_16x16x32_bf16 a[204:207], v[140:143], v[184:187], a[204:207]\n"
    "v_mfma_f32_16x16x32_bf16 a[108:111], v[172:175], v[140:143], a[108:111]\n"
    "ds_read_b64_tr_b16 v[126:127], v15 offset:17664\n"
    "v_mfma_f32_16x16x32_bf16 a[236:239], v[140:143], v[188:191], a[236:239]\n"
    "buffer_load_dwordx4 v7, s[24:27], s60 offen offset:3072 lds\n"
    "v_mfma_f32_16x16x32_bf16 a[16:19], v[160:163], v[144:147], a[16:19]\n"
    "ds_read_b128 v[64:67], v12\n"
    "s_add_u32 s60, s60, 0x8000\n"
    "v_mfma_f32_16x16x32_bf16 a[144:147], v[144:147], v[176:179], a[144:147]\n"
    "s_add_u32 m0, m0, 0x1000\n"
    "v_mfma_f32_16x16x32_bf16 a[48:51], v[164:167], v[144:147], a[48:51]\n"
    "ds_read_b128 v[68:71], v12 offset:2048\n"
    "v_mfma_f32_16x16x32_bf16 a[176:179], v[144:147], v[180:183], a[176:179]\n"
    "buffer_load_dwordx4 v4, s[24:27], s60 offen lds\n"
    "v_mfma_f32_16x16x32_bf16 a[80:83], v[168:171], v[144:147], a[80:83]\n"
    "ds_read_b128 v[72:75], v12 offset:4096\n"
    "v_mfma_f32_16x16x32_bf16 a[208:211], v[144:147], v[184:187], a[208:211]\n"
    "v_mfma_f32_16x16x32_bf16 a[112:115], v[172:175], v[144:147], a[112:115]\n"
    "ds_read_b128 v[76:79], v12 offset:6144\n"
    "v_mfma_f32_16x16x32_bf16 a[240:243], v[144:147], v[188:191], a[240:243]\n"
    "buffer_load_dwordx4 v5, s[24:27], s60 offen offset:1024 lds\n"
    "v_mfma_f32_16x16x32_bf16 a[20:23], v[160:163], v[148:151], a[20:23]\n"
    "ds_read_b128 v[80:83], v12 offset:8192\n"
    "v_mfma_f32_16x16x32_bf16 a[148:151], v[148:151], v[176:179], a[148:151]\n"
    "v_mfma_f32_16x16x32_bf16 a[52:55], v[164:167], v[148:151], a[52:55]\n"
    "ds_read_b128 v[84:87], v12 offset:10240\n"
    "v_mfma_f32_16x16x32_bf16 a[180:183], v[148:151], v[180:183], a[180:183]\n"
    "buffer_load_dwordx4 v6, s[24:27], s60 offen offset:2048 lds\n"
    "v_mfma_f32_16x16x32_bf16 a[84:87], v[168:171], v[148:151], a[84:87]\n"
    "ds_read_b128 v[88:91], v12 offset:12288\n"
    "v_mfma_f32_16x16x32_bf16 a[212:215], v[148:151], v[184:187], a[212:215]\n"
    "v_mfma_f32_16x16x32_bf16 a[116:119], v[172:175], v[148:151], a[116:119]\n"
    "ds_read_b128 v[92:95], v12 offset:14336\n"
    "v_mfma_f32_16x16x32_bf16 a[244:247], v[148:151], v[188:191], a[244:247]\n"
    "buffer_load_dwordx4 v7, s[24:27], s60 offen offset:3072 lds\n"
    "v_mfma_f32_16x16x32_bf16 a[24:27], v[160:163], v[152:155], a[24:27]\n"
    "s_add_u32 s61, s54, 0x80000\n"
    "s_add_u32 m0, s16, 0xc000\n"
    "v_mfma_f32_16x16x32_bf16 a[152:155], v[152:155], v[176:179], a[152:155]\n"
    "v_mfma_f32_16x16x32_bf16 a[56:59], v[164:167], v[152:155], a[56:59]\n"
    "buffer_load_dwordx4 v8, s[28:31], s61 offen lds\n"
    "v_mfma_f32_16x16x32_bf16 a[184:187], v[152:155], v[180:183], a[184:187]\n"
    "v_mfma_f32_16x16x32_bf16 a[88:91], v[168:171], v[152:155], a[88:91]\n"
    "v_mfma_f32_16x16x32_bf16 a[216:219], v[152:155], v[184:187], a[216:219]\n"
    "buffer_load_dwordx4 v9, s[28:31], s61 offen offset:1024 lds\n"
    "v_mfma_f32_16x16x32_bf16 a[120:123], v[172:175], v[152:155], a[120:123]\n"
    "v_mfma_f32_16x16x32_bf16 a[248:251], v[152:155], v[188:191], a[248:251]\n"
    "v_mfma_f32_16x16x32_bf16 a[28:31], v[160:163], v[156:159], a[28:31]\n"
    "buffer_load_dwordx4 v10, s[28:31], s61 offen offset:2048 lds\n"
    "v_mfma_f32_16x16x32_bf16 a[156:159], v[156:159], v[176:179], a[156:159]\n"
    "v_mfma_f32_16x16x32_bf16 a[60:63], v[164:167], v[156:159], a[60:63]\n"
    "v_mfma_f32_16x16x32_bf16 a[188:191], v[156:159], v[180:183], a[188:191]\n"
    "buffer_load_dwordx4 v11, s[28:31], s61 offen offset:3072 lds\n"
    "v_mfma_f32_16x16x32_bf16 a[92:95], v[168:171], v[156:159], a[92:95]\n"
    "v_mfma_f32_16x16x32_bf16 a[220:223], v[156:159], v[184:187], a[220:223]\n"
    "v_mfma_f32_16x16x32_bf16 a[124:127], v[172:175], v[156:159], a[124:127]\n"
    "v_mfma_f32_16x16x32_bf16 a[252:255], v[156:159], v[188:191], a[252:255]\n"
    "s_waitcnt lgkmcnt(0)\n"
    "v_mfma_f32_16x16x32_bf16 a[0:3], v[96:99], v[64:67], a[0:3]\n"
    "ds_read_b64_tr_b16 v[160:161], v14 offset:8192\n"
    "v_mfma_f32_16x16x32_bf16 a[128:131], v[64:67], v[112:115], a[128:131]\n"
    "v_mfma_f32_16x16x32_bf16 a[32:35], v[100:103], v[64:67], a[32:35]\n"
    "ds_read_b64_tr_b16 v[162:163], v14 offset:9216\n"
    "v_mfma_f32_16x16x32_bf16 a[160:163], v[64:67], v[116:119], a[160:163]\n"
    "v_mfma_f32_16x16x32_bf16 a[64:67], v[104:107], v[64:67], a[64:67]\n"
    "ds_read_b64_tr_b16 v[176:177], v14 offset:24576\n"
    "v_mfma_f32_16x16x32_bf16 a[192:195], v[64:67], v[120:123], a[192:195]\n"
    "v_mfma_f32_16x16x32_bf16 a[96:99], v[108:111], v[64:67], a[96:99]\n"
    "ds_read_b64_tr_b16 v[178:179], v14 offset:25600\n"
    "v_mfma_f32_16x16x32_bf16 a[224:227], v[64:67], v[124:127], a[224:227]\n"
    "v_mfma_f32_16x16x32_bf16 a[4:7], v[96:99], v[68:71], a[4:7]\n"
    "ds_read_b64_tr_b16 v[164:165], v15 offset:8192\n"
    "v_mfma_f32_16x16x32_bf16 a[132:135], v[68:71], v[112:115], a[132:135]\n"
    "v_mfma_f32_16x16x32_bf16 a[36:39], v[100:103], v[68:71], a[36:39]\n"
    "ds_read_b64_tr_b16 v[166:167], v15 offset:9216\n"
    "v_mfma_f32_16x16x32_bf16 a[164:167], v[68:71], v[116:119], a[164:167]\n"
    "v_mfma_f32_16x16x32_bf16 a[68:71], v[104:107], v[68:71], a[68:71]\n"
    "ds_read_b64_tr_b16 v[180:181], v15 offset:24576\n"
    "v_mfma_f32_16x16x32_bf16 a[196:199], v[68:71], v[120:123], a[196:199]\n"
    "v_mfma_f32_16x16x32_bf16 a[100:103], v[108:111], v[68:71], a[100:103]\n"
    "ds_read_b64_tr_b16 v[182:183], v15 offset:25600\n"
    "v_mfma_f32_16x16x32_bf16 a[228:231], v[68:71], v[124:127], a[228:231]\n"
    "v_mfma_f32_16x16x32_bf16 a[8:11], v[96:99], v[72:75], a[8:11]\n"
    "ds_read_b64_tr_b16 v[168:169], v14 offset:8448\n"
    "v_mfma_f32_16x16x32_bf16 a[136:139], v[72:75], v[112:115], a[136:139]\n"
    "v_mfma_f32_16x16x32_bf16 a[40:43], v[100:103], v[72:75], a[40:43]\n"
    "ds_read_b64_tr_b16 v[170:171], v14 offset:9472\n"
    "v_mfma_f32_16x16x32_bf16 a[168:171], v[72:75], v[116:119], a[168:171]\n"
    "v_mfma_f32_16x16x32_bf16 a[72:75], v[104:107], v[72:75], a[72:75]\n"
    "ds_read_b64_tr_b16 v[184:185], v14 offset:24832\n"
    "v_mfma_f32_16x16x32_bf16 a[200:203], v[72:75], v[120:123], a[200:203]\n"
    "v_mfma_f32_16x16x32_bf16 a[104:107], v[108:111], v[72:75], a[104:107]\n"
    "ds_read_b64_tr_b16 v[186:187], v14 offset:25856\n"
    "v_mfma_f32_16x16x32_bf16 a[232:235], v[72:75], v[124:127], a[232:235]\n"
    "v_mfma_f32_16x16x32_bf16 a[12:15], v[96:99], v[76:79], a[12:15]\n"
    "ds_read_b64_tr_b16 v[172:173], v15 offset:8448\n"
    "v_mfma_f32_16x16x32_bf16 a[140:143], v[76:79], v[112:115], a[140:143]\n"
    "v_mfma_f32_16x16x32_bf16 a[44:47], v[100:103], v[76:79], a[44:47]\n"
    "ds_read_b64_tr_b16 v[174:175], v15 offset:9472\n"
    "v_mfma_f32_16x16x32_bf16 a[172:175], v[76:79], v[116:119], a[172:175]\n"
    "v_mfma_f32_16x16x32_bf16 a[76:79], v[104:107], v[76:79], a[76:79]\n"
    "ds_read_b64_tr_b16 v[188:189], v15 offset:24832\n"
    "v_mfma_f32_16x16x32_bf16 a[204:207], v[76:79], v[120:123], a[204:207]\n"
    "v_mfma_f32_16x16x32_bf16 a[108:111], v[108:111], v[76:79], a[108:111]\n"
    "ds_read_b64_tr_b16 v[190:191], v15 offset:25856\n"
    "v_mfma_f32_16x16x32_bf16 a[236:239], v[76:79], v[124:127], a[236:239]\n"
    "v_mfma_f32_16x16x32_bf16 a[16:19], v[96:99], v[80:83], a[16:19]\n"
    "ds_read_b128 v[128:131], v13\n"
    "v_mfma_f32_16x16x32_bf16 a[144:147], v[80:83], v[112:115], a[144:147]\n"
    "v_mfma_f32_16x16x32_bf16 a[48:51], v[100:103], v[80:83], a[48:51]\n"
    "ds_read_b128 v[132:135], v13 offset:2048\n"
    "v_mfma_f32_16x16x32_bf16 a[176:179], v[80:83], v[116:119], a[176:179]\n"
    "v_mfma_f32_16x16x32_bf16 a[80:83], v[104:107], v[80:83], a[80:83]\n"
    "ds_read_b128 v[136:139], v13 offset:4096\n"
    "v_mfma_f32_16x16x32_bf16 a[208:211], v[80:83], v[120:123], a[208:211]\n"
    "v_mfma_f32_16x16x32_bf16 a[112:115], v[108:111], v[80:83], a[112:115]\n"
    "ds_read_b128 v[140:143], v13 offset:6144\n"
    "v_mfma_f32_16x16x32_bf16 a[240:243], v[80:83], v[124:127], a[240:243]\n"
    "v_mfma_f32_16x16x32_bf16 a[20:23], v[96:99], v[84:87], a[20:23]\n"
    "ds_read_b128 v[144:147], v13 offset:8192\n"
    "v_mfma_f32_16x16x32_bf16 a[148:151], v[84:87], v[112:115], a[148:151]\n"
    "v_mfma_f32_16x16x32_bf16 a[52:55], v[100:103], v[84:87], a[52:55]\n"
    "ds_read_b128 v[148:151], v13 offset:10240\n"
    "v_mfma_f32_16x16x32_bf16 a[180:183], v[84:87], v[116:119], a[180:183]\n"
    "v_mfma_f32_16x16x32_bf16 a[84:87], v[104:107], v[84:87], a[84:87]\n"
    "ds_read_b128 v[152:155], v13 offset:12288\n"
    "v_mfma_f32_16x16x32_bf16 a[212:215], v[84:87], v[120:123], a[212:215]\n"
    "v_mfma_f32_16x16x32_bf16 a[116:119], v[108:111], v[84:87], a[116:119]\n"
    "ds_read_b128 v[156:159], v13 offset:14336\n"
    "v_mfma_f32_16x16x32_bf16 a[244:247], v[84:87], v[124:127], a[244:247]\n"
    "v_mfma_f32_16x16x32_bf16 a[24:27], v[96:99], v[88:91], a[24:27]\n"
    "v_mfma_f32_16x16x32_bf16 a[152:155], v[88:91], v[112:115], a[152:155]\n"
    "v_mfma_f32_16x16x32_bf16 a[56:59], v[100:103], v[88:91], a[56:59]\n"
    "v_mfma_f32_16x16x32_bf16 a[184:187], v[88:91], v[116:119], a[184:187]\n"
    "v_mfma_f32_16x16x32_bf16 a[88:91], v[104:107], v[88:91], a[88:91]\n"
    "v_xor_b32 v12, 0x10000, v12\n"
    "v_xor_b32 v13, 0x10000, v13\n"
    "v_mfma_f32_16x16x32_bf16 a[216:219], v[88:91], v[120:123], a[216:219]\n"
    "v_add_u32 v14, 0x8000, v14\n"
    "v_add_u32 v15, 0x8000, v15\n"
    "v_mfma_f32_16x16x32_bf16 a[120:123], v[108:111], v[88:91], a[120:123]\n"
    "v_mfma_f32_16x16x32_bf16 a[248:251], v[88:91], v[124:127], a[248:251]\n"
    "v_mfma_f32_16x16x32_bf16 a[28:31], v[96:99], v[92:95], a[28:31]\n"
    "v_mfma_f32_16x16x32_bf16 a[156:159], v[92:95], v[112:115], a[156:159]\n"
    "v_mfma_f32_16x16x32_bf16 a[60:63], v[100:103], v[92:95], a[60:63]\n"
    "v_mfma_f32_16x16x32_bf16 a[188:191], v[92:95], v[116:119], a[188:191]\n"
    "v_mfma_f32_16x16x32_bf16 a[92:95], v[104:107], v[92:95], a[92:95]\n"
    "v_mfma_f32_16x16x32_bf16 a[220:223], v[92:95], v[120:123], a[220:223]\n"
    "v_mfma_f32_16x16x32_bf16 a[124:127], v[108:111], v[92:95], a[124:127]\n"
    "v_mfma_f32_16x16x32_bf16 a[252:255], v[92:95], v[124:127], a[252:255]\n"
    "s_waitcnt vmcnt(4) lgkmcnt(0)\n"
    "s_barrier\n"
    "s_lshl_b32 s23, s18, 3\n"
    "s_add_u32 s23, s23, 0x3000\n"
    "s_cmp_eq_u32 s19, 0\n"
    "s_cselect_b32 s23, s23, 0x7ff00000\n"
    "v_mov_b32 v60, s4\n"
    "v_mov_b32 v61, s5\n"
    "s_mov_b64 exec, 1\n"
    "buffer_store_dwordx2 v[60:61], v2, s[44:47], s23 offen sc1\n"
    "s_mov_b64 exec, -1\n"
    "v_mfma_f32_16x16x32_bf16 a[0:3], v[160:163], v[128:131], a[0:3]\n"
    "s_add_u32 s23, s17, 0\n"
    "v_and_b32 v20, 15, v1\n"
    "v_mfma_f32_16x16x32_bf16 a[128:131], v[128:131], v[176:179], a[128:131]\n"
    "v_lshrrev_b32 v21, 1, v20\n"
    "v_lshrrev_b32 v22, 4, v1\n"
    "v_mfma_f32_16x16x32_bf16 a[32:35], v[164:167], v[128:131], a[32:35]\n"
    "v_xor_b32 v21, v21, v22\n"
    "v_lshlrev_b32 v21, 4, v21\n"
    "v_mfma_f32_16x16x32_bf16 a[160:163], v[128:131], v[180:183], a[160:163]\n"
    "v_lshlrev_b32 v20, 7, v20\n"
    "v_add_u32 v20, v20, v21\n"
    "v_mfma_f32_16x16x32_bf16 a[64:67], v[168:171], v[128:131], a[64:67]\n"
    "v_add_u32 v12, s23, v20\n"
    "v_xor_b32 v13, 64, v12\n"
    "v_mfma_f32_16x16x32_bf16 a[192:195], v[128:131], v[184:187], a[192:195]\n"
    "s_mov_b32 s23, 0x8000\n"
    "v_lshrrev_b32 v20, 4, v1\n"
    "v_mfma_f32_16x16x32_bf16 a[96:99], v[172:175], v[128:131], a[96:99]\n"
    "v_lshlrev_b32 v21, 11, v20\n"
    "v_and_b32 v20, 1, v20\n"
    "v_mfma_f32_16x16x32_bf16 a[224:227], v[128:131], v[188:191], a[224:227]\n"
    "v_lshl_add_u32 v21, v20, 5, v21\n"
    "v_bfe_u32 v20, v1, 2, 2\n"
    "v_mfma_f32_16x16x32_bf16 a[4:7], v[160:163], v[132:135], a[4:7]\n"
    "v_lshl_add_u32 v21, v20, 6, v21\n"
    "v_and_b32 v20, 3, v1\n"
    "v_mfma_f32_16x16x32_bf16 a[132:135], v[132:135], v[176:179], a[132:135]\n"
    "v_lshl_add_u32 v21, v20, 3, v21\n"
    "v_add_u32 v14, s23, v21\n"
    "v_mfma_f32_16x16x32_bf16 a[36:39], v[164:167], v[132:135], a[36:39]\n"
    "v_xor_b32 v15, 32, v14\n"
    "v_mfma_f32_16x16x32_bf16 a[164:167], v[132:135], v[180:183], a[164:167]\n"
    "s_add_u32 s61, s54, 0x100000\n"
    "s_add_u32 m0, s16, 0x18000\n"
    "v_mfma_f32_16x16x32_bf16 a[68:71], v[168:171], v[132:135], a[68:71]\n"
    "ds_read_b128 v[96:99], v12\n"
    "v_mfma_f32_16x16x32_bf16 a[196:199], v[132:135], v[184:187], a[196:199]\n"
    "buffer_load_dwordx4 v8, s[28:31], s61 offen lds\n"
    "v_mfma_f32_16x16x32_bf16 a[100:103], v[172:175], v[132:135], a[100:103]\n"
    "ds_read_b128 v[100:103], v12 offset:2048\n"
    "v_mfma_f32_16x16x32_bf16 a[228:231], v[132:135], v[188:191], a[228:231]\n"
    "ds_read_b128 v[104:107], v12 offset:4096\n"
    "v_mfma_f32_16x16x32_bf16 a[8:11], v[160:163], v[136:139], a[8:11]\n"
    "ds_read_b128 v[108:111], v12 offset:6144\n"
    "v_mfma_f32_16x16x32_bf16 a[136:139], v[136:139], v[176:179], a[136:139]\n"
    "buffer_load_dwordx4 v9, s[28:31], s61 offen offset:1024 lds\n"
    "v_mfma_f32_16x16x32_bf16 a[40:43], v[164:167], v[136:139], a[40:43]\n"
    "ds_read_b64_tr_b16 v[64:65], v14\n"
    "v_mfma_f32_16x16x32_bf16 a[168:171], v[136:139], v[180:183], a[168:171]\n"
    "ds_read_b64_tr_b16 v[66:67], v14 offset:1024\n"
    "v_mfma_f32_16x16x32_bf16 a[72:75], v[168:171], v[136:139], a[72:75]\n"
    "ds_read_b64_tr_b16 v[68:69], v15\n"
    "v_mfma_f32_16x16x32_bf16 a[200:203], v[136:139], v[184:187], a[200:203]\n"
    "buffer_load_dwordx4 v10, s[28:31], s61 offen offset:2048 lds\n"
    "v_mfma_f32_16x16x32_bf16 a[104:107], v[172:175], v[136:139], a[104:107]\n"
    "ds_read_b64_tr_b16 v[70:71], v15 offset:1024\n"
    "v_accvgpr_read_b32 v16, a0\n"
    "v_mfma_f32_16x16x32_bf16 a[232:235], v[136:139], v[188:191], a[232:235]\n"
    "ds_read_b64_tr_b16 v[72:73], v14 offset:256\n"
    "v_accvgpr_read_b32 v17, a1\n"
    "v_mfma_f32_16x16x32_bf16 a[12:15], v[160:163], v[140:143], a[12:15]\n"
    "ds_read_b64_tr_b16 v[74:75], v14 offset:1280\n"
    "v_accvgpr_read_b32 v18, a2\n"
    "v_mfma_f32_16x16x32_bf16 a[140:143], v[140:143], v[176:179], a[140:143]\n"
    "buffer_load_dwordx4 v11, s[28:31], s61 offen offset:3072 lds\n"
    "v_mfma_f32_16x16x32_bf16 a[44:47], v[164:167], v[140:143], a[44:47]\n"
    "ds_read_b64_tr_b16 v[76:77], v15 offset:256\n"
    "v_accvgpr_read_b32 v19, a3\n"
    "v_mfma_f32_16x16x32_bf16 a[172:175], v[140:143], v[180:183], a[172:175]\n"
    "s_add_u32 s60, s63, 0x100\n"
    "s_add_u32 m0, s17, 0x10000\n"
    "v_mfma_f32_16x16x32_bf16 a[76:79], v[168:171], v[140:143], a[76:79]\n"
    "ds_read_b64_tr_b16 v[78:79], v15 offset:1280\n"
    "v_accvgpr_read_b32 v20, a32\n"
    "v_mfma_f32_16x16x32_bf16 a[204:207], v[140:143], v[184:187], a[204:207]\n"
    "buffer_load_dwordx4 v4, s[24:27], s60 offen lds\n"
    "v_mfma_f32_16x16x32_bf16 a[108:111], v[172:175], v[140:143], a[108:111]\n"
    "ds_read_b64_tr_b16 v[80:81], v14 offset:512\n"
    "v_accvgpr_read_b32 v21, a33\n"
    "v_mfma_f32_16x16x32_bf16 a[236:239], v[140:143], v[188:191], a[236:239]\n"
    "ds_read_b64_tr_b16 v[82:83], v14 offset:1536\n"
    "v_accvgpr_read_b32 v22, a34\n"
    "v_mfma_f32_16x16x32_bf16 a[16:19], v[160:163], v[144:147], a[16:19]\n"
    "ds_read_b64_tr_b16 v[84:85], v15 offset:512\n"
    "v_accvgpr_read_b32 v23, a35\n"
    "v_mfma_f32_16x16x32_bf16 a[144:147], v[144:147], v[176:179], a[144:147]\n"
    "buffer_load_dwordx4 v5, s[24:27], s60 offen offset:1024 lds\n"
    "v_mfma_f32_16x16x32_bf16 a[48:51], v[164:167], v[144:147], a[48:51]\n"
    "ds_read_b64_tr_b16 v[86:87], v15 offset:1536\n"
    "v_cvt_pk_bf16_f32 v24, v16, v17\n"
    "v_mfma_f32_16x16x32_bf16 a[176:179], v[144:147], v[180:183], a[176:179]\n"
    "ds_read_b64_tr_b16 v[88:89], v14 offset:768\n"
    "v_cvt_pk_bf16_f32 v25, v18, v19\n"
    "v_mfma_f32_16x16x32_bf16 a[80:83], v[168:171], v[144:147], a[80:83]\n"
    "ds_read_b64_tr_b16 v[90:91], v14 offset:1792\n"
    "v_cvt_pk_bf16_f32 v26, v20, v21\n"
    "v_mfma_f32_16x16x32_bf16 a[208:211], v[144:147], v[184:187], a[208:211]\n"
    "buffer_load_dwordx4 v6, s[24:27], s60 offen offset:2048 lds\n"
    "v_mfma_f32_16x16x32_bf16 a[112:115], v[172:175], v[144:147], a[112:115]\n"
    "ds_read_b64_tr_b16 v[92:93], v15 offset:768\n"
    "v_cvt_pk_bf16_f32 v27, v22, v23\n"
    "v_mfma_f32_16x16x32_bf16 a[240:243], v[144:147], v[188:191], a[240:243]\n"
    "ds_read_b64_tr_b16 v[94:95], v15 offset:1792\n"
    "s_add_u32 s22, s48, 0\n"
    "v_mfma_f32_16x16x32_bf16 a[20:23], v[160:163], v[148:151], a[20:23]\n"
    "buffer_store_dwordx4 v[24:27], v2, s[44:47], s22 offen sc1\n"
    "v_mfma_f32_16x16x32_bf16 a[148:151], v[148:151], v[176:179], a[148:151]\n"
    "buffer_load_dwordx4 v7, s[24:27], s60 offen offset:3072 lds\n"
    "v_mfma_f32_16x16x32_bf16 a[52:55], v[164:167], v[148:151], a[52:55]\n"
    "v_accvgpr_read_b32 v16, a64\n"
    "v_accvgpr_read_b32 v17, a65\n"
    "v_mfma_f32_16x16x32_bf16 a[180:183], v[148:151], v[180:183], a[180:183]\n"
    "s_add_u32 s60, s60, 0x8000\n"
    "s_add_u32 m0, m0, 0x1000\n"
    "v_mfma_f32_16x16x32_bf16 a[84:87], v[168:171], v[148:151], a[84:87]\n"
    "v_accvgpr_read_b32 v18, a66\n"
    "v_accvgpr_read_b32 v19, a67\n"
    "v_mfma_f32_16x16x32_bf16 a[212:215], v[148:151], v[184:187], a[212:215]\n"
    "buffer_load_dwordx4 v4, s[24:27], s60 offen lds\n"
    "v_mfma_f32_16x16x32_bf16 a[116:119], v[172:175], v[148:151], a[116:119]\n"
    "v_accvgpr_read_b32 v20, a96\n"
    "v_accvgpr_read_b32 v21, a97\n"
    "v_mfma_f32_16x16x32_bf16 a[244:247], v[148:151], v[188:191], a[244:247]\n"
    "v_accvgpr_read_b32 v22, a98\n"
    "v_accvgpr_read_b32 v23, a99\n"
    "v_mfma_f32_16x16x32_bf16 a[24:27], v[160:163], v[152:155], a[24:27]\n"
    "v_cvt_pk_bf16_f32 v28, v16, v17\n"
    "v_cvt_pk_bf16_f32 v29, v18, v19\n"
    "v_mfma_f32_16x16x32_bf16 a[152:155], v[152:155], v[176:179], a[152:155]\n"
    "buffer_load_dwordx4 v5, s[24:27], s60 offen offset:1024 lds\n"
    "v_mfma_f32_16x16x32_bf16 a[56:59], v[164:167], v[152:155], a[56:59]\n"
    "v_cvt_pk_bf16_f32 v30, v20, v21\n"
    "v_cvt_pk_bf16_f32 v31, v22, v23\n"
    "v_mfma_f32_16x16x32_bf16 a[184:187], v[152:155], v[180:183], a[184:187]\n"
    "s_add_u32 s22, s48, 0\n"
    "v_mfma_f32_16x16x32_bf16 a[88:91], v[168:171], v[152:155], a[88:91]\n"
    "buffer_store_dwordx4 v[28:31], v2, s[44:47], s22 offen offset:1024 sc1\n"
    "v_mfma_f32_16x16x32_bf16 a[216:219], v[152:155], v[184:187], a[216:219]\n"
    "buffer_load_dwordx4 v6, s[24:27], s60 offen offset:2048 lds\n"
    "v_mfma_f32_16x16x32_bf16 a[120:123], v[172:175], v[152:155], a[120:123]\n"
    "v_accvgpr_read_b32 v16, a4\n"
    "v_accvgpr_read_b32 v17, a5\n"
    "v_mfma_f32_16x16x32_bf16 a[248:251], v[152:155], v[188:191], a[248:251]\n"
    "v_accvgpr_read_b32 v18, a6\n"
    "v_accvgpr_read_b32 v19, a7\n"
    "v_mfma_f32_16x16x32_bf16 a[28:31], v[160:163], v[156:159], a[28:31]\n"
    "v_accvgpr_read_b32 v20, a36\n"
    "v_accvgpr_read_b32 v21, a37\n"
    "v_mfma_f32_16x16x32_bf16 a[156:159], v[156:159], v[176:179], a[156:159]\n"
    "buffer_load_dwordx4 v7, s[24:27], s60 offen offset:3072 lds\n"
    "v_mfma_f32_16x16x32_bf16 a[60:63], v[164:167], v[156:159], a[60:63]\n"
    "v_accvgpr_read_b32 v22, a38\n"
    "v_accvgpr_read_b32 v23, a39\n"
    "v_mfma_f32_16x16x32_bf16 a[188:191], v[156:159], v[180:183], a[188:191]\n"
    "v_cvt_pk_bf16_f32 v24, v16, v17\n"
    "v_cvt_pk_bf16_f32 v25, v18, v19\n"
    "v_mfma_f32_16x16x32_bf16 a[92:95], v[168:171], v[156:159], a[92:95]\n"
    "v_cvt_pk_bf16_f32 v26, v20, v21\n"
    "v_cvt_pk_bf16_f32 v27, v22, v23\n"
    "v_mfma_f32_16x16x32_bf16 a[220:223], v[156:159], v[184:187], a[220:223]\n"
    "s_add_u32 s22, s48, 0x1000\n"
    "v_mfma_f32_16x16x32_bf16 a[124:127], v[172:175], v[156:159], a[124:127]\n"
    "buffer_store_dwordx4 v[24:27], v2, s[44:47], s22 offen sc1\n"
    "v_mfma_f32_16x16x32_bf16 a[252:255], v[156:159], v[188:191], a[252:255]\n"
    "v_accvgpr_read_b32 v16, a68\n"
    "v_accvgpr_read_b32 v17, a69\n"
    "v_accvgpr_read_b32 v18, a70\n"
    "v_accvgpr_read_b32 v19, a71\n"
    "v_accvgpr_read_b32 v20, a100\n"
    "v_accvgpr_read_b32 v21, a101\n"
    "v_accvgpr_read_b32 v22, a102\n"
    "v_accvgpr_read_b32 v23, a103\n"
    "v_cvt_pk_bf16_f32 v28, v16, v17\n"
    "v_cvt_pk_bf16_f32 v29, v18, v19\n"
    "v_cvt_pk_bf16_f32 v30, v20, v21\n"
    "v_cvt_pk_bf16_f32 v31, v22, v23\n"
    "s_add_u32 s22, s48, 0x1000\n"
    "buffer_store_dwordx4 v[28:31], v2, s[44:47], s22 offen offset:1024 sc1\n"
    "s_waitcnt lgkmcnt(0)\n"
    "v_mfma_f32_16x16x32_bf16 v[128:131], v[64:67], v[96:99], 0\n"
    "ds_read_b128 v[112:115], v13\n"
    "v_mfma_f32_16x16x32_bf16 v[132:135], v[64:67], v[100:103], 0\n"
    "ds_read_b128 v[116:119], v13 offset:2048\n"
    "v_mfma_f32_16x16x32_bf16 v[136:139], v[64:67], v[104:107], 0\n"
    "ds_read_b128 v[120:123], v13 offset:4096\n"
    "v_mfma_f32_16x16x32_bf16 v[140:143], v[64:67], v[108:111], 0\n"
    "ds_read_b128 v[124:127], v13 offset:6144\n"
    "v_mfma_f32_16x16x32_bf16 v[144:147], v[68:71], v[96:99], 0\n"
    "ds_read_b64_tr_b16 v[32:33], v14 offset:8192\n"
    "v_accvgpr_read_b32 v16, a128\n"
    "v_mfma_f32_16x16x32_bf16 v[148:151], v[68:71], v[100:103], 0\n"
    "ds_read_b64_tr_b16 v[34:35], v14 offset:9216\n"
    "v_accvgpr_read_b32 v17, a129\n"
    "v_mfma_f32_16x16x32_bf16 v[152:155], v[68:71], v[104:107], 0\n"
    "ds_read_b64_tr_b16 v[36:37], v15 offset:8192\n"
    "v_accvgpr_read_b32 v18, a130\n"
    "v_mfma_f32_16x16x32_bf16 v[156:159], v[68:71], v[108:111], 0\n"
    "ds_read_b64_tr_b16 v[38:39], v15 offset:9216\n"
    "v_accvgpr_read_b32 v19, a131\n"
    "v_mfma_f32_16x16x32_bf16 v[160:163], v[72:75], v[96:99], 0\n"
    "ds_read_b64_tr_b16 v[40:41], v14 offset:8448\n"
    "v_accvgpr_read_b32 v20, a132\n"
    "v_mfma_f32_16x16x32_bf16 v[164:167], v[72:75], v[100:103], 0\n"
    "ds_read_b64_tr_b16 v[42:43], v14 offset:9472\n"
    "v_accvgpr_read_b32 v21, a133\n"
    "v_mfma_f32_16x16x32_bf16 v[168:171], v[72:75], v[104:107], 0\n"
    "ds_read_b64_tr_b16 v[44:45], v15 offset:8448\n"
    "v_accvgpr_read_b32 v22, a134\n"
    "v_mfma_f32_16x16x32_bf16 v[172:175], v[72:75], v[108:111], 0\n"
    "ds_read_b64_tr_b16 v[46:47], v15 offset:9472\n"
    "v_accvgpr_read_b32 v23, a135\n"
    "v_mfma_f32_16x16x32_bf16 v[176:179], v[76:79], v[96:99], 0\n"
    "ds_read_b64_tr_b16 v[48:49], v14 offset:8704\n"
    "v_cvt_pk_bf16_f32 v24, v16, v17\n"
    "v_mfma_f32_16x16x32_bf16 v[180:183], v[76:79], v[100:103], 0\n"
    "ds_read_b64_tr_b16 v[50:51], v14 offset:9728\n"
    "v_cvt_pk_bf16_f32 v25, v18, v19\n"
    "v_mfma_f32_16x16x32_bf16 v[184:187], v[76:79], v[104:107], 0\n"
    "ds_read_b64_tr_b16 v[52:53], v15 offset:8704\n"
    "v_cvt_pk_bf16_f32 v26, v20, v21\n"
    "v_mfma_f32_16x16x32_bf16 v[188:191], v[76:79], v[108:111], 0\n"
    "ds_read_b64_tr_b16 v[54:55], v15 offset:9728\n"
    "v_cvt_pk_bf16_f32 v27, v22, v23\n"
    "v_mfma_f32_16x16x32_bf16 v[192:195], v[80:83], v[96:99], 0\n"
    "ds_read_b64_tr_b16 v[56:57], v14 offset:8960\n"
    "s_add_u32 s22, s49, 0\n"
    "v_mfma_f32_16x16x32_bf16 v[196:199], v[80:83], v[100:103], 0\n"
    "ds_read_b64_tr_b16 v[58:59], v14 offset:9984\n"
    "v_mfma_f32_16x16x32_bf16 v[200:203], v[80:83], v[104:107], 0\n"
    "ds_read_b64_tr_b16 v[60:61], v15 offset:8960\n"
    "v_mfma_f32_16x16x32_bf16 v[204:207], v[80:83], v[108:111], 0\n"
    "ds_read_b64_tr_b16 v[62:63], v15 offset:9984\n"
    "v_mfma_f32_16x16x32_bf16 v[208:211], v[84:87], v[96:99], 0\n"
    "v_add_u32 v12, 0x20000, v12\n"
    "v_add_u32 v13, 0x20000, v13\n"
    "v_mfma_f32_16x16x32_bf16 v[212:215], v[84:87], v[100:103], 0\n"
    "v_add_u32 v14, 0x4000, v14\n"
    "v_add_u32 v15, 0x4000, v15\n"
    "v_mfma_f32_16x16x32_bf16 v[216:219], v[84:87], v[104:107], 0\n"
    "buffer_store_dwordx4 v[24:27], v2, s[44:47], s22 offen sc1\n"
    "v_mfma_f32_16x16x32_bf16 v[220:223], v[84:87], v[108:111], 0\n"
    "v_accvgpr_read_b32 v16, a160\n"
    "v_accvgpr_read_b32 v17, a161\n"
    "v_mfma_f32_16x16x32_bf16 v[224:227], v[88:91], v[96:99], 0\n"
    "v_accvgpr_read_b32 v18, a162\n"
    "v_accvgpr_read_b32 v19, a163\n"
    "v_mfma_f32_16x16x32_bf16 v[228:231], v[88:91], v[100:103], 0\n"
    "v_accvgpr_read_b32 v20, a164\n"
    "v_accvgpr_read_b32 v21, a165\n"
    "v_mfma_f32_16x16x32_bf16 v[232:235], v[88:91], v[104:107], 0\n"
    "v_accvgpr_read_b32 v22, a166\n"
    "v_accvgpr_read_b32 v23, a167\n"
    "v_mfma_f32_16x16x32_bf16 v[236:239], v[88:91], v[108:111], 0\n"
    "v_cvt_pk_bf16_f32 v28, v16, v17\n"
    "v_cvt_pk_bf16_f32 v29, v18, v19\n"
    "v_mfma_f32_16x16x32_bf16 v[240:243], v[92:95], v[96:99], 0\n"
    "v_cvt_pk_bf16_f32 v30, v20, v21\n"
    "v_cvt_pk_bf16_f32 v31, v22, v23\n"
    "v_mfma_f32_16x16x32_bf16 v[244:247], v[92:95], v[100:103], 0\n"
    "s_add_u32 s22, s49, 0\n"
    "v_mfma_f32_16x16x32_bf16 v[248:251], v[92:95], v[104:107], 0\n"
    "buffer_store_dwordx4 v[28:31], v2, s[44:47], s22 offen offset:1024 sc1\n"
    "v_mfma_f32_16x16x32_bf16 v[252:255], v[92:95], v[108:111], 0\n"
    "s_waitcnt vmcnt(19) lgkmcnt(0)\n"
    "s_barrier\n"
    "v_mfma_f32_16x16x32_bf16 v[128:131], v[32:35], v[112:115], v[128:131]\n"
    "ds_read_b128 v[96:99], v12\n"
    "v_accvgpr_read_b32 v16, a192\n"
    "v_mfma_f32_16x16x32_bf16 v[132:135], v[32:35], v[116:119], v[132:135]\n"
    "ds_read_b128 v[100:103], v12 offset:2048\n"
    "s_add_u32 s61, s54, 0x180000\n"
    "v_mfma_f32_16x16x32_bf16 v[136:139], v[32:35], v[120:123], v[136:139]\n"
    "ds_read_b128 v[104:107], v12 offset:4096\n"
    "s_add_u32 m0, s16, 0x8000\n"
    "v_mfma_f32_16x16x32_bf16 v[140:143], v[32:35], v[124:127], v[140:143]\n"
    "buffer_load_dwordx4 v8, s[28:31], s61 offen lds\n"
    "v_mfma_f32_16x16x32_bf16 v[144:147], v[36:39], v[112:115], v[144:147]\n"
    "ds_read_b128 v[108:111], v12 offset:6144\n"
    "v_accvgpr_read_b32 v17, a193\n"
    "v_mfma_f32_16x16x32_bf16 v[148:151], v[36:39], v[116:119], v[148:151]\n"
    "ds_read_b64_tr_b16 v[64:65], v14\n"
    "v_accvgpr_read_b32 v18, a194\n"
    "v_mfma_f32_16x16x32_bf16 v[152:155], v[36:39], v[120:123], v[152:155]\n"
    "ds_read_b64_tr_b16 v[66:67], v14 offset:1024\n"
    "v_accvgpr_read_b32 v19, a195\n"
    "v_mfma_f32_16x16x32_bf16 v[156:159], v[36:39], v[124:127], v[156:159]\n"
    "buffer_load_dwordx4 v9, s[28:31], s61 offen offset:1024 lds\n"
    "v_mfma_f32_16x16x32_bf16 v[160:163], v[40:43], v[112:115], v[160:163]\n"
    "ds_read_b64_tr_b16 v[68:69], v15\n"
    "v_accvgpr_read_b32 v20, a196\n"
    "v_mfma_f32_16x16x32_bf16 v[164:167], v[40:43], v[116:119], v[164:167]\n"
    "ds_read_b64_tr_b16 v[70:71], v15 offset:1024\n"
    "v_accvgpr_read_b32 v21, a197\n"
    "v_mfma_f32_16x16x32_bf16 v[168:171], v[40:43], v[120:123], v[168:171]\n"
    "ds_read_b64_tr_b16 v[72:73], v14 offset:256\n"
    "v_accvgpr_read_b32 v22, a198\n"
    "v_mfma_f32_16x16x32_bf16 v[172:175], v[40:43], v[124:127], v[172:175]\n"
    "buffer_load_dwordx4 v10, s[28:31], s61 offen offset:2048 lds\n"
    "v_mfma_f32_16x16x32_bf16 v[176:179], v[44:47], v[112:115], v[176:179]\n"
    "ds_read_b64_tr_b16 v[74:75], v14 offset:1280\n"
    "v_accvgpr_read_b32 v23, a199\n"
    "v_mfma_f32_16x16x32_bf16 v[180:183], v[44:47], v[116:119], v[180:183]\n"
    "ds_read_b64_tr_b16 v[76:77], v15 offset:256\n"
    "v_cvt_pk_bf16_f32 v24, v16, v17\n"
    "v_mfma_f32_16x16x32_bf16 v[184:187], v[44:47], v[120:123], v[184:187]\n"
    "ds_read_b64_tr_b16 v[78:79], v15 offset:1280\n"
    "v_cvt_pk_bf16_f32 v25, v18, v19\n"
    "v_mfma_f32_16x16x32_bf16 v[188:191], v[44:47], v[124:127], v[188:191]\n"
    "ds_read_b64_tr_b16 v[80:81], v14 offset:512\n"
    "v_cvt_pk_bf16_f32 v26, v20, v21\n"
    "v_mfma_f32_16x16x32_bf16 v[192:195], v[48:51], v[112:115], v[192:195]\n"
    "buffer_load_dwordx4 v11, s[28:31], s61 offen offset:3072 lds\n"
    "v_mfma_f32_16x16x32_bf16 v[196:199], v[48:51], v[116:119], v[196:199]\n"
    "ds_read_b64_tr_b16 v[82:83], v14 offset:1536\n"
    "v_cvt_pk_bf16_f32 v27, v22, v23\n"
    "v_mfma_f32_16x16x32_bf16 v[200:203], v[48:51], v[120:123], v[200:203]\n"
    "ds_read_b64_tr_b16 v[84:85], v15 offset:512\n"
    "s_add_u32 s60, s63, 0x180\n"
    "v_mfma_f32_16x16x32_bf16 v[204:207], v[48:51], v[124:127], v[204:207]\n"
    "ds_read_b64_tr_b16 v[86:87], v15 offset:1536\n"
    "s_add_u32 m0, s17, 0\n"
    "v_mfma_f32_16x16x32_bf16 v[208:211], v[52:55], v[112:115], v[208:211]\n"
    "buffer_load_dwordx4 v4, s[24:27], s60 offen lds\n"
    "v_mfma_f32_16x16x32_bf16 v[212:215], v[52:55], v[116:119], v[212:215]\n"
    "ds_read_b64_tr_b16 v[88:89], v14 offset:768\n"
    "s_add_u32 s22, s49, 0\n"
    "v_mfma_f32_16x16x32_bf16 v[216:219], v[52:55], v[120:123], v[216:219]\n"
    "ds_read_b64_tr_b16 v[90:91], v14 offset:1792\n"
    "v_mfma_f32_16x16x32_bf16 v[220:223], v[52:55], v[124:127], v[220:223]\n"
    "ds_read_b64_tr_b16 v[92:93], v15 offset:768\n"
    "v_mfma_f32_16x16x32_bf16 v[224:227], v[56:59], v[112:115], v[224:227]\n"
    "buffer_load_dwordx4 v5, s[24:27], s60 offen offset:1024 lds\n"
    "v_mfma_f32_16x16x32_bf16 v[228:231], v[56:59], v[116:119], v[228:231]\n"
    "ds_read_b64_tr_b16 v[94:95], v15 offset:1792\n"
    "v_mfma_f32_16x16x32_bf16 v[232:235], v[56:59], v[120:123], v[232:235]\n"
    "buffer_store_dwordx4 v[24:27], v2, s[44:47], s22 offen offset:2048 sc1\n"
    "v_mfma_f32_16x16x32_bf16 v[236:239], v[56:59], v[124:127], v[236:239]\n"
    "v_accvgpr_read_b32 v16, a224\n"
    "v_accvgpr_read_b32 v17, a225\n"
    "v_mfma_f32_16x16x32_bf16 v[240:243], v[60:63], v[112:115], v[240:243]\n"
    "v_accvgpr_read_b32 v18, a226\n"
    "v_accvgpr_read_b32 v19, a227\n"
    "v_mfma_f32_16x16x32_bf16 v[244:247], v[60:63], v[116:119], v[244:247]\n"
    "v_accvgpr_read_b32 v20, a228\n"
    "v_accvgpr_read_b32 v21, a229\n"
    "v_mfma_f32_16x16x32_bf16 v[248:251], v[60:63], v[120:123], v[248:251]\n"
    "v_accvgpr_read_b32 v22, a230\n"
    "v_accvgpr_read_b32 v23, a231\n"
    "v_mfma_f32_16x16x32_bf16 v[252:255], v[60:63], v[124:127], v[252:255]\n"
    "v_cvt_pk_bf16_f32 v28, v16, v17\n"
    "v_cvt_pk_bf16_f32 v29, v18, v19\n"
    "v_cvt_pk_bf16_f32 v30, v20, v21\n"
    "v_cvt_pk_bf16_f32 v31, v22, v23\n"
    "s_add_u32 s22, s49, 0\n"
    "buffer_store_dwordx4 v[28:31], v2, s[44:47], s22 offen offset:3072 sc1\n"
    "s_waitcnt lgkmcnt(0)\n"
    "v_mfma_f32_16x16x32_bf16 v[128:131], v[64:67], v[96:99], v[128:131]\n"
    "ds_read_b128 v[112:115], v13\n"
    "v_accvgpr_read_b32 v16, a8\n"
    "v_mfma_f32_16x16x32_bf16 v[132:135], v[64:67], v[100:103], v[132:135]\n"
    "ds_read_b128 v[116:119], v13 offset:2048\n"
    "v_accvgpr_read_b32 v17, a9\n"
    "v_mfma_f32_16x16x32_bf16 v[136:139], v[64:67], v[104:107], v[136:139]\n"
    "ds_read_b128 v[120:123], v13 offset:4096\n"
    "v_accvgpr_read_b32 v18, a10\n"
    "v_mfma_f32_16x16x32_bf16 v[140:143], v[64:67], v[108:111], v[140:143]\n"
    "buffer_load_dwordx4 v6, s[24:27], s60 offen offset:2048 lds\n"
    "v_mfma_f32_16x16x32_bf16 v[144:147], v[68:71], v[96:99], v[144:147]\n"
    "ds_read_b128 v[124:127], v13 offset:6144\n"
    "v_accvgpr_read_b32 v19, a11\n"
    "v_mfma_f32_16x16x32_bf16 v[148:151], v[68:71], v[100:103], v[148:151]\n"
    "ds_read_b64_tr_b16 v[32:33], v14 offset:8192\n"
    "v_accvgpr_read_b32 v20, a40\n"
    "v_mfma_f32_16x16x32_bf16 v[152:155], v[68:71], v[104:107], v[152:155]\n"
    "ds_read_b64_tr_b16 v[34:35], v14 offset:9216\n"
    "v_accvgpr_read_b32 v21, a41\n"
    "v_mfma_f32_16x16x32_bf16 v[156:159], v[68:71], v[108:111], v[156:159]\n"
    "buffer_load_dwordx4 v7, s[24:27], s60 offen offset:3072 lds\n"
    "v_mfma_f32_16x16x32_bf16 v[160:163], v[72:75], v[96:99], v[160:163]\n"
    "ds_read_b64_tr_b16 v[36:37], v15 offset:8192\n"
    "v_accvgpr_read_b32 v22, a42\n"
    "v_mfma_f32_16x16x32_bf16 v[164:167], v[72:75], v[100:103], v[164:167]\n"
    "ds_read_b64_tr_b16 v[38:39], v15 offset:9216\n"
    "s_add_u32 s60, s60, 0x8000\n"
    "v_mfma_f32_16x16x32_bf16 v[168:171], v[72:75], v[104:107], v[168:171]\n"
    "ds_read_b64_tr_b16 v[40:41], v14 offset:8448\n"
    "s_add_u32 m0, m0, 0x1000\n"
    "v_mfma_f32_16x16x32_bf16 v[172:175], v[72:75], v[108:111], v[172:175]\n"
    "buffer_load_dwordx4 v4, s[24:27], s60 offen lds\n"
    "v_mfma_f32_16x16x32_bf16 v[176:179], v[76:79], v[96:99], v[176:179]\n"
    "ds_read_b64_tr_b16 v[42:43], v14 offset:9472\n"
    "v_accvgpr_read_b32 v23, a43\n"
    "v_mfma_f32_16x16x32_bf16 v[180:183], v[76:79], v[100:103], v[180:183]\n"
    "ds_read_b64_tr_b16 v[44:45], v15 offset:8448\n"
    "v_cvt_pk_bf16_f32 v24, v16, v17\n"
    "v_mfma_f32_16x16x32_bf16 v[184:187], v[76:79], v[104:107], v[184:187]\n"
    "ds_read_b64_tr_b16 v[46:47], v15 offset:9472\n"
    "v_cvt_pk_bf16_f32 v25, v18, v19\n"
    "v_mfma_f32_16x16x32_bf16 v[188:191], v[76:79], v[108:111], v[188:191]\n"
    "ds_read_b64_tr_b16 v[48:49], v14 offset:8704\n"
    "v_cvt_pk_bf16_f32 v26, v20, v21\n"
    "v_mfma_f32_16x16x32_bf16 v[192:195], v[80:83], v[96:99], v[192:195]\n"
    "buffer_load_dwordx4 v5, s[24:27], s60 offen offset:1024 lds\n"
    "v_mfma_f32_16x16x32_bf16 v[196:199], v[80:83], v[100:103], v[196:199]\n"
    "ds_read_b64_tr_b16 v[50:51], v14 offset:9728\n"
    "v_cvt_pk_bf16_f32 v27, v22, v23\n"
    "v_mfma_f32_16x16x32_bf16 v[200:203], v[80:83], v[104:107], v[200:203]\n"
    "ds_read_b64_tr_b16 v[52:53], v15 offset:8704\n"
    "s_add_u32 s22, s48, 0x2000\n"
    "v_mfma_f32_16x16x32_bf16 v[204:207], v[80:83], v[108:111], v[204:207]\n"
    "ds_read_b64_tr_b16 v[54:55], v15 offset:9728\n"
    "v_mfma_f32_16x16x32_bf16 v[208:211], v[84:87], v[96:99], v[208:211]\n"
    "buffer_load_dwordx4 v6, s[24:27], s60 offen offset:2048 lds\n"
    "v_mfma_f32_16x16x32_bf16 v[212:215], v[84:87], v[100:103], v[212:215]\n"
    "ds_read_b64_tr_b16 v[56:57], v14 offset:8960\n"
    "v_mfma_f32_16x16x32_bf16 v[216:219], v[84:87], v[104:107], v[216:219]\n"
    "ds_read_b64_tr_b16 v[58:59], v14 offset:9984\n"
    "v_mfma_f32_16x16x32_bf16 v[220:223], v[84:87], v[108:111], v[220:223]\n"
    "ds_read_b64_tr_b16 v[60:61], v15 offset:8960\n"
    "v_mfma_f32_16x16x32_bf16 v[224:227], v[88:91], v[96:99], v[224:227]\n"
    "buffer_load_dwordx4 v7, s[24:27], s60 offen offset:3072 lds\n"
    "v_mfma_f32_16x16x32_bf16 v[228:231], v[88:91], v[100:103], v[228:231]\n"
    "ds_read_b64_tr_b16 v[62:63], v15 offset:9984\n"
    "v_mfma_f32_16x16x32_bf16 v[232:235], v[88:91], v[104:107], v[232:235]\n"
    "v_subrev_u32 v12, 0x10000, v12\n"
    "v_subrev_u32 v13, 0x10000, v13\n"
    "v_mfma_f32_16x16x32_bf16 v[236:239], v[88:91], v[108:111], v[236:239]\n"
    "v_add_u32 v14, 0xc000, v14\n"
    "v_add_u32 v15, 0xc000, v15\n"
    "v_mfma_f32_16x16x32_bf16 v[240:243], v[92:95], v[96:99], v[240:243]\n"
    "buffer_store_dwordx4 v[24:27], v2, s[44:47], s22 offen sc1\n"
    "v_mfma_f32_16x16x32_bf16 v[244:247], v[92:95], v[100:103], v[244:247]\n"
    "v_accvgpr_read_b32 v16, a72\n"
    "v_accvgpr_read_b32 v17, a73\n"
    "v_mfma_f32_16x16x32_bf16 v[248:251], v[92:95], v[104:107], v[248:251]\n"
    "v_accvgpr_read_b32 v18, a74\n"
    "v_accvgpr_read_b32 v19, a75\n"
    "v_mfma_f32_16x16x32_bf16 v[252:255], v[92:95], v[108:111], v[252:255]\n"
    "v_accvgpr_read_b32 v20, a104\n"
    "v_accvgpr_read_b32 v21, a105\n"
    "v_accvgpr_read_b32 v22, a106\n"
    "v_accvgpr_read_b32 v23, a107\n"
    "v_cvt_pk_bf16_f32 v28, v16, v17\n"
    "v_cvt_pk_bf16_f32 v29, v18, v19\n"
    "v_cvt_pk_bf16_f32 v30, v20, v21\n"
    "v_cvt_pk_bf16_f32 v31, v22, v23\n"
    "s_add_u32 s22, s48, 0x2000\n"
    "buffer_store_dwordx4 v[28:31], v2, s[44:47], s22 offen offset:1024 sc1\n"
    "s_waitcnt vmcnt(20) lgkmcnt(0)\n"
    "s_barrier\n"
    "v_mfma_f32_16x16x32_bf16 v[128:131], v[32:35], v[112:115], v[128:131]\n"
    "ds_read_b128 v[96:99], v12\n"
    "v_accvgpr_read_b32 v16, a12\n"
    "v_mfma_f32_16x16x32_bf16 v[132:135], v[32:35], v[116:119], v[132:135]\n"
    "ds_read_b128 v[100:103], v12 offset:2048\n"
    "s_add_u32 s61, s54, 0x200000\n"
    "v_mfma_f32_16x16x32_bf16 v[136:139], v[32:35], v[120:123], v[136:139]\n"
    "ds_read_b128 v[104:107], v12 offset:4096\n"
    "s_add_u32 m0, s16, 0xc000\n"
    "v_mfma_f32_16x16x32_bf16 v[140:143], v[32:35], v[124:127], v[140:143]\n"
    "buffer_load_dwordx4 v8, s[28:31], s61 offen lds\n"
    "v_mfma_f32_16x16x32_bf16 v[144:147], v[36:39], v[112:115], v[144:147]\n"
    "ds_read_b128 v[108:111], v12 offset:6144\n"
    "v_accvgpr_read_b32 v17, a13\n"
    "v_mfma_f32_16x16x32_bf16 v[148:151], v[36:39], v[116:119], v[148:151]\n"
    "ds_read_b64_tr_b16 v[64:65], v14\n"
    "v_accvgpr_read_b32 v18, a14\n"
    "v_mfma_f32_16x16x32_bf16 v[152:155], v[36:39], v[120:123], v[152:155]\n"
    "ds_read_b64_tr_b16 v[66:67], v14 offset:1024\n"
    "v_accvgpr_read_b32 v19, a15\n"
    "v_mfma_f32_16x16x32_bf16 v[156:159], v[36:39], v[124:127], v[156:159]\n"
    "buffer_load_dwordx4 v9, s[28:31], s61 offen offset:1024 lds\n"
    "v_mfma_f32_16x16x32_bf16 v[160:163], v[40:43], v[112:115], v[160:163]\n"
    "ds_read_b64_tr_b16 v[68:69], v15\n"
    "v_accvgpr_read_b32 v20, a44\n"
    "v_mfma_f32_16x16x32_bf16 v[164:167], v[40:43], v[116:119], v[164:167]\n"
    "ds_read_b64_tr_b16 v[70:71], v15 offset:1024\n"
    "v_accvgpr_read_b32 v21, a45\n"
    "v_mfma_f32_16x16x32_bf16 v[168:171], v[40:43], v[120:123], v[168:171]\n"
    "ds_read_b64_tr_b16 v[72:73], v14 offset:256\n"
    "v_accvgpr_read_b32 v22, a46\n"
    "v_mfma_f32_16x16x32_bf16 v[172:175], v[40:43], v[124:127], v[172:175]\n"
    "buffer_load_dwordx4 v10, s[28:31], s61 offen offset:2048 lds\n"
    "v_mfma_f32_16x16x32_bf16 v[176:179], v[44:47], v[112:115], v[176:179]\n"
    "ds_read_b64_tr_b16 v[74:75], v14 offset:1280\n"
    "v_accvgpr_read_b32 v23, a47\n"
    "v_mfma_f32_16x16x32_bf16 v[180:183], v[44:47], v[116:119], v[180:183]\n"
    "ds_read_b64_tr_b16 v[76:77], v15 offset:256\n"
    "v_cvt_pk_bf16_f32 v24, v16, v17\n"
    "v_mfma_f32_16x16x32_bf16 v[184:187], v[44:47], v[120:123], v[184:187]\n"
    "ds_read_b64_tr_b16 v[78:79], v15 offset:1280\n"
    "v_cvt_pk_bf16_f32 v25, v18, v19\n"
    "v_mfma_f32_16x16x32_bf16 v[188:191], v[44:47], v[124:127], v[188:191]\n"
    "ds_read_b64_tr_b16 v[80:81], v14 offset:512\n"
    "v_cvt_pk_bf16_f32 v26, v20, v21\n"
    "v_mfma_f32_16x16x32_bf16 v[192:195], v[48:51], v[112:115], v[192:195]\n"
    "buffer_load_dwordx4 v11, s[28:31], s61 offen offset:3072 lds\n"
    "v_mfma_f32_16x16x32_bf16 v[196:199], v[48:51], v[116:119], v[196:199]\n"
    "ds_read_b64_tr_b16 v[82:83], v14 offset:1536\n"
    "v_cvt_pk_bf16_f32 v27, v22, v23\n"
    "v_mfma_f32_16x16x32_bf16 v[200:203], v[48:51], v[120:123], v[200:203]\n"
    "ds_read_b64_tr_b16 v[84:85], v15 offset:512\n"
    "s_add_u32 s60, s63, 0x200\n"
    "v_mfma_f32_16x16x32_bf16 v[204:207], v[48:51], v[124:127], v[204:207]\n"
    "ds_read_b64_tr_b16 v[86:87], v15 offset:1536\n"
    "s_add_u32 m0, s17, 0x20000\n"
    "v_mfma_f32_16x16x32_bf16 v[208:211], v[52:55], v[112:115], v[208:211]\n"
    "buffer_load_dwordx4 v4, s[24:27], s60 offen lds\n"
    "v_mfma_f32_16x16x32_bf16 v[212:215], v[52:55], v[116:119], v[212:215]\n"
    "ds_read_b64_tr_b16 v[88:89], v14 offset:768\n"
    "s_add_u32 s22, s48, 0x3000\n"
    "v_mfma_f32_16x16x32_bf16 v[216:219], v[52:55], v[120:123], v[216:219]\n"
    "ds_read_b64_tr_b16 v[90:91], v14 offset:1792\n"
    "v_mfma_f32_16x16x32_bf16 v[220:223], v[52:55], v[124:127], v[220:223]\n"
    "ds_read_b64_tr_b16 v[92:93], v15 offset:768\n"
    "v_mfma_f32_16x16x32_bf16 v[224:227], v[56:59], v[112:115], v[224:227]\n"
    "buffer_load_dwordx4 v5, s[24:27], s60 offen offset:1024 lds\n"
    "v_mfma_f32_16x16x32_bf16 v[228:231], v[56:59], v[116:119], v[228:231]\n"
    "ds_read_b64_tr_b16 v[94:95], v15 offset:1792\n"
    "v_mfma_f32_16x16x32_bf16 v[232:235], v[56:59], v[120:123], v[232:235]\n"
    "buffer_store_dwordx4 v[24:27], v2, s[44:47], s22 offen sc1\n"
    "v_mfma_f32_16x16x32_bf16 v[236:239], v[56:59], v[124:127], v[236:239]\n"
    "v_accvgpr_read_b32 v16, a76\n"
    "v_accvgpr_read_b32 v17, a77\n"
    "v_mfma_f32_16x16x32_bf16 v[240:243], v[60:63], v[112:115], v[240:243]\n"
    "v_accvgpr_read_b32 v18, a78\n"
    "v_accvgpr_read_b32 v19, a79\n"
    "v_mfma_f32_16x16x32_bf16 v[244:247], v[60:63], v[116:119], v[244:247]\n"
    "v_accvgpr_read_b32 v20, a108\n"
    "v_accvgpr_read_b32 v21, a109\n"
    "v_mfma_f32_16x16x32_bf16 v[248:251], v[60:63], v[120:123], v[248:251]\n"
    "v_accvgpr_read_b32 v22, a110\n"
    "v_accvgpr_read_b32 v23, a111\n"
    "v_mfma_f32_16x16x32_bf16 v[252:255], v[60:63], v[124:127], v[252:255]\n"
    "v_cvt_pk_bf16_f32 v28, v16, v17\n"
    "v_cvt_pk_bf16_f32 v29, v18, v19\n"
    "v_cvt_pk_bf16_f32 v30, v20, v21\n"
    "v_cvt_pk_bf16_f32 v31, v22, v23\n"
    "s_add_u32 s22, s48, 0x3000\n"
    "buffer_store_dwordx4 v[28:31], v2, s[44:47], s22 offen offset:1024 sc1\n"
    "s_waitcnt lgkmcnt(0)\n"
    "v_mfma_f32_16x16x32_bf16 v[128:131], v[64:67], v[96:99], v[128:131]\n"
    "ds_read_b128 v[112:115], v13\n"
    "v_accvgpr_read_b32 v16, a136\n"
    "v_mfma_f32_16x16x32_bf16 v[132:135], v[64:67], v[100:103], v[132:135]\n"
    "ds_read_b128 v[116:119], v13 offset:2048\n"
    "v_accvgpr_read_b32 v17, a137\n"
    "v_mfma_f32_16x16x32_bf16 v[136:139], v[64:67], v[104:107], v[136:139]\n"
    "ds_read_b128 v[120:123], v13 offset:4096\n"
    "v_accvgpr_read_b32 v18, a138\n"
    "v_mfma_f32_16x16x32_bf16 v[140:143], v[64:67], v[108:111], v[140:143]\n"
    "buffer_load_dwordx4 v6, s[24:27], s60 offen offset:2048 lds\n"
    "v_mfma_f32_16x16x32_bf16 v[144:147], v[68:71], v[96:99], v[144:147]\n"
    "ds_read_b128 v[124:127], v13 offset:6144\n"
    "v_accvgpr_read_b32 v19, a139\n"
    "v_mfma_f32_16x16x32_bf16 v[148:151], v[68:71], v[100:103], v[148:151]\n"
    "ds_read_b64_tr_b16 v[32:33], v14 offset:8192\n"
    "v_accvgpr_read_b32 v20, a140\n"
    "v_mfma_f32_16x16x32_bf16 v[152:155], v[68:71], v[104:107], v[152:155]\n"
    "ds_read_b64_tr_b16 v[34:35], v14 offset:9216\n"
    "v_accvgpr_read_b32 v21, a141\n"
    "v_mfma_f32_16x16x32_bf16 v[156:159], v[68:71], v[108:111], v[156:159]\n"
    "buffer_load_dwordx4 v7, s[24:27], s60 offen offset:3072 lds\n"
    "v_mfma_f32_16x16x32_bf16 v[160:163], v[72:75], v[96:99], v[160:163]\n"
    "ds_read_b64_tr_b16 v[36:37], v15 offset:8192\n"
    "v_accvgpr_read_b32 v22, a142\n"
    "v_mfma_f32_16x16x32_bf16 v[164:167], v[72:75], v[100:103], v[164:167]\n"
    "ds_read_b64_tr_b16 v[38:39], v15 offset:9216\n"
    "s_add_u32 s60, s60, 0x8000\n"
    "v_mfma_f32_16x16x32_bf16 v[168:171], v[72:75], v[104:107], v[168:171]\n"
    "ds_read_b64_tr_b16 v[40:41], v14 offset:8448\n"
    "s_add_u32 m0, m0, 0x1000\n"
    "v_mfma_f32_16x16x32_bf16 v[172:175], v[72:75], v[108:111], v[172:175]\n"
    "buffer_load_dwordx4 v4, s[24:27], s60 offen lds\n"
    "v_mfma_f32_16x16x32_bf16 v[176:179], v[76:79], v[96:99], v[176:179]\n"
    "ds_read_b64_tr_b16 v[42:43], v14 offset:9472\n"
    "v_accvgpr_read_b32 v23, a143\n"
    "v_mfma_f32_16x16x32_bf16 v[180:183], v[76:79], v[100:103], v[180:183]\n"
    "ds_read_b64_tr_b16 v[44:45], v15 offset:8448\n"
    "v_cvt_pk_bf16_f32 v24, v16, v17\n"
    "v_mfma_f32_16x16x32_bf16 v[184:187], v[76:79], v[104:107], v[184:187]\n"
    "ds_read_b64_tr_b16 v[46:47], v15 offset:9472\n"
    "v_cvt_pk_bf16_f32 v25, v18, v19\n"
    "v_mfma_f32_16x16x32_bf16 v[188:191], v[76:79], v[108:111], v[188:191]\n"
    "ds_read_b64_tr_b16 v[48:49], v14 offset:8704\n"
    "v_cvt_pk_bf16_f32 v26, v20, v21\n"
    "v_mfma_f32_16x16x32_bf16 v[192:195], v[80:83], v[96:99], v[192:195]\n"
    "buffer_load_dwordx4 v5, s[24:27], s60 offen offset:1024 lds\n"
    "v_mfma_f32_16x16x32_bf16 v[196:199], v[80:83], v[100:103], v[196:199]\n"
    "ds_read_b64_tr_b16 v[50:51], v14 offset:9728\n"
    "v_cvt_pk_bf16_f32 v27, v22, v23\n"
    "v_mfma_f32_16x16x32_bf16 v[200:203], v[80:83], v[104:107], v[200:203]\n"
    "ds_read_b64_tr_b16 v[52:53], v15 offset:8704\n"
    "s_add_u32 s22, s49, 0x2000\n"
    "v_mfma_f32_16x16x32_bf16 v[204:207], v[80:83], v[108:111], v[204:207]\n"
    "ds_read_b64_tr_b16 v[54:55], v15 offset:9728\n"
    "v_mfma_f32_16x16x32_bf16 v[208:211], v[84:87], v[96:99], v[208:211]\n"
    "buffer_load_dwordx4 v6, s[24:27], s60 offen offset:2048 lds\n"
    "v_mfma_f32_16x16x32_bf16 v[212:215], v[84:87], v[100:103], v[212:215]\n"
    "ds_read_b64_tr_b16 v[56:57], v14 offset:8960\n"
    "v_mfma_f32_16x16x32_bf16 v[216:219], v[84:87], v[104:107], v[216:219]\n"
    "ds_read_b64_tr_b16 v[58:59], v14 offset:9984\n"
    "v_mfma_f32_16x16x32_bf16 v[220:223], v[84:87], v[108:111], v[220:223]\n"
    "ds_read_b64_tr_b16 v[60:61], v15 offset:8960\n"
    "v_mfma_f32_16x16x32_bf16 v[224:227], v[88:91], v[96:99], v[224:227]\n"
    "buffer_load_dwordx4 v7, s[24:27], s60 offen offset:3072 lds\n"
    "v_mfma_f32_16x16x32_bf16 v[228:231], v[88:91], v[100:103], v[228:231]\n"
    "ds_read_b64_tr_b16 v[62:63], v15 offset:9984\n"
    "v_mfma_f32_16x16x32_bf16 v[232:235], v[88:91], v[104:107], v[232:235]\n"
    "v_subrev_u32 v12, 0x10000, v12\n"
    "v_subrev_u32 v13, 0x10000, v13\n"
    "v_mfma_f32_16x16x32_bf16 v[236:239], v[88:91], v[108:111], v[236:239]\n"
    "v_subrev_u32 v14, 0x10000, v14\n"
    "v_subrev_u32 v15, 0x10000, v15\n"
    "v_mfma_f32_16x16x32_bf16 v[240:243], v[92:95], v[96:99], v[240:243]\n"
    "buffer_store_dwordx4 v[24:27], v2, s[44:47], s22 offen sc1\n"
    "v_mfma_f32_16x16x32_bf16 v[244:247], v[92:95], v[100:103], v[244:247]\n"
    "v_accvgpr_read_b32 v16, a168\n"
    "v_accvgpr_read_b32 v17, a169\n"
    "v_mfma_f32_16x16x32_bf16 v[248:251], v[92:95], v[104:107], v[248:251]\n"
    "v_accvgpr_read_b32 v18, a170\n"
    "v_accvgpr_read_b32 v19, a171\n"
    "v_mfma_f32_16x16x32_bf16 v[252:255], v[92:95], v[108:111], v[252:255]\n"
    "v_accvgpr_read_b32 v20, a172\n"
    "v_accvgpr_read_b32 v21, a173\n"
    "v_accvgpr_read_b32 v22, a174\n"
    "v_accvgpr_read_b32 v23, a175\n"
    "v_cvt_pk_bf16_f32 v28, v16, v17\n"
    "v_cvt_pk_bf16_f32 v29, v18, v19\n"
    "v_cvt_pk_bf16_f32 v30, v20, v21\n"
    "v_cvt_pk_bf16_f32 v31, v22, v23\n"
    "s_add_u32 s22, s49, 0x2000\n"
    "buffer_store_dwordx4 v[28:31], v2, s[44:47], s22 offen offset:1024 sc1\n"
    "s_waitcnt vmcnt(18) lgkmcnt(0)\n"
    "s_barrier\n"
    "v_mfma_f32_16x16x32_bf16 v[128:131], v[32:35], v[112:115], v[128:131]\n"
    "ds_read_b128 v[96:99], v12\n"
    "v_accvgpr_read_b32 v16, a200\n"
    "v_mfma_f32_16x16x32_bf16 v[132:135], v[32:35], v[116:119], v[132:135]\n"
    "ds_read_b128 v[100:103], v12 offset:2048\n"
    "s_add_u32 s61, s54, 0x280000\n"
    "v_mfma_f32_16x16x32_bf16 v[136:139], v[32:35], v[120:123], v[136:139]\n"
    "ds_read_b128 v[104:107], v12 offset:4096\n"
    "s_add_u32 m0, s16, 0x18000\n"
    "v_mfma_f32_16x16x32_bf16 v[140:143], v[32:35], v[124:127], v[140:143]\n"
    "buffer_load_dwordx4 v8, s[28:31], s61 offen lds\n"
    "v_mfma_f32_16x16x32_bf16 v[144:147], v[36:39], v[112:115], v[144:147]\n"
    "ds_read_b128 v[108:111], v12 offset:6144\n"
    "v_accvgpr_read_b32 v17, a201\n"
    "v_mfma_f32_16x16x32_bf16 v[148:151], v[36:39], v[116:119], v[148:151]\n"
    "ds_read_b64_tr_b16 v[64:65], v14\n"
    "v_accvgpr_read_b32 v18, a202\n"
    "v_mfma_f32_16x16x32_bf16 v[152:155], v[36:39], v[120:123], v[152:155]\n"
    "ds_read_b64_tr_b16 v[66:67], v14 offset:1024\n"
    "v_accvgpr_read_b32 v19, a203\n"
    "v_mfma_f32_16x16x32_bf16 v[156:159], v[36:39], v[124:127], v[156:159]\n"
    "buffer_load_dwordx4 v9, s[28:31], s61 offen offset:1024 lds\n"
    "v_mfma_f32_16x16x32_bf16 v[160:163], v[40:43], v[112:115], v[160:163]\n"
    "ds_read_b64_tr_b16 v[68:69], v15\n"
    "v_accvgpr_read_b32 v20, a204\n"
    "v_mfma_f32_16x16x32_bf16 v[164:167], v[40:43], v[116:119], v[164:167]\n"
    "ds_read_b64_tr_b16 v[70:71], v15 offset:1024\n"
    "v_accvgpr_read_b32 v21, a205\n"
    "v_mfma_f32_16x16x32_bf16 v[168:171], v[40:43], v[120:123], v[168:171]\n"
    "ds_read_b64_tr_b16 v[72:73], v14 offset:256\n"
    "v_accvgpr_read_b32 v22, a206\n"
    "v_mfma_f32_16x16x32_bf16 v[172:175], v[40:43], v[124:127], v[172:175]\n"
    "buffer_load_dwordx4 v10, s[28:31], s61 offen offset:2048 lds\n"
    "v_mfma_f32_16x16x32_bf16 v[176:179], v[44:47], v[112:115], v[176:179]\n"
    "ds_read_b64_tr_b16 v[74:75], v14 offset:1280\n"
    "v_accvgpr_read_b32 v23, a207\n"
    "v_mfma_f32_16x16x32_bf16 v[180:183], v[44:47], v[116:119], v[180:183]\n"
    "ds_read_b64_tr_b16 v[76:77], v15 offset:256\n"
    "v_cvt_pk_bf16_f32 v24, v16, v17\n"
    "v_mfma_f32_16x16x32_bf16 v[184:187], v[44:47], v[120:123], v[184:187]\n"
    "ds_read_b64_tr_b16 v[78:79], v15 offset:1280\n"
    "v_cvt_pk_bf16_f32 v25, v18, v19\n"
    "v_mfma_f32_16x16x32_bf16 v[188:191], v[44:47], v[124:127], v[188:191]\n"
    "ds_read_b64_tr_b16 v[80:81], v14 offset:512\n"
    "v_cvt_pk_bf16_f32 v26, v20, v21\n"
    "v_mfma_f32_16x16x32_bf16 v[192:195], v[48:51], v[112:115], v[192:195]\n"
    "buffer_load_dwordx4 v11, s[28:31], s61 offen offset:3072 lds\n"
    "v_mfma_f32_16x16x32_bf16 v[196:199], v[48:51], v[116:119], v[196:199]\n"
    "ds_read_b64_tr_b16 v[82:83], v14 offset:1536\n"
    "v_cvt_pk_bf16_f32 v27, v22, v23\n"
    "v_mfma_f32_16x16x32_bf16 v[200:203], v[48:51], v[120:123], v[200:203]\n"
    "ds_read_b64_tr_b16 v[84:85], v15 offset:512\n"
    "s_add_u32 s60, s63, 0x280\n"
    "v_mfma_f32_16x16x32_bf16 v[204:207], v[48:51], v[124:127], v[204:207]\n"
    "ds_read_b64_tr_b16 v[86:87], v15 offset:1536\n"
    "s_add_u32 m0, s17, 0x10000\n"
    "v_mfma_f32_16x16x32_bf16 v[208:211], v[52:55], v[112:115], v[208:211]\n"
    "buffer_load_dwordx4 v4, s[24:27], s60 offen lds\n"
    "v_mfma_f32_16x16x32_bf16 v[212:215], v[52:55], v[116:119], v[212:215]\n"
    "ds_read_b64_tr_b16 v[88:89], v14 offset:768\n"
    "s_add_u32 s22, s49, 0x2000\n"
    "v_mfma_f32_16x16x32_bf16 v[216:219], v[52:55], v[120:123], v[216:219]\n"
    "ds_read_b64_tr_b16 v[90:91], v14 offset:1792\n"
    "v_mfma_f32_16x16x32_bf16 v[220:223], v[52:55], v[124:127], v[220:223]\n"
    "ds_read_b64_tr_b16 v[92:93], v15 offset:768\n"
    "v_mfma_f32_16x16x32_bf16 v[224:227], v[56:59], v[112:115], v[224:227]\n"
    "buffer_load_dwordx4 v5, s[24:27], s60 offen offset:1024 lds\n"
    "v_mfma_f32_16x16x32_bf16 v[228:231], v[56:59], v[116:119], v[228:231]\n"
    "ds_read_b64_tr_b16 v[94:95], v15 offset:1792\n"
    "v_mfma_f32_16x16x32_bf16 v[232:235], v[56:59], v[120:123], v[232:235]\n"
    "buffer_store_dwordx4 v[24:27], v2, s[44:47], s22 offen offset:2048 sc1\n"
    "v_mfma_f32_16x16x32_bf16 v[236:239], v[56:59], v[124:127], v[236:239]\n"
    "v_accvgpr_read_b32 v16, a232\n"
    "v_accvgpr_read_b32 v17, a233\n"
    "v_mfma_f32_16x16x32_bf16 v[240:243], v[60:63], v[112:115], v[240:243]\n"
    "v_accvgpr_read_b32 v18, a234\n"
    "v_accvgpr_read_b32 v19, a235\n"
    "v_mfma_f32_16x16x32_bf16 v[244:247], v[60:63], v[116:119], v[244:247]\n"
    "v_accvgpr_read_b32 v20, a236\n"
    "v_accvgpr_read_b32 v21, a237\n"
    "v_mfma_f32_16x16x32_bf16 v[248:251], v[60:63], v[120:123], v[248:251]\n"
    "v_accvgpr_read_b32 v22, a238\n"
    "v_accvgpr_read_b32 v23, a239\n"
    "v_mfma_f32_16x16x32_bf16 v[252:255], v[60:63], v[124:127], v[252:255]\n"
    "v_cvt_pk_bf16_f32 v28, v16, v17\n"
    "v_cvt_pk_bf16_f32 v29, v18, v19\n"
    "v_cvt_pk_bf16_f32 v30, v20, v21\n"
    "v_cvt_pk_bf16_f32 v31, v22, v23\n"
    "s_add_u32 s22, s49, 0x2000\n"
    "buffer_store_dwordx4 v[28:31], v2, s[44:47], s22 offen offset:3072 sc1\n"
    "s_waitcnt lgkmcnt(0)\n"
    "v_mfma_f32_16x16x32_bf16 v[128:131], v[64:67], v[96:99], v[128:131]\n"
    "ds_read_b128 v[112:115], v13\n"
    "v_accvgpr_read_b32 v16, a16\n"
    "v_mfma_f32_16x16x32_bf16 v[132:135], v[64:67], v[100:103], v[132:135]\n"
    "ds_read_b128 v[116:119], v13 offset:2048\n"
    "v_accvgpr_read_b32 v17, a17\n"
    "v_mfma_f32_16x16x32_bf16 v[136:139], v[64:67], v[104:107], v[136:139]\n"
    "ds_read_b128 v[120:123], v13 offset:4096\n"
    "v_accvgpr_read_b32 v18, a18\n"
    "v_mfma_f32_16x16x32_bf16 v[140:143], v[64:67], v[108:111], v[140:143]\n"
    "buffer_load_dwordx4 v6, s[24:27], s60 offen offset:2048 lds\n"
    "v_mfma_f32_16x16x32_bf16 v[144:147], v[68:71], v[96:99], v[144:147]\n"
    "ds_read_b128 v[124:127], v13 offset:6144\n"
    "v_accvgpr_read_b32 v19, a19\n"
    "v_mfma_f32_16x16x32_bf16 v[148:151], v[68:71], v[100:103], v[148:151]\n"
    "ds_read_b64_tr_b16 v[32:33], v14 offset:8192\n"
    "v_accvgpr_read_b32 v20, a48\n"
    "v_mfma_f32_16x16x32_bf16 v[152:155], v[68:71], v[104:107], v[152:155]\n"
    "ds_read_b64_tr_b16 v[34:35], v14 offset:9216\n"
    "v_accvgpr_read_b32 v21, a49\n"
    "v_mfma_f32_16x16x32_bf16 v[156:159], v[68:71], v[108:111], v[156:159]\n"
    "buffer_load_dwordx4 v7, s[24:27], s60 offen offset:3072 lds\n"
    "v_mfma_f32_16x16x32_bf16 v[160:163], v[72:75], v[96:99], v[160:163]\n"
    "ds_read_b64_tr_b16 v[36:37], v15 offset:8192\n"
    "v_accvgpr_read_b32 v22, a50\n"
    "v_mfma_f32_16x16x32_bf16 v[164:167], v[72:75], v[100:103], v[164:167]\n"
    "ds_read_b64_tr_b16 v[38:39], v15 offset:9216\n"
    "s_add_u32 s60, s60, 0x8000\n"
    "v_mfma_f32_16x16x32_bf16 v[168:171], v[72:75], v[104:107], v[168:171]\n"
    "ds_read_b64_tr_b16 v[40:41], v14 offset:8448\n"
    "s_add_u32 m0, m0, 0x1000\n"
    "v_mfma_f32_16x16x32_bf16 v[172:175], v[72:75], v[108:111], v[172:175]\n"
    "buffer_load_dwordx4 v4, s[24:27], s60 offen lds\n"
    "v_mfma_f32_16x16x32_bf16 v[176:179], v[76:79], v[96:99], v[176:179]\n"
    "ds_read_b64_tr_b16 v[42:43], v14 offset:9472\n"
    "v_accvgpr_read_b32 v23, a51\n"
    "v_mfma_f32_16x16x32_bf16 v[180:183], v[76:79], v[100:103], v[180:183]\n"
    "ds_read_b64_tr_b16 v[44:45], v15 offset:8448\n"
    "v_cvt_pk_bf16_f32 v24, v16, v17\n"
    "v_mfma_f32_16x16x32_bf16 v[184:187], v[76:79], v[104:107], v[184:187]\n"
    "ds_read_b64_tr_b16 v[46:47], v15 offset:9472\n"
    "v_cvt_pk_bf16_f32 v25, v18, v19\n"
    "v_mfma_f32_16x16x32_bf16 v[188:191], v[76:79], v[108:111], v[188:191]\n"
    "ds_read_b64_tr_b16 v[48:49], v14 offset:8704\n"
    "v_cvt_pk_bf16_f32 v26, v20, v21\n"
    "v_mfma_f32_16x16x32_bf16 v[192:195], v[80:83], v[96:99], v[192:195]\n"
    "buffer_load_dwordx4 v5, s[24:27], s60 offen offset:1024 lds\n"
    "v_mfma_f32_16x16x32_bf16 v[196:199], v[80:83], v[100:103], v[196:199]\n"
    "ds_read_b64_tr_b16 v[50:51], v14 offset:9728\n"
    "v_cvt_pk_bf16_f32 v27, v22, v23\n"
    "v_mfma_f32_16x16x32_bf16 v[200:203], v[80:83], v[104:107], v[200:203]\n"
    "ds_read_b64_tr_b16 v[52:53], v15 offset:8704\n"
    "s_add_u32 s22, s48, 0x8000\n"
    "v_mfma_f32_16x16x32_bf16 v[204:207], v[80:83], v[108:111], v[204:207]\n"
    "ds_read_b64_tr_b16 v[54:55], v15 offset:9728\n"
    "v_mfma_f32_16x16x32_bf16 v[208:211], v[84:87], v[96:99], v[208:211]\n"
    "buffer_load_dwordx4 v6, s[24:27], s60 offen offset:2048 lds\n"
    "v_mfma_f32_16x16x32_bf16 v[212:215], v[84:87], v[100:103], v[212:215]\n"
    "ds_read_b64_tr_b16 v[56:57], v14 offset:8960\n"
    "v_mfma_f32_16x16x32_bf16 v[216:219], v[84:87], v[104:107], v[216:219]\n"
    "ds_read_b64_tr_b16 v[58:59], v14 offset:9984\n"
    "v_mfma_f32_16x16x32_bf16 v[220:223], v[84:87], v[108:111], v[220:223]\n"
    "ds_read_b64_tr_b16 v[60:61], v15 offset:8960\n"
    "v_mfma_f32_16x16x32_bf16 v[224:227], v[88:91], v[96:99], v[224:227]\n"
    "buffer_load_dwordx4 v7, s[24:27], s60 offen offset:3072 lds\n"
    "v_mfma_f32_16x16x32_bf16 v[228:231], v[88:91], v[100:103], v[228:231]\n"
    "ds_read_b64_tr_b16 v[62:63], v15 offset:9984\n"
    "v_mfma_f32_16x16x32_bf16 v[232:235], v[88:91], v[104:107], v[232:235]\n"
    "v_add_u32 v12, 0x20000, v12\n"
    "v_add_u32 v13, 0x20000, v13\n"
    "v_mfma_f32_16x16x32_bf16 v[236:239], v[88:91], v[108:111], v[236:239]\n"
    "v_add_u32 v14, 0x4000, v14\n"
    "v_add_u32 v15, 0x4000, v15\n"
    "v_mfma_f32_16x16x32_bf16 v[240:243], v[92:95], v[96:99], v[240:243]\n"
    "buffer_store_dwordx4 v[24:27], v2, s[44:47], s22 offen sc1\n"
    "v_mfma_f32_16x16x32_bf16 v[244:247], v[92:95], v[100:103], v[244:247]\n"
    "v_accvgpr_read_b32 v16, a80\n"
    "v_accvgpr_read_b32 v17, a81\n"
    "v_mfma_f32_16x16x32_bf16 v[248:251], v[92:95], v[104:107], v[248:251]\n"
    "v_accvgpr_read_b32 v18, a82\n"
    "v_accvgpr_read_b32 v19, a83\n"
    "v_mfma_f32_16x16x32_bf16 v[252:255], v[92:95], v[108:111], v[252:255]\n"
    "v_accvgpr_read_b32 v20, a112\n"
    "v_accvgpr_read_b32 v21, a113\n"
    "v_accvgpr_read_b32 v22, a114\n"
    "v_accvgpr_read_b32 v23, a115\n"
    "v_cvt_pk_bf16_f32 v28, v16, v17\n"
    "v_cvt_pk_bf16_f32 v29, v18, v19\n"
    "v_cvt_pk_bf16_f32 v30, v20, v21\n"
    "v_cvt_pk_bf16_f32 v31, v22, v23\n"
    "s_add_u32 s22, s48, 0x8000\n"
    "buffer_store_dwordx4 v[28:31], v2, s[44:47], s22 offen offset:1024 sc1\n"
    "s_waitcnt vmcnt(18) lgkmcnt(0)\n"
    "s_barrier\n"
    "v_mfma_f32_16x16x32_bf16 v[128:131], v[32:35], v[112:115], v[128:131]\n"
    "ds_read_b128 v[96:99], v12\n"
    "v_accvgpr_read_b32 v16, a20\n"
    "v_mfma_f32_16x16x32_bf16 v[132:135], v[32:35], v[116:119], v[132:135]\n"
    "ds_read_b128 v[100:103], v12 offset:2048\n"
    "s_add_u32 s61, s54, 0x300000\n"
    "v_mfma_f32_16x16x32_bf16 v[136:139], v[32:35], v[120:123], v[136:139]\n"
    "ds_read_b128 v[104:107], v12 offset:4096\n"
    "s_add_u32 m0, s16, 0x8000\n"
    "v_mfma_f32_16x16x32_bf16 v[140:143], v[32:35], v[124:127], v[140:143]\n"
    "buffer_load_dwordx4 v8, s[28:31], s61 offen lds\n"
    "v_mfma_f32_16x16x32_bf16 v[144:147], v[36:39], v[112:115], v[144:147]\n"
    "ds_read_b128 v[108:111], v12 offset:6144\n"
    "v_accvgpr_read_b32 v17, a21\n"
    "v_mfma_f32_16x16x32_bf16 v[148:151], v[36:39], v[116:119], v[148:151]\n"
    "ds_read_b64_tr_b16 v[64:65], v14\n"
    "v_accvgpr_read_b32 v18, a22\n"
    "v_mfma_f32_16x16x32_bf16 v[152:155], v[36:39], v[120:123], v[152:155]\n"
    "ds_read_b64_tr_b16 v[66:67], v14 offset:1024\n"
    "v_accvgpr_read_b32 v19, a23\n"
    "v_mfma_f32_16x16x32_bf16 v[156:159], v[36:39], v[124:127], v[156:159]\n"
    "buffer_load_dwordx4 v9, s[28:31], s61 offen offset:1024 lds\n"
    "v_mfma_f32_16x16x32_bf16 v[160:163], v[40:43], v[112:115], v[160:163]\n"
    "ds_read_b64_tr_b16 v[68:69], v15\n"
    "v_accvgpr_read_b32 v20, a52\n"
    "v_mfma_f32_16x16x32_bf16 v[164:167], v[40:43], v[116:119], v[164:167]\n"
    "ds_read_b64_tr_b16 v[70:71], v15 offset:1024\n"
    "v_accvgpr_read_b32 v21, a53\n"
    "v_mfma_f32_16x16x32_bf16 v[168:171], v[40:43], v[120:123], v[168:171]\n"
    "ds_read_b64_tr_b16 v[72:73], v14 offset:256\n"
    "v_accvgpr_read_b32 v22, a54\n"
    "v_mfma_f32_16x16x32_bf16 v[172:175], v[40:43], v[124:127], v[172:175]\n"
    "buffer_load_dwordx4 v10, s[28:31], s61 offen offset:2048 lds\n"
    "v_mfma_f32_16x16x32_bf16 v[176:179], v[44:47], v[112:115], v[176:179]\n"
    "ds_read_b64_tr_b16 v[74:75], v14 offset:1280\n"
    "v_accvgpr_read_b32 v23, a55\n"
    "v_mfma_f32_16x16x32_bf16 v[180:183], v[44:47], v[116:119], v[180:183]\n"
    "ds_read_b64_tr_b16 v[76:77], v15 offset:256\n"
    "v_cvt_pk_bf16_f32 v24, v16, v17\n"
    "v_mfma_f32_16x16x32_bf16 v[184:187], v[44:47], v[120:123], v[184:187]\n"
    "ds_read_b64_tr_b16 v[78:79], v15 offset:1280\n"
    "v_cvt_pk_bf16_f32 v25, v18, v19\n"
    "v_mfma_f32_16x16x32_bf16 v[188:191], v[44:47], v[124:127], v[188:191]\n"
    "ds_read_b64_tr_b16 v[80:81], v14 offset:512\n"
    "v_cvt_pk_bf16_f32 v26, v20, v21\n"
    "v_mfma_f32_16x16x32_bf16 v[192:195], v[48:51], v[112:115], v[192:195]\n"
    "buffer_load_dwordx4 v11, s[28:31], s61 offen offset:3072 lds\n"
    "v_mfma_f32_16x16x32_bf16 v[196:199], v[48:51], v[116:119], v[196:199]\n"
    "ds_read_b64_tr_b16 v[82:83], v14 offset:1536\n"
    "v_cvt_pk_bf16_f32 v27, v22, v23\n"
    "v_mfma_f32_16x16x32_bf16 v[200:203], v[48:51], v[120:123], v[200:203]\n"
    "ds_read_b64_tr_b16 v[84:85], v15 offset:512\n"
    "s_add_u32 s60, s63, 0x300\n"
    "v_mfma_f32_16x16x32_bf16 v[204:207], v[48:51], v[124:127], v[204:207]\n"
    "ds_read_b64_tr_b16 v[86:87], v15 offset:1536\n"
    "s_add_u32 m0, s17, 0\n"
    "v_mfma_f32_16x16x32_bf16 v[208:211], v[52:55], v[112:115], v[208:211]\n"
    "buffer_load_dwordx4 v4, s[24:27], s60 offen lds\n"
    "v_mfma_f32_16x16x32_bf16 v[212:215], v[52:55], v[116:119], v[212:215]\n"
    "ds_read_b64_tr_b16 v[88:89], v14 offset:768\n"
    "s_add_u32 s22, s48, 0x9000\n"
    "v_mfma_f32_16x16x32_bf16 v[216:219], v[52:55], v[120:123], v[216:219]\n"
    "ds_read_b64_tr_b16 v[90:91], v14 offset:1792\n"
    "v_mfma_f32_16x16x32_bf16 v[220:223], v[52:55], v[124:127], v[220:223]\n"
    "ds_read_b64_tr_b16 v[92:93], v15 offset:768\n"
    "v_mfma_f32_16x16x32_bf16 v[224:227], v[56:59], v[112:115], v[224:227]\n"
    "buffer_load_dwordx4 v5, s[24:27], s60 offen offset:1024 lds\n"
    "v_mfma_f32_16x16x32_bf16 v[228:231], v[56:59], v[116:119], v[228:231]\n"
    "ds_read_b64_tr_b16 v[94:95], v15 offset:1792\n"
    "v_mfma_f32_16x16x32_bf16 v[232:235], v[56:59], v[120:123], v[232:235]\n"
    "buffer_store_dwordx4 v[24:27], v2, s[44:47], s22 offen sc1\n"
    "v_mfma_f32_16x16x32_bf16 v[236:239], v[56:59], v[124:127], v[236:239]\n"
    "v_accvgpr_read_b32 v16, a84\n"
    "v_accvgpr_read_b32 v17, a85\n"
    "v_mfma_f32_16x16x32_bf16 v[240:243], v[60:63], v[112:115], v[240:243]\n"
    "v_accvgpr_read_b32 v18, a86\n"
    "v_accvgpr_read_b32 v19, a87\n"
    "v_mfma_f32_16x16x32_bf16 v[244:247], v[60:63], v[116:119], v[244:247]\n"
    "v_accvgpr_read_b32 v20, a116\n"
    "v_accvgpr_read_b32 v21, a117\n"
    "v_mfma_f32_16x16x32_bf16 v[248:251], v[60:63], v[120:123], v[248:251]\n"
    "v_accvgpr_read_b32 v22, a118\n"
    "v_accvgpr_read_b32 v23, a119\n"
    "v_mfma_f32_16x16x32_bf16 v[252:255], v[60:63], v[124:127], v[252:255]\n"
    "v_cvt_pk_bf16_f32 v28, v16, v17\n"
    "v_cvt_pk_bf16_f32 v29, v18, v19\n"
    "v_cvt_pk_bf16_f32 v30, v20, v21\n"
    "v_cvt_pk_bf16_f32 v31, v22, v23\n"
    "s_add_u32 s22, s48, 0x9000\n"
    "buffer_store_dwordx4 v[28:31], v2, s[44:47], s22 offen offset:1024 sc1\n"
    "s_waitcnt lgkmcnt(0)\n"
    "v_mfma_f32_16x16x32_bf16 v[128:131], v[64:67], v[96:99], v[128:131]\n"
    "ds_read_b128 v[112:115], v13\n"
    "v_accvgpr_read_b32 v16, a144\n"
    "v_mfma_f32_16x16x32_bf16 v[132:135], v[64:67], v[100:103], v[132:135]\n"
    "ds_read_b128 v[116:119], v13 offset:2048\n"
    "v_accvgpr_read_b32 v17, a145\n"
    "v_mfma_f32_16x16x32_bf16 v[136:139], v[64:67], v[104:107], v[136:139]\n"
    "ds_read_b128 v[120:123], v13 offset:4096\n"
    "v_accvgpr_read_b32 v18, a146\n"
    "v_mfma_f32_16x16x32_bf16 v[140:143], v[64:67], v[108:111], v[140:143]\n"
    "buffer_load_dwordx4 v6, s[24:27], s60 offen offset:2048 lds\n"
    "v_mfma_f32_16x16x32_bf16 v[144:147], v[68:71], v[96:99], v[144:147]\n"
    "ds_read_b128 v[124:127], v13 offset:6144\n"
    "v_accvgpr_read_b32 v19, a147\n"
    "v_mfma_f32_16x16x32_bf16 v[148:151], v[68:71], v[100:103], v[148:151]\n"
    "ds_read_b64_tr_b16 v[32:33], v14 offset:8192\n"
    "v_accvgpr_read_b32 v20, a148\n"
    "v_mfma_f32_16x16x32_bf16 v[152:155], v[68:71], v[104:107], v[152:155]\n"
    "ds_read_b64_tr_b16 v[34:35], v14 offset:9216\n"
    "v_accvgpr_read_b32 v21, a149\n"
    "v_mfma_f32_16x16x32_bf16 v[156:159], v[68:71], v[108:111], v[156:159]\n"
    "buffer_load_dwordx4 v7, s[24:27], s60 offen offset:3072 lds\n"
    "v_mfma_f32_16x16x32_bf16 v[160:163], v[72:75], v[96:99], v[160:163]\n"
    "ds_read_b64_tr_b16 v[36:37], v15 offset:8192\n"
    "v_accvgpr_read_b32 v22, a150\n"
    "v_mfma_f32_16x16x32_bf16 v[164:167], v[72:75], v[100:103], v[164:167]\n"
    "ds_read_b64_tr_b16 v[38:39], v15 offset:9216\n"
    "s_add_u32 s60, s60, 0x8000\n"
    "v_mfma_f32_16x16x32_bf16 v[168:171], v[72:75], v[104:107], v[168:171]\n"
    "ds_read_b64_tr_b16 v[40:41], v14 offset:8448\n"
    "s_add_u32 m0, m0, 0x1000\n"
    "v_mfma_f32_16x16x32_bf16 v[172:175], v[72:75], v[108:111], v[172:175]\n"
    "buffer_load_dwordx4 v4, s[24:27], s60 offen lds\n"
    "v_mfma_f32_16x16x32_bf16 v[176:179], v[76:79], v[96:99], v[176:179]\n"
    "ds_read_b64_tr_b16 v[42:43], v14 offset:9472\n"
    "v_accvgpr_read_b32 v23, a151\n"
    "v_mfma_f32_16x16x32_bf16 v[180:183], v[76:79], v[100:103], v[180:183]\n"
    "ds_read_b64_tr_b16 v[44:45], v15 offset:8448\n"
    "v_cvt_pk_bf16_f32 v24, v16, v17\n"
    "v_mfma_f32_16x16x32_bf16 v[184:187], v[76:79], v[104:107], v[184:187]\n"
    "ds_read_b64_tr_b16 v[46:47], v15 offset:9472\n"
    "v_cvt_pk_bf16_f32 v25, v18, v19\n"
    "v_mfma_f32_16x16x32_bf16 v[188:191], v[76:79], v[108:111], v[188:191]\n"
    "ds_read_b64_tr_b16 v[48:49], v14 offset:8704\n"
    "v_cvt_pk_bf16_f32 v26, v20, v21\n"
    "v_mfma_f32_16x16x32_bf16 v[192:195], v[80:83], v[96:99], v[192:195]\n"
    "buffer_load_dwordx4 v5, s[24:27], s60 offen offset:1024 lds\n"
    "v_mfma_f32_16x16x32_bf16 v[196:199], v[80:83], v[100:103], v[196:199]\n"
    "ds_read_b64_tr_b16 v[50:51], v14 offset:9728\n"
    "v_cvt_pk_bf16_f32 v27, v22, v23\n"
    "v_mfma_f32_16x16x32_bf16 v[200:203], v[80:83], v[104:107], v[200:203]\n"
    "ds_read_b64_tr_b16 v[52:53], v15 offset:8704\n"
    "s_add_u32 s22, s49, 0x8000\n"
    "v_mfma_f32_16x16x32_bf16 v[204:207], v[80:83], v[108:111], v[204:207]\n"
    "ds_read_b64_tr_b16 v[54:55], v15 offset:9728\n"
    "v_mfma_f32_16x16x32_bf16 v[208:211], v[84:87], v[96:99], v[208:211]\n"
    "buffer_load_dwordx4 v6, s[24:27], s60 offen offset:2048 lds\n"
    "v_mfma_f32_16x16x32_bf16 v[212:215], v[84:87], v[100:103], v[212:215]\n"
    "ds_read_b64_tr_b16 v[56:57], v14 offset:8960\n"
    "v_mfma_f32_16x16x32_bf16 v[216:219], v[84:87], v[104:107], v[216:219]\n"
    "ds_read_b64_tr_b16 v[58:59], v14 offset:9984\n"
    "v_mfma_f32_16x16x32_bf16 v[220:223], v[84:87], v[108:111], v[220:223]\n"
    "ds_read_b64_tr_b16 v[60:61], v15 offset:8960\n"
    "v_mfma_f32_16x16x32_bf16 v[224:227], v[88:91], v[96:99], v[224:227]\n"
    "buffer_load_dwordx4 v7, s[24:27], s60 offen offset:3072 lds\n"
    "v_mfma_f32_16x16x32_bf16 v[228:231], v[88:91], v[100:103], v[228:231]\n"
    "ds_read_b64_tr_b16 v[62:63], v15 offset:9984\n"
    "v_mfma_f32_16x16x32_bf16 v[232:235], v[88:91], v[104:107], v[232:235]\n"
    "v_subrev_u32 v12, 0x10000, v12\n"
    "v_subrev_u32 v13, 0x10000, v13\n"
    "v_mfma_f32_16x16x32_bf16 v[236:239], v[88:91], v[108:111], v[236:239]\n"
    "v_add_u32 v14, 0xc000, v14\n"
    "v_add_u32 v15, 0xc000, v15\n"
    "v_mfma_f32_16x16x32_bf16 v[240:243], v[92:95], v[96:99], v[240:243]\n"
    "buffer_store_dwordx4 v[24:27], v2, s[44:47], s22 offen sc1\n"
    "v_mfma_f32_16x16x32_bf16 v[244:247], v[92:95], v[100:103], v[244:247]\n"
    "v_accvgpr_read_b32 v16, a176\n"
    "v_accvgpr_read_b32 v17, a177\n"
    "v_mfma_f32_16x16x32_bf16 v[248:251], v[92:95], v[104:107], v[248:251]\n"
    "v_accvgpr_read_b32 v18, a178\n"
    "v_accvgpr_read_b32 v19, a179\n"
    "v_mfma_f32_16x16x32_bf16 v[252:255], v[92:95], v[108:111], v[252:255]\n"
    "v_accvgpr_read_b32 v20, a180\n"
    "v_accvgpr_read_b32 v21, a181\n"
    "v_accvgpr_read_b32 v22, a182\n"
    "v_accvgpr_read_b32 v23, a183\n"
    "v_cvt_pk_bf16_f32 v28, v16, v17\n"
    "v_cvt_pk_bf16_f32 v29, v18, v19\n"
    "v_cvt_pk_bf16_f32 v30, v20, v21\n"
    "v_cvt_pk_bf16_f32 v31, v22, v23\n"
    "s_add_u32 s22, s49, 0x8000\n"
    "buffer_store_dwordx4 v[28:31], v2, s[44:47], s22 offen offset:1024 sc1\n"
    "s_waitcnt vmcnt(18) lgkmcnt(0)\n"
    "s_barrier\n"
    "v_mfma_f32_16x16x32_bf16 v[128:131], v[32:35], v[112:115], v[128:131]\n"
    "ds_read_b128 v[96:99], v12\n"
    "v_accvgpr_read_b32 v16, a208\n"
    "v_mfma_f32_16x16x32_bf16 v[132:135], v[32:35], v[116:119], v[132:135]\n"
    "ds_read_b128 v[100:103], v12 offset:2048\n"
    "s_add_u32 s61, s54, 0x380000\n"
    "v_mfma_f32_16x16x32_bf16 v[136:139], v[32:35], v[120:123], v[136:139]\n"
    "ds_read_b128 v[104:107], v12 offset:4096\n"
    "s_add_u32 m0, s16, 0xc000\n"
    "v_mfma_f32_16x16x32_bf16 v[140:143], v[32:35], v[124:127], v[140:143]\n"
    "buffer_load_dwordx4 v8, s[28:31], s61 offen lds\n"
    "v_mfma_f32_16x16x32_bf16 v[144:147], v[36:39], v[112:115], v[144:147]\n"
    "ds_read_b128 v[108:111], v12 offset:6144\n"
    "v_accvgpr_read_b32 v17, a209\n"
    "v_mfma_f32_16x16x32_bf16 v[148:151], v[36:39], v[116:119], v[148:151]\n"
    "ds_read_b64_tr_b16 v[64:65], v14\n"
    "v_accvgpr_read_b32 v18, a210\n"
    "v_mfma_f32_16x16x32_bf16 v[152:155], v[36:39], v[120:123], v[152:155]\n"
    "ds_read_b64_tr_b16 v[66:67], v14 offset:1024\n"
    "v_accvgpr_read_b32 v19, a211\n"
    "v_mfma_f32_16x16x32_bf16 v[156:159], v[36:39], v[124:127], v[156:159]\n"
    "buffer_load_dwordx4 v9, s[28:31], s61 offen offset:1024 lds\n"
    "v_mfma_f32_16x16x32_bf16 v[160:163], v[40:43], v[112:115], v[160:163]\n"
    "ds_read_b64_tr_b16 v[68:69], v15\n"
    "v_accvgpr_read_b32 v20, a212\n"
    "v_mfma_f32_16x16x32_bf16 v[164:167], v[40:43], v[116:119], v[164:167]\n"
    "ds_read_b64_tr_b16 v[70:71], v15 offset:1024\n"
    "v_accvgpr_read_b32 v21, a213\n"
    "v_mfma_f32_16x16x32_bf16 v[168:171], v[40:43], v[120:123], v[168:171]\n"
    "ds_read_b64_tr_b16 v[72:73], v14 offset:256\n"
    "v_accvgpr_read_b32 v22, a214\n"
    "v_mfma_f32_16x16x32_bf16 v[172:175], v[40:43], v[124:127], v[172:175]\n"
    "buffer_load_dwordx4 v10, s[28:31], s61 offen offset:2048 lds\n"
    "v_mfma_f32_16x16x32_bf16 v[176:179], v[44:47], v[112:115], v[176:179]\n"
    "ds_read_b64_tr_b16 v[74:75], v14 offset:1280\n"
    "v_accvgpr_read_b32 v23, a215\n"
    "v_mfma_f32_16x16x32_bf16 v[180:183], v[44:47], v[116:119], v[180:183]\n"
    "ds_read_b64_tr_b16 v[76:77], v15 offset:256\n"
    "v_cvt_pk_bf16_f32 v24, v16, v17\n"
    "v_mfma_f32_16x16x32_bf16 v[184:187], v[44:47], v[120:123], v[184:187]\n"
    "ds_read_b64_tr_b16 v[78:79], v15 offset:1280\n"
    "v_cvt_pk_bf16_f32 v25, v18, v19\n"
    "v_mfma_f32_16x16x32_bf16 v[188:191], v[44:47], v[124:127], v[188:191]\n"
    "ds_read_b64_tr_b16 v[80:81], v14 offset:512\n"
    "v_cvt_pk_bf16_f32 v26, v20, v21\n"
    "v_mfma_f32_16x16x32_bf16 v[192:195], v[48:51], v[112:115], v[192:195]\n"
    "buffer_load_dwordx4 v11, s[28:31], s61 offen offset:3072 lds\n"
    "v_mfma_f32_16x16x32_bf16 v[196:199], v[48:51], v[116:119], v[196:199]\n"
    "ds_read_b64_tr_b16 v[82:83], v14 offset:1536\n"
    "v_cvt_pk_bf16_f32 v27, v22, v23\n"
    "v_mfma_f32_16x16x32_bf16 v[200:203], v[48:51], v[120:123], v[200:203]\n"
    "ds_read_b64_tr_b16 v[84:85], v15 offset:512\n"
    "s_add_u32 s60, s63, 0x380\n"
    "v_mfma_f32_16x16x32_bf16 v[204:207], v[48:51], v[124:127], v[204:207]\n"
    "ds_read_b64_tr_b16 v[86:87], v15 offset:1536\n"
    "s_add_u32 m0, s17, 0x20000\n"
    "v_mfma_f32_16x16x32_bf16 v[208:211], v[52:55], v[112:115], v[208:211]\n"
    "buffer_load_dwordx4 v4, s[24:27], s60 offen lds\n"
    "v_mfma_f32_16x16x32_bf16 v[212:215], v[52:55], v[116:119], v[212:215]\n"
    "ds_read_b64_tr_b16 v[88:89], v14 offset:768\n"
    "s_add_u32 s22, s49, 0x8000\n"
    "v_mfma_f32_16x16x32_bf16 v[216:219], v[52:55], v[120:123], v[216:219]\n"
    "ds_read_b64_tr_b16 v[90:91], v14 offset:1792\n"
    "v_mfma_f32_16x16x32_bf16 v[220:223], v[52:55], v[124:127], v[220:223]\n"
    "ds_read_b64_tr_b16 v[92:93], v15 offset:768\n"
    "v_mfma_f32_16x16x32_bf16 v[224:227], v[56:59], v[112:115], v[224:227]\n"
    "buffer_load_dwordx4 v5, s[24:27], s60 offen offset:1024 lds\n"
    "v_mfma_f32_16x16x32_bf16 v[228:231], v[56:59], v[116:119], v[228:231]\n"
    "ds_read_b64_tr_b16 v[94:95], v15 offset:1792\n"
    "v_mfma_f32_16x16x32_bf16 v[232:235], v[56:59], v[120:123], v[232:235]\n"
    "buffer_store_dwordx4 v[24:27], v2, s[44:47], s22 offen offset:2048 sc1\n"
    "v_mfma_f32_16x16x32_bf16 v[236:239], v[56:59], v[124:127], v[236:239]\n"
    "v_accvgpr_read_b32 v16, a240\n"
    "v_accvgpr_read_b32 v17, a241\n"
    "v_mfma_f32_16x16x32_bf16 v[240:243], v[60:63], v[112:115], v[240:243]\n"
    "v_accvgpr_read_b32 v18, a242\n"
    "v_accvgpr_read_b32 v19, a243\n"
    "v_mfma_f32_16x16x32_bf16 v[244:247], v[60:63], v[116:119], v[244:247]\n"
    "v_accvgpr_read_b32 v20, a244\n"
    "v_accvgpr_read_b32 v21, a245\n"
    "v_mfma_f32_16x16x32_bf16 v[248:251], v[60:63], v[120:123], v[248:251]\n"
    "v_accvgpr_read_b32 v22, a246\n"
    "v_accvgpr_read_b32 v23, a247\n"
    "v_mfma_f32_16x16x32_bf16 v[252:255], v[60:63], v[124:127], v[252:255]\n"
    "v_cvt_pk_bf16_f32 v28, v16, v17\n"
    "v_cvt_pk_bf16_f32 v29, v18, v19\n"
    "v_cvt_pk_bf16_f32 v30, v20, v21\n"
    "v_cvt_pk_bf16_f32 v31, v22, v23\n"
    "s_add_u32 s22, s49, 0x8000\n"
    "buffer_store_dwordx4 v[28:31], v2, s[44:47], s22 offen offset:3072 sc1\n"
    "s_waitcnt lgkmcnt(0)\n"
    "v_mfma_f32_16x16x32_bf16 v[128:131], v[64:67], v[96:99], v[128:131]\n"
    "ds_read_b128 v[112:115], v13\n"
    "v_accvgpr_read_b32 v16, a24\n"
    "v_mfma_f32_16x16x32_bf16 v[132:135], v[64:67], v[100:103], v[132:135]\n"
    "ds_read_b128 v[116:119], v13 offset:2048\n"
    "v_accvgpr_read_b32 v17, a25\n"
    "v_mfma_f32_16x16x32_bf16 v[136:139], v[64:67], v[104:107], v[136:139]\n"
    "ds_read_b128 v[120:123], v13 offset:4096\n"
    "v_accvgpr_read_b32 v18, a26\n"
    "v_mfma_f32_16x16x32_bf16 v[140:143], v[64:67], v[108:111], v[140:143]\n"
    "buffer_load_dwordx4 v6, s[24:27], s60 offen offset:2048 lds\n"
    "v_mfma_f32_16x16x32_bf16 v[144:147], v[68:71], v[96:99], v[144:147]\n"
    "ds_read_b128 v[124:127], v13 offset:6144\n"
    "v_accvgpr_read_b32 v19, a27\n"
    "v_mfma_f32_16x16x32_bf16 v[148:151], v[68:71], v[100:103], v[148:151]\n"
    "ds_read_b64_tr_b16 v[32:33], v14 offset:8192\n"
    "v_accvgpr_read_b32 v20, a56\n"
    "v_mfma_f32_16x16x32_bf16 v[152:155], v[68:71], v[104:107], v[152:155]\n"
    "ds_read_b64_tr_b16 v[34:35], v14 offset:9216\n"
    "v_accvgpr_read_b32 v21, a57\n"
    "v_mfma_f32_16x16x32_bf16 v[156:159], v[68:71], v[108:111], v[156:159]\n"
    "buffer_load_dwordx4 v7, s[24:27], s60 offen offset:3072 lds\n"
    "v_mfma_f32_16x16x32_bf16 v[160:163], v[72:75], v[96:99], v[160:163]\n"
    "ds_read_b64_tr_b16 v[36:37], v15 offset:8192\n"
    "v_accvgpr_read_b32 v22, a58\n"
    "v_mfma_f32_16x16x32_bf16 v[164:167], v[72:75], v[100:103], v[164:167]\n"
    "ds_read_b64_tr_b16 v[38:39], v15 offset:9216\n"
    "s_add_u32 s60, s60, 0x8000\n"
    "v_mfma_f32_16x16x32_bf16 v[168:171], v[72:75], v[104:107], v[168:171]\n"
    "ds_read_b64_tr_b16 v[40:41], v14 offset:8448\n"
    "s_add_u32 m0, m0, 0x1000\n"
    "v_mfma_f32_16x16x32_bf16 v[172:175], v[72:75], v[108:111], v[172:175]\n"
    "buffer_load_dwordx4 v4, s[24:27], s60 offen lds\n"
    "v_mfma_f32_16x16x32_bf16 v[176:179], v[76:79], v[96:99], v[176:179]\n"
    "ds_read_b64_tr_b16 v[42:43], v14 offset:9472\n"
    "v_accvgpr_read_b32 v23, a59\n"
    "v_mfma_f32_16x16x32_bf16 v[180:183], v[76:79], v[100:103], v[180:183]\n"
    "ds_read_b64_tr_b16 v[44:45], v15 offset:8448\n"
    "v_cvt_pk_bf16_f32 v24, v16, v17\n"
    "v_mfma_f32_16x16x32_bf16 v[184:187], v[76:79], v[104:107], v[184:187]\n"
    "ds_read_b64_tr_b16 v[46:47], v15 offset:9472\n"
    "v_cvt_pk_bf16_f32 v25, v18, v19\n"
    "v_mfma_f32_16x16x32_bf16 v[188:191], v[76:79], v[108:111], v[188:191]\n"
    "ds_read_b64_tr_b16 v[48:49], v14 offset:8704\n"
    "v_cvt_pk_bf16_f32 v26, v20, v21\n"
    "v_mfma_f32_16x16x32_bf16 v[192:195], v[80:83], v[96:99], v[192:195]\n"
    "buffer_load_dwordx4 v5, s[24:27], s60 offen offset:1024 lds\n"
    "v_mfma_f32_16x16x32_bf16 v[196:199], v[80:83], v[100:103], v[196:199]\n"
    "ds_read_b64_tr_b16 v[50:51], v14 offset:9728\n"
    "v_cvt_pk_bf16_f32 v27, v22, v23\n"
    "v_mfma_f32_16x16x32_bf16 v[200:203], v[80:83], v[104:107], v[200:203]\n"
    "ds_read_b64_tr_b16 v[52:53], v15 offset:8704\n"
    "s_add_u32 s22, s48, 0xa000\n"
    "v_mfma_f32_16x16x32_bf16 v[204:207], v[80:83], v[108:111], v[204:207]\n"
    "ds_read_b64_tr_b16 v[54:55], v15 offset:9728\n"
    "v_mfma_f32_16x16x32_bf16 v[208:211], v[84:87], v[96:99], v[208:211]\n"
    "buffer_load_dwordx4 v6, s[24:27], s60 offen offset:2048 lds\n"
    "v_mfma_f32_16x16x32_bf16 v[212:215], v[84:87], v[100:103], v[212:215]\n"
    "ds_read_b64_tr_b16 v[56:57], v14 offset:8960\n"
    "v_mfma_f32_16x16x32_bf16 v[216:219], v[84:87], v[104:107], v[216:219]\n"
    "ds_read_b64_tr_b16 v[58:59], v14 offset:9984\n"
    "v_mfma_f32_16x16x32_bf16 v[220:223], v[84:87], v[108:111], v[220:223]\n"
    "ds_read_b64_tr_b16 v[60:61], v15 offset:8960\n"
    "v_mfma_f32_16x16x32_bf16 v[224:227], v[88:91], v[96:99], v[224:227]\n"
    "buffer_load_dwordx4 v7, s[24:27], s60 offen offset:3072 lds\n"
    "v_mfma_f32_16x16x32_bf16 v[228:231], v[88:91], v[100:103], v[228:231]\n"
    "ds_read_b64_tr_b16 v[62:63], v15 offset:9984\n"
    "v_mfma_f32_16x16x32_bf16 v[232:235], v[88:91], v[104:107], v[232:235]\n"
    "v_subrev_u32 v12, 0x10000, v12\n"
    "v_subrev_u32 v13, 0x10000, v13\n"
    "v_mfma_f32_16x16x32_bf16 v[236:239], v[88:91], v[108:111], v[236:239]\n"
    "v_subrev_u32 v14, 0x10000, v14\n"
    "v_subrev_u32 v15, 0x10000, v15\n"
    "v_mfma_f32_16x16x32_bf16 v[240:243], v[92:95], v[96:99], v[240:243]\n"
    "buffer_store_dwordx4 v[24:27], v2, s[44:47], s22 offen sc1\n"
    "v_mfma_f32_16x16x32_bf16 v[244:247], v[92:95], v[100:103], v[244:247]\n"
    "v_accvgpr_read_b32 v16, a88\n"
    "v_accvgpr_read_b32 v17, a89\n"
    "v_mfma_f32_16x16x32_bf16 v[248:251], v[92:95], v[104:107], v[248:251]\n"
    "v_accvgpr_read_b32 v18, a90\n"
    "v_accvgpr_read_b32 v19, a91\n"
    "v_mfma_f32_16x16x32_bf16 v[252:255], v[92:95], v[108:111], v[252:255]\n"
    "v_accvgpr_read_b32 v20, a120\n"
    "v_accvgpr_read_b32 v21, a121\n"
    "v_accvgpr_read_b32 v22, a122\n"
    "v_accvgpr_read_b32 v23, a123\n"
    "v_cvt_pk_bf16_f32 v28, v16, v17\n"
    "v_cvt_pk_bf16_f32 v29, v18, v19\n"
    "v_cvt_pk_bf16_f32 v30, v20, v21\n"
    "v_cvt_pk_bf16_f32 v31, v22, v23\n"
    "s_add_u32 s22, s48, 0xa000\n"
    "buffer_store_dwordx4 v[28:31], v2, s[44:47], s22 offen offset:1024 sc1\n"
    "s_waitcnt vmcnt(18) lgkmcnt(0)\n"
    "s_barrier\n"
    "v_mfma_f32_16x16x32_bf16 v[128:131], v[32:35], v[112:115], v[128:131]\n"
    "ds_read_b128 v[96:99], v12\n"
    "v_accvgpr_read_b32 v16, a28\n"
    "v_mfma_f32_16x16x32_bf16 v[132:135], v[32:35], v[116:119], v[132:135]\n"
    "ds_read_b128 v[100:103], v12 offset:2048\n"
    "v_accvgpr_read_b32 v17, a29\n"
    "v_mfma_f32_16x16x32_bf16 v[136:139], v[32:35], v[120:123], v[136:139]\n"
    "ds_read_b128 v[104:107], v12 offset:4096\n"
    "v_accvgpr_read_b32 v18, a30\n"
    "v_mfma_f32_16x16x32_bf16 v[140:143], v[32:35], v[124:127], v[140:143]\n"
    "ds_read_b128 v[108:111], v12 offset:6144\n"
    "v_accvgpr_read_b32 v19, a31\n"
    "v_mfma_f32_16x16x32_bf16 v[144:147], v[36:39], v[112:115], v[144:147]\n"
    "ds_read_b64_tr_b16 v[64:65], v14\n"
    "v_accvgpr_read_b32 v20, a60\n"
    "v_mfma_f32_16x16x32_bf16 v[148:151], v[36:39], v[116:119], v[148:151]\n"
    "ds_read_b64_tr_b16 v[66:67], v14 offset:1024\n"
    "v_accvgpr_read_b32 v21, a61\n"
    "v_mfma_f32_16x16x32_bf16 v[152:155], v[36:39], v[120:123], v[152:155]\n"
    "ds_read_b64_tr_b16 v[68:69], v15\n"
    "v_accvgpr_read_b32 v22, a62\n"
    "v_mfma_f32_16x16x32_bf16 v[156:159], v[36:39], v[124:127], v[156:159]\n"
    "ds_read_b64_tr_b16 v[70:71], v15 offset:1024\n"
    "v_accvgpr_read_b32 v23, a63\n"
    "v_mfma_f32_16x16x32_bf16 v[160:163], v[40:43], v[112:115], v[160:163]\n"
    "ds_read_b64_tr_b16 v[72:73], v14 offset:256\n"
    "v_cvt_pk_bf16_f32 v24, v16, v17\n"
    "v_mfma_f32_16x16x32_bf16 v[164:167], v[40:43], v[116:119], v[164:167]\n"
    "ds_read_b64_tr_b16 v[74:75], v14 offset:1280\n"
    "v_cvt_pk_bf16_f32 v25, v18, v19\n"
    "v_mfma_f32_16x16x32_bf16 v[168:171], v[40:43], v[120:123], v[168:171]\n"
    "ds_read_b64_tr_b16 v[76:77], v15 offset:256\n"
    "v_cvt_pk_bf16_f32 v26, v20, v21\n"
    "v_mfma_f32_16x16x32_bf16 v[172:175], v[40:43], v[124:127], v[172:175]\n"
    "ds_read_b64_tr_b16 v[78:79], v15 offset:1280\n"
    "v_cvt_pk_bf16_f32 v27, v22, v23\n"
    "v_mfma_f32_16x16x32_bf16 v[176:179], v[44:47], v[112:115], v[176:179]\n"
    "ds_read_b64_tr_b16 v[80:81], v14 offset:512\n"
    "s_add_u32 s22, s48, 0xb000\n"
    "v_mfma_f32_16x16x32_bf16 v[180:183], v[44:47], v[116:119], v[180:183]\n"
    "ds_read_b64_tr_b16 v[82:83], v14 offset:1536\n"
    "v_mfma_f32_16x16x32_bf16 v[184:187], v[44:47], v[120:123], v[184:187]\n"
    "ds_read_b64_tr_b16 v[84:85], v15 offset:512\n"
    "v_mfma_f32_16x16x32_bf16 v[188:191], v[44:47], v[124:127], v[188:191]\n"
    "ds_read_b64_tr_b16 v[86:87], v15 offset:1536\n"
    "v_mfma_f32_16x16x32_bf16 v[192:195], v[48:51], v[112:115], v[192:195]\n"
    "ds_read_b64_tr_b16 v[88:89], v14 offset:768\n"
    "v_mfma_f32_16x16x32_bf16 v[196:199], v[48:51], v[116:119], v[196:199]\n"
    "ds_read_b64_tr_b16 v[90:91], v14 offset:1792\n"
    "v_mfma_f32_16x16x32_bf16 v[200:203], v[48:51], v[120:123], v[200:203]\n"
    "ds_read_b64_tr_b16 v[92:93], v15 offset:768\n"
    "v_mfma_f32_16x16x32_bf16 v[204:207], v[48:51], v[124:127], v[204:207]\n"
    "ds_read_b64_tr_b16 v[94:95], v15 offset:1792\n"
    "v_mfma_f32_16x16x32_bf16 v[208:211], v[52:55], v[112:115], v[208:211]\n"
    "buffer_store_dwordx4 v[24:27], v2, s[44:47], s22 offen sc1\n"
    "v_mfma_f32_16x16x32_bf16 v[212:215], v[52:55], v[116:119], v[212:215]\n"
    "v_accvgpr_read_b32 v16, a92\n"
    "v_accvgpr_read_b32 v17, a93\n"
    "v_mfma_f32_16x16x32_bf16 v[216:219], v[52:55], v[120:123], v[216:219]\n"
    "v_accvgpr_read_b32 v18, a94\n"
    "v_accvgpr_read_b32 v19, a95\n"
    "v_mfma_f32_16x16x32_bf16 v[220:223], v[52:55], v[124:127], v[220:223]\n"
    "v_accvgpr_read_b32 v20, a124\n"
    "v_accvgpr_read_b32 v21, a125\n"
    "v_mfma_f32_16x16x32_bf16 v[224:227], v[56:59], v[112:115], v[224:227]\n"
    "v_accvgpr_read_b32 v22, a126\n"
    "v_accvgpr_read_b32 v23, a127\n"
    "v_mfma_f32_16x16x32_bf16 v[228:231], v[56:59], v[116:119], v[228:231]\n"
    "v_cvt_pk_bf16_f32 v28, v16, v17\n"
    "v_cvt_pk_bf16_f32 v29, v18, v19\n"
    "v_mfma_f32_16x16x32_bf16 v[232:235], v[56:59], v[120:123], v[232:235]\n"
    "v_cvt_pk_bf16_f32 v30, v20, v21\n"
    "v_cvt_pk_bf16_f32 v31, v22, v23\n"
    "v_mfma_f32_16x16x32_bf16 v[236:239], v[56:59], v[124:127], v[236:239]\n"
    "s_add_u32 s22, s48, 0xb000\n"
    "v_mfma_f32_16x16x32_bf16 v[240:243], v[60:63], v[112:115], v[240:243]\n"
    "buffer_store_dwordx4 v[28:31], v2, s[44:47], s22 offen offset:1024 sc1\n"
    "v_mfma_f32_16x16x32_bf16 v[244:247], v[60:63], v[116:119], v[244:247]\n"
    "v_accvgpr_read_b32 v16, a152\n"
    "v_accvgpr_read_b32 v17, a153\n"
    "v_mfma_f32_16x16x32_bf16 v[248:251], v[60:63], v[120:123], v[248:251]\n"
    "v_accvgpr_read_b32 v18, a154\n"
    "v_accvgpr_read_b32 v19, a155\n"
    "v_mfma_f32_16x16x32_bf16 v[252:255], v[60:63], v[124:127], v[252:255]\n"
    "v_accvgpr_read_b32 v20, a156\n"
    "v_accvgpr_read_b32 v21, a157\n"
    "v_accvgpr_read_b32 v22, a158\n"
    "v_accvgpr_read_b32 v23, a159\n"
    "v_cvt_pk_bf16_f32 v24, v16, v17\n"
    "v_cvt_pk_bf16_f32 v25, v18, v19\n"
    "v_cvt_pk_bf16_f32 v26, v20, v21\n"
    "v_cvt_pk_bf16_f32 v27, v22, v23\n"
    "s_add_u32 s22, s49, 0xa000\n"
    "buffer_store_dwordx4 v[24:27], v2, s[44:47], s22 offen sc1\n"
    "s_waitcnt lgkmcnt(0)\n"
    "v_mfma_f32_16x16x32_bf16 v[128:131], v[64:67], v[96:99], v[128:131]\n"
    "ds_read_b128 v[112:115], v13\n"
    "v_accvgpr_read_b32 v16, a184\n"
    "v_mfma_f32_16x16x32_bf16 v[132:135], v[64:67], v[100:103], v[132:135]\n"
    "ds_read_b128 v[116:119], v13 offset:2048\n"
    "v_accvgpr_read_b32 v17, a185\n"
    "v_mfma_f32_16x16x32_bf16 v[136:139], v[64:67], v[104:107], v[136:139]\n"
    "ds_read_b128 v[120:123], v13 offset:4096\n"
    "v_accvgpr_read_b32 v18, a186\n"
    "v_mfma_f32_16x16x32_bf16 v[140:143], v[64:67], v[108:111], v[140:143]\n"
    "ds_read_b128 v[124:127], v13 offset:6144\n"
    "v_accvgpr_read_b32 v19, a187\n"
    "v_mfma_f32_16x16x32_bf16 v[144:147], v[68:71], v[96:99], v[144:147]\n"
    "ds_read_b64_tr_b16 v[32:33], v14 offset:8192\n"
    "v_accvgpr_read_b32 v20, a188\n"
    "v_mfma_f32_16x16x32_bf16 v[148:151], v[68:71], v[100:103], v[148:151]\n"
    "ds_read_b64_tr_b16 v[34:35], v14 offset:9216\n"
    "v_accvgpr_read_b32 v21, a189\n"
    "v_mfma_f32_16x16x32_bf16 v[152:155], v[68:71], v[104:107], v[152:155]\n"
    "ds_read_b64_tr_b16 v[36:37], v15 offset:8192\n"
    "v_accvgpr_read_b32 v22, a190\n"
    "v_mfma_f32_16x16x32_bf16 v[156:159], v[68:71], v[108:111], v[156:159]\n"
    "ds_read_b64_tr_b16 v[38:39], v15 offset:9216\n"
    "v_accvgpr_read_b32 v23, a191\n"
    "v_mfma_f32_16x16x32_bf16 v[160:163], v[72:75], v[96:99], v[160:163]\n"
    "ds_read_b64_tr_b16 v[40:41], v14 offset:8448\n"
    "v_cvt_pk_bf16_f32 v28, v16, v17\n"
    "v_mfma_f32_16x16x32_bf16 v[164:167], v[72:75], v[100:103], v[164:167]\n"
    "ds_read_b64_tr_b16 v[42:43], v14 offset:9472\n"
    "v_cvt_pk_bf16_f32 v29, v18, v19\n"
    "v_mfma_f32_16x16x32_bf16 v[168:171], v[72:75], v[104:107], v[168:171]\n"
    "ds_read_b64_tr_b16 v[44:45], v15 offset:8448\n"
    "v_cvt_pk_bf16_f32 v30, v20, v21\n"
    "v_mfma_f32_16x16x32_bf16 v[172:175], v[72:75], v[108:111], v[172:175]\n"
    "ds_read_b64_tr_b16 v[46:47], v15 offset:9472\n"
    "v_cvt_pk_bf16_f32 v31, v22, v23\n"
    "v_mfma_f32_16x16x32_bf16 v[176:179], v[76:79], v[96:99], v[176:179]\n"
    "ds_read_b64_tr_b16 v[48:49], v14 offset:8704\n"
    "s_add_u32 s22, s49, 0xa000\n"
    "v_mfma_f32_16x16x32_bf16 v[180:183], v[76:79], v[100:103], v[180:183]\n"
    "ds_read_b64_tr_b16 v[50:51], v14 offset:9728\n"
    "v_mfma_f32_16x16x32_bf16 v[184:187], v[76:79], v[104:107], v[184:187]\n"
    "ds_read_b64_tr_b16 v[52:53], v15 offset:8704\n"
    "v_mfma_f32_16x16x32_bf16 v[188:191], v[76:79], v[108:111], v[188:191]\n"
    "ds_read_b64_tr_b16 v[54:55], v15 offset:9728\n"
    "v_mfma_f32_16x16x32_bf16 v[192:195], v[80:83], v[96:99], v[192:195]\n"
    "ds_read_b64_tr_b16 v[56:57], v14 offset:8960\n"
    "v_mfma_f32_16x16x32_bf16 v[196:199], v[80:83], v[100:103], v[196:199]\n"
    "ds_read_b64_tr_b16 v[58:59], v14 offset:9984\n"
    "v_mfma_f32_16x16x32_bf16 v[200:203], v[80:83], v[104:107], v[200:203]\n"
    "ds_read_b64_tr_b16 v[60:61], v15 offset:8960\n"
    "v_mfma_f32_16x16x32_bf16 v[204:207], v[80:83], v[108:111], v[204:207]\n"
    "ds_read_b64_tr_b16 v[62:63], v15 offset:9984\n"
    "v_mfma_f32_16x16x32_bf16 v[208:211], v[84:87], v[96:99], v[208:211]\n"
    "v_add_u32 v12, 0x20000, v12\n"
    "v_add_u32 v13, 0x20000, v13\n"
    "v_mfma_f32_16x16x32_bf16 v[212:215], v[84:87], v[100:103], v[212:215]\n"
    "v_add_u32 v14, 0x4000, v14\n"
    "v_add_u32 v15, 0x4000, v15\n"
    "v_mfma_f32_16x16x32_bf16 v[216:219], v[84:87], v[104:107], v[216:219]\n"
    "buffer_store_dwordx4 v[28:31], v2, s[44:47], s22 offen offset:1024 sc1\n"
    "v_mfma_f32_16x16x32_bf16 v[220:223], v[84:87], v[108:111], v[220:223]\n"
    "v_accvgpr_read_b32 v16, a216\n"
    "v_accvgpr_read_b32 v17, a217\n"
    "v_mfma_f32_16x16x32_bf16 v[224:227], v[88:91], v[96:99], v[224:227]\n"
    "v_accvgpr_read_b32 v18, a218\n"
    "v_accvgpr_read_b32 v19, a219\n"
    "v_mfma_f32_16x16x32_bf16 v[228:231], v[88:91], v[100:103], v[228:231]\n"
    "v_accvgpr_read_b32 v20, a220\n"
    "v_accvgpr_read_b32 v21, a221\n"
    "v_mfma_f32_16x16x32_bf16 v[232:235], v[88:91], v[104:107], v[232:235]\n"
    "v_accvgpr_read_b32 v22, a222\n"
    "v_accvgpr_read_b32 v23, a223\n"
    "v_mfma_f32_16x16x32_bf16 v[236:239], v[88:91], v[108:111], v[236:239]\n"
    "v_cvt_pk_bf16_f32 v24, v16, v17\n"
    "v_cvt_pk_bf16_f32 v25, v18, v19\n"
    "v_mfma_f32_16x16x32_bf16 v[240:243], v[92:95], v[96:99], v[240:243]\n"
    "v_cvt_pk_bf16_f32 v26, v20, v21\n"
    "v_cvt_pk_bf16_f32 v27, v22, v23\n"
    "v_mfma_f32_16x16x32_bf16 v[244:247], v[92:95], v[100:103], v[244:247]\n"
    "s_add_u32 s22, s49, 0xa000\n"
    "v_mfma_f32_16x16x32_bf16 v[248:251], v[92:95], v[104:107], v[248:251]\n"
    "buffer_store_dwordx4 v[24:27], v2, s[44:47], s22 offen offset:2048 sc1\n"
    "v_mfma_f32_16x16x32_bf16 v[252:255], v[92:95], v[108:111], v[252:255]\n"
    "v_accvgpr_read_b32 v16, a248\n"
    "v_accvgpr_read_b32 v17, a249\n"
    "v_accvgpr_read_b32 v18, a250\n"
    "v_accvgpr_read_b32 v19, a251\n"
    "v_accvgpr_read_b32 v20, a252\n"
    "v_accvgpr_read_b32 v21, a253\n"
    "v_accvgpr_read_b32 v22, a254\n"
    "v_accvgpr_read_b32 v23, a255\n"
    "v_cvt_pk_bf16_f32 v28, v16, v17\n"
    "v_cvt_pk_bf16_f32 v29, v18, v19\n"
    "v_cvt_pk_bf16_f32 v30, v20, v21\n"
    "v_cvt_pk_bf16_f32 v31, v22, v23\n"
    "s_add_u32 s22, s49, 0xa000\n"
    "buffer_store_dwordx4 v[28:31], v2, s[44:47], s22 offen offset:3072 sc1\n"
    "s_waitcnt vmcnt(8) lgkmcnt(0)\n"
    "s_barrier\n"
    "v_mfma_f32_16x16x32_bf16 v[128:131], v[32:35], v[112:115], v[128:131]\n"
    "ds_read_b128 v[96:99], v12\n"
    "s_mov_b64 s[64:65], s[86:87]\n"
    "v_mfma_f32_16x16x32_bf16 v[132:135], v[32:35], v[116:119], v[132:135]\n"
    "ds_read_b128 v[100:103], v12 offset:2048\n"
    "s_mov_b32 s66, 0x800000\n"
    "v_mfma_f32_16x16x32_bf16 v[136:139], v[32:35], v[120:123], v[136:139]\n"
    "ds_read_b128 v[104:107], v12 offset:4096\n"
    "s_mov_b32 s67, 0x20000\n"
    "v_mfma_f32_16x16x32_bf16 v[140:143], v[32:35], v[124:127], v[140:143]\n"
    "ds_read_b128 v[108:111], v12 offset:6144\n"
    "s_and_b32 s22, s18, 7\n"
    "v_mfma_f32_16x16x32_bf16 v[144:147], v[36:39], v[112:115], v[144:147]\n"
    "ds_read_b64_tr_b16 v[64:65], v14\n"
    "s_lshr_b32 s23, s18, 3\n"
    "v_mfma_f32_16x16x32_bf16 v[148:151], v[36:39], v[116:119], v[148:151]\n"
    "ds_read_b64_tr_b16 v[66:67], v14 offset:1024\n"
    "s_mul_i32 s59, s23, 37\n"
    "v_mfma_f32_16x16x32_bf16 v[152:155], v[36:39], v[120:123], v[152:155]\n"
    "ds_read_b64_tr_b16 v[68:69], v15\n"
    "s_lshr_b32 s59, s59, 8\n"
    "v_mfma_f32_16x16x32_bf16 v[156:159], v[36:39], v[124:127], v[156:159]\n"
    "ds_read_b64_tr_b16 v[70:71], v15 offset:1024\n"
    "s_mul_i32 s34, s59, 7\n"
    "v_mfma_f32_16x16x32_bf16 v[160:163], v[40:43], v[112:115], v[160:163]\n"
    "ds_read_b64_tr_b16 v[72:73], v14 offset:256\n"
    "s_sub_u32 s57, s23, s34\n"
    "v_mfma_f32_16x16x32_bf16 v[164:167], v[40:43], v[116:119], v[164:167]\n"
    "ds_read_b64_tr_b16 v[74:75], v14 offset:1280\n"
    "s_lshr_b32 s56, s22, 1\n"
    "v_mfma_f32_16x16x32_bf16 v[168:171], v[40:43], v[120:123], v[168:171]\n"
    "ds_read_b64_tr_b16 v[76:77], v15 offset:256\n"
    "s_lshl_b32 s56, s56, 2\n"
    "v_mfma_f32_16x16x32_bf16 v[172:175], v[40:43], v[124:127], v[172:175]\n"
    "ds_read_b64_tr_b16 v[78:79], v15 offset:1280\n"
    "s_add_u32 s56, s56, s59\n"
    "v_mfma_f32_16x16x32_bf16 v[176:179], v[44:47], v[112:115], v[176:179]\n"
    "ds_read_b64_tr_b16 v[80:81], v14 offset:512\n"
    "s_and_b32 s58, s22, 1\n"
    "v_mfma_f32_16x16x32_bf16 v[180:183], v[44:47], v[116:119], v[180:183]\n"
    "ds_read_b64_tr_b16 v[82:83], v14 offset:1536\n"
    "s_lshr_b32 s59, s59, 2\n"
    "v_mfma_f32_16x16x32_bf16 v[184:187], v[44:47], v[120:123], v[184:187]\n"
    "ds_read_b64_tr_b16 v[84:85], v15 offset:512\n"
    "s_lshl_b32 s13, s56, 19\n"
    "v_mfma_f32_16x16x32_bf16 v[188:191], v[44:47], v[124:127], v[188:191]\n"
    "ds_read_b64_tr_b16 v[86:87], v15 offset:1536\n"
    "s_lshl_b32 s22, s19, 17\n"
    "v_mfma_f32_16x16x32_bf16 v[192:195], v[48:51], v[112:115], v[192:195]\n"
    "ds_read_b64_tr_b16 v[88:89], v14 offset:768\n"
    "s_add_u32 s13, s13, s22\n"
    "v_mfma_f32_16x16x32_bf16 v[196:199], v[48:51], v[116:119], v[196:199]\n"
    "ds_read_b64_tr_b16 v[90:91], v14 offset:1792\n"
    "s_mul_i32 s22, s58, 0xa00\n"
    "v_mfma_f32_16x16x32_bf16 v[200:203], v[48:51], v[120:123], v[200:203]\n"
    "ds_read_b64_tr_b16 v[92:93], v15 offset:768\n"
    "s_add_u32 s13, s13, s22\n"
    "v_mfma_f32_16x16x32_bf16 v[204:207], v[48:51], v[124:127], v[204:207]\n"
    "ds_read_b64_tr_b16 v[94:95], v15 offset:1792\n"
    "s_mul_i32 s71, s58, 40\n"
    "v_mfma_f32_16x16x32_bf16 v[208:211], v[52:55], v[112:115], v[208:211]\n"
    "s_lshr_b32 s22, s19, 1\n"
    "s_add_u32 s71, s71, s22\n"
    "v_mfma_f32_16x16x32_bf16 v[212:215], v[52:55], v[116:119], v[212:215]\n"
    "s_mul_i32 s71, s71, 56\n"
    "s_lshl_b32 s22, s57, 3\n"
    "v_mfma_f32_16x16x32_bf16 v[216:219], v[52:55], v[120:123], v[216:219]\n"
    "s_add_u32 s71, s71, s22\n"
    "s_and_b32 s22, s19, 1\n"
    "v_mfma_f32_16x16x32_bf16 v[220:223], v[52:55], v[124:127], v[220:223]\n"
    "s_lshl_b32 s22, s22, 2\n"
    "s_add_u32 s71, s71, s22\n"
    "v_mfma_f32_16x16x32_bf16 v[224:227], v[56:59], v[112:115], v[224:227]\n"
    "s_lshl_b32 s71, s71, 10\n"
    "s_add_u32 s71, s71, 0x4800000\n"
    "v_mfma_f32_16x16x32_bf16 v[228:231], v[56:59], v[116:119], v[228:231]\n"
    "s_cmp_eq_u32 s59, 1\n"
    "s_cselect_b32 s71, 0x7ff00000, s71\n"
    "s_cselect_b32 s13, 0x7ff00000, s13\n"
    "v_mfma_f32_16x16x32_bf16 v[232:235], v[56:59], v[120:123], v[232:235]\n"
    "v_mfma_f32_16x16x32_bf16 v[236:239], v[56:59], v[124:127], v[236:239]\n"
    "v_mfma_f32_16x16x32_bf16 v[240:243], v[60:63], v[112:115], v[240:243]\n"
    "v_mfma_f32_16x16x32_bf16 v[244:247], v[60:63], v[116:119], v[244:247]\n"
    "v_mfma_f32_16x16x32_bf16 v[248:251], v[60:63], v[120:123], v[248:251]\n"
    "v_mfma_f32_16x16x32_bf16 v[252:255], v[60:63], v[124:127], v[252:255]\n"
    "s_waitcnt lgkmcnt(0)\n"
    "v_mfma_f32_16x16x32_bf16 v[128:131], v[64:67], v[96:99], v[128:131]\n"
    "ds_read_b128 v[112:115], v13\n"
    "v_mfma_f32_16x16x32_bf16 v[132:135], v[64:67], v[100:103], v[132:135]\n"
    "ds_read_b128 v[116:119], v13 offset:2048\n"
    "v_mfma_f32_16x16x32_bf16 v[136:139], v[64:67], v[104:107], v[136:139]\n"
    "ds_read_b128 v[120:123], v13 offset:4096\n"
    "v_mfma_f32_16x16x32_bf16 v[140:143], v[64:67], v[108:111], v[140:143]\n"
    "ds_read_b128 v[124:127], v13 offset:6144\n"
    "v_mfma_f32_16x16x32_bf16 v[144:147], v[68:71], v[96:99], v[144:147]\n"
    "ds_read_b64_tr_b16 v[32:33], v14 offset:8192\n"
    "v_mfma_f32_16x16x32_bf16 v[148:151], v[68:71], v[100:103], v[148:151]\n"
    "ds_read_b64_tr_b16 v[34:35], v14 offset:9216\n"
    "v_mfma_f32_16x16x32_bf16 v[152:155], v[68:71], v[104:107], v[152:155]\n"
    "ds_read_b64_tr_b16 v[36:37], v15 offset:8192\n"
    "v_mfma_f32_16x16x32_bf16 v[156:159], v[68:71], v[108:111], v[156:159]\n"
    "ds_read_b64_tr_b16 v[38:39], v15 offset:9216\n"
    "v_mfma_f32_16x16x32_bf16 v[160:163], v[72:75], v[96:99], v[160:163]\n"
    "ds_read_b64_tr_b16 v[40:41], v14 offset:8448\n"
    "v_mfma_f32_16x16x32_bf16 v[164:167], v[72:75], v[100:103], v[164:167]\n"
    "ds_read_b64_tr_b16 v[42:43], v14 offset:9472\n"
    "v_mfma_f32_16x16x32_bf16 v[168:171], v[72:75], v[104:107], v[168:171]\n"
    "ds_read_b64_tr_b16 v[44:45], v15 offset:8448\n"
    "v_mfma_f32_16x16x32_bf16 v[172:175], v[72:75], v[108:111], v[172:175]\n"
    "ds_read_b64_tr_b16 v[46:47], v15 offset:9472\n"
    "v_mfma_f32_16x16x32_bf16 v[176:179], v[76:79], v[96:99], v[176:179]\n"
    "ds_read_b64_tr_b16 v[48:49], v14 offset:8704\n"
    "v_mfma_f32_16x16x32_bf16 v[180:183], v[76:79], v[100:103], v[180:183]\n"
    "ds_read_b64_tr_b16 v[50:51], v14 offset:9728\n"
    "v_mfma_f32_16x16x32_bf16 v[184:187], v[76:79], v[104:107], v[184:187]\n"
    "ds_read_b64_tr_b16 v[52:53], v15 offset:8704\n"
    "v_mfma_f32_16x16x32_bf16 v[188:191], v[76:79], v[108:111], v[188:191]\n"
    "ds_read_b64_tr_b16 v[54:55], v15 offset:9728\n"
    "v_mfma_f32_16x16x32_bf16 v[192:195], v[80:83], v[96:99], v[192:195]\n"
    "ds_read_b64_tr_b16 v[56:57], v14 offset:8960\n"
    "v_mfma_f32_16x16x32_bf16 v[196:199], v[80:83], v[100:103], v[196:199]\n"
    "ds_read_b64_tr_b16 v[58:59], v14 offset:9984\n"
    "v_mfma_f32_16x16x32_bf16 v[200:203], v[80:83], v[104:107], v[200:203]\n"
    "ds_read_b64_tr_b16 v[60:61], v15 offset:8960\n"
    "v_mfma_f32_16x16x32_bf16 v[204:207], v[80:83], v[108:111], v[204:207]\n"
    "ds_read_b64_tr_b16 v[62:63], v15 offset:9984\n"
    "v_mfma_f32_16x16x32_bf16 v[208:211], v[84:87], v[96:99], v[208:211]\n"
    "v_mfma_f32_16x16x32_bf16 v[212:215], v[84:87], v[100:103], v[212:215]\n"
    "v_mfma_f32_16x16x32_bf16 v[216:219], v[84:87], v[104:107], v[216:219]\n"
    "v_mfma_f32_16x16x32_bf16 v[220:223], v[84:87], v[108:111], v[220:223]\n"
    "v_mfma_f32_16x16x32_bf16 v[224:227], v[88:91], v[96:99], v[224:227]\n"
    "v_mfma_f32_16x16x32_bf16 v[228:231], v[88:91], v[100:103], v[228:231]\n"
    "v_mfma_f32_16x16x32_bf16 v[232:235], v[88:91], v[104:107], v[232:235]\n"
    "v_mfma_f32_16x16x32_bf16 v[236:239], v[88:91], v[108:111], v[236:239]\n"
    "v_mfma_f32_16x16x32_bf16 v[240:243], v[92:95], v[96:99], v[240:243]\n"
    "v_mfma_f32_16x16x32_bf16 v[244:247], v[92:95], v[100:103], v[244:247]\n"
    "v_mfma_f32_16x16x32_bf16 v[248:251], v[92:95], v[104:107], v[248:251]\n"
    "v_mfma_f32_16x16x32_bf16 v[252:255], v[92:95], v[108:111], v[252:255]\n"
    "s_waitcnt lgkmcnt(0)\n"
    "s_waitcnt vmcnt(0)\n"
    "s_barrier\n"
    "s_lshl_b32 s22, s20, 4\n"
    "s_add_u32 s22, s22, s21\n"
    "s_lshl_b32 s22, s22, 3\n"
    "s_add_u32 s22, s22, 0x400\n"
    "s_cmp_eq_u32 s19, 0\n"
    "s_cselect_b32 s22, s22, 0x7ff00000\n"
    "v_mov_b32 v16, s4\n"
    "v_mov_b32 v17, s5\n"
    "s_mov_b64 exec, 1\n"
    "buffer_store_dwordx2 v[16:17], v2, s[44:47], s22 offen sc1\n"
    "s_mov_b64 exec, -1\n"
    "v_mfma_f32_16x16x32_bf16 v[128:131], v[32:35], v[112:115], v[128:131]\n"
    "s_lshl_b32 s22, s20, 20\n"
    "s_add_u32 s22, s22, 0x100000\n"
    "v_mfma_f32_16x16x32_bf16 v[132:135], v[32:35], v[116:119], v[132:135]\n"
    "s_add_u32 s36, s96, s22\n"
    "s_addc_u32 s37, s97, 0\n"
    "v_mfma_f32_16x16x32_bf16 v[136:139], v[32:35], v[120:123], v[136:139]\n"
    "s_mov_b32 s38, 0x88000\n"
    "s_mov_b64 s[40:41], s[36:37]\n"
    "v_mfma_f32_16x16x32_bf16 v[140:143], v[32:35], v[124:127], v[140:143]\n"
    "s_sub_u32 s22, 32, s70\n"
    "s_lshl_b32 s42, s22, 15\n"
    "v_mfma_f32_16x16x32_bf16 v[144:147], v[36:39], v[112:115], v[144:147]\n"
    "s_mov_b32 s49, 0\n"
    "s_and_b32 s22, s19, 1\n"
    "v_mfma_f32_16x16x32_bf16 v[148:151], v[36:39], v[116:119], v[148:151]\n"
    "s_add_u32 s22, s22, s70\n"
    "v_mfma_f32_16x16x32_bf16 v[152:155], v[36:39], v[120:123], v[152:155]\n"
    "s_cmp_lt_u32 s19, 2\n"
    "s_cselect_b32 s50, 16, s22\n"
    "s_cselect_b32 s52, 1, 0\n"
    "v_mfma_f32_16x16x32_bf16 v[156:159], v[36:39], v[124:127], v[156:159]\n"
    "s_sub_u32 s63, 15, s22\n"
    "v_mfma_f32_16x16x32_bf16 v[160:163], v[40:43], v[112:115], v[160:163]\n"
    "s_cmp_eq_u32 s68, s50\n"
    "s_cselect_b32 s51, 1, 0\n"
    "v_mfma_f32_16x16x32_bf16 v[164:167], v[40:43], v[116:119], v[164:167]\n"
    "s_add_u32 s54, s16, 0x10000\n"
    "s_add_u32 s55, s16, 0xc000\n"
    "v_mfma_f32_16x16x32_bf16 v[168:171], v[40:43], v[120:123], v[168:171]\n"
    "s_add_u32 s60, s16, 0xf8000\n"
    "s_add_u32 s61, s16, 0x104000\n"
    "v_mfma_f32_16x16x32_bf16 v[172:175], v[40:43], v[124:127], v[172:175]\n"
    "s_mov_b32 s62, s16\n"
    "s_lshl_b32 s11, s19, 9\n"
    "v_mfma_f32_16x16x32_bf16 v[176:179], v[44:47], v[112:115], v[176:179]\n"
    "s_add_u32 s11, s11, 0x3000\n"
    "s_lshl_b32 s12, s20, 7\n"
    "v_mfma_f32_16x16x32_bf16 v[180:183], v[44:47], v[116:119], v[180:183]\n"
    "s_add_u32 s12, s12, 0x400\n"
    "s_and_b32 s8, s18, 7\n"
    "v_mfma_f32_16x16x32_bf16 v[184:187], v[44:47], v[120:123], v[184:187]\n"
    "s_lshr_b32 s9, s18, 3\n"
    "s_and_b32 s23, s8, 1\n"
    "v_mfma_f32_16x16x32_bf16 v[188:191], v[44:47], v[124:127], v[188:191]\n"
    "s_lshl_b32 s34, s23, 11\n"
    "s_lshl_b32 s23, s23, 5\n"
    "v_mfma_f32_16x16x32_bf16 v[192:195], v[48:51], v[112:115], v[192:195]\n"
    "s_add_u32 s23, s23, s9\n"
    "s_mul_i32 s10, s23, 0xe000\n"
    "v_mfma_f32_16x16x32_bf16 v[196:199], v[48:51], v[116:119], v[196:199]\n"
    "s_add_u32 s10, s10, 0x4800000\n"
    "s_lshr_b32 s23, s8, 1\n"
    "v_mfma_f32_16x16x32_bf16 v[200:203], v[48:51], v[120:123], v[200:203]\n"
    "s_lshl_b32 s23, s23, 5\n"
    "s_add_u32 s23, s23, s9\n"
    "v_mfma_f32_16x16x32_bf16 v[204:207], v[48:51], v[124:127], v[204:207]\n"
    "s_lshl_b32 s23, s23, 16\n"
    "s_add_u32 s23, s23, s34\n"
    "v_mfma_f32_16x16x32_bf16 v[208:211], v[52:55], v[112:115], v[208:211]\n"
    "s_mov_b32 s26, 0xe000\n"
    "v_mfma_f32_16x16x32_bf16 v[212:215], v[52:55], v[116:119], v[212:215]\n"
    "s_cmp_eq_u32 s19, 1\n"
    "s_cselect_b32 s10, s23, s10\n"
    "s_cselect_b64 s[24:25], s[86:87], s[96:97]\n"
    "s_cselect_b32 s26, 0xf800, s26\n"
    "s_cselect_b32 s35, 14, 13\n"
    "v_mfma_f32_16x16x32_bf16 v[216:219], v[52:55], v[120:123], v[216:219]\n"
    "s_lshl_b32 s6, 15, s35\n"
    "s_lshl_b32 s7, -1, s35\n"
    "v_mfma_f32_16x16x32_bf16 v[220:223], v[52:55], v[124:127], v[220:223]\n"
    "s_add_u32 s24, s24, s10\n"
    "s_addc_u32 s25, s25, 0\n"
    "v_mfma_f32_16x16x32_bf16 v[224:227], v[56:59], v[112:115], v[224:227]\n"
    "v_mfma_f32_16x16x32_bf16 v[228:231], v[56:59], v[116:119], v[228:231]\n"
    "v_mfma_f32_16x16x32_bf16 v[232:235], v[56:59], v[120:123], v[232:235]\n"
    "v_mfma_f32_16x16x32_bf16 v[236:239], v[56:59], v[124:127], v[236:239]\n"
    "v_mfma_f32_16x16x32_bf16 v[240:243], v[60:63], v[112:115], v[240:243]\n"
    "v_mfma_f32_16x16x32_bf16 v[244:247], v[60:63], v[116:119], v[244:247]\n"
    "v_mfma_f32_16x16x32_bf16 v[248:251], v[60:63], v[120:123], v[248:251]\n"
    "v_mfma_f32_16x16x32_bf16 v[252:255], v[60:63], v[124:127], v[252:255]\n"
    "s_waitcnt lgkmcnt(0)\n"
    "v_mov_b32 v4, 0x3e0293ee\n"
    "v_mov_b32 v5, 0x3e0293ee\n"
    "v_pk_mul_f32 v[128:129], v[128:129], v[4:5]\n"
    "v_pk_mul_f32 v[130:131], v[130:131], v[4:5]\n"
    "v_pk_mul_f32 v[144:145], v[144:145], v[4:5]\n"
    "v_pk_mul_f32 v[146:147], v[146:147], v[4:5]\n"
    "v_cvt_pk_bf16_f32 v64, v128, v129\n"
    "v_cvt_pk_bf16_f32 v65, v130, v131\n"
    "v_cvt_pk_bf16_f32 v66, v144, v145\n"
    "v_cvt_pk_bf16_f32 v67, v146, v147\n"
    "v_pk_mul_f32 v[132:133], v[132:133], v[4:5]\n"
    "v_pk_mul_f32 v[134:135], v[134:135], v[4:5]\n"
    "v_pk_mul_f32 v[148:149], v[148:149], v[4:5]\n"
    "v_pk_mul_f32 v[150:151], v[150:151], v[4:5]\n"
    "v_cvt_pk_bf16_f32 v80, v132, v133\n"
    "v_cvt_pk_bf16_f32 v81, v134, v135\n"
    "v_cvt_pk_bf16_f32 v82, v148, v149\n"
    "v_cvt_pk_bf16_f32 v83, v150, v151\n"
    "v_pk_mul_f32 v[136:137], v[136:137], v[4:5]\n"
    "v_pk_mul_f32 v[138:139], v[138:139], v[4:5]\n"
    "v_pk_mul_f32 v[152:153], v[152:153], v[4:5]\n"
    "v_pk_mul_f32 v[154:155], v[154:155], v[4:5]\n"
    "v_cvt_pk_bf16_f32 v96, v136, v137\n"
    "v_cvt_pk_bf16_f32 v97, v138, v139\n"
    "v_cvt_pk_bf16_f32 v98, v152, v153\n"
    "v_cvt_pk_bf16_f32 v99, v154, v155\n"
    "v_pk_mul_f32 v[140:141], v[140:141], v[4:5]\n"
    "v_pk_mul_f32 v[142:143], v[142:143], v[4:5]\n"
    "v_pk_mul_f32 v[156:157], v[156:157], v[4:5]\n"
    "v_pk_mul_f32 v[158:159], v[158:159], v[4:5]\n"
    "v_cvt_pk_bf16_f32 v112, v140, v141\n"
    "v_cvt_pk_bf16_f32 v113, v142, v143\n"
    "v_cvt_pk_bf16_f32 v114, v156, v157\n"
    "v_cvt_pk_bf16_f32 v115, v158, v159\n"
    "v_pk_mul_f32 v[160:161], v[160:161], v[4:5]\n"
    "v_pk_mul_f32 v[162:163], v[162:163], v[4:5]\n"
    "v_pk_mul_f32 v[176:177], v[176:177], v[4:5]\n"
    "v_pk_mul_f32 v[178:179], v[178:179], v[4:5]\n"
    "v_cvt_pk_bf16_f32 v68, v160, v161\n"
    "v_cvt_pk_bf16_f32 v69, v162, v163\n"
    "v_cvt_pk_bf16_f32 v70, v176, v177\n"
    "v_cvt_pk_bf16_f32 v71, v178, v179\n"
    "v_pk_mul_f32 v[164:165], v[164:165], v[4:5]\n"
    "v_pk_mul_f32 v[166:167], v[166:167], v[4:5]\n"
    "v_pk_mul_f32 v[180:181], v[180:181], v[4:5]\n"
    "v_pk_mul_f32 v[182:183], v[182:183], v[4:5]\n"
    "v_cvt_pk_bf16_f32 v84, v164, v165\n"
    "v_cvt_pk_bf16_f32 v85, v166, v167\n"
    "v_cvt_pk_bf16_f32 v86, v180, v181\n"
    "v_cvt_pk_bf16_f32 v87, v182, v183\n"
    "v_pk_mul_f32 v[168:169], v[168:169], v[4:5]\n"
    "v_pk_mul_f32 v[170:171], v[170:171], v[4:5]\n"
    "v_pk_mul_f32 v[184:185], v[184:185], v[4:5]\n"
    "v_pk_mul_f32 v[186:187], v[186:187], v[4:5]\n"
    "v_cvt_pk_bf16_f32 v100, v168, v169\n"
    "v_cvt_pk_bf16_f32 v101, v170, v171\n"
    "v_cvt_pk_bf16_f32 v102, v184, v185\n"
    "v_cvt_pk_bf16_f32 v103, v186, v187\n"
    "v_pk_mul_f32 v[172:173], v[172:173], v[4:5]\n"
    "v_pk_mul_f32 v[174:175], v[174:175], v[4:5]\n"
    "v_pk_mul_f32 v[188:189], v[188:189], v[4:5]\n"
    "v_pk_mul_f32 v[190:191], v[190:191], v[4:5]\n"
    "v_cvt_pk_bf16_f32 v116, v172, v173\n"
    "v_cvt_pk_bf16_f32 v117, v174, v175\n"
    "v_cvt_pk_bf16_f32 v118, v188, v189\n"
    "v_cvt_pk_bf16_f32 v119, v190, v191\n"
    "v_pk_mul_f32 v[192:193], v[192:193], v[4:5]\n"
    "v_pk_mul_f32 v[194:195], v[194:195], v[4:5]\n"
    "v_pk_mul_f32 v[208:209], v[208:209], v[4:5]\n"
    "v_pk_mul_f32 v[210:211], v[210:211], v[4:5]\n"
    "v_cvt_pk_bf16_f32 v72, v192, v193\n"
    "v_cvt_pk_bf16_f32 v73, v194, v195\n"
    "v_cvt_pk_bf16_f32 v74, v208, v209\n"
    "v_cvt_pk_bf16_f32 v75, v210, v211\n"
    "v_pk_mul_f32 v[196:197], v[196:197], v[4:5]\n"
    "v_pk_mul_f32 v[198:199], v[198:199], v[4:5]\n"
    "v_pk_mul_f32 v[212:213], v[212:213], v[4:5]\n"
    "v_pk_mul_f32 v[214:215], v[214:215], v[4:5]\n"
    "v_cvt_pk_bf16_f32 v88, v196, v197\n"
    "v_cvt_pk_bf16_f32 v89, v198, v199\n"
    "v_cvt_pk_bf16_f32 v90, v212, v213\n"
    "v_cvt_pk_bf16_f32 v91, v214, v215\n"
    "v_pk_mul_f32 v[200:201], v[200:201], v[4:5]\n"
    "v_pk_mul_f32 v[202:203], v[202:203], v[4:5]\n"
    "v_pk_mul_f32 v[216:217], v[216:217], v[4:5]\n"
    "v_pk_mul_f32 v[218:219], v[218:219], v[4:5]\n"
    "v_cvt_pk_bf16_f32 v104, v200, v201\n"
    "v_cvt_pk_bf16_f32 v105, v202, v203\n"
    "v_cvt_pk_bf16_f32 v106, v216, v217\n"
    "v_cvt_pk_bf16_f32 v107, v218, v219\n"
    "v_pk_mul_f32 v[204:205], v[204:205], v[4:5]\n"
    "v_pk_mul_f32 v[206:207], v[206:207], v[4:5]\n"
    "v_pk_mul_f32 v[220:221], v[220:221], v[4:5]\n"
    "v_pk_mul_f32 v[222:223], v[222:223], v[4:5]\n"
    "v_cvt_pk_bf16_f32 v120, v204, v205\n"
    "v_cvt_pk_bf16_f32 v121, v206, v207\n"
    "v_cvt_pk_bf16_f32 v122, v220, v221\n"
    "v_cvt_pk_bf16_f32 v123, v222, v223\n"
    "v_pk_mul_f32 v[224:225], v[224:225], v[4:5]\n"
    "v_pk_mul_f32 v[226:227], v[226:227], v[4:5]\n"
    "v_pk_mul_f32 v[240:241], v[240:241], v[4:5]\n"
    "v_pk_mul_f32 v[242:243], v[242:243], v[4:5]\n"
    "v_cvt_pk_bf16_f32 v76, v224, v225\n"
    "v_cvt_pk_bf16_f32 v77, v226, v227\n"
    "v_cvt_pk_bf16_f32 v78, v240, v241\n"
    "v_cvt_pk_bf16_f32 v79, v242, v243\n"
    "v_pk_mul_f32 v[228:229], v[228:229], v[4:5]\n"
    "v_pk_mul_f32 v[230:231], v[230:231], v[4:5]\n"
    "v_pk_mul_f32 v[244:245], v[244:245], v[4:5]\n"
    "v_pk_mul_f32 v[246:247], v[246:247], v[4:5]\n"
    "v_cvt_pk_bf16_f32 v92, v228, v229\n"
    "v_cvt_pk_bf16_f32 v93, v230, v231\n"
    "v_cvt_pk_bf16_f32 v94, v244, v245\n"
    "v_cvt_pk_bf16_f32 v95, v246, v247\n"
    "v_pk_mul_f32 v[232:233], v[232:233], v[4:5]\n"
    "v_pk_mul_f32 v[234:235], v[234:235], v[4:5]\n"
    "v_pk_mul_f32 v[248:249], v[248:249], v[4:5]\n"
    "v_pk_mul_f32 v[250:251], v[250:251], v[4:5]\n"
    "v_cvt_pk_bf16_f32 v108, v232, v233\n"
    "v_cvt_pk_bf16_f32 v109, v234, v235\n"
    "v_cvt_pk_bf16_f32 v110, v248, v249\n"
    "v_cvt_pk_bf16_f32 v111, v250, v251\n"
    "v_pk_mul_f32 v[236:237], v[236:237], v[4:5]\n"
    "v_pk_mul_f32 v[238:239], v[238:239], v[4:5]\n"
    "v_pk_mul_f32 v[252:253], v[252:253], v[4:5]\n"
    "v_pk_mul_f32 v[254:255], v[254:255], v[4:5]\n"
    "v_cvt_pk_bf16_f32 v124, v236, v237\n"
    "v_cvt_pk_bf16_f32 v125, v238, v239\n"
    "v_cvt_pk_bf16_f32 v126, v252, v253\n"
    "v_cvt_pk_bf16_f32 v127, v254, v255\n"
    "v_min_u32 v10, 63, v1\n"
    "v_mul_u32_u24 v10, 8, v10\n"
    "buffer_load_dwordx2 v[8:9], v10, s[44:47], s11 offen sc1\n"
    "v_min_u32 v14, 7, v1\n"
    "v_mul_u32_u24 v14, 8, v14\n"
    "buffer_load_dwordx2 v[12:13], v14, s[44:47], s12 offen sc1\n"
    "s_and_b32 s22, s19, 1\n"
    "s_lshl_b32 s22, s22, 14\n"
    "s_add_u32 s22, s22, 0x20000\n"
    "v_add_u32 v7, s22, v2\n"
    "s_cmp_ge_u32 s19, 2\n"
    "s_cbranch_scc1 NOQS_%=\n"
    "ds_write_b128 v7, v[64:67]\n"
    "ds_write_b128 v7, v[68:71] offset:1024\n"
    "ds_write_b128 v7, v[72:75] offset:2048\n"
    "ds_write_b128 v7, v[76:79] offset:3072\n"
    "ds_write_b128 v7, v[80:83] offset:4096\n"
    "ds_write_b128 v7, v[84:87] offset:5120\n"
    "ds_write_b128 v7, v[88:91] offset:6144\n"
    "ds_write_b128 v7, v[92:95] offset:7168\n"
    "s_waitcnt lgkmcnt(4)\n"
    "ds_write_b128 v7, v[96:99] offset:8192\n"
    "ds_write_b128 v7, v[100:103] offset:9216\n"
    "ds_write_b128 v7, v[104:107] offset:10240\n"
    "ds_write_b128 v7, v[108:111] offset:11264\n"
    "ds_write_b128 v7, v[112:115] offset:12288\n"
    "ds_write_b128 v7, v[116:119] offset:13312\n"
    "ds_write_b128 v7, v[120:123] offset:14336\n"
    "ds_write_b128 v7, v[124:127] offset:15360\n"
    "NOQS_%=:\n"
    "s_cmp_ge_u32 s19, 2\n"
    "s_cselect_b32 s26, 0, s26\n"
    "v_mov_b32 v244, 0x3f803f80\n"
    "v_mov_b32 v245, 0x3f803f80\n"
    "v_mov_b32 v246, 0x3f803f80\n"
    "v_mov_b32 v247, 0x3f803f80\n"
    "v_and_b32 v4, 15, v1\n"
    "v_lshrrev_b32 v5, 4, v1\n"
    "v_lshlrev_b32 v19, 7, v4\n"
    "v_lshl_add_u32 v6, v5, 12, v19\n"
    "v_lshl_add_u32 v19, v5, 11, v19\n"
    "s_cmp_eq_u32 s19, 1\n"
    "s_cselect_b64 vcc, -1, 0\n"
    "v_cndmask_b32 v19, v19, v6, vcc\n"
    "v_lshlrev_b32 v5, 2, v5\n"
    "v_sub_u32 v4, v4, v5\n"
    "v_mov_b32 v6, 0xff800000\n"
    "v_cmp_gt_i32 vcc, 0, v4\n"
    "v_mov_b32 v60, 0\n"
    "v_mov_b32 v52, 0\n"
    "v_cndmask_b32 v252, 0, v6, vcc\n"
    "v_cmp_gt_i32 vcc, 1, v4\n"
    "v_mov_b32 v61, 0\n"
    "v_mov_b32 v53, 0\n"
    "v_cndmask_b32 v253, 0, v6, vcc\n"
    "v_cmp_gt_i32 vcc, 2, v4\n"
    "v_mov_b32 v62, 0\n"
    "v_mov_b32 v54, 0\n"
    "v_cndmask_b32 v254, 0, v6, vcc\n"
    "v_cmp_gt_i32 vcc, 3, v4\n"
    "v_mov_b32 v63, 0\n"
    "v_mov_b32 v55, 0\n"
    "v_cndmask_b32 v255, 0, v6, vcc\n"
    "s_waitcnt vmcnt(0)\n"
    "v_cmp_ne_u64 vcc, s[4:5], v[12:13]\n"
    "s_nop 0\n"
    "s_cbranch_vccnz PS2_%=\n"
    "PR2_%=:\n"
    "s_add_u32 m0, s16, 0x18000\n"
    "s_nop 0\n"
    "buffer_load_dwordx4 v2, s[36:39], s16 offen sc1 lds\n"
    "v_mfma_f32_16x16x32_bf16 a[64:67], v[60:63], v[60:63], 0\n"
    "buffer_load_dwordx4 v2, s[36:39], s16 offen offset:1024 sc1 lds\n"
    "v_mfma_f32_16x16x32_bf16 a[68:71], v[60:63], v[60:63], 0\n"
    "buffer_load_dwordx4 v2, s[36:39], s16 offen offset:2048 sc1 lds\n"
    "v_mfma_f32_16x16x32_bf16 a[72:75], v[60:63], v[60:63], 0\n"
    "buffer_load_dwordx4 v2, s[36:39], s16 offen offset:3072 sc1 lds\n"
    "v_mfma_f32_16x16x32_bf16 a[76:79], v[60:63], v[60:63], 0\n"
    "s_add_u32 s22, s16, 0x4000\n"
    "s_add_u32 m0, s16, 0xc000\n"
    "s_nop 0\n"
    "buffer_load_dwordx4 v2, s[36:39], s22 offen sc1 lds\n"
    "v_mfma_f32_16x16x32_bf16 a[80:83], v[60:63], v[60:63], 0\n"
    "buffer_load_dwordx4 v2, s[36:39], s22 offen offset:1024 sc1 lds\n"
    "v_mfma_f32_16x16x32_bf16 a[84:87], v[60:63], v[60:63], 0\n"
    "buffer_load_dwordx4 v2, s[36:39], s22 offen offset:2048 sc1 lds\n"
    "v_mfma_f32_16x16x32_bf16 a[88:91], v[60:63], v[60:63], 0\n"
    "buffer_load_dwordx4 v2, s[36:39], s22 offen offset:3072 sc1 lds\n"
    "v_mfma_f32_16x16x32_bf16 a[92:95], v[60:63], v[60:63], 0\n"
    "s_add_u32 s22, s16, 0x8000\n"
    "s_add_u32 m0, s16, 0x8000\n"
    "s_nop 0\n"
    "buffer_load_dwordx4 v2, s[36:39], s22 offen sc1 lds\n"
    "v_mfma_f32_16x16x32_bf16 a[96:99], v[60:63], v[60:63], 0\n"
    "buffer_load_dwordx4 v2, s[36:39], s22 offen offset:1024 sc1 lds\n"
    "v_mfma_f32_16x16x32_bf16 a[100:103], v[60:63], v[60:63], 0\n"
    "buffer_load_dwordx4 v2, s[36:39], s22 offen offset:2048 sc1 lds\n"
    "v_mfma_f32_16x16x32_bf16 a[104:107], v[60:63], v[60:63], 0\n"
    "buffer_load_dwordx4 v2, s[36:39], s22 offen offset:3072 sc1 lds\n"
    "v_mfma_f32_16x16x32_bf16 a[108:111], v[60:63], v[60:63], 0\n"
    "v_mfma_f32_16x16x32_bf16 a[112:115], v[60:63], v[60:63], 0\n"
    "v_mfma_f32_16x16x32_bf16 a[116:119], v[60:63], v[60:63], 0\n"
    "v_mfma_f32_16x16x32_bf16 a[120:123], v[60:63], v[60:63], 0\n"
    "v_mfma_f32_16x16x32_bf16 a[124:127], v[60:63], v[60:63], 0\n"
    "v_add_u32 v3, 0x18000, v2\n"
    "v_cmp_ne_u64 vcc, s[4:5], v[8:9]\n"
    "s_nop 0\n"
    "s_cbranch_vccnz PS4_%=\n"
    "PR4_%=:\n"
    "s_cmp_lg_u32 s68, 0\n"
    "s_cbranch_scc1 NOM0_%=\n"
    "v_mov_b32 v60, v252\n"
    "v_mov_b32 v61, v253\n"
    "v_mov_b32 v62, v254\n"
    "v_mov_b32 v63, v255\n"
    "v_mov_b32 v52, 0xff800000\n"
    "v_mov_b32 v53, 0xff800000\n"
    "v_mov_b32 v54, 0xff800000\n"
    "v_mov_b32 v55, 0xff800000\n"
    "NOM0_%=:\n"
    "s_waitcnt vmcnt(0) lgkmcnt(0)\n"
    "s_barrier\n"
    "ds_read_b128 a[128:131], v3\n"
    "ds_read_b128 a[132:135], v3 offset:1024\n"
    "ds_read_b128 a[136:139], v3 offset:2048\n"
    "ds_read_b128 a[140:143], v3 offset:3072\n"
    "ds_read_b128 a[144:147], v3 offset:4096\n"
    "ds_read_b128 a[148:151], v3 offset:5120\n"
    "ds_read_b128 a[152:155], v3 offset:6144\n"
    "ds_read_b128 a[156:159], v3 offset:7168\n"
    "ds_read_b128 a[160:163], v3 offset:8192\n"
    "ds_read_b128 a[164:167], v3 offset:9216\n"
    "s_waitcnt lgkmcnt(4)\n"
    "ds_read_b128 a[168:171], v3 offset:10240\n"
    "ds_read_b128 a[172:175], v3 offset:11264\n"
    "ds_read_b128 a[176:179], v3 offset:12288\n"
    "ds_read_b128 a[180:183], v3 offset:13312\n"
    "ds_read_b128 a[184:187], v3 offset:14336\n"
    "ds_read_b128 a[188:191], v3 offset:15360\n"
    "v_add_u32 v3, 0x8000, v2\n"
    "s_waitcnt lgkmcnt(0)\n"
    "s_mov_b32 s31, 0xc0\n"
    "FIRST_%=:\n"
    "v_mfma_f32_16x16x32_bf16 v[128:131], a[128:131], v[64:67], v[60:63]\n"
    "s_add_u32 m0, s62, 0\n"
    "s_nop 0\n"
    "buffer_load_dwordx4 v2, s[36:39], s54 offen sc1 lds\n"
    "ds_read_b128 a[192:195], v3 offset:16384\n"
    "v_mfma_f32_16x16x32_bf16 v[128:131], a[132:135], v[68:71], v[128:131]\n"
    "ds_read_b128 a[224:227], v3 offset:24576\n"
    "v_mfma_f32_16x16x32_bf16 v[128:131], a[136:139], v[72:75], v[128:131]\n"
    "ds_read_b128 a[196:199], v3 offset:17408\n"
    "v_mfma_f32_16x16x32_bf16 v[128:131], a[140:143], v[76:79], v[128:131]\n"
    "ds_read_b128 a[228:231], v3 offset:25600\n"
    "v_mfma_f32_16x16x32_bf16 v[144:147], a[128:131], v[80:83], 0\n"
    "buffer_load_dwordx4 v2, s[36:39], s54 offen offset:1024 sc1 lds\n"
    "ds_read_b128 a[200:203], v3 offset:18432\n"
    "v_mfma_f32_16x16x32_bf16 v[144:147], a[132:135], v[84:87], v[144:147]\n"
    "ds_read_b128 a[232:235], v3 offset:26624\n"
    "v_mfma_f32_16x16x32_bf16 v[144:147], a[136:139], v[88:91], v[144:147]\n"
    "ds_read_b128 a[204:207], v3 offset:19456\n"
    "v_mfma_f32_16x16x32_bf16 v[144:147], a[140:143], v[92:95], v[144:147]\n"
    "ds_read_b128 a[236:239], v3 offset:27648\n"
    "v_mfma_f32_16x16x32_bf16 v[132:135], a[144:147], v[64:67], v[52:55]\n"
    "ds_read_b128 a[208:211], v3 offset:20480\n"
    "v_mfma_f32_16x16x32_bf16 v[132:135], a[148:151], v[68:71], v[132:135]\n"
    "buffer_load_dwordx4 v2, s[36:39], s54 offen offset:2048 sc1 lds\n"
    "ds_read_b128 a[240:243], v3 offset:28672\n"
    "v_mfma_f32_16x16x32_bf16 v[132:135], a[152:155], v[72:75], v[132:135]\n"
    "ds_read_b128 a[212:215], v3 offset:21504\n"
    "v_mfma_f32_16x16x32_bf16 v[132:135], a[156:159], v[76:79], v[132:135]\n"
    "ds_read_b128 a[244:247], v3 offset:29696\n"
    "v_mfma_f32_16x16x32_bf16 v[148:151], a[144:147], v[80:83], v[60:63]\n"
    "ds_read_b128 a[216:219], v3 offset:22528\n"
    "v_mfma_f32_16x16x32_bf16 v[148:151], a[148:151], v[84:87], v[148:151]\n"
    "ds_read_b128 a[248:251], v3 offset:30720\n"
    "v_mfma_f32_16x16x32_bf16 v[148:151], a[152:155], v[88:91], v[148:151]\n"
    "buffer_load_dwordx4 v2, s[36:39], s54 offen offset:3072 sc1 lds\n"
    "s_add_u32 s54, s54, 0x8000\n"
    "s_add_u32 m0, s62, 0x4000\n"
    "ds_read_b128 a[220:223], v3 offset:23552\n"
    "v_mfma_f32_16x16x32_bf16 v[148:151], a[156:159], v[92:95], v[148:151]\n"
    "ds_read_b128 a[252:255], v3 offset:31744\n"
    "v_mfma_f32_16x16x32_bf16 v[136:139], a[160:163], v[64:67], v[52:55]\n"
    "v_mfma_f32_16x16x32_bf16 v[136:139], a[164:167], v[68:71], v[136:139]\n"
    "v_mfma_f32_16x16x32_bf16 v[136:139], a[168:171], v[72:75], v[136:139]\n"
    "buffer_load_dwordx4 v2, s[36:39], s55 offen sc1 lds\n"
    "v_mfma_f32_16x16x32_bf16 v[136:139], a[172:175], v[76:79], v[136:139]\n"
    "v_mfma_f32_16x16x32_bf16 v[152:155], a[160:163], v[80:83], v[52:55]\n"
    "v_mfma_f32_16x16x32_bf16 v[152:155], a[164:167], v[84:87], v[152:155]\n"
    "v_mfma_f32_16x16x32_bf16 v[152:155], a[168:171], v[88:91], v[152:155]\n"
    "v_mfma_f32_16x16x32_bf16 v[152:155], a[172:175], v[92:95], v[152:155]\n"
    "buffer_load_dwordx4 v2, s[36:39], s55 offen offset:1024 sc1 lds\n"
    "v_mfma_f32_16x16x32_bf16 v[140:143], a[176:179], v[64:67], v[52:55]\n"
    "v_mfma_f32_16x16x32_bf16 v[140:143], a[180:183], v[68:71], v[140:143]\n"
    "v_mfma_f32_16x16x32_bf16 v[140:143], a[184:187], v[72:75], v[140:143]\n"
    "v_mfma_f32_16x16x32_bf16 v[140:143], a[188:191], v[76:79], v[140:143]\n"
    "v_mfma_f32_16x16x32_bf16 v[156:159], a[176:179], v[80:83], v[52:55]\n"
    "buffer_load_dwordx4 v2, s[36:39], s55 offen offset:2048 sc1 lds\n"
    "v_mfma_f32_16x16x32_bf16 v[156:159], a[180:183], v[84:87], v[156:159]\n"
    "v_mfma_f32_16x16x32_bf16 v[156:159], a[184:187], v[88:91], v[156:159]\n"
    "v_mfma_f32_16x16x32_bf16 v[156:159], a[188:191], v[92:95], v[156:159]\n"
    "v_mfma_f32_16x16x32_bf16 v[160:163], a[128:131], v[96:99], 0\n"
    "s_nop 0\n"
    "s_nop 0\n"
    "v_mfma_f32_16x16x32_bf16 v[160:163], a[132:135], v[100:103], v[160:163]\n"
    "buffer_load_dwordx4 v2, s[36:39], s55 offen offset:3072 sc1 lds\n"
    "s_add_u32 s55, s55, 0x8000\n"
    "s_add_u32 m0, s62, 0x10000\n"
    "s_nop 0\n"
    "s_nop 0\n"
    "v_mfma_f32_16x16x32_bf16 v[160:163], a[136:139], v[104:107], v[160:163]\n"
    "s_nop 0\n"
    "s_nop 0\n"
    "v_mfma_f32_16x16x32_bf16 v[160:163], a[140:143], v[108:111], v[160:163]\n"
    "v_max3_f32 v5, v128, v129, v130\n"
    "v_max3_f32 v6, v144, v145, v146\n"
    "v_mfma_f32_16x16x32_bf16 v[176:179], a[128:131], v[112:115], 0\n"
    "v_max3_f32 v5, v5, v131, v132\n"
    "v_max3_f32 v6, v6, v147, v148\n"
    "v_mfma_f32_16x16x32_bf16 v[176:179], a[132:135], v[116:119], v[176:179]\n"
    "buffer_load_dwordx4 v2, s[40:43], s60 offen sc1 lds\n"
    "v_max3_f32 v5, v5, v133, v134\n"
    "v_max3_f32 v6, v6, v149, v150\n"
    "v_mfma_f32_16x16x32_bf16 v[176:179], a[136:139], v[120:123], v[176:179]\n"
    "v_max3_f32 v5, v5, v135, v136\n"
    "v_max3_f32 v6, v6, v151, v152\n"
    "v_mfma_f32_16x16x32_bf16 v[176:179], a[140:143], v[124:127], v[176:179]\n"
    "v_max3_f32 v5, v5, v137, v138\n"
    "v_max3_f32 v6, v6, v153, v154\n"
    "v_mfma_f32_16x16x32_bf16 v[164:167], a[144:147], v[96:99], 0\n"
    "v_max3_f32 v5, v5, v139, v140\n"
    "v_max3_f32 v6, v6, v155, v156\n"
    "v_mfma_f32_16x16x32_bf16 v[164:167], a[148:151], v[100:103], v[164:167]\n"
    "buffer_load_dwordx4 v2, s[40:43], s60 offen offset:1024 sc1 lds\n"
    "v_max3_f32 v5, v5, v141, v142\n"
    "v_max3_f32 v6, v6, v157, v158\n"
    "v_mfma_f32_16x16x32_bf16 v[164:167], a[152:155], v[104:107], v[164:167]\n"
    "v_max_f32 v5, v5, v143\n"
    "v_max_f32 v6, v6, v159\n"
    "v_mfma_f32_16x16x32_bf16 v[164:167], a[156:159], v[108:111], v[164:167]\n"
    "v_mov_b32 v9, v5\n"
    "v_mov_b32 v10, v6\n"
    "v_mfma_f32_16x16x32_bf16 v[180:183], a[144:147], v[112:115], 0\n"
    "s_nop 0\n"
    "v_permlane32_swap_b32 v5, v9\n"
    "v_mfma_f32_16x16x32_bf16 v[180:183], a[148:151], v[116:119], v[180:183]\n"
    "buffer_load_dwordx4 v2, s[40:43], s60 offen offset:2048 sc1 lds\n"
    "v_permlane32_swap_b32 v6, v10\n"
    "v_max_f32 v5, v5, v9\n"
    "v_mfma_f32_16x16x32_bf16 v[180:183], a[152:155], v[120:123], v[180:183]\n"
    "v_max_f32 v6, v6, v10\n"
    "v_mov_b32 v9, v5\n"
    "v_mfma_f32_16x16x32_bf16 v[180:183], a[156:159], v[124:127], v[180:183]\n"
    "v_mov_b32 v10, v6\n"
    "s_nop 0\n"
    "v_mfma_f32_16x16x32_bf16 v[168:171], a[160:163], v[96:99], v[60:63]\n"
    "v_permlane16_swap_b32 v5, v9\n"
    "v_permlane16_swap_b32 v6, v10\n"
    "v_mfma_f32_16x16x32_bf16 v[168:171], a[164:167], v[100:103], v[168:171]\n"
    "buffer_load_dwordx4 v2, s[40:43], s60 offen offset:3072 sc1 lds\n"
    "s_sub_u32 s60, s60, 0x8000\n"
    "s_add_u32 m0, s62, 0x14000\n"
    "v_max_f32 v5, v5, v9\n"
    "v_max_f32 v6, v6, v10\n"
    "v_mfma_f32_16x16x32_bf16 v[168:171], a[168:171], v[104:107], v[168:171]\n"
    "v_sub_f32 v128, v128, v5\n"
    "v_sub_f32 v129, v129, v5\n"
    "v_mfma_f32_16x16x32_bf16 v[168:171], a[172:175], v[108:111], v[168:171]\n"
    "v_sub_f32 v130, v130, v5\n"
    "v_sub_f32 v131, v131, v5\n"
    "v_mfma_f32_16x16x32_bf16 v[184:187], a[160:163], v[112:115], 0\n"
    "v_sub_f32 v144, v144, v6\n"
    "v_sub_f32 v145, v145, v6\n"
    "v_mfma_f32_16x16x32_bf16 v[184:187], a[164:167], v[116:119], v[184:187]\n"
    "buffer_load_dwordx4 v2, s[40:43], s61 offen sc1 lds\n"
    "v_sub_f32 v146, v146, v6\n"
    "v_sub_f32 v147, v147, v6\n"
    "v_mfma_f32_16x16x32_bf16 v[184:187], a[168:171], v[120:123], v[184:187]\n"
    "v_sub_f32 v132, v132, v5\n"
    "v_sub_f32 v133, v133, v5\n"
    "v_mfma_f32_16x16x32_bf16 v[184:187], a[172:175], v[124:127], v[184:187]\n"
    "v_sub_f32 v134, v134, v5\n"
    "v_sub_f32 v135, v135, v5\n"
    "v_mfma_f32_16x16x32_bf16 v[172:175], a[176:179], v[96:99], v[52:55]\n"
    "v_sub_f32 v148, v148, v6\n"
    "v_sub_f32 v149, v149, v6\n"
    "v_mfma_f32_16x16x32_bf16 v[172:175], a[180:183], v[100:103], v[172:175]\n"
    "buffer_load_dwordx4 v2, s[40:43], s61 offen offset:1024 sc1 lds\n"
    "v_sub_f32 v150, v150, v6\n"
    "v_sub_f32 v151, v151, v6\n"
    "v_mfma_f32_16x16x32_bf16 v[172:175], a[184:187], v[104:107], v[172:175]\n"
    "v_sub_f32 v136, v136, v5\n"
    "v_sub_f32 v137, v137, v5\n"
    "v_mfma_f32_16x16x32_bf16 v[172:175], a[188:191], v[108:111], v[172:175]\n"
    "v_sub_f32 v138, v138, v5\n"
    "v_sub_f32 v139, v139, v5\n"
    "v_mfma_f32_16x16x32_bf16 v[188:191], a[176:179], v[112:115], v[60:63]\n"
    "v_sub_f32 v152, v152, v6\n"
    "v_sub_f32 v153, v153, v6\n"
    "v_mfma_f32_16x16x32_bf16 v[188:191], a[180:183], v[116:119], v[188:191]\n"
    "buffer_load_dwordx4 v2, s[40:43], s61 offen offset:2048 sc1 lds\n"
    "v_sub_f32 v154, v154, v6\n"
    "v_sub_f32 v155, v155, v6\n"
    "v_mfma_f32_16x16x32_bf16 v[188:191], a[184:187], v[120:123], v[188:191]\n"
    "v_sub_f32 v140, v140, v5\n"
    "v_sub_f32 v141, v141, v5\n"
    "v_mfma_f32_16x16x32_bf16 v[188:191], a[188:191], v[124:127], v[188:191]\n"
    "v_sub_f32 v142, v142, v5\n"
    "v_sub_f32 v143, v143, v5\n"
    "v_sub_f32 v156, v156, v6\n"
    "v_sub_f32 v157, v157, v6\n"
    "v_sub_f32 v158, v158, v6\n"
    "v_sub_f32 v159, v159, v6\n"
    "v_exp_f32 v248, v128\n"
    "buffer_load_dwordx4 v2, s[40:43], s61 offen offset:3072 sc1 lds\n"
    "s_sub_u32 s61, s61, 0x8000\n"
    "s_xor_b32 s62, s62, 0x8000\n"
    "v_exp_f32 v249, v129\n"
    "v_exp_f32 v250, v130\n"
    "v_cvt_pk_bf16_f32 v192, v248, v249\n"
    "v_exp_f32 v251, v131\n"
    "v_exp_f32 v248, v144\n"
    "v_cvt_pk_bf16_f32 v193, v250, v251\n"
    "v_exp_f32 v249, v145\n"
    "v_exp_f32 v250, v146\n"
    "v_cvt_pk_bf16_f32 v200, v248, v249\n"
    "v_exp_f32 v251, v147\n"
    "v_exp_f32 v248, v132\n"
    "v_cvt_pk_bf16_f32 v201, v250, v251\n"
    "v_exp_f32 v249, v133\n"
    "v_exp_f32 v250, v134\n"
    "v_cvt_pk_bf16_f32 v194, v248, v249\n"
    "v_exp_f32 v251, v135\n"
    "v_exp_f32 v248, v148\n"
    "v_cvt_pk_bf16_f32 v195, v250, v251\n"
    "v_exp_f32 v249, v149\n"
    "v_exp_f32 v250, v150\n"
    "v_cvt_pk_bf16_f32 v202, v248, v249\n"
    "v_exp_f32 v251, v151\n"
    "v_exp_f32 v248, v136\n"
    "v_cvt_pk_bf16_f32 v203, v250, v251\n"
    "v_exp_f32 v249, v137\n"
    "v_exp_f32 v250, v138\n"
    "v_cvt_pk_bf16_f32 v196, v248, v249\n"
    "v_exp_f32 v251, v139\n"
    "v_exp_f32 v248, v152\n"
    "v_cvt_pk_bf16_f32 v197, v250, v251\n"
    "v_exp_f32 v249, v153\n"
    "v_exp_f32 v250, v154\n"
    "v_cvt_pk_bf16_f32 v204, v248, v249\n"
    "v_exp_f32 v251, v155\n"
    "v_exp_f32 v248, v140\n"
    "v_cvt_pk_bf16_f32 v205, v250, v251\n"
    "v_exp_f32 v249, v141\n"
    "v_exp_f32 v250, v142\n"
    "v_cvt_pk_bf16_f32 v198, v248, v249\n"
    "v_exp_f32 v251, v143\n"
    "v_exp_f32 v248, v156\n"
    "v_cvt_pk_bf16_f32 v199, v250, v251\n"
    "v_exp_f32 v249, v157\n"
    "v_exp_f32 v250, v158\n"
    "v_cvt_pk_bf16_f32 v206, v248, v249\n"
    "v_exp_f32 v251, v159\n"
    "s_nop 0\n"
    "v_cvt_pk_bf16_f32 v207, v250, v251\n"
    "v_sub_f32 v20, 0, v5\n"
    "v_sub_f32 v21, 0, v5\n"
    "v_sub_f32 v22, 0, v5\n"
    "v_sub_f32 v23, 0, v5\n"
    "v_mfma_f32_16x16x32_bf16 v[224:227], v[244:247], v[192:195], 0\n"
    "v_mfma_f32_16x16x32_bf16 v[224:227], v[244:247], v[196:199], v[224:227]\n"
    "v_mfma_f32_16x16x32_bf16 v[228:231], v[244:247], v[200:203], 0\n"
    "v_mfma_f32_16x16x32_bf16 v[228:231], v[244:247], v[204:207], v[228:231]\n"
    "s_waitcnt lgkmcnt(0)\n"
    "v_mfma_f32_16x16x32_bf16 a[0:3], a[192:195], v[192:195], 0\n"
    "ds_read_b128 a[128:131], v3\n"
    "v_sub_f32 v24, 0, v5\n"
    "v_sub_f32 v25, 0, v5\n"
    "v_sub_f32 v26, 0, v5\n"
    "v_mfma_f32_16x16x32_bf16 a[0:3], a[224:227], v[196:199], a[0:3]\n"
    "ds_read_b128 a[132:135], v3 offset:1024\n"
    "v_sub_f32 v27, 0, v5\n"
    "v_sub_f32 v32, 0, v6\n"
    "v_sub_f32 v33, 0, v6\n"
    "v_mfma_f32_16x16x32_bf16 a[32:35], a[192:195], v[200:203], 0\n"
    "ds_read_b128 a[136:139], v3 offset:2048\n"
    "v_sub_f32 v34, 0, v6\n"
    "v_sub_f32 v35, 0, v6\n"
    "v_sub_f32 v36, 0, v6\n"
    "v_mfma_f32_16x16x32_bf16 a[32:35], a[224:227], v[204:207], a[32:35]\n"
    "ds_read_b128 a[140:143], v3 offset:3072\n"
    "v_sub_f32 v37, 0, v6\n"
    "v_sub_f32 v38, 0, v6\n"
    "v_sub_f32 v39, 0, v6\n"
    "v_mfma_f32_16x16x32_bf16 a[4:7], a[196:199], v[192:195], 0\n"
    "ds_read_b128 a[144:147], v3 offset:4096\n"
    "v_sub_f32 v40, 0, v6\n"
    "v_sub_f32 v41, 0, v6\n"
    "v_sub_f32 v42, 0, v6\n"
    "v_mfma_f32_16x16x32_bf16 a[4:7], a[228:231], v[196:199], a[4:7]\n"
    "ds_read_b128 a[148:151], v3 offset:5120\n"
    "v_sub_f32 v43, 0, v6\n"
    "v_max3_f32 v5, v160, v161, v162\n"
    "v_max3_f32 v6, v176, v177, v178\n"
    "v_mfma_f32_16x16x32_bf16 a[36:39], a[196:199], v[200:203], 0\n"
    "ds_read_b128 a[152:155], v3 offset:6144\n"
    "v_max3_f32 v5, v5, v163, v164\n"
    "v_max3_f32 v6, v6, v179, v180\n"
    "v_max3_f32 v5, v5, v165, v166\n"
    "v_mfma_f32_16x16x32_bf16 a[36:39], a[228:231], v[204:207], a[36:39]\n"
    "ds_read_b128 a[156:159], v3 offset:7168\n"
    "v_max3_f32 v6, v6, v181, v182\n"
    "v_max3_f32 v5, v5, v167, v168\n"
    "v_max3_f32 v6, v6, v183, v184\n"
    "v_mfma_f32_16x16x32_bf16 a[8:11], a[200:203], v[192:195], 0\n"
    "ds_read_b128 a[160:163], v3 offset:8192\n"
    "v_max3_f32 v5, v5, v169, v170\n"
    "v_max3_f32 v6, v6, v185, v186\n"
    "v_max3_f32 v5, v5, v171, v172\n"
    "v_mfma_f32_16x16x32_bf16 a[8:11], a[232:235], v[196:199], a[8:11]\n"
    "ds_read_b128 a[164:167], v3 offset:9216\n"
    "v_max3_f32 v6, v6, v187, v188\n"
    "v_max3_f32 v5, v5, v173, v174\n"
    "v_max3_f32 v6, v6, v189, v190\n"
    "v_mfma_f32_16x16x32_bf16 a[40:43], a[200:203], v[200:203], 0\n"
    "ds_read_b128 a[168:171], v3 offset:10240\n"
    "v_max_f32 v5, v5, v175\n"
    "v_max_f32 v6, v6, v191\n"
    "v_mov_b32 v9, v5\n"
    "v_mfma_f32_16x16x32_bf16 a[40:43], a[232:235], v[204:207], a[40:43]\n"
    "ds_read_b128 a[172:175], v3 offset:11264\n"
    "v_mov_b32 v10, v6\n"
    "s_nop 0\n"
    "v_permlane32_swap_b32 v5, v9\n"
    "v_mfma_f32_16x16x32_bf16 a[12:15], a[204:207], v[192:195], 0\n"
    "ds_read_b128 a[176:179], v3 offset:12288\n"
    "v_permlane32_swap_b32 v6, v10\n"
    "v_max_f32 v5, v5, v9\n"
    "v_max_f32 v6, v6, v10\n"
    "v_mfma_f32_16x16x32_bf16 a[12:15], a[236:239], v[196:199], a[12:15]\n"
    "ds_read_b128 a[180:183], v3 offset:13312\n"
    "v_mov_b32 v9, v5\n"
    "v_mov_b32 v10, v6\n"
    "s_nop 0\n"
    "v_mfma_f32_16x16x32_bf16 a[44:47], a[204:207], v[200:203], 0\n"
    "ds_read_b128 a[184:187], v3 offset:14336\n"
    "v_permlane16_swap_b32 v5, v9\n"
    "v_permlane16_swap_b32 v6, v10\n"
    "v_max_f32 v5, v5, v9\n"
    "v_mfma_f32_16x16x32_bf16 a[44:47], a[236:239], v[204:207], a[44:47]\n"
    "ds_read_b128 a[188:191], v3 offset:15360\n"
    "v_max_f32 v6, v6, v10\n"
    "v_sub_f32 v160, v160, v5\n"
    "v_sub_f32 v161, v161, v5\n"
    "v_mfma_f32_16x16x32_bf16 a[16:19], a[208:211], v[192:195], 0\n"
    "v_sub_f32 v162, v162, v5\n"
    "v_sub_f32 v163, v163, v5\n"
    "v_sub_f32 v176, v176, v6\n"
    "v_sub_f32 v177, v177, v6\n"
    "v_mfma_f32_16x16x32_bf16 a[16:19], a[240:243], v[196:199], a[16:19]\n"
    "v_sub_f32 v178, v178, v6\n"
    "v_sub_f32 v179, v179, v6\n"
    "v_sub_f32 v164, v164, v5\n"
    "v_sub_f32 v165, v165, v5\n"
    "v_mfma_f32_16x16x32_bf16 a[48:51], a[208:211], v[200:203], 0\n"
    "v_sub_f32 v166, v166, v5\n"
    "v_sub_f32 v167, v167, v5\n"
    "v_sub_f32 v180, v180, v6\n"
    "v_sub_f32 v181, v181, v6\n"
    "v_mfma_f32_16x16x32_bf16 a[48:51], a[240:243], v[204:207], a[48:51]\n"
    "v_sub_f32 v182, v182, v6\n"
    "v_sub_f32 v183, v183, v6\n"
    "v_sub_f32 v168, v168, v5\n"
    "v_sub_f32 v169, v169, v5\n"
    "v_mfma_f32_16x16x32_bf16 a[20:23], a[212:215], v[192:195], 0\n"
    "v_sub_f32 v170, v170, v5\n"
    "v_sub_f32 v171, v171, v5\n"
    "v_sub_f32 v184, v184, v6\n"
    "v_sub_f32 v185, v185, v6\n"
    "v_mfma_f32_16x16x32_bf16 a[20:23], a[244:247], v[196:199], a[20:23]\n"
    "v_sub_f32 v186, v186, v6\n"
    "v_sub_f32 v187, v187, v6\n"
    "v_sub_f32 v172, v172, v5\n"
    "v_sub_f32 v173, v173, v5\n"
    "v_mfma_f32_16x16x32_bf16 a[52:55], a[212:215], v[200:203], 0\n"
    "v_sub_f32 v174, v174, v5\n"
    "v_sub_f32 v175, v175, v5\n"
    "v_sub_f32 v188, v188, v6\n"
    "v_sub_f32 v189, v189, v6\n"
    "v_mfma_f32_16x16x32_bf16 a[52:55], a[244:247], v[204:207], a[52:55]\n"
    "v_sub_f32 v190, v190, v6\n"
    "v_sub_f32 v191, v191, v6\n"
    "v_exp_f32 v248, v160\n"
    "v_mfma_f32_16x16x32_bf16 a[24:27], a[216:219], v[192:195], 0\n"
    "v_exp_f32 v249, v161\n"
    "v_exp_f32 v250, v162\n"
    "v_mfma_f32_16x16x32_bf16 a[24:27], a[248:251], v[196:199], a[24:27]\n"
    "v_cvt_pk_bf16_f32 v208, v248, v249\n"
    "v_exp_f32 v251, v163\n"
    "v_mfma_f32_16x16x32_bf16 a[56:59], a[216:219], v[200:203], 0\n"
    "v_exp_f32 v248, v176\n"
    "v_cvt_pk_bf16_f32 v209, v250, v251\n"
    "v_mfma_f32_16x16x32_bf16 a[56:59], a[248:251], v[204:207], a[56:59]\n"
    "v_exp_f32 v249, v177\n"
    "v_exp_f32 v250, v178\n"
    "v_mfma_f32_16x16x32_bf16 a[28:31], a[220:223], v[192:195], 0\n"
    "v_cvt_pk_bf16_f32 v216, v248, v249\n"
    "v_exp_f32 v251, v179\n"
    "v_mfma_f32_16x16x32_bf16 a[28:31], a[252:255], v[196:199], a[28:31]\n"
    "v_exp_f32 v248, v164\n"
    "v_cvt_pk_bf16_f32 v217, v250, v251\n"
    "v_mfma_f32_16x16x32_bf16 a[60:63], a[220:223], v[200:203], 0\n"
    "v_exp_f32 v249, v165\n"
    "v_exp_f32 v250, v166\n"
    "v_mfma_f32_16x16x32_bf16 a[60:63], a[252:255], v[204:207], a[60:63]\n"
    "v_cvt_pk_bf16_f32 v210, v248, v249\n"
    "v_exp_f32 v251, v167\n"
    "v_exp_f32 v248, v180\n"
    "v_cvt_pk_bf16_f32 v211, v250, v251\n"
    "v_exp_f32 v249, v181\n"
    "v_exp_f32 v250, v182\n"
    "v_cvt_pk_bf16_f32 v218, v248, v249\n"
    "v_exp_f32 v251, v183\n"
    "v_exp_f32 v248, v168\n"
    "v_cvt_pk_bf16_f32 v219, v250, v251\n"
    "v_exp_f32 v249, v169\n"
    "s_nop 0\n"
    "v_cvt_pk_bf16_f32 v212, v248, v249\n"
    "v_sub_f32 v44, 0, v5\n"
    "v_sub_f32 v45, 0, v5\n"
    "v_sub_f32 v46, 0, v5\n"
    "v_sub_f32 v47, 0, v5\n"
    "v_sub_f32 v48, 0, v5\n"
    "v_sub_f32 v49, 0, v5\n"
    "v_sub_f32 v50, 0, v5\n"
    "v_sub_f32 v51, 0, v5\n"
    "v_sub_f32 v52, 0, v5\n"
    "v_sub_f32 v53, 0, v5\n"
    "v_sub_f32 v54, 0, v5\n"
    "v_sub_f32 v55, 0, v5\n"
    "v_sub_f32 v56, 0, v6\n"
    "v_sub_f32 v57, 0, v6\n"
    "v_sub_f32 v58, 0, v6\n"
    "v_sub_f32 v59, 0, v6\n"
    "v_sub_f32 v60, 0, v6\n"
    "v_sub_f32 v61, 0, v6\n"
    "v_sub_f32 v62, 0, v6\n"
    "v_sub_f32 v63, 0, v6\n"
    "v_mov_b32 v240, v224\n"
    "v_mov_b32 v241, v228\n"
    "v_mov_b32 v242, 0\n"
    "v_mov_b32 v243, 0\n"
    "v_xor_b32 v3, 0x8000, v3\n"
    "s_waitcnt vmcnt(0) lgkmcnt(0)\n"
    "s_barrier\n"
    "s_cmp_eq_u32 s49, 0\n"
    "s_cbranch_scc1 NOSIG_%=\n"
    "v_mov_b32 v8, s4\n"
    "v_mov_b32 v9, s5\n"
    "v_mov_b32 v10, s4\n"
    "v_mov_b32 v11, s5\n"
    "s_mov_b64 exec, 1\n"
    "buffer_store_dwordx4 v[8:11], v2, s[44:47], s49 offen sc1\n"
    "s_mov_b64 exec, -1\n"
    "s_mov_b32 s49, 0\n"
    "NOSIG_%=:\n"
    "LOOP_%=:\n"
    "s_cmp_eq_u32 s50, 0\n"
    "s_cbranch_scc1 ENDP_%=\n"
    "s_cmp_eq_u32 s50, s51\n"
    "s_cbranch_scc1 MASK_%=\n"
    "STEP_%=:\n"
    "v_mfma_f32_16x16x32_bf16 v[128:131], a[128:131], v[64:67], v[20:23]\n"
    "v_exp_f32 v250, v170\n"
    "v_mfma_f32_16x16x32_bf16 v[128:131], a[132:135], v[68:71], v[128:131]\n"
    "s_add_u32 m0, s62, 0\n"
    "v_mfma_f32_16x16x32_bf16 v[128:131], a[136:139], v[72:75], v[128:131]\n"
    "v_exp_f32 v251, v171\n"
    "v_mfma_f32_16x16x32_bf16 v[128:131], a[140:143], v[76:79], v[128:131]\n"
    "v_cvt_pk_bf16_f32 v213, v250, v251\n"
    "v_mfma_f32_16x16x32_bf16 v[144:147], a[128:131], v[80:83], v[32:35]\n"
    "v_exp_f32 v248, v184\n"
    "v_mfma_f32_16x16x32_bf16 v[144:147], a[132:135], v[84:87], v[144:147]\n"
    "buffer_load_dwordx4 v2, s[36:39], s54 offen sc1 lds\n"
    "v_mfma_f32_16x16x32_bf16 v[144:147], a[136:139], v[88:91], v[144:147]\n"
    "v_exp_f32 v249, v185\n"
    "v_mfma_f32_16x16x32_bf16 v[144:147], a[140:143], v[92:95], v[144:147]\n"
    "v_cvt_pk_bf16_f32 v220, v248, v249\n"
    "v_mfma_f32_16x16x32_bf16 v[132:135], a[144:147], v[64:67], v[24:27]\n"
    "v_exp_f32 v250, v186\n"
    "v_mfma_f32_16x16x32_bf16 v[132:135], a[148:151], v[68:71], v[132:135]\n"
    "buffer_load_dwordx4 v2, s[36:39], s54 offen offset:1024 sc1 lds\n"
    "v_mfma_f32_16x16x32_bf16 v[132:135], a[152:155], v[72:75], v[132:135]\n"
    "v_exp_f32 v251, v187\n"
    "v_mfma_f32_16x16x32_bf16 v[132:135], a[156:159], v[76:79], v[132:135]\n"
    "v_cvt_pk_bf16_f32 v221, v250, v251\n"
    "v_mfma_f32_16x16x32_bf16 v[148:151], a[144:147], v[80:83], v[36:39]\n"
    "v_exp_f32 v248, v172\n"
    "v_mfma_f32_16x16x32_bf16 v[148:151], a[148:151], v[84:87], v[148:151]\n"
    "buffer_load_dwordx4 v2, s[36:39], s54 offen offset:2048 sc1 lds\n"
    "v_mfma_f32_16x16x32_bf16 v[148:151], a[152:155], v[88:91], v[148:151]\n"
    "v_exp_f32 v249, v173\n"
    "v_mfma_f32_16x16x32_bf16 v[148:151], a[156:159], v[92:95], v[148:151]\n"
    "v_cvt_pk_bf16_f32 v214, v248, v249\n"
    "v_mfma_f32_16x16x32_bf16 v[136:139], a[160:163], v[64:67], v[24:27]\n"
    "v_exp_f32 v250, v174\n"
    "v_mfma_f32_16x16x32_bf16 v[136:139], a[164:167], v[68:71], v[136:139]\n"
    "buffer_load_dwordx4 v2, s[36:39], s54 offen offset:3072 sc1 lds\n"
    "v_mfma_f32_16x16x32_bf16 v[136:139], a[168:171], v[72:75], v[136:139]\n"
    "v_exp_f32 v251, v175\n"
    "v_mfma_f32_16x16x32_bf16 v[136:139], a[172:175], v[76:79], v[136:139]\n"
    "v_cvt_pk_bf16_f32 v215, v250, v251\n"
    "s_add_u32 m0, s62, 0x4000\n"
    "v_mfma_f32_16x16x32_bf16 v[152:155], a[160:163], v[80:83], v[40:43]\n"
    "v_exp_f32 v248, v188\n"
    "v_mfma_f32_16x16x32_bf16 v[152:155], a[164:167], v[84:87], v[152:155]\n"
    "buffer_load_dwordx4 v2, s[36:39], s55 offen sc1 lds\n"
    "v_mfma_f32_16x16x32_bf16 v[152:155], a[168:171], v[88:91], v[152:155]\n"
    "v_exp_f32 v249, v189\n"
    "v_mfma_f32_16x16x32_bf16 v[152:155], a[172:175], v[92:95], v[152:155]\n"
    "v_cvt_pk_bf16_f32 v222, v248, v249\n"
    "s_add_u32 s54, s54, 0x8000\n"
    "v_mfma_f32_16x16x32_bf16 v[140:143], a[176:179], v[64:67], v[24:27]\n"
    "v_exp_f32 v250, v190\n"
    "v_mfma_f32_16x16x32_bf16 v[140:143], a[180:183], v[68:71], v[140:143]\n"
    "v_exp_f32 v251, v191\n"
    "v_mfma_f32_16x16x32_bf16 v[232:235], v[244:247], v[208:211], 0\n"
    "v_cvt_pk_bf16_f32 v223, v250, v251\n"
    "v_mfma_f32_16x16x32_bf16 v[232:235], v[244:247], v[212:215], v[232:235]\n"
    "buffer_load_dwordx4 v2, s[36:39], s55 offen offset:1024 sc1 lds\n"
    "v_mfma_f32_16x16x32_bf16 v[236:239], v[244:247], v[216:219], 0\n"
    "v_mfma_f32_16x16x32_bf16 v[236:239], v[244:247], v[220:223], v[236:239]\n"
    "buffer_load_dwordx4 v2, s[36:39], s55 offen offset:2048 sc1 lds\n"
    "v_mfma_f32_16x16x32_bf16 v[140:143], a[184:187], v[72:75], v[140:143]\n"
    "s_sub_u32 s50, s50, 1\n"
    "s_nop 0\n"
    "v_mfma_f32_16x16x32_bf16 v[140:143], a[188:191], v[76:79], v[140:143]\n"
    "buffer_load_dwordx4 v2, s[36:39], s55 offen offset:3072 sc1 lds\n"
    "v_mfma_f32_16x16x32_bf16 v[156:159], a[176:179], v[80:83], v[40:43]\n"
    "v_exp_f32 v248, v128\n"
    "v_mfma_f32_16x16x32_bf16 v[156:159], a[180:183], v[84:87], v[156:159]\n"
    "v_max_f32 v4, v232, v236\n"
    "v_cmp_lt_f32 vcc, 0x49800000, v4\n"
    "v_mfma_f32_16x16x32_bf16 v[156:159], a[184:187], v[88:91], v[156:159]\n"
    "v_exp_f32 v249, v129\n"
    "v_mfma_f32_16x16x32_bf16 v[156:159], a[188:191], v[92:95], v[156:159]\n"
    "v_cvt_pk_bf16_f32 v192, v248, v249\n"
    "s_cbranch_vccnz SCA_%=\n"
    "SRA_%=:\n"
    "v_mfma_f32_16x16x32_bf16 a[64:67], a[192:195], v[208:211], a[64:67]\n"
    "v_exp_f32 v250, v130\n"
    "v_mfma_f32_16x16x32_bf16 a[64:67], a[224:227], v[212:215], a[64:67]\n"
    "s_add_u32 m0, s62, 0x10000\n"
    "s_add_u32 s55, s55, 0x8000\n"
    "v_mfma_f32_16x16x32_bf16 a[96:99], a[192:195], v[216:219], a[96:99]\n"
    "v_exp_f32 v251, v131\n"
    "v_mfma_f32_16x16x32_bf16 a[96:99], a[224:227], v[220:223], a[96:99]\n"
    "ds_read_b128 a[192:195], v3 offset:16384\n"
    "v_cvt_pk_bf16_f32 v193, v250, v251\n"
    "v_mfma_f32_16x16x32_bf16 a[68:71], a[196:199], v[208:211], a[68:71]\n"
    "v_exp_f32 v248, v144\n"
    "v_mfma_f32_16x16x32_bf16 a[68:71], a[228:231], v[212:215], a[68:71]\n"
    "buffer_load_dwordx4 v2, s[40:43], s60 offen sc1 lds\n"
    "v_mfma_f32_16x16x32_bf16 a[100:103], a[196:199], v[216:219], a[100:103]\n"
    "v_exp_f32 v249, v145\n"
    "v_mfma_f32_16x16x32_bf16 a[100:103], a[228:231], v[220:223], a[100:103]\n"
    "ds_read_b128 a[224:227], v3 offset:24576\n"
    "v_cvt_pk_bf16_f32 v200, v248, v249\n"
    "v_mfma_f32_16x16x32_bf16 a[72:75], a[200:203], v[208:211], a[72:75]\n"
    "v_exp_f32 v250, v146\n"
    "v_mfma_f32_16x16x32_bf16 a[72:75], a[232:235], v[212:215], a[72:75]\n"
    "buffer_load_dwordx4 v2, s[40:43], s60 offen offset:1024 sc1 lds\n"
    "v_mfma_f32_16x16x32_bf16 a[104:107], a[200:203], v[216:219], a[104:107]\n"
    "v_exp_f32 v251, v147\n"
    "v_mfma_f32_16x16x32_bf16 a[104:107], a[232:235], v[220:223], a[104:107]\n"
    "ds_read_b128 a[196:199], v3 offset:17408\n"
    "v_cvt_pk_bf16_f32 v201, v250, v251\n"
    "v_mfma_f32_16x16x32_bf16 a[76:79], a[204:207], v[208:211], a[76:79]\n"
    "v_exp_f32 v248, v132\n"
    "v_mfma_f32_16x16x32_bf16 a[76:79], a[236:239], v[212:215], a[76:79]\n"
    "buffer_load_dwordx4 v2, s[40:43], s60 offen offset:2048 sc1 lds\n"
    "v_mfma_f32_16x16x32_bf16 a[108:111], a[204:207], v[216:219], a[108:111]\n"
    "v_exp_f32 v249, v133\n"
    "v_mfma_f32_16x16x32_bf16 a[108:111], a[236:239], v[220:223], a[108:111]\n"
    "ds_read_b128 a[228:231], v3 offset:25600\n"
    "v_cvt_pk_bf16_f32 v194, v248, v249\n"
    "v_mfma_f32_16x16x32_bf16 a[80:83], a[208:211], v[208:211], a[80:83]\n"
    "v_exp_f32 v250, v134\n"
    "v_mfma_f32_16x16x32_bf16 a[80:83], a[240:243], v[212:215], a[80:83]\n"
    "buffer_load_dwordx4 v2, s[40:43], s60 offen offset:3072 sc1 lds\n"
    "v_mfma_f32_16x16x32_bf16 a[112:115], a[208:211], v[216:219], a[112:115]\n"
    "v_exp_f32 v251, v135\n"
    "v_mfma_f32_16x16x32_bf16 a[112:115], a[240:243], v[220:223], a[112:115]\n"
    "v_cvt_pk_bf16_f32 v195, v250, v251\n"
    "s_add_u32 m0, s62, 0x14000\n"
    "v_mfma_f32_16x16x32_bf16 a[84:87], a[212:215], v[208:211], a[84:87]\n"
    "v_exp_f32 v248, v148\n"
    "v_mfma_f32_16x16x32_bf16 a[84:87], a[244:247], v[212:215], a[84:87]\n"
    "buffer_load_dwordx4 v2, s[40:43], s61 offen sc1 lds\n"
    "v_mfma_f32_16x16x32_bf16 a[116:119], a[212:215], v[216:219], a[116:119]\n"
    "v_exp_f32 v249, v149\n"
    "v_mfma_f32_16x16x32_bf16 a[116:119], a[244:247], v[220:223], a[116:119]\n"
    "ds_read_b128 a[200:203], v3 offset:18432\n"
    "v_cvt_pk_bf16_f32 v202, v248, v249\n"
    "v_mfma_f32_16x16x32_bf16 a[88:91], a[216:219], v[208:211], a[88:91]\n"
    "v_exp_f32 v250, v150\n"
    "v_mfma_f32_16x16x32_bf16 a[88:91], a[248:251], v[212:215], a[88:91]\n"
    "buffer_load_dwordx4 v2, s[40:43], s61 offen offset:1024 sc1 lds\n"
    "v_mfma_f32_16x16x32_bf16 a[120:123], a[216:219], v[216:219], a[120:123]\n"
    "v_exp_f32 v251, v151\n"
    "v_mfma_f32_16x16x32_bf16 a[120:123], a[248:251], v[220:223], a[120:123]\n"
    "ds_read_b128 a[232:235], v3 offset:26624\n"
    "v_cvt_pk_bf16_f32 v203, v250, v251\n"
    "v_mfma_f32_16x16x32_bf16 a[92:95], a[220:223], v[208:211], a[92:95]\n"
    "v_exp_f32 v248, v136\n"
    "v_mfma_f32_16x16x32_bf16 a[92:95], a[252:255], v[212:215], a[92:95]\n"
    "buffer_load_dwordx4 v2, s[40:43], s61 offen offset:2048 sc1 lds\n"
    "v_mfma_f32_16x16x32_bf16 a[124:127], a[220:223], v[216:219], a[124:127]\n"
    "v_exp_f32 v249, v137\n"
    "v_mfma_f32_16x16x32_bf16 a[124:127], a[252:255], v[220:223], a[124:127]\n"
    "ds_read_b128 a[204:207], v3 offset:19456\n"
    "v_cvt_pk_bf16_f32 v196, v248, v249\n"
    "v_mfma_f32_16x16x32_bf16 v[160:163], a[128:131], v[96:99], v[44:47]\n"
    "v_exp_f32 v250, v138\n"
    "v_mfma_f32_16x16x32_bf16 v[160:163], a[132:135], v[100:103], v[160:163]\n"
    "buffer_load_dwordx4 v2, s[40:43], s61 offen offset:3072 sc1 lds\n"
    "v_mfma_f32_16x16x32_bf16 v[160:163], a[136:139], v[104:107], v[160:163]\n"
    "v_exp_f32 v251, v139\n"
    "v_mfma_f32_16x16x32_bf16 v[160:163], a[140:143], v[108:111], v[160:163]\n"
    "ds_read_b128 a[236:239], v3 offset:27648\n"
    "v_cvt_pk_bf16_f32 v197, v250, v251\n"
    "v_mfma_f32_16x16x32_bf16 v[176:179], a[128:131], v[112:115], v[56:59]\n"
    "v_exp_f32 v248, v152\n"
    "v_mfma_f32_16x16x32_bf16 v[176:179], a[132:135], v[116:119], v[176:179]\n"
    "buffer_load_dword v29, v19, s[24:27], s6 offen sc1\n"
    "v_mfma_f32_16x16x32_bf16 v[176:179], a[136:139], v[120:123], v[176:179]\n"
    "v_exp_f32 v249, v153\n"
    "v_mfma_f32_16x16x32_bf16 v[176:179], a[140:143], v[124:127], v[176:179]\n"
    "ds_read_b128 a[208:211], v3 offset:20480\n"
    "v_cvt_pk_bf16_f32 v204, v248, v249\n"
    "v_mfma_f32_16x16x32_bf16 v[164:167], a[144:147], v[96:99], v[44:47]\n"
    "v_exp_f32 v250, v154\n"
    "v_mfma_f32_16x16x32_bf16 v[164:167], a[148:151], v[100:103], v[164:167]\n"
    "ds_read_b128 a[240:243], v3 offset:28672\n"
    "s_sub_u32 s61, s61, 0x8000\n"
    "v_mfma_f32_16x16x32_bf16 v[164:167], a[152:155], v[104:107], v[164:167]\n"
    "v_exp_f32 v251, v155\n"
    "v_mfma_f32_16x16x32_bf16 v[164:167], a[156:159], v[108:111], v[164:167]\n"
    "ds_read_b128 a[212:215], v3 offset:21504\n"
    "v_cvt_pk_bf16_f32 v205, v250, v251\n"
    "v_mfma_f32_16x16x32_bf16 v[180:183], a[144:147], v[112:115], v[56:59]\n"
    "v_exp_f32 v248, v140\n"
    "v_mfma_f32_16x16x32_bf16 v[180:183], a[148:151], v[116:119], v[180:183]\n"
    "ds_read_b128 a[244:247], v3 offset:29696\n"
    "s_xor_b32 s62, s62, 0x8000\n"
    "v_mfma_f32_16x16x32_bf16 v[180:183], a[152:155], v[120:123], v[180:183]\n"
    "v_exp_f32 v249, v141\n"
    "v_mfma_f32_16x16x32_bf16 v[180:183], a[156:159], v[124:127], v[180:183]\n"
    "ds_read_b128 a[216:219], v3 offset:22528\n"
    "v_cvt_pk_bf16_f32 v198, v248, v249\n"
    "v_mfma_f32_16x16x32_bf16 v[168:171], a[160:163], v[96:99], v[48:51]\n"
    "v_exp_f32 v250, v142\n"
    "v_mfma_f32_16x16x32_bf16 v[168:171], a[164:167], v[100:103], v[168:171]\n"
    "ds_read_b128 a[248:251], v3 offset:30720\n"
    "v_add_f32 v242, v242, v232\n"
    "v_mfma_f32_16x16x32_bf16 v[168:171], a[168:171], v[104:107], v[168:171]\n"
    "v_exp_f32 v251, v143\n"
    "v_mfma_f32_16x16x32_bf16 v[168:171], a[172:175], v[108:111], v[168:171]\n"
    "ds_read_b128 a[220:223], v3 offset:23552\n"
    "v_cvt_pk_bf16_f32 v199, v250, v251\n"
    "v_mfma_f32_16x16x32_bf16 v[184:187], a[160:163], v[112:115], v[56:59]\n"
    "v_exp_f32 v248, v156\n"
    "v_mfma_f32_16x16x32_bf16 v[184:187], a[164:167], v[116:119], v[184:187]\n"
    "ds_read_b128 a[252:255], v3 offset:31744\n"
    "v_add_f32 v243, v243, v236\n"
    "v_mfma_f32_16x16x32_bf16 v[184:187], a[168:171], v[120:123], v[184:187]\n"
    "v_exp_f32 v249, v157\n"
    "v_mfma_f32_16x16x32_bf16 v[184:187], a[172:175], v[124:127], v[184:187]\n"
    "ds_read_b128 a[128:131], v3\n"
    "v_cvt_pk_bf16_f32 v206, v248, v249\n"
    "v_mfma_f32_16x16x32_bf16 v[172:175], a[176:179], v[96:99], v[52:55]\n"
    "v_exp_f32 v250, v158\n"
    "v_mfma_f32_16x16x32_bf16 v[172:175], a[180:183], v[100:103], v[172:175]\n"
    "v_exp_f32 v251, v159\n"
    "v_mfma_f32_16x16x32_bf16 v[224:227], v[244:247], v[192:195], 0\n"
    "ds_read_b128 a[132:135], v3 offset:1024\n"
    "v_cvt_pk_bf16_f32 v207, v250, v251\n"
    "v_mfma_f32_16x16x32_bf16 v[224:227], v[244:247], v[196:199], v[224:227]\n"
    "ds_read_b128 a[136:139], v3 offset:2048\n"
    "v_mfma_f32_16x16x32_bf16 v[228:231], v[244:247], v[200:203], 0\n"
    "ds_read_b128 a[140:143], v3 offset:3072\n"
    "v_mfma_f32_16x16x32_bf16 v[228:231], v[244:247], v[204:207], v[228:231]\n"
    "ds_read_b128 a[144:147], v3 offset:4096\n"
    "v_mfma_f32_16x16x32_bf16 v[172:175], a[184:187], v[104:107], v[172:175]\n"
    "ds_read_b128 a[148:151], v3 offset:5120\n"
    "v_mfma_f32_16x16x32_bf16 v[172:175], a[188:191], v[108:111], v[172:175]\n"
    "ds_read_b128 a[152:155], v3 offset:6144\n"
    "v_mfma_f32_16x16x32_bf16 v[188:191], a[176:179], v[112:115], v[60:63]\n"
    "v_exp_f32 v248, v160\n"
    "v_mfma_f32_16x16x32_bf16 v[188:191], a[180:183], v[116:119], v[188:191]\n"
    "v_max_f32 v4, v224, v228\n"
    "v_cmp_lt_f32 vcc, 0x49800000, v4\n"
    "v_mfma_f32_16x16x32_bf16 v[188:191], a[184:187], v[120:123], v[188:191]\n"
    "v_exp_f32 v249, v161\n"
    "v_mfma_f32_16x16x32_bf16 v[188:191], a[188:191], v[124:127], v[188:191]\n"
    "v_cvt_pk_bf16_f32 v208, v248, v249\n"
    "s_cbranch_vccnz SCB_%=\n"
    "SRB_%=:\n"
    "s_waitcnt lgkmcnt(7)\n"
    "v_mfma_f32_16x16x32_bf16 a[0:3], a[192:195], v[192:195], a[0:3]\n"
    "v_exp_f32 v250, v162\n"
    "v_mfma_f32_16x16x32_bf16 a[0:3], a[224:227], v[196:199], a[0:3]\n"
    "ds_read_b128 a[156:159], v3 offset:7168\n"
    "v_add_f32 v240, v240, v224\n"
    "v_mfma_f32_16x16x32_bf16 a[32:35], a[192:195], v[200:203], a[32:35]\n"
    "v_exp_f32 v251, v163\n"
    "v_mfma_f32_16x16x32_bf16 a[32:35], a[224:227], v[204:207], a[32:35]\n"
    "ds_read_b128 a[160:163], v3 offset:8192\n"
    "v_cvt_pk_bf16_f32 v209, v250, v251\n"
    "v_mfma_f32_16x16x32_bf16 a[4:7], a[196:199], v[192:195], a[4:7]\n"
    "v_exp_f32 v248, v176\n"
    "v_mfma_f32_16x16x32_bf16 a[4:7], a[228:231], v[196:199], a[4:7]\n"
    "ds_read_b128 a[164:167], v3 offset:9216\n"
    "v_add_f32 v241, v241, v228\n"
    "v_mfma_f32_16x16x32_bf16 a[36:39], a[196:199], v[200:203], a[36:39]\n"
    "v_exp_f32 v249, v177\n"
    "v_mfma_f32_16x16x32_bf16 a[36:39], a[228:231], v[204:207], a[36:39]\n"
    "ds_read_b128 a[168:171], v3 offset:10240\n"
    "v_cvt_pk_bf16_f32 v216, v248, v249\n"
    "v_mfma_f32_16x16x32_bf16 a[8:11], a[200:203], v[192:195], a[8:11]\n"
    "v_exp_f32 v250, v178\n"
    "v_mfma_f32_16x16x32_bf16 a[8:11], a[232:235], v[196:199], a[8:11]\n"
    "ds_read_b128 a[172:175], v3 offset:11264\n"
    "s_sub_u32 s60, s60, 0x8000\n"
    "v_mfma_f32_16x16x32_bf16 a[40:43], a[200:203], v[200:203], a[40:43]\n"
    "v_exp_f32 v251, v179\n"
    "v_mfma_f32_16x16x32_bf16 a[40:43], a[232:235], v[204:207], a[40:43]\n"
    "ds_read_b128 a[176:179], v3 offset:12288\n"
    "v_cvt_pk_bf16_f32 v217, v250, v251\n"
    "v_mfma_f32_16x16x32_bf16 a[12:15], a[204:207], v[192:195], a[12:15]\n"
    "v_exp_f32 v248, v164\n"
    "v_mfma_f32_16x16x32_bf16 a[12:15], a[236:239], v[196:199], a[12:15]\n"
    "ds_read_b128 a[180:183], v3 offset:13312\n"
    "v_mfma_f32_16x16x32_bf16 a[44:47], a[204:207], v[200:203], a[44:47]\n"
    "v_exp_f32 v249, v165\n"
    "v_mfma_f32_16x16x32_bf16 a[44:47], a[236:239], v[204:207], a[44:47]\n"
    "ds_read_b128 a[184:187], v3 offset:14336\n"
    "v_cvt_pk_bf16_f32 v210, v248, v249\n"
    "v_mfma_f32_16x16x32_bf16 a[16:19], a[208:211], v[192:195], a[16:19]\n"
    "v_exp_f32 v250, v166\n"
    "v_mfma_f32_16x16x32_bf16 a[16:19], a[240:243], v[196:199], a[16:19]\n"
    "ds_read_b128 a[188:191], v3 offset:15360\n"
    "v_xor_b32 v3, 0x8000, v3\n"
    "v_mfma_f32_16x16x32_bf16 a[48:51], a[208:211], v[200:203], a[48:51]\n"
    "v_exp_f32 v251, v167\n"
    "v_mfma_f32_16x16x32_bf16 a[48:51], a[240:243], v[204:207], a[48:51]\n"
    "v_cvt_pk_bf16_f32 v211, v250, v251\n"
    "v_mfma_f32_16x16x32_bf16 a[20:23], a[212:215], v[192:195], a[20:23]\n"
    "v_exp_f32 v248, v180\n"
    "v_mfma_f32_16x16x32_bf16 a[20:23], a[244:247], v[196:199], a[20:23]\n"
    "v_mfma_f32_16x16x32_bf16 a[52:55], a[212:215], v[200:203], a[52:55]\n"
    "v_exp_f32 v249, v181\n"
    "v_mfma_f32_16x16x32_bf16 a[52:55], a[244:247], v[204:207], a[52:55]\n"
    "v_cvt_pk_bf16_f32 v218, v248, v249\n"
    "v_mfma_f32_16x16x32_bf16 a[24:27], a[216:219], v[192:195], a[24:27]\n"
    "v_exp_f32 v250, v182\n"
    "v_mfma_f32_16x16x32_bf16 a[24:27], a[248:251], v[196:199], a[24:27]\n"
    "s_add_u32 s6, s6, s7\n"
    "v_mfma_f32_16x16x32_bf16 a[56:59], a[216:219], v[200:203], a[56:59]\n"
    "v_exp_f32 v251, v183\n"
    "v_mfma_f32_16x16x32_bf16 a[56:59], a[248:251], v[204:207], a[56:59]\n"
    "v_cvt_pk_bf16_f32 v219, v250, v251\n"
    "v_mfma_f32_16x16x32_bf16 a[28:31], a[220:223], v[192:195], a[28:31]\n"
    "v_exp_f32 v248, v168\n"
    "v_mfma_f32_16x16x32_bf16 a[28:31], a[252:255], v[196:199], a[28:31]\n"
    "s_cmp_gt_u32 s50, s51\n"
    "v_mfma_f32_16x16x32_bf16 a[60:63], a[220:223], v[200:203], a[60:63]\n"
    "v_exp_f32 v249, v169\n"
    "v_mfma_f32_16x16x32_bf16 a[60:63], a[252:255], v[204:207], a[60:63]\n"
    "v_cvt_pk_bf16_f32 v212, v248, v249\n"
    "s_waitcnt vmcnt(1) lgkmcnt(0)\n"
    "s_barrier\n"
    "s_cbranch_scc1 STEP_%=\n"
    "s_branch LOOP_%=\n"
    "MASK_%=:\n"
    "v_add_f32 v20, v20, v252\n"
    "v_mov_b32 v24, 0xff800000\n"
    "v_add_f32 v21, v21, v253\n"
    "v_mov_b32 v25, 0xff800000\n"
    "v_add_f32 v22, v22, v254\n"
    "v_mov_b32 v26, 0xff800000\n"
    "v_add_f32 v23, v23, v255\n"
    "v_mov_b32 v27, 0xff800000\n"
    "v_add_f32 v36, v36, v252\n"
    "v_mov_b32 v40, 0xff800000\n"
    "v_add_f32 v37, v37, v253\n"
    "v_mov_b32 v41, 0xff800000\n"
    "v_add_f32 v38, v38, v254\n"
    "v_mov_b32 v42, 0xff800000\n"
    "v_add_f32 v39, v39, v255\n"
    "v_mov_b32 v43, 0xff800000\n"
    "v_add_f32 v48, v48, v252\n"
    "v_mov_b32 v52, 0xff800000\n"
    "v_add_f32 v49, v49, v253\n"
    "v_mov_b32 v53, 0xff800000\n"
    "v_add_f32 v50, v50, v254\n"
    "v_mov_b32 v54, 0xff800000\n"
    "v_add_f32 v51, v51, v255\n"
    "v_mov_b32 v55, 0xff800000\n"
    "v_add_f32 v60, v60, v252\n"
    "v_add_f32 v61, v61, v253\n"
    "v_add_f32 v62, v62, v254\n"
    "v_add_f32 v63, v63, v255\n"
    "s_nop 1\n"
    "s_branch STEP_%=\n"
    "SLOW0_%=:\n"
    "s_nop 7\n"
    "s_nop 7\n"
    "v_max3_f32 v5, v128, v129, v130\n"
    "v_max3_f32 v5, v5, v131, v132\n"
    "v_max3_f32 v5, v5, v133, v134\n"
    "v_max3_f32 v5, v5, v135, v136\n"
    "v_max3_f32 v5, v5, v137, v138\n"
    "v_max3_f32 v5, v5, v139, v140\n"
    "v_max3_f32 v5, v5, v141, v142\n"
    "v_max_f32 v5, v5, v143\n"
    "v_mov_b32 v9, v5\n"
    "s_nop 1\n"
    "v_permlane32_swap_b32 v5, v9\n"
    "v_max_f32 v5, v5, v9\n"
    "v_mov_b32 v9, v5\n"
    "s_nop 1\n"
    "v_permlane16_swap_b32 v5, v9\n"
    "v_max_f32 v5, v5, v9\n"
    "v_max_f32 v5, 0, v5\n"
    "v_sub_f32 v7, 0, v5\n"
    "v_exp_f32 v7, v7\n"
    "v_sub_f32 v20, v20, v5\n"
    "v_sub_f32 v21, v21, v5\n"
    "v_sub_f32 v22, v22, v5\n"
    "v_sub_f32 v23, v23, v5\n"
    "v_sub_f32 v24, v24, v5\n"
    "v_sub_f32 v25, v25, v5\n"
    "v_sub_f32 v26, v26, v5\n"
    "v_sub_f32 v27, v27, v5\n"
    "v_mul_f32 v240, v240, v7\n"
    "v_accvgpr_read_b32 v10, a0\n"
    "v_accvgpr_read_b32 v11, a1\n"
    "v_accvgpr_read_b32 v12, a2\n"
    "v_accvgpr_read_b32 v13, a3\n"
    "v_mul_f32 v10, v10, v7\n"
    "v_mul_f32 v11, v11, v7\n"
    "v_mul_f32 v12, v12, v7\n"
    "v_mul_f32 v13, v13, v7\n"
    "v_accvgpr_write_b32 a0, v10\n"
    "v_accvgpr_write_b32 a1, v11\n"
    "v_accvgpr_write_b32 a2, v12\n"
    "v_accvgpr_write_b32 a3, v13\n"
    "v_accvgpr_read_b32 v10, a4\n"
    "v_accvgpr_read_b32 v11, a5\n"
    "v_accvgpr_read_b32 v12, a6\n"
    "v_accvgpr_read_b32 v13, a7\n"
    "v_mul_f32 v10, v10, v7\n"
    "v_mul_f32 v11, v11, v7\n"
    "v_mul_f32 v12, v12, v7\n"
    "v_mul_f32 v13, v13, v7\n"
    "v_accvgpr_write_b32 a4, v10\n"
    "v_accvgpr_write_b32 a5, v11\n"
    "v_accvgpr_write_b32 a6, v12\n"
    "v_accvgpr_write_b32 a7, v13\n"
    "v_accvgpr_read_b32 v10, a8\n"
    "v_accvgpr_read_b32 v11, a9\n"
    "v_accvgpr_read_b32 v12, a10\n"
    "v_accvgpr_read_b32 v13, a11\n"
    "v_mul_f32 v10, v10, v7\n"
    "v_mul_f32 v11, v11, v7\n"
    "v_mul_f32 v12, v12, v7\n"
    "v_mul_f32 v13, v13, v7\n"
    "v_accvgpr_write_b32 a8, v10\n"
    "v_accvgpr_write_b32 a9, v11\n"
    "v_accvgpr_write_b32 a10, v12\n"
    "v_accvgpr_write_b32 a11, v13\n"
    "v_accvgpr_read_b32 v10, a12\n"
    "v_accvgpr_read_b32 v11, a13\n"
    "v_accvgpr_read_b32 v12, a14\n"
    "v_accvgpr_read_b32 v13, a15\n"
    "v_mul_f32 v10, v10, v7\n"
    "v_mul_f32 v11, v11, v7\n"
    "v_mul_f32 v12, v12, v7\n"
    "v_mul_f32 v13, v13, v7\n"
    "v_accvgpr_write_b32 a12, v10\n"
    "v_accvgpr_write_b32 a13, v11\n"
    "v_accvgpr_write_b32 a14, v12\n"
    "v_accvgpr_write_b32 a15, v13\n"
    "v_accvgpr_read_b32 v10, a16\n"
    "v_accvgpr_read_b32 v11, a17\n"
    "v_accvgpr_read_b32 v12, a18\n"
    "v_accvgpr_read_b32 v13, a19\n"
    "v_mul_f32 v10, v10, v7\n"
    "v_mul_f32 v11, v11, v7\n"
    "v_mul_f32 v12, v12, v7\n"
    "v_mul_f32 v13, v13, v7\n"
    "v_accvgpr_write_b32 a16, v10\n"
    "v_accvgpr_write_b32 a17, v11\n"
    "v_accvgpr_write_b32 a18, v12\n"
    "v_accvgpr_write_b32 a19, v13\n"
    "v_accvgpr_read_b32 v10, a20\n"
    "v_accvgpr_read_b32 v11, a21\n"
    "v_accvgpr_read_b32 v12, a22\n"
    "v_accvgpr_read_b32 v13, a23\n"
    "v_mul_f32 v10, v10, v7\n"
    "v_mul_f32 v11, v11, v7\n"
    "v_mul_f32 v12, v12, v7\n"
    "v_mul_f32 v13, v13, v7\n"
    "v_accvgpr_write_b32 a20, v10\n"
    "v_accvgpr_write_b32 a21, v11\n"
    "v_accvgpr_write_b32 a22, v12\n"
    "v_accvgpr_write_b32 a23, v13\n"
    "v_accvgpr_read_b32 v10, a24\n"
    "v_accvgpr_read_b32 v11, a25\n"
    "v_accvgpr_read_b32 v12, a26\n"
    "v_accvgpr_read_b32 v13, a27\n"
    "v_mul_f32 v10, v10, v7\n"
    "v_mul_f32 v11, v11, v7\n"
    "v_mul_f32 v12, v12, v7\n"
    "v_mul_f32 v13, v13, v7\n"
    "v_accvgpr_write_b32 a24, v10\n"
    "v_accvgpr_write_b32 a25, v11\n"
    "v_accvgpr_write_b32 a26, v12\n"
    "v_accvgpr_write_b32 a27, v13\n"
    "v_accvgpr_read_b32 v10, a28\n"
    "v_accvgpr_read_b32 v11, a29\n"
    "v_accvgpr_read_b32 v12, a30\n"
    "v_accvgpr_read_b32 v13, a31\n"
    "v_mul_f32 v10, v10, v7\n"
    "v_mul_f32 v11, v11, v7\n"
    "v_mul_f32 v12, v12, v7\n"
    "v_mul_f32 v13, v13, v7\n"
    "v_accvgpr_write_b32 a28, v10\n"
    "v_accvgpr_write_b32 a29, v11\n"
    "v_accvgpr_write_b32 a30, v12\n"
    "v_accvgpr_write_b32 a31, v13\n"
    "v_sub_f32 v128, v128, v5\n"
    "v_sub_f32 v129, v129, v5\n"
    "v_sub_f32 v130, v130, v5\n"
    "v_sub_f32 v131, v131, v5\n"
    "v_sub_f32 v132, v132, v5\n"
    "v_sub_f32 v133, v133, v5\n"
    "v_sub_f32 v134, v134, v5\n"
    "v_sub_f32 v135, v135, v5\n"
    "v_sub_f32 v136, v136, v5\n"
    "v_sub_f32 v137, v137, v5\n"
    "v_sub_f32 v138, v138, v5\n"
    "v_sub_f32 v139, v139, v5\n"
    "v_sub_f32 v140, v140, v5\n"
    "v_sub_f32 v141, v141, v5\n"
    "v_sub_f32 v142, v142, v5\n"
    "v_sub_f32 v143, v143, v5\n"
    "v_max3_f32 v6, v144, v145, v146\n"
    "v_max3_f32 v6, v6, v147, v148\n"
    "v_max3_f32 v6, v6, v149, v150\n"
    "v_max3_f32 v6, v6, v151, v152\n"
    "v_max3_f32 v6, v6, v153, v154\n"
    "v_max3_f32 v6, v6, v155, v156\n"
    "v_max3_f32 v6, v6, v157, v158\n"
    "v_max_f32 v6, v6, v159\n"
    "v_mov_b32 v9, v6\n"
    "s_nop 1\n"
    "v_permlane32_swap_b32 v6, v9\n"
    "v_max_f32 v6, v6, v9\n"
    "v_mov_b32 v9, v6\n"
    "s_nop 1\n"
    "v_permlane16_swap_b32 v6, v9\n"
    "v_max_f32 v6, v6, v9\n"
    "v_max_f32 v6, 0, v6\n"
    "v_sub_f32 v8, 0, v6\n"
    "v_exp_f32 v8, v8\n"
    "v_sub_f32 v32, v32, v6\n"
    "v_sub_f32 v33, v33, v6\n"
    "v_sub_f32 v34, v34, v6\n"
    "v_sub_f32 v35, v35, v6\n"
    "v_sub_f32 v36, v36, v6\n"
    "v_sub_f32 v37, v37, v6\n"
    "v_sub_f32 v38, v38, v6\n"
    "v_sub_f32 v39, v39, v6\n"
    "v_sub_f32 v40, v40, v6\n"
    "v_sub_f32 v41, v41, v6\n"
    "v_sub_f32 v42, v42, v6\n"
    "v_sub_f32 v43, v43, v6\n"
    "v_mul_f32 v241, v241, v8\n"
    "v_accvgpr_read_b32 v10, a32\n"
    "v_accvgpr_read_b32 v11, a33\n"
    "v_accvgpr_read_b32 v12, a34\n"
    "v_accvgpr_read_b32 v13, a35\n"
    "v_mul_f32 v10, v10, v8\n"
    "v_mul_f32 v11, v11, v8\n"
    "v_mul_f32 v12, v12, v8\n"
    "v_mul_f32 v13, v13, v8\n"
    "v_accvgpr_write_b32 a32, v10\n"
    "v_accvgpr_write_b32 a33, v11\n"
    "v_accvgpr_write_b32 a34, v12\n"
    "v_accvgpr_write_b32 a35, v13\n"
    "v_accvgpr_read_b32 v10, a36\n"
    "v_accvgpr_read_b32 v11, a37\n"
    "v_accvgpr_read_b32 v12, a38\n"
    "v_accvgpr_read_b32 v13, a39\n"
    "v_mul_f32 v10, v10, v8\n"
    "v_mul_f32 v11, v11, v8\n"
    "v_mul_f32 v12, v12, v8\n"
    "v_mul_f32 v13, v13, v8\n"
    "v_accvgpr_write_b32 a36, v10\n"
    "v_accvgpr_write_b32 a37, v11\n"
    "v_accvgpr_write_b32 a38, v12\n"
    "v_accvgpr_write_b32 a39, v13\n"
    "v_accvgpr_read_b32 v10, a40\n"
    "v_accvgpr_read_b32 v11, a41\n"
    "v_accvgpr_read_b32 v12, a42\n"
    "v_accvgpr_read_b32 v13, a43\n"
    "v_mul_f32 v10, v10, v8\n"
    "v_mul_f32 v11, v11, v8\n"
    "v_mul_f32 v12, v12, v8\n"
    "v_mul_f32 v13, v13, v8\n"
    "v_accvgpr_write_b32 a40, v10\n"
    "v_accvgpr_write_b32 a41, v11\n"
    "v_accvgpr_write_b32 a42, v12\n"
    "v_accvgpr_write_b32 a43, v13\n"
    "v_accvgpr_read_b32 v10, a44\n"
    "v_accvgpr_read_b32 v11, a45\n"
    "v_accvgpr_read_b32 v12, a46\n"
    "v_accvgpr_read_b32 v13, a47\n"
    "v_mul_f32 v10, v10, v8\n"
    "v_mul_f32 v11, v11, v8\n"
    "v_mul_f32 v12, v12, v8\n"
    "v_mul_f32 v13, v13, v8\n"
    "v_accvgpr_write_b32 a44, v10\n"
    "v_accvgpr_write_b32 a45, v11\n"
    "v_accvgpr_write_b32 a46, v12\n"
    "v_accvgpr_write_b32 a47, v13\n"
    "v_accvgpr_read_b32 v10, a48\n"
    "v_accvgpr_read_b32 v11, a49\n"
    "v_accvgpr_read_b32 v12, a50\n"
    "v_accvgpr_read_b32 v13, a51\n"
    "v_mul_f32 v10, v10, v8\n"
    "v_mul_f32 v11, v11, v8\n"
    "v_mul_f32 v12, v12, v8\n"
    "v_mul_f32 v13, v13, v8\n"
    "v_accvgpr_write_b32 a48, v10\n"
    "v_accvgpr_write_b32 a49, v11\n"
    "v_accvgpr_write_b32 a50, v12\n"
    "v_accvgpr_write_b32 a51, v13\n"
    "v_accvgpr_read_b32 v10, a52\n"
    "v_accvgpr_read_b32 v11, a53\n"
    "v_accvgpr_read_b32 v12, a54\n"
    "v_accvgpr_read_b32 v13, a55\n"
    "v_mul_f32 v10, v10, v8\n"
    "v_mul_f32 v11, v11, v8\n"
    "v_mul_f32 v12, v12, v8\n"
    "v_mul_f32 v13, v13, v8\n"
    "v_accvgpr_write_b32 a52, v10\n"
    "v_accvgpr_write_b32 a53, v11\n"
    "v_accvgpr_write_b32 a54, v12\n"
    "v_accvgpr_write_b32 a55, v13\n"
    "v_accvgpr_read_b32 v10, a56\n"
    "v_accvgpr_read_b32 v11, a57\n"
    "v_accvgpr_read_b32 v12, a58\n"
    "v_accvgpr_read_b32 v13, a59\n"
    "v_mul_f32 v10, v10, v8\n"
    "v_mul_f32 v11, v11, v8\n"
    "v_mul_f32 v12, v12, v8\n"
    "v_mul_f32 v13, v13, v8\n"
    "v_accvgpr_write_b32 a56, v10\n"
    "v_accvgpr_write_b32 a57, v11\n"
    "v_accvgpr_write_b32 a58, v12\n"
    "v_accvgpr_write_b32 a59, v13\n"
    "v_accvgpr_read_b32 v10, a60\n"
    "v_accvgpr_read_b32 v11, a61\n"
    "v_accvgpr_read_b32 v12, a62\n"
    "v_accvgpr_read_b32 v13, a63\n"
    "v_mul_f32 v10, v10, v8\n"
    "v_mul_f32 v11, v11, v8\n"
    "v_mul_f32 v12, v12, v8\n"
    "v_mul_f32 v13, v13, v8\n"
    "v_accvgpr_write_b32 a60, v10\n"
    "v_accvgpr_write_b32 a61, v11\n"
    "v_accvgpr_write_b32 a62, v12\n"
    "v_accvgpr_write_b32 a63, v13\n"
    "v_sub_f32 v144, v144, v6\n"
    "v_sub_f32 v145, v145, v6\n"
    "v_sub_f32 v146, v146, v6\n"
    "v_sub_f32 v147, v147, v6\n"
    "v_sub_f32 v148, v148, v6\n"
    "v_sub_f32 v149, v149, v6\n"
    "v_sub_f32 v150, v150, v6\n"
    "v_sub_f32 v151, v151, v6\n"
    "v_sub_f32 v152, v152, v6\n"
    "v_sub_f32 v153, v153, v6\n"
    "v_sub_f32 v154, v154, v6\n"
    "v_sub_f32 v155, v155, v6\n"
    "v_sub_f32 v156, v156, v6\n"
    "v_sub_f32 v157, v157, v6\n"
    "v_sub_f32 v158, v158, v6\n"
    "v_sub_f32 v159, v159, v6\n"
    "v_exp_f32 v10, v128\n"
    "v_exp_f32 v11, v129\n"
    "v_exp_f32 v12, v130\n"
    "v_exp_f32 v13, v131\n"
    "v_cvt_pk_bf16_f32 v192, v10, v11\n"
    "v_cvt_pk_bf16_f32 v193, v12, v13\n"
    "v_exp_f32 v10, v144\n"
    "v_exp_f32 v11, v145\n"
    "v_exp_f32 v12, v146\n"
    "v_exp_f32 v13, v147\n"
    "v_cvt_pk_bf16_f32 v200, v10, v11\n"
    "v_cvt_pk_bf16_f32 v201, v12, v13\n"
    "v_exp_f32 v10, v132\n"
    "v_exp_f32 v11, v133\n"
    "v_exp_f32 v12, v134\n"
    "v_exp_f32 v13, v135\n"
    "v_cvt_pk_bf16_f32 v194, v10, v11\n"
    "v_cvt_pk_bf16_f32 v195, v12, v13\n"
    "v_exp_f32 v10, v148\n"
    "v_exp_f32 v11, v149\n"
    "v_exp_f32 v12, v150\n"
    "v_exp_f32 v13, v151\n"
    "v_cvt_pk_bf16_f32 v202, v10, v11\n"
    "v_cvt_pk_bf16_f32 v203, v12, v13\n"
    "v_exp_f32 v10, v136\n"
    "v_exp_f32 v11, v137\n"
    "v_exp_f32 v12, v138\n"
    "v_exp_f32 v13, v139\n"
    "v_cvt_pk_bf16_f32 v196, v10, v11\n"
    "v_cvt_pk_bf16_f32 v197, v12, v13\n"
    "v_exp_f32 v10, v152\n"
    "v_exp_f32 v11, v153\n"
    "v_exp_f32 v12, v154\n"
    "v_exp_f32 v13, v155\n"
    "v_cvt_pk_bf16_f32 v204, v10, v11\n"
    "v_cvt_pk_bf16_f32 v205, v12, v13\n"
    "v_exp_f32 v10, v140\n"
    "v_exp_f32 v11, v141\n"
    "v_exp_f32 v12, v142\n"
    "v_exp_f32 v13, v143\n"
    "v_cvt_pk_bf16_f32 v198, v10, v11\n"
    "v_cvt_pk_bf16_f32 v199, v12, v13\n"
    "v_exp_f32 v10, v156\n"
    "v_exp_f32 v11, v157\n"
    "v_exp_f32 v12, v158\n"
    "v_exp_f32 v13, v159\n"
    "v_cvt_pk_bf16_f32 v206, v10, v11\n"
    "v_cvt_pk_bf16_f32 v207, v12, v13\n"
    "s_nop 1\n"
    "v_mfma_f32_16x16x32_bf16 v[224:227], v[244:247], v[192:195], 0\n"
    "v_mfma_f32_16x16x32_bf16 v[228:231], v[244:247], v[200:203], 0\n"
    "v_mfma_f32_16x16x32_bf16 v[224:227], v[244:247], v[196:199], v[224:227]\n"
    "v_mfma_f32_16x16x32_bf16 v[228:231], v[244:247], v[204:207], v[228:231]\n"
    "s_nop 7\n"
    "s_nop 1\n"
    "s_branch SRB_%=\n"
    "SCB_%=:\n"
    "s_mov_b32 s53, 0\n"
    "s_branch SLOW0_%=\n"
    "SLOW1_%=:\n"
    "s_nop 7\n"
    "s_nop 7\n"
    "v_max3_f32 v5, v160, v161, v162\n"
    "v_max3_f32 v5, v5, v163, v164\n"
    "v_max3_f32 v5, v5, v165, v166\n"
    "v_max3_f32 v5, v5, v167, v168\n"
    "v_max3_f32 v5, v5, v169, v170\n"
    "v_max3_f32 v5, v5, v171, v172\n"
    "v_max3_f32 v5, v5, v173, v174\n"
    "v_max_f32 v5, v5, v175\n"
    "v_mov_b32 v9, v5\n"
    "s_nop 1\n"
    "v_permlane32_swap_b32 v5, v9\n"
    "v_max_f32 v5, v5, v9\n"
    "v_mov_b32 v9, v5\n"
    "s_nop 1\n"
    "v_permlane16_swap_b32 v5, v9\n"
    "v_max_f32 v5, v5, v9\n"
    "v_max_f32 v5, 0, v5\n"
    "v_sub_f32 v7, 0, v5\n"
    "v_exp_f32 v7, v7\n"
    "v_sub_f32 v44, v44, v5\n"
    "v_sub_f32 v45, v45, v5\n"
    "v_sub_f32 v46, v46, v5\n"
    "v_sub_f32 v47, v47, v5\n"
    "v_sub_f32 v48, v48, v5\n"
    "v_sub_f32 v49, v49, v5\n"
    "v_sub_f32 v50, v50, v5\n"
    "v_sub_f32 v51, v51, v5\n"
    "v_sub_f32 v52, v52, v5\n"
    "v_sub_f32 v53, v53, v5\n"
    "v_sub_f32 v54, v54, v5\n"
    "v_sub_f32 v55, v55, v5\n"
    "v_mul_f32 v242, v242, v7\n"
    "v_accvgpr_read_b32 v10, a64\n"
    "v_accvgpr_read_b32 v11, a65\n"
    "v_accvgpr_read_b32 v12, a66\n"
    "v_accvgpr_read_b32 v13, a67\n"
    "v_mul_f32 v10, v10, v7\n"
    "v_mul_f32 v11, v11, v7\n"
    "v_mul_f32 v12, v12, v7\n"
    "v_mul_f32 v13, v13, v7\n"
    "v_accvgpr_write_b32 a64, v10\n"
    "v_accvgpr_write_b32 a65, v11\n"
    "v_accvgpr_write_b32 a66, v12\n"
    "v_accvgpr_write_b32 a67, v13\n"
    "v_accvgpr_read_b32 v10, a68\n"
    "v_accvgpr_read_b32 v11, a69\n"
    "v_accvgpr_read_b32 v12, a70\n"
    "v_accvgpr_read_b32 v13, a71\n"
    "v_mul_f32 v10, v10, v7\n"
    "v_mul_f32 v11, v11, v7\n"
    "v_mul_f32 v12, v12, v7\n"
    "v_mul_f32 v13, v13, v7\n"
    "v_accvgpr_write_b32 a68, v10\n"
    "v_accvgpr_write_b32 a69, v11\n"
    "v_accvgpr_write_b32 a70, v12\n"
    "v_accvgpr_write_b32 a71, v13\n"
    "v_accvgpr_read_b32 v10, a72\n"
    "v_accvgpr_read_b32 v11, a73\n"
    "v_accvgpr_read_b32 v12, a74\n"
    "v_accvgpr_read_b32 v13, a75\n"
    "v_mul_f32 v10, v10, v7\n"
    "v_mul_f32 v11, v11, v7\n"
    "v_mul_f32 v12, v12, v7\n"
    "v_mul_f32 v13, v13, v7\n"
    "v_accvgpr_write_b32 a72, v10\n"
    "v_accvgpr_write_b32 a73, v11\n"
    "v_accvgpr_write_b32 a74, v12\n"
    "v_accvgpr_write_b32 a75, v13\n"
    "v_accvgpr_read_b32 v10, a76\n"
    "v_accvgpr_read_b32 v11, a77\n"
    "v_accvgpr_read_b32 v12, a78\n"
    "v_accvgpr_read_b32 v13, a79\n"
    "v_mul_f32 v10, v10, v7\n"
    "v_mul_f32 v11, v11, v7\n"
    "v_mul_f32 v12, v12, v7\n"
    "v_mul_f32 v13, v13, v7\n"
    "v_accvgpr_write_b32 a76, v10\n"
    "v_accvgpr_write_b32 a77, v11\n"
    "v_accvgpr_write_b32 a78, v12\n"
    "v_accvgpr_write_b32 a79, v13\n"
    "v_accvgpr_read_b32 v10, a80\n"
    "v_accvgpr_read_b32 v11, a81\n"
    "v_accvgpr_read_b32 v12, a82\n"
    "v_accvgpr_read_b32 v13, a83\n"
    "v_mul_f32 v10, v10, v7\n"
    "v_mul_f32 v11, v11, v7\n"
    "v_mul_f32 v12, v12, v7\n"
    "v_mul_f32 v13, v13, v7\n"
    "v_accvgpr_write_b32 a80, v10\n"
    "v_accvgpr_write_b32 a81, v11\n"
    "v_accvgpr_write_b32 a82, v12\n"
    "v_accvgpr_write_b32 a83, v13\n"
    "v_accvgpr_read_b32 v10, a84\n"
    "v_accvgpr_read_b32 v11, a85\n"
    "v_accvgpr_read_b32 v12, a86\n"
    "v_accvgpr_read_b32 v13, a87\n"
    "v_mul_f32 v10, v10, v7\n"
    "v_mul_f32 v11, v11, v7\n"
    "v_mul_f32 v12, v12, v7\n"
    "v_mul_f32 v13, v13, v7\n"
    "v_accvgpr_write_b32 a84, v10\n"
    "v_accvgpr_write_b32 a85, v11\n"
    "v_accvgpr_write_b32 a86, v12\n"
    "v_accvgpr_write_b32 a87, v13\n"
    "v_accvgpr_read_b32 v10, a88\n"
    "v_accvgpr_read_b32 v11, a89\n"
    "v_accvgpr_read_b32 v12, a90\n"
    "v_accvgpr_read_b32 v13, a91\n"
    "v_mul_f32 v10, v10, v7\n"
    "v_mul_f32 v11, v11, v7\n"
    "v_mul_f32 v12, v12, v7\n"
    "v_mul_f32 v13, v13, v7\n"
    "v_accvgpr_write_b32 a88, v10\n"
    "v_accvgpr_write_b32 a89, v11\n"
    "v_accvgpr_write_b32 a90, v12\n"
    "v_accvgpr_write_b32 a91, v13\n"
    "v_accvgpr_read_b32 v10, a92\n"
    "v_accvgpr_read_b32 v11, a93\n"
    "v_accvgpr_read_b32 v12, a94\n"
    "v_accvgpr_read_b32 v13, a95\n"
    "v_mul_f32 v10, v10, v7\n"
    "v_mul_f32 v11, v11, v7\n"
    "v_mul_f32 v12, v12, v7\n"
    "v_mul_f32 v13, v13, v7\n"
    "v_accvgpr_write_b32 a92, v10\n"
    "v_accvgpr_write_b32 a93, v11\n"
    "v_accvgpr_write_b32 a94, v12\n"
    "v_accvgpr_write_b32 a95, v13\n"
    "v_sub_f32 v160, v160, v5\n"
    "v_sub_f32 v161, v161, v5\n"
    "v_sub_f32 v162, v162, v5\n"
    "v_sub_f32 v163, v163, v5\n"
    "v_sub_f32 v164, v164, v5\n"
    "v_sub_f32 v165, v165, v5\n"
    "v_sub_f32 v166, v166, v5\n"
    "v_sub_f32 v167, v167, v5\n"
    "v_sub_f32 v168, v168, v5\n"
    "v_sub_f32 v169, v169, v5\n"
    "v_sub_f32 v170, v170, v5\n"
    "v_sub_f32 v171, v171, v5\n"
    "v_sub_f32 v172, v172, v5\n"
    "v_sub_f32 v173, v173, v5\n"
    "v_sub_f32 v174, v174, v5\n"
    "v_sub_f32 v175, v175, v5\n"
    "v_max3_f32 v6, v176, v177, v178\n"
    "v_max3_f32 v6, v6, v179, v180\n"
    "v_max3_f32 v6, v6, v181, v182\n"
    "v_max3_f32 v6, v6, v183, v184\n"
    "v_max3_f32 v6, v6, v185, v186\n"
    "v_max3_f32 v6, v6, v187, v188\n"
    "v_max3_f32 v6, v6, v189, v190\n"
    "v_max_f32 v6, v6, v191\n"
    "v_mov_b32 v9, v6\n"
    "s_nop 1\n"
    "v_permlane32_swap_b32 v6, v9\n"
    "v_max_f32 v6, v6, v9\n"
    "v_mov_b32 v9, v6\n"
    "s_nop 1\n"
    "v_permlane16_swap_b32 v6, v9\n"
    "v_max_f32 v6, v6, v9\n"
    "v_max_f32 v6, 0, v6\n"
    "v_sub_f32 v8, 0, v6\n"
    "v_exp_f32 v8, v8\n"
    "v_sub_f32 v56, v56, v6\n"
    "v_sub_f32 v57, v57, v6\n"
    "v_sub_f32 v58, v58, v6\n"
    "v_sub_f32 v59, v59, v6\n"
    "v_sub_f32 v60, v60, v6\n"
    "v_sub_f32 v61, v61, v6\n"
    "v_sub_f32 v62, v62, v6\n"
    "v_sub_f32 v63, v63, v6\n"
    "v_mul_f32 v243, v243, v8\n"
    "v_accvgpr_read_b32 v10, a96\n"
    "v_accvgpr_read_b32 v11, a97\n"
    "v_accvgpr_read_b32 v12, a98\n"
    "v_accvgpr_read_b32 v13, a99\n"
    "v_mul_f32 v10, v10, v8\n"
    "v_mul_f32 v11, v11, v8\n"
    "v_mul_f32 v12, v12, v8\n"
    "v_mul_f32 v13, v13, v8\n"
    "v_accvgpr_write_b32 a96, v10\n"
    "v_accvgpr_write_b32 a97, v11\n"
    "v_accvgpr_write_b32 a98, v12\n"
    "v_accvgpr_write_b32 a99, v13\n"
    "v_accvgpr_read_b32 v10, a100\n"
    "v_accvgpr_read_b32 v11, a101\n"
    "v_accvgpr_read_b32 v12, a102\n"
    "v_accvgpr_read_b32 v13, a103\n"
    "v_mul_f32 v10, v10, v8\n"
    "v_mul_f32 v11, v11, v8\n"
    "v_mul_f32 v12, v12, v8\n"
    "v_mul_f32 v13, v13, v8\n"
    "v_accvgpr_write_b32 a100, v10\n"
    "v_accvgpr_write_b32 a101, v11\n"
    "v_accvgpr_write_b32 a102, v12\n"
    "v_accvgpr_write_b32 a103, v13\n"
    "v_accvgpr_read_b32 v10, a104\n"
    "v_accvgpr_read_b32 v11, a105\n"
    "v_accvgpr_read_b32 v12, a106\n"
    "v_accvgpr_read_b32 v13, a107\n"
    "v_mul_f32 v10, v10, v8\n"
    "v_mul_f32 v11, v11, v8\n"
    "v_mul_f32 v12, v12, v8\n"
    "v_mul_f32 v13, v13, v8\n"
    "v_accvgpr_write_b32 a104, v10\n"
    "v_accvgpr_write_b32 a105, v11\n"
    "v_accvgpr_write_b32 a106, v12\n"
    "v_accvgpr_write_b32 a107, v13\n"
    "v_accvgpr_read_b32 v10, a108\n"
    "v_accvgpr_read_b32 v11, a109\n"
    "v_accvgpr_read_b32 v12, a110\n"
    "v_accvgpr_read_b32 v13, a111\n"
    "v_mul_f32 v10, v10, v8\n"
    "v_mul_f32 v11, v11, v8\n"
    "v_mul_f32 v12, v12, v8\n"
    "v_mul_f32 v13, v13, v8\n"
    "v_accvgpr_write_b32 a108, v10\n"
    "v_accvgpr_write_b32 a109, v11\n"
    "v_accvgpr_write_b32 a110, v12\n"
    "v_accvgpr_write_b32 a111, v13\n"
    "v_accvgpr_read_b32 v10, a112\n"
    "v_accvgpr_read_b32 v11, a113\n"
    "v_accvgpr_read_b32 v12, a114\n"
    "v_accvgpr_read_b32 v13, a115\n"
    "v_mul_f32 v10, v10, v8\n"
    "v_mul_f32 v11, v11, v8\n"
    "v_mul_f32 v12, v12, v8\n"
    "v_mul_f32 v13, v13, v8\n"
    "v_accvgpr_write_b32 a112, v10\n"
    "v_accvgpr_write_b32 a113, v11\n"
    "v_accvgpr_write_b32 a114, v12\n"
    "v_accvgpr_write_b32 a115, v13\n"
    "v_accvgpr_read_b32 v10, a116\n"
    "v_accvgpr_read_b32 v11, a117\n"
    "v_accvgpr_read_b32 v12, a118\n"
    "v_accvgpr_read_b32 v13, a119\n"
    "v_mul_f32 v10, v10, v8\n"
    "v_mul_f32 v11, v11, v8\n"
    "v_mul_f32 v12, v12, v8\n"
    "v_mul_f32 v13, v13, v8\n"
    "v_accvgpr_write_b32 a116, v10\n"
    "v_accvgpr_write_b32 a117, v11\n"
    "v_accvgpr_write_b32 a118, v12\n"
    "v_accvgpr_write_b32 a119, v13\n"
    "v_accvgpr_read_b32 v10, a120\n"
    "v_accvgpr_read_b32 v11, a121\n"
    "v_accvgpr_read_b32 v12, a122\n"
    "v_accvgpr_read_b32 v13, a123\n"
    "v_mul_f32 v10, v10, v8\n"
    "v_mul_f32 v11, v11, v8\n"
    "v_mul_f32 v12, v12, v8\n"
    "v_mul_f32 v13, v13, v8\n"
    "v_accvgpr_write_b32 a120, v10\n"
    "v_accvgpr_write_b32 a121, v11\n"
    "v_accvgpr_write_b32 a122, v12\n"
    "v_accvgpr_write_b32 a123, v13\n"
    "v_accvgpr_read_b32 v10, a124\n"
    "v_accvgpr_read_b32 v11, a125\n"
    "v_accvgpr_read_b32 v12, a126\n"
    "v_accvgpr_read_b32 v13, a127\n"
    "v_mul_f32 v10, v10, v8\n"
    "v_mul_f32 v11, v11, v8\n"
    "v_mul_f32 v12, v12, v8\n"
    "v_mul_f32 v13, v13, v8\n"
    "v_accvgpr_write_b32 a124, v10\n"
    "v_accvgpr_write_b32 a125, v11\n"
    "v_accvgpr_write_b32 a126, v12\n"
    "v_accvgpr_write_b32 a127, v13\n"
    "v_sub_f32 v176, v176, v6\n"
    "v_sub_f32 v177, v177, v6\n"
    "v_sub_f32 v178, v178, v6\n"
    "v_sub_f32 v179, v179, v6\n"
    "v_sub_f32 v180, v180, v6\n"
    "v_sub_f32 v181, v181, v6\n"
    "v_sub_f32 v182, v182, v6\n"
    "v_sub_f32 v183, v183, v6\n"
    "v_sub_f32 v184, v184, v6\n"
    "v_sub_f32 v185, v185, v6\n"
    "v_sub_f32 v186, v186, v6\n"
    "v_sub_f32 v187, v187, v6\n"
    "v_sub_f32 v188, v188, v6\n"
    "v_sub_f32 v189, v189, v6\n"
    "v_sub_f32 v190, v190, v6\n"
    "v_sub_f32 v191, v191, v6\n"
    "v_exp_f32 v10, v160\n"
    "v_exp_f32 v11, v161\n"
    "v_exp_f32 v12, v162\n"
    "v_exp_f32 v13, v163\n"
    "v_cvt_pk_bf16_f32 v208, v10, v11\n"
    "v_cvt_pk_bf16_f32 v209, v12, v13\n"
    "v_exp_f32 v10, v176\n"
    "v_exp_f32 v11, v177\n"
    "v_exp_f32 v12, v178\n"
    "v_exp_f32 v13, v179\n"
    "v_cvt_pk_bf16_f32 v216, v10, v11\n"
    "v_cvt_pk_bf16_f32 v217, v12, v13\n"
    "v_exp_f32 v10, v164\n"
    "v_exp_f32 v11, v165\n"
    "v_exp_f32 v12, v166\n"
    "v_exp_f32 v13, v167\n"
    "v_cvt_pk_bf16_f32 v210, v10, v11\n"
    "v_cvt_pk_bf16_f32 v211, v12, v13\n"
    "v_exp_f32 v10, v180\n"
    "v_exp_f32 v11, v181\n"
    "v_exp_f32 v12, v182\n"
    "v_exp_f32 v13, v183\n"
    "v_cvt_pk_bf16_f32 v218, v10, v11\n"
    "v_cvt_pk_bf16_f32 v219, v12, v13\n"
    "v_exp_f32 v10, v168\n"
    "v_exp_f32 v11, v169\n"
    "v_exp_f32 v12, v170\n"
    "v_exp_f32 v13, v171\n"
    "v_cvt_pk_bf16_f32 v212, v10, v11\n"
    "v_cvt_pk_bf16_f32 v213, v12, v13\n"
    "v_exp_f32 v10, v184\n"
    "v_exp_f32 v11, v185\n"
    "v_exp_f32 v12, v186\n"
    "v_exp_f32 v13, v187\n"
    "v_cvt_pk_bf16_f32 v220, v10, v11\n"
    "v_cvt_pk_bf16_f32 v221, v12, v13\n"
    "v_exp_f32 v10, v172\n"
    "v_exp_f32 v11, v173\n"
    "v_exp_f32 v12, v174\n"
    "v_exp_f32 v13, v175\n"
    "v_cvt_pk_bf16_f32 v214, v10, v11\n"
    "v_cvt_pk_bf16_f32 v215, v12, v13\n"
    "v_exp_f32 v10, v188\n"
    "v_exp_f32 v11, v189\n"
    "v_exp_f32 v12, v190\n"
    "v_exp_f32 v13, v191\n"
    "v_cvt_pk_bf16_f32 v222, v10, v11\n"
    "v_cvt_pk_bf16_f32 v223, v12, v13\n"
    "s_nop 1\n"
    "v_mfma_f32_16x16x32_bf16 v[232:235], v[244:247], v[208:211], 0\n"
    "v_mfma_f32_16x16x32_bf16 v[236:239], v[244:247], v[216:219], 0\n"
    "v_mfma_f32_16x16x32_bf16 v[232:235], v[244:247], v[212:215], v[232:235]\n"
    "v_mfma_f32_16x16x32_bf16 v[236:239], v[244:247], v[220:223], v[236:239]\n"
    "s_nop 7\n"
    "s_nop 1\n"
    "s_cmp_eq_u32 s53, 0\n"
    "s_cbranch_scc1 SRA_%=\n"
    "s_cmp_eq_u32 s53, 1\n"
    "s_cbranch_scc1 SRC_%=\n"
    "s_branch SRD_%=\n"
    "SCA_%=:\n"
    "s_mov_b32 s53, 0\n"
    "s_branch SLOW1_%=\n"
    "SCC_%=:\n"
    "s_mov_b32 s53, 1\n"
    "s_branch SLOW1_%=\n"
    "SCD_%=:\n"
    "s_mov_b32 s53, 2\n"
    "s_branch SLOW1_%=\n"
    "ENDP_%=:\n"
    "s_cmp_lg_u32 s52, 0\n"
    "s_cbranch_scc1 FIN_%=\n"
    "v_and_b32 v13, 15, v1\n"
    "v_lshlrev_b32 v13, 2, v13\n"
    "s_lshl_b32 s22, s68, 8\n"
    "s_add_u32 s22, s22, 0x3c00000\n"
    "buffer_load_dword v14, v13, s[44:47], s22 offen sc1\n"
    "buffer_load_dword v15, v13, s[44:47], s22 offen offset:64 sc1\n"
    "buffer_load_dword v16, v13, s[44:47], s22 offen offset:128 sc1\n"
    "buffer_load_dword v17, v13, s[44:47], s22 offen offset:192 sc1\n"
    "v_add_u32 v3, 0x10000, v3\n"
    "s_and_b32 s23, s19, 1\n"
    "s_lshl_b32 s23, s23, 14\n"
    "s_add_u32 s23, s23, 0x20000\n"
    "v_add_u32 v200, s23, v2\n"
    "v_exp_f32 v250, v170\n"
    "s_add_u32 m0, s62, 0\n"
    "s_nop 0\n"
    "buffer_load_dwordx4 v2, s[36:39], s54 offen sc1 lds\n"
    "v_exp_f32 v251, v171\n"
    "v_exp_f32 v248, v184\n"
    "v_cvt_pk_bf16_f32 v213, v250, v251\n"
    "v_exp_f32 v249, v185\n"
    "v_exp_f32 v250, v186\n"
    "v_cvt_pk_bf16_f32 v220, v248, v249\n"
    "v_exp_f32 v251, v187\n"
    "v_exp_f32 v248, v172\n"
    "buffer_load_dwordx4 v2, s[36:39], s54 offen offset:1024 sc1 lds\n"
    "v_cvt_pk_bf16_f32 v221, v250, v251\n"
    "v_exp_f32 v249, v173\n"
    "v_exp_f32 v250, v174\n"
    "v_cvt_pk_bf16_f32 v214, v248, v249\n"
    "v_exp_f32 v251, v175\n"
    "v_exp_f32 v248, v188\n"
    "v_cvt_pk_bf16_f32 v215, v250, v251\n"
    "v_exp_f32 v249, v189\n"
    "v_exp_f32 v250, v190\n"
    "v_cvt_pk_bf16_f32 v222, v248, v249\n"
    "buffer_load_dwordx4 v2, s[36:39], s54 offen offset:2048 sc1 lds\n"
    "v_exp_f32 v251, v191\n"
    "s_nop 0\n"
    "v_cvt_pk_bf16_f32 v223, v250, v251\n"
    "s_nop 1\n"
    "v_mfma_f32_16x16x32_bf16 v[232:235], v[244:247], v[208:211], 0\n"
    "v_mfma_f32_16x16x32_bf16 v[232:235], v[244:247], v[212:215], v[232:235]\n"
    "v_mfma_f32_16x16x32_bf16 v[236:239], v[244:247], v[216:219], 0\n"
    "buffer_load_dwordx4 v2, s[36:39], s54 offen offset:3072 sc1 lds\n"
    "s_add_u32 s54, s54, 0x8000\n"
    "s_add_u32 m0, s62, 0x4000\n"
    "v_mfma_f32_16x16x32_bf16 v[236:239], v[244:247], v[220:223], v[236:239]\n"
    "s_mul_i32 s48, s68, 0xb8\n"
    "s_lshl_b32 s22, s20, 2\n"
    "s_add_u32 s48, s48, s22\n"
    "s_lshl_b32 s48, s48, 12\n"
    "s_add_u32 s48, s48, 0x2100000\n"
    "s_lshr_b32 s49, s68, 1\n"
    "s_lshl_b32 s49, s49, 2\n"
    "s_lshr_b32 s22, s20, 3\n"
    "s_add_u32 s49, s49, s22\n"
    "s_lshl_b32 s49, s49, 1\n"
    "s_and_b32 s22, s68, 1\n"
    "s_add_u32 s49, s49, s22\n"
    "buffer_load_dwordx4 v2, s[36:39], s55 offen sc1 lds\n"
    "s_lshl_b32 s49, s49, 3\n"
    "s_and_b32 s22, s20, 7\n"
    "s_add_u32 s49, s49, s22\n"
    "s_lshl_b32 s49, s49, 4\n"
    "s_add_u32 s49, s49, 0x4000\n"
    "v_max_f32 v4, v232, v236\n"
    "v_cmp_lt_f32 vcc, 0x49800000, v4\n"
    "s_nop 0\n"
    "s_cbranch_vccnz SCC_%=\n"
    "SRC_%=:\n"
    "v_add_f32 v242, v242, v232\n"
    "v_add_f32 v243, v243, v236\n"
    "v_mfma_f32_16x16x32_bf16 a[64:67], a[192:195], v[208:211], a[64:67]\n"
    "ds_read_b128 a[128:131], v3\n"
    "s_waitcnt vmcnt(5)\n"
    "v_mfma_f32_16x16x32_bf16 a[64:67], a[224:227], v[212:215], a[64:67]\n"
    "buffer_load_dwordx4 v2, s[36:39], s55 offen offset:1024 sc1 lds\n"
    "ds_read_b128 a[132:135], v3 offset:1024\n"
    "v_mfma_f32_16x16x32_bf16 a[96:99], a[192:195], v[216:219], a[96:99]\n"
    "v_rcp_f32 v192, v240\n"
    "v_mfma_f32_16x16x32_bf16 a[96:99], a[224:227], v[220:223], a[96:99]\n"
    "ds_read_b128 a[136:139], v3 offset:2048\n"
    "v_mfma_f32_16x16x32_bf16 a[68:71], a[196:199], v[208:211], a[68:71]\n"
    "v_rcp_f32 v194, v241\n"
    "v_mfma_f32_16x16x32_bf16 a[68:71], a[228:231], v[212:215], a[68:71]\n"
    "ds_read_b128 a[140:143], v3 offset:3072\n"
    "s_nop 0\n"
    "v_mfma_f32_16x16x32_bf16 a[100:103], a[196:199], v[216:219], a[100:103]\n"
    "buffer_load_dwordx4 v2, s[36:39], s55 offen offset:2048 sc1 lds\n"
    "ds_read_b128 a[144:147], v3 offset:4096\n"
    "v_mul_f32 v192, v192, v14\n"
    "v_mfma_f32_16x16x32_bf16 a[100:103], a[228:231], v[220:223], a[100:103]\n"
    "ds_read_b128 a[148:151], v3 offset:5120\n"
    "v_mov_b32 v193, v192\n"
    "v_mfma_f32_16x16x32_bf16 a[72:75], a[200:203], v[208:211], a[72:75]\n"
    "ds_read_b128 a[152:155], v3 offset:6144\n"
    "v_mul_f32 v194, v194, v15\n"
    "v_mfma_f32_16x16x32_bf16 a[72:75], a[232:235], v[212:215], a[72:75]\n"
    "ds_read_b128 a[156:159], v3 offset:7168\n"
    "v_mov_b32 v195, v194\n"
    "v_mfma_f32_16x16x32_bf16 a[104:107], a[200:203], v[216:219], a[104:107]\n"
    "buffer_load_dwordx4 v2, s[36:39], s55 offen offset:3072 sc1 lds\n"
    "s_add_u32 s55, s55, 0x8000\n"
    "s_add_u32 m0, s62, 0x10000\n"
    "ds_read_b128 a[160:163], v3 offset:8192\n"
    "v_accvgpr_read_b32 v128, a0\n"
    "v_mfma_f32_16x16x32_bf16 a[104:107], a[232:235], v[220:223], a[104:107]\n"
    "ds_read_b128 a[164:167], v3 offset:9216\n"
    "v_accvgpr_read_b32 v129, a1\n"
    "v_mfma_f32_16x16x32_bf16 a[76:79], a[204:207], v[208:211], a[76:79]\n"
    "ds_read_b128 a[168:171], v3 offset:10240\n"
    "v_accvgpr_read_b32 v130, a2\n"
    "v_mfma_f32_16x16x32_bf16 a[76:79], a[236:239], v[212:215], a[76:79]\n"
    "ds_read_b128 a[172:175], v3 offset:11264\n"
    "v_accvgpr_read_b32 v131, a3\n"
    "v_mfma_f32_16x16x32_bf16 a[108:111], a[204:207], v[216:219], a[108:111]\n"
    "buffer_load_dwordx4 v2, s[40:43], s60 offen sc1 lds\n"
    "ds_read_b128 a[176:179], v3 offset:12288\n"
    "v_accvgpr_read_b32 v132, a4\n"
    "v_mfma_f32_16x16x32_bf16 a[108:111], a[236:239], v[220:223], a[108:111]\n"
    "ds_read_b128 a[180:183], v3 offset:13312\n"
    "v_accvgpr_read_b32 v133, a5\n"
    "v_mfma_f32_16x16x32_bf16 a[80:83], a[208:211], v[208:211], a[80:83]\n"
    "ds_read_b128 a[184:187], v3 offset:14336\n"
    "v_accvgpr_read_b32 v134, a6\n"
    "v_mfma_f32_16x16x32_bf16 a[80:83], a[240:243], v[212:215], a[80:83]\n"
    "ds_read_b128 a[188:191], v3 offset:15360\n"
    "v_accvgpr_read_b32 v135, a7\n"
    "v_mfma_f32_16x16x32_bf16 a[112:115], a[208:211], v[216:219], a[112:115]\n"
    "buffer_load_dwordx4 v2, s[40:43], s60 offen offset:1024 sc1 lds\n"
    "ds_read_b128 v[64:67], v200\n"
    "v_mul_f32 v128, v128, v192\n"
    "v_mfma_f32_16x16x32_bf16 a[112:115], a[240:243], v[220:223], a[112:115]\n"
    "ds_read_b128 v[68:71], v200 offset:1024\n"
    "v_mul_f32 v129, v129, v192\n"
    "v_mfma_f32_16x16x32_bf16 a[84:87], a[212:215], v[208:211], a[84:87]\n"
    "ds_read_b128 v[72:75], v200 offset:2048\n"
    "v_mul_f32 v130, v130, v192\n"
    "v_mfma_f32_16x16x32_bf16 a[84:87], a[244:247], v[212:215], a[84:87]\n"
    "ds_read_b128 v[76:79], v200 offset:3072\n"
    "v_mul_f32 v131, v131, v192\n"
    "v_mfma_f32_16x16x32_bf16 a[116:119], a[212:215], v[216:219], a[116:119]\n"
    "buffer_load_dwordx4 v2, s[40:43], s60 offen offset:2048 sc1 lds\n"
    "ds_read_b128 v[80:83], v200 offset:4096\n"
    "v_mul_f32 v132, v132, v192\n"
    "v_mfma_f32_16x16x32_bf16 a[116:119], a[244:247], v[220:223], a[116:119]\n"
    "ds_read_b128 v[84:87], v200 offset:5120\n"
    "v_mul_f32 v133, v133, v192\n"
    "v_mfma_f32_16x16x32_bf16 a[88:91], a[216:219], v[208:211], a[88:91]\n"
    "ds_read_b128 v[88:91], v200 offset:6144\n"
    "v_mul_f32 v134, v134, v192\n"
    "v_mfma_f32_16x16x32_bf16 a[88:91], a[248:251], v[212:215], a[88:91]\n"
    "ds_read_b128 v[92:95], v200 offset:7168\n"
    "v_mul_f32 v135, v135, v192\n"
    "v_mfma_f32_16x16x32_bf16 a[120:123], a[216:219], v[216:219], a[120:123]\n"
    "buffer_load_dwordx4 v2, s[40:43], s60 offen offset:3072 sc1 lds\n"
    "s_sub_u32 s60, s60, 0x8000\n"
    "s_add_u32 m0, s62, 0x14000\n"
    "ds_read_b128 v[96:99], v200 offset:8192\n"
    "v_cvt_pk_bf16_f32 v136, v128, v129\n"
    "v_mfma_f32_16x16x32_bf16 a[120:123], a[248:251], v[220:223], a[120:123]\n"
    "ds_read_b128 v[100:103], v200 offset:9216\n"
    "v_cvt_pk_bf16_f32 v137, v130, v131\n"
    "v_mfma_f32_16x16x32_bf16 a[92:95], a[220:223], v[208:211], a[92:95]\n"
    "ds_read_b128 v[104:107], v200 offset:10240\n"
    "v_cvt_pk_bf16_f32 v138, v132, v133\n"
    "v_mfma_f32_16x16x32_bf16 a[92:95], a[252:255], v[212:215], a[92:95]\n"
    "ds_read_b128 v[108:111], v200 offset:11264\n"
    "v_cvt_pk_bf16_f32 v139, v134, v135\n"
    "v_mfma_f32_16x16x32_bf16 a[124:127], a[220:223], v[216:219], a[124:127]\n"
    "buffer_load_dwordx4 v2, s[40:43], s61 offen sc1 lds\n"
    "ds_read_b128 v[112:115], v200 offset:12288\n"
    "s_add_u32 s22, s48, 0\n"
    "v_mfma_f32_16x16x32_bf16 a[124:127], a[252:255], v[220:223], a[124:127]\n"
    "ds_read_b128 v[116:119], v200 offset:13312\n"
    "ds_read_b128 v[120:123], v200 offset:14336\n"
    "ds_read_b128 v[124:127], v200 offset:15360\n"
    "buffer_store_dwordx4 v[136:139], v2, s[44:47], s22 offen sc1\n"
    "v_accvgpr_read_b32 v140, a8\n"
    "v_accvgpr_read_b32 v141, a9\n"
    "v_accvgpr_read_b32 v142, a10\n"
    "v_accvgpr_read_b32 v143, a11\n"
    "v_accvgpr_read_b32 v144, a12\n"
    "v_accvgpr_read_b32 v145, a13\n"
    "v_accvgpr_read_b32 v146, a14\n"
    "buffer_load_dwordx4 v2, s[40:43], s61 offen offset:1024 sc1 lds\n"
    "v_accvgpr_read_b32 v147, a15\n"
    "v_pk_mul_f32 v[140:141], v[140:141], v[192:193]\n"
    "v_pk_mul_f32 v[142:143], v[142:143], v[192:193]\n"
    "v_pk_mul_f32 v[144:145], v[144:145], v[192:193]\n"
    "v_pk_mul_f32 v[146:147], v[146:147], v[192:193]\n"
    "v_cvt_pk_bf16_f32 v148, v140, v141\n"
    "v_cvt_pk_bf16_f32 v149, v142, v143\n"
    "v_cvt_pk_bf16_f32 v150, v144, v145\n"
    "v_cvt_pk_bf16_f32 v151, v146, v147\n"
    "s_add_u32 s22, s48, 0x1000\n"
    "buffer_store_dwordx4 v[148:151], v2, s[44:47], s22 offen sc1\n"
    "v_accvgpr_read_b32 v128, a16\n"
    "v_accvgpr_read_b32 v129, a17\n"
    "v_accvgpr_read_b32 v130, a18\n"
    "v_accvgpr_read_b32 v131, a19\n"
    "buffer_load_dwordx4 v2, s[40:43], s61 offen offset:2048 sc1 lds\n"
    "v_accvgpr_read_b32 v132, a20\n"
    "v_accvgpr_read_b32 v133, a21\n"
    "v_accvgpr_read_b32 v134, a22\n"
    "v_accvgpr_read_b32 v135, a23\n"
    "v_pk_mul_f32 v[128:129], v[128:129], v[192:193]\n"
    "v_pk_mul_f32 v[130:131], v[130:131], v[192:193]\n"
    "v_pk_mul_f32 v[132:133], v[132:133], v[192:193]\n"
    "v_pk_mul_f32 v[134:135], v[134:135], v[192:193]\n"
    "v_cvt_pk_bf16_f32 v136, v128, v129\n"
    "v_cvt_pk_bf16_f32 v137, v130, v131\n"
    "v_cvt_pk_bf16_f32 v138, v132, v133\n"
    "v_cvt_pk_bf16_f32 v139, v134, v135\n"
    "s_add_u32 s22, s48, 0x2000\n"
    "buffer_store_dwordx4 v[136:139], v2, s[44:47], s22 offen sc1\n"
    "v_accvgpr_read_b32 v140, a24\n"
    "buffer_load_dwordx4 v2, s[40:43], s61 offen offset:3072 sc1 lds\n"
    "s_sub_u32 s61, s61, 0x8000\n"
    "s_xor_b32 s62, s62, 0x8000\n"
    "v_accvgpr_read_b32 v141, a25\n"
    "v_accvgpr_read_b32 v142, a26\n"
    "v_accvgpr_read_b32 v143, a27\n"
    "v_accvgpr_read_b32 v144, a28\n"
    "v_accvgpr_read_b32 v145, a29\n"
    "v_accvgpr_read_b32 v146, a30\n"
    "v_accvgpr_read_b32 v147, a31\n"
    "v_pk_mul_f32 v[140:141], v[140:141], v[192:193]\n"
    "v_pk_mul_f32 v[142:143], v[142:143], v[192:193]\n"
    "v_pk_mul_f32 v[144:145], v[144:145], v[192:193]\n"
    "v_pk_mul_f32 v[146:147], v[146:147], v[192:193]\n"
    "v_cvt_pk_bf16_f32 v148, v140, v141\n"
    "v_cvt_pk_bf16_f32 v149, v142, v143\n"
    "v_cvt_pk_bf16_f32 v150, v144, v145\n"
    "v_cvt_pk_bf16_f32 v151, v146, v147\n"
    "s_add_u32 s22, s48, 0x3000\n"
    "buffer_store_dwordx4 v[148:151], v2, s[44:47], s22 offen sc1\n"
    "v_accvgpr_read_b32 v128, a32\n"
    "v_accvgpr_read_b32 v129, a33\n"
    "v_accvgpr_read_b32 v130, a34\n"
    "v_accvgpr_read_b32 v131, a35\n"
    "v_accvgpr_read_b32 v132, a36\n"
    "v_accvgpr_read_b32 v133, a37\n"
    "v_accvgpr_read_b32 v134, a38\n"
    "v_accvgpr_read_b32 v135, a39\n"
    "v_pk_mul_f32 v[128:129], v[128:129], v[194:195]\n"
    "v_pk_mul_f32 v[130:131], v[130:131], v[194:195]\n"
    "v_pk_mul_f32 v[132:133], v[132:133], v[194:195]\n"
    "v_pk_mul_f32 v[134:135], v[134:135], v[194:195]\n"
    "v_cvt_pk_bf16_f32 v136, v128, v129\n"
    "v_cvt_pk_bf16_f32 v137, v130, v131\n"
    "v_cvt_pk_bf16_f32 v138, v132, v133\n"
    "v_cvt_pk_bf16_f32 v139, v134, v135\n"
    "s_add_u32 s22, s48, 0\n"
    "buffer_store_dwordx4 v[136:139], v2, s[44:47], s22 offen offset:1024 sc1\n"
    "v_accvgpr_read_b32 v140, a40\n"
    "v_accvgpr_read_b32 v141, a41\n"
    "v_accvgpr_read_b32 v142, a42\n"
    "v_accvgpr_read_b32 v143, a43\n"
    "v_accvgpr_read_b32 v144, a44\n"
    "v_accvgpr_read_b32 v145, a45\n"
    "v_accvgpr_read_b32 v146, a46\n"
    "v_accvgpr_read_b32 v147, a47\n"
    "v_pk_mul_f32 v[140:141], v[140:141], v[194:195]\n"
    "v_pk_mul_f32 v[142:143], v[142:143], v[194:195]\n"
    "v_pk_mul_f32 v[144:145], v[144:145], v[194:195]\n"
    "v_pk_mul_f32 v[146:147], v[146:147], v[194:195]\n"
    "v_cvt_pk_bf16_f32 v148, v140, v141\n"
    "v_cvt_pk_bf16_f32 v149, v142, v143\n"
    "v_cvt_pk_bf16_f32 v150, v144, v145\n"
    "v_cvt_pk_bf16_f32 v151, v146, v147\n"
    "s_add_u32 s22, s48, 0x1000\n"
    "buffer_store_dwordx4 v[148:151], v2, s[44:47], s22 offen offset:1024 sc1\n"
    "v_accvgpr_read_b32 v128, a48\n"
    "v_accvgpr_read_b32 v129, a49\n"
    "v_accvgpr_read_b32 v130, a50\n"
    "v_accvgpr_read_b32 v131, a51\n"
    "v_accvgpr_read_b32 v132, a52\n"
    "v_accvgpr_read_b32 v133, a53\n"
    "v_accvgpr_read_b32 v134, a54\n"
    "v_accvgpr_read_b32 v135, a55\n"
    "v_pk_mul_f32 v[128:129], v[128:129], v[194:195]\n"
    "v_pk_mul_f32 v[130:131], v[130:131], v[194:195]\n"
    "v_pk_mul_f32 v[132:133], v[132:133], v[194:195]\n"
    "v_pk_mul_f32 v[134:135], v[134:135], v[194:195]\n"
    "v_cvt_pk_bf16_f32 v136, v128, v129\n"
    "v_cvt_pk_bf16_f32 v137, v130, v131\n"
    "v_cvt_pk_bf16_f32 v138, v132, v133\n"
    "v_cvt_pk_bf16_f32 v139, v134, v135\n"
    "s_add_u32 s22, s48, 0x2000\n"
    "buffer_store_dwordx4 v[136:139], v2, s[44:47], s22 offen offset:1024 sc1\n"
    "v_accvgpr_read_b32 v140, a56\n"
    "v_accvgpr_read_b32 v141, a57\n"
    "v_accvgpr_read_b32 v142, a58\n"
    "v_accvgpr_read_b32 v143, a59\n"
    "v_accvgpr_read_b32 v144, a60\n"
    "v_accvgpr_read_b32 v145, a61\n"
    "v_accvgpr_read_b32 v146, a62\n"
    "v_accvgpr_read_b32 v147, a63\n"
    "v_pk_mul_f32 v[140:141], v[140:141], v[194:195]\n"
    "v_pk_mul_f32 v[142:143], v[142:143], v[194:195]\n"
    "v_pk_mul_f32 v[144:145], v[144:145], v[194:195]\n"
    "v_pk_mul_f32 v[146:147], v[146:147], v[194:195]\n"
    "v_cvt_pk_bf16_f32 v148, v140, v141\n"
    "v_cvt_pk_bf16_f32 v149, v142, v143\n"
    "v_cvt_pk_bf16_f32 v150, v144, v145\n"
    "v_cvt_pk_bf16_f32 v151, v146, v147\n"
    "s_add_u32 s22, s48, 0x3000\n"
    "buffer_store_dwordx4 v[148:151], v2, s[44:47], s22 offen offset:1024 sc1\n"
    "v_rcp_f32 v188, v242\n"
    "v_rcp_f32 v190, v243\n"
    "s_nop 0\n"
    "v_mul_f32 v188, v188, v16\n"
    "v_mov_b32 v189, v188\n"
    "v_mul_f32 v190, v190, v17\n"
    "v_mov_b32 v191, v190\n"
    "v_mov_b32 v32, 0\n"
    "v_mov_b32 v33, 0\n"
    "v_mov_b32 v34, 0\n"
    "v_mov_b32 v35, 0\n"
    "v_accvgpr_read_b32 v152, a64\n"
    "v_accvgpr_read_b32 v153, a65\n"
    "v_accvgpr_read_b32 v154, a66\n"
    "v_accvgpr_read_b32 v155, a67\n"
    "v_accvgpr_read_b32 v156, a68\n"
    "v_accvgpr_read_b32 v157, a69\n"
    "v_accvgpr_read_b32 v158, a70\n"
    "v_accvgpr_read_b32 v159, a71\n"
    "v_pk_mul_f32 v[152:153], v[152:153], v[188:189]\n"
    "v_pk_mul_f32 v[154:155], v[154:155], v[188:189]\n"
    "v_pk_mul_f32 v[156:157], v[156:157], v[188:189]\n"
    "v_pk_mul_f32 v[158:159], v[158:159], v[188:189]\n"
    "v_cvt_pk_bf16_f32 v160, v152, v153\n"
    "v_cvt_pk_bf16_f32 v161, v154, v155\n"
    "v_cvt_pk_bf16_f32 v162, v156, v157\n"
    "v_cvt_pk_bf16_f32 v163, v158, v159\n"
    "s_add_u32 s22, s48, 0\n"
    "buffer_store_dwordx4 v[160:163], v2, s[44:47], s22 offen offset:2048 sc1\n"
    "v_accvgpr_read_b32 v164, a72\n"
    "v_accvgpr_read_b32 v165, a73\n"
    "v_accvgpr_read_b32 v166, a74\n"
    "v_accvgpr_read_b32 v167, a75\n"
    "v_accvgpr_read_b32 v168, a76\n"
    "v_accvgpr_read_b32 v169, a77\n"
    "v_accvgpr_read_b32 v170, a78\n"
    "v_accvgpr_read_b32 v171, a79\n"
    "v_pk_mul_f32 v[164:165], v[164:165], v[188:189]\n"
    "v_pk_mul_f32 v[166:167], v[166:167], v[188:189]\n"
    "v_pk_mul_f32 v[168:169], v[168:169], v[188:189]\n"
    "v_pk_mul_f32 v[170:171], v[170:171], v[188:189]\n"
    "v_cvt_pk_bf16_f32 v172, v164, v165\n"
    "v_cvt_pk_bf16_f32 v173, v166, v167\n"
    "v_cvt_pk_bf16_f32 v174, v168, v169\n"
    "v_cvt_pk_bf16_f32 v175, v170, v171\n"
    "s_add_u32 s22, s48, 0x1000\n"
    "buffer_store_dwordx4 v[172:175], v2, s[44:47], s22 offen offset:2048 sc1\n"
    "v_accvgpr_read_b32 v152, a80\n"
    "v_accvgpr_read_b32 v153, a81\n"
    "v_accvgpr_read_b32 v154, a82\n"
    "v_accvgpr_read_b32 v155, a83\n"
    "v_accvgpr_read_b32 v156, a84\n"
    "v_accvgpr_read_b32 v157, a85\n"
    "v_accvgpr_read_b32 v158, a86\n"
    "v_accvgpr_read_b32 v159, a87\n"
    "v_pk_mul_f32 v[152:153], v[152:153], v[188:189]\n"
    "v_pk_mul_f32 v[154:155], v[154:155], v[188:189]\n"
    "v_pk_mul_f32 v[156:157], v[156:157], v[188:189]\n"
    "v_pk_mul_f32 v[158:159], v[158:159], v[188:189]\n"
    "v_cvt_pk_bf16_f32 v160, v152, v153\n"
    "v_cvt_pk_bf16_f32 v161, v154, v155\n"
    "v_cvt_pk_bf16_f32 v162, v156, v157\n"
    "v_cvt_pk_bf16_f32 v163, v158, v159\n"
    "s_add_u32 s22, s48, 0x2000\n"
    "buffer_store_dwordx4 v[160:163], v2, s[44:47], s22 offen offset:2048 sc1\n"
    "v_accvgpr_read_b32 v164, a88\n"
    "v_accvgpr_read_b32 v165, a89\n"
    "v_accvgpr_read_b32 v166, a90\n"
    "v_accvgpr_read_b32 v167, a91\n"
    "v_accvgpr_read_b32 v168, a92\n"
    "v_accvgpr_read_b32 v169, a93\n"
    "v_accvgpr_read_b32 v170, a94\n"
    "v_accvgpr_read_b32 v171, a95\n"
    "v_pk_mul_f32 v[164:165], v[164:165], v[188:189]\n"
    "v_pk_mul_f32 v[166:167], v[166:167], v[188:189]\n"
    "v_pk_mul_f32 v[168:169], v[168:169], v[188:189]\n"
    "v_pk_mul_f32 v[170:171], v[170:171], v[188:189]\n"
    "v_cvt_pk_bf16_f32 v172, v164, v165\n"
    "v_cvt_pk_bf16_f32 v173, v166, v167\n"
    "v_cvt_pk_bf16_f32 v174, v168, v169\n"
    "v_cvt_pk_bf16_f32 v175, v170, v171\n"
    "s_add_u32 s22, s48, 0x3000\n"
    "buffer_store_dwordx4 v[172:175], v2, s[44:47], s22 offen offset:2048 sc1\n"
    "v_mov_b32 v60, v252\n"
    "v_mov_b32 v61, v253\n"
    "v_mov_b32 v62, v254\n"
    "v_mov_b32 v63, v255\n"
    "v_mov_b32 v52, 0xff800000\n"
    "v_mov_b32 v53, 0xff800000\n"
    "v_mov_b32 v54, 0xff800000\n"
    "v_mov_b32 v55, 0xff800000\n"
    "v_mfma_f32_16x16x32_bf16 a[64:67], v[32:35], v[32:35], 0\n"
    "v_accvgpr_read_b32 v128, a96\n"
    "v_accvgpr_read_b32 v129, a97\n"
    "v_accvgpr_read_b32 v130, a98\n"
    "v_accvgpr_read_b32 v131, a99\n"
    "v_accvgpr_read_b32 v132, a100\n"
    "v_mfma_f32_16x16x32_bf16 a[68:71], v[32:35], v[32:35], 0\n"
    "v_accvgpr_read_b32 v133, a101\n"
    "v_accvgpr_read_b32 v134, a102\n"
    "v_accvgpr_read_b32 v135, a103\n"
    "v_mul_f32 v128, v128, v190\n"
    "v_mul_f32 v129, v129, v190\n"
    "v_mfma_f32_16x16x32_bf16 a[72:75], v[32:35], v[32:35], 0\n"
    "v_mul_f32 v130, v130, v190\n"
    "v_mul_f32 v131, v131, v190\n"
    "v_mul_f32 v132, v132, v190\n"
    "v_mul_f32 v133, v133, v190\n"
    "v_mul_f32 v134, v134, v190\n"
    "v_mfma_f32_16x16x32_bf16 a[76:79], v[32:35], v[32:35], 0\n"
    "v_mul_f32 v135, v135, v190\n"
    "v_cvt_pk_bf16_f32 v136, v128, v129\n"
    "v_cvt_pk_bf16_f32 v137, v130, v131\n"
    "v_cvt_pk_bf16_f32 v138, v132, v133\n"
    "v_cvt_pk_bf16_f32 v139, v134, v135\n"
    "v_mfma_f32_16x16x32_bf16 a[80:83], v[32:35], v[32:35], 0\n"
    "s_add_u32 s22, s48, 0\n"
    "v_mfma_f32_16x16x32_bf16 a[84:87], v[32:35], v[32:35], 0\n"
    "buffer_store_dwordx4 v[136:139], v2, s[44:47], s22 offen offset:3072 sc1\n"
    "v_accvgpr_read_b32 v140, a104\n"
    "v_accvgpr_read_b32 v141, a105\n"
    "v_accvgpr_read_b32 v142, a106\n"
    "v_mfma_f32_16x16x32_bf16 a[88:91], v[32:35], v[32:35], 0\n"
    "v_accvgpr_read_b32 v143, a107\n"
    "v_accvgpr_read_b32 v144, a108\n"
    "v_accvgpr_read_b32 v145, a109\n"
    "v_accvgpr_read_b32 v146, a110\n"
    "v_accvgpr_read_b32 v147, a111\n"
    "v_mfma_f32_16x16x32_bf16 a[92:95], v[32:35], v[32:35], 0\n"
    "v_mul_f32 v140, v140, v190\n"
    "v_mul_f32 v141, v141, v190\n"
    "v_mul_f32 v142, v142, v190\n"
    "v_mul_f32 v143, v143, v190\n"
    "v_mul_f32 v144, v144, v190\n"
    "v_mul_f32 v145, v145, v190\n"
    "v_mul_f32 v146, v146, v190\n"
    "v_mul_f32 v147, v147, v190\n"
    "v_cvt_pk_bf16_f32 v148, v140, v141\n"
    "v_cvt_pk_bf16_f32 v149, v142, v143\n"
    "v_cvt_pk_bf16_f32 v150, v144, v145\n"
    "v_cvt_pk_bf16_f32 v151, v146, v147\n"
    "s_add_u32 s22, s48, 0x1000\n"
    "buffer_store_dwordx4 v[148:151], v2, s[44:47], s22 offen offset:3072 sc1\n"
    "v_accvgpr_read_b32 v128, a112\n"
    "v_accvgpr_read_b32 v129, a113\n"
    "v_accvgpr_read_b32 v130, a114\n"
    "v_accvgpr_read_b32 v131, a115\n"
    "v_accvgpr_read_b32 v132, a116\n"
    "v_accvgpr_read_b32 v133, a117\n"
    "v_accvgpr_read_b32 v134, a118\n"
    "v_accvgpr_read_b32 v135, a119\n"
    "v_pk_mul_f32 v[128:129], v[128:129], v[190:191]\n"
    "v_pk_mul_f32 v[130:131], v[130:131], v[190:191]\n"
    "v_pk_mul_f32 v[132:133], v[132:133], v[190:191]\n"
    "v_pk_mul_f32 v[134:135], v[134:135], v[190:191]\n"
    "v_cvt_pk_bf16_f32 v136, v128, v129\n"
    "v_cvt_pk_bf16_f32 v137, v130, v131\n"
    "v_cvt_pk_bf16_f32 v138, v132, v133\n"
    "v_cvt_pk_bf16_f32 v139, v134, v135\n"
    "s_add_u32 s22, s48, 0x2000\n"
    "buffer_store_dwordx4 v[136:139], v2, s[44:47], s22 offen offset:3072 sc1\n"
    "v_accvgpr_read_b32 v140, a120\n"
    "v_accvgpr_read_b32 v141, a121\n"
    "v_accvgpr_read_b32 v142, a122\n"
    "v_accvgpr_read_b32 v143, a123\n"
    "v_accvgpr_read_b32 v144, a124\n"
    "v_accvgpr_read_b32 v145, a125\n"
    "v_accvgpr_read_b32 v146, a126\n"
    "v_accvgpr_read_b32 v147, a127\n"
    "v_pk_mul_f32 v[140:141], v[140:141], v[190:191]\n"
    "v_pk_mul_f32 v[142:143], v[142:143], v[190:191]\n"
    "v_pk_mul_f32 v[144:145], v[144:145], v[190:191]\n"
    "v_pk_mul_f32 v[146:147], v[146:147], v[190:191]\n"
    "v_cvt_pk_bf16_f32 v148, v140, v141\n"
    "v_cvt_pk_bf16_f32 v149, v142, v143\n"
    "v_cvt_pk_bf16_f32 v150, v144, v145\n"
    "v_cvt_pk_bf16_f32 v151, v146, v147\n"
    "s_add_u32 s22, s48, 0x3000\n"
    "buffer_store_dwordx4 v[148:151], v2, s[44:47], s22 offen offset:3072 sc1\n"
    "v_mfma_f32_16x16x32_bf16 a[96:99], v[32:35], v[32:35], 0\n"
    "v_mfma_f32_16x16x32_bf16 a[100:103], v[32:35], v[32:35], 0\n"
    "v_mfma_f32_16x16x32_bf16 a[104:107], v[32:35], v[32:35], 0\n"
    "v_mfma_f32_16x16x32_bf16 a[108:111], v[32:35], v[32:35], 0\n"
    "v_mfma_f32_16x16x32_bf16 a[112:115], v[32:35], v[32:35], 0\n"
    "v_mfma_f32_16x16x32_bf16 a[116:119], v[32:35], v[32:35], 0\n"
    "v_mfma_f32_16x16x32_bf16 a[120:123], v[32:35], v[32:35], 0\n"
    "v_mfma_f32_16x16x32_bf16 a[124:127], v[32:35], v[32:35], 0\n"
    "v_xor_b32 v3, 0x8000, v3\n"
    "s_mov_b32 s52, 1\n"
    "s_mov_b32 s51, 0\n"
    "s_mov_b32 s9, 0\n"
    "s_mov_b32 s68, s69\n"
    "s_sub_u32 s50, s63, 1\n"
    "s_waitcnt vmcnt(13) lgkmcnt(0)\n"
    "s_barrier\n"
    "s_cmp_lg_u32 s63, 0\n"
    "s_cbranch_scc1 FIRST_%=\n"
    "v_mfma_f32_16x16x32_bf16 a[0:3], v[32:35], v[32:35], 0\n"
    "v_mfma_f32_16x16x32_bf16 a[4:7], v[32:35], v[32:35], 0\n"
    "v_mfma_f32_16x16x32_bf16 a[8:11], v[32:35], v[32:35], 0\n"
    "v_mfma_f32_16x16x32_bf16 a[12:15], v[32:35], v[32:35], 0\n"
    "v_mfma_f32_16x16x32_bf16 a[16:19], v[32:35], v[32:35], 0\n"
    "v_mfma_f32_16x16x32_bf16 a[20:23], v[32:35], v[32:35], 0\n"
    "v_mfma_f32_16x16x32_bf16 a[24:27], v[32:35], v[32:35], 0\n"
    "v_mfma_f32_16x16x32_bf16 a[28:31], v[32:35], v[32:35], 0\n"
    "v_mfma_f32_16x16x32_bf16 a[32:35], v[32:35], v[32:35], 0\n"
    "v_mfma_f32_16x16x32_bf16 a[36:39], v[32:35], v[32:35], 0\n"
    "v_mfma_f32_16x16x32_bf16 a[40:43], v[32:35], v[32:35], 0\n"
    "v_mfma_f32_16x16x32_bf16 a[44:47], v[32:35], v[32:35], 0\n"
    "v_mfma_f32_16x16x32_bf16 a[48:51], v[32:35], v[32:35], 0\n"
    "v_mfma_f32_16x16x32_bf16 a[52:55], v[32:35], v[32:35], 0\n"
    "v_mfma_f32_16x16x32_bf16 a[56:59], v[32:35], v[32:35], 0\n"
    "v_mfma_f32_16x16x32_bf16 a[60:63], v[32:35], v[32:35], 0\n"
    "v_mov_b32 v240, 0\n"
    "v_mov_b32 v241, 0\n"
    "v_mov_b32 v242, 0\n"
    "v_mov_b32 v243, 0\n"
    "v_mov_b32 v10, 0x7f800000\n"
    "v_mov_b32 v11, 0x7f800000\n"
    "v_mov_b32 v12, 0x7f800000\n"
    "v_mov_b32 v13, 0x7f800000\n"
    "s_waitcnt vmcnt(0)\n"
    "v_mov_b32 v4, s4\n"
    "v_mov_b32 v5, s5\n"
    "v_mov_b32 v6, s4\n"
    "v_mov_b32 v7, s5\n"
    "s_mov_b64 exec, 1\n"
    "buffer_store_dwordx4 v[4:7], v2, s[44:47], s49 offen sc1\n"
    "s_mov_b64 exec, -1\n"
    "s_mov_b32 s52, 2\n"
    "FIN_%=:\n"
    "v_and_b32 v200, 15, v1\n"
    "v_lshlrev_b32 v200, 2, v200\n"
    "s_lshl_b32 s22, s69, 8\n"
    "s_add_u32 s22, s22, 0x3c00000\n"
    "buffer_load_dword v14, v200, s[44:47], s22 offen sc1\n"
    "buffer_load_dword v15, v200, s[44:47], s22 offen offset:64 sc1\n"
    "buffer_load_dword v16, v200, s[44:47], s22 offen offset:128 sc1\n"
    "buffer_load_dword v17, v200, s[44:47], s22 offen offset:192 sc1\n"
    "s_cmp_eq_u32 s52, 2\n"
    "s_cbranch_scc1 EM2_%=\n"
    "v_exp_f32 v250, v170\n"
    "s_add_u32 m0, s16, 0x4000\n"
    "s_nop 0\n"
    "buffer_load_dwordx4 v2, s[44:47], s71 offen sc1 lds\n"
    "v_exp_f32 v251, v171\n"
    "v_exp_f32 v248, v184\n"
    "v_cvt_pk_bf16_f32 v213, v250, v251\n"
    "v_exp_f32 v249, v185\n"
    "v_exp_f32 v250, v186\n"
    "v_cvt_pk_bf16_f32 v220, v248, v249\n"
    "v_exp_f32 v251, v187\n"
    "buffer_load_dwordx4 v2, s[44:47], s71 offen offset:1024 sc1 lds\n"
    "v_exp_f32 v248, v172\n"
    "v_cvt_pk_bf16_f32 v221, v250, v251\n"
    "v_exp_f32 v249, v173\n"
    "v_exp_f32 v250, v174\n"
    "v_cvt_pk_bf16_f32 v214, v248, v249\n"
    "v_exp_f32 v251, v175\n"
    "v_exp_f32 v248, v188\n"
    "v_cvt_pk_bf16_f32 v215, v250, v251\n"
    "v_exp_f32 v249, v189\n"
    "buffer_load_dwordx4 v2, s[44:47], s71 offen offset:2048 sc1 lds\n"
    "v_exp_f32 v250, v190\n"
    "v_cvt_pk_bf16_f32 v222, v248, v249\n"
    "v_exp_f32 v251, v191\n"
    "s_nop 0\n"
    "v_cvt_pk_bf16_f32 v223, v250, v251\n"
    "s_nop 1\n"
    "v_mfma_f32_16x16x32_bf16 v[232:235], v[244:247], v[208:211], 0\n"
    "v_mfma_f32_16x16x32_bf16 v[232:235], v[244:247], v[212:215], v[232:235]\n"
    "buffer_load_dwordx4 v2, s[44:47], s71 offen offset:3072 sc1 lds\n"
    "v_mfma_f32_16x16x32_bf16 v[236:239], v[244:247], v[216:219], 0\n"
    "v_mfma_f32_16x16x32_bf16 v[236:239], v[244:247], v[220:223], v[236:239]\n"
    "v_lshrrev_b32 v200, 3, v1\n"
    "v_lshlrev_b32 v200, 12, v200\n"
    "v_lshrrev_b32 v201, 4, v1\n"
    "v_and_b32 v202, 7, v1\n"
    "v_xor_b32 v201, v201, v202\n"
    "v_lshlrev_b32 v201, 4, v201\n"
    "v_xor_b32 v202, 0, v201\n"
    "v_add_u32 v202, v202, v200\n"
    "s_add_u32 s71, s71, 0x1c000\n"
    "s_add_u32 m0, s16, 0xc000\n"
    "s_nop 0\n"
    "buffer_load_dwordx4 v2, s[44:47], s71 offen sc1 lds\n"
    "v_add_u32 v192, 0, v202\n"
    "v_xor_b32 v202, 64, v201\n"
    "v_add_u32 v202, v202, v200\n"
    "v_add_u32 v193, 0x7c00, v202\n"
    "v_xor_b32 v202, 0, v201\n"
    "v_add_u32 v202, v202, v200\n"
    "v_add_u32 v194, 0xf800, v202\n"
    "v_xor_b32 v202, 64, v201\n"
    "v_add_u32 v202, v202, v200\n"
    "v_add_u32 v195, 0x17400, v202\n"
    "v_max_f32 v4, v232, v236\n"
    "buffer_load_dwordx4 v2, s[44:47], s71 offen offset:1024 sc1 lds\n"
    "v_cmp_lt_f32 vcc, 0x49800000, v4\n"
    "s_nop 0\n"
    "s_cbranch_vccnz SCD_%=\n"
    "SRD_%=:\n"
    "v_add_f32 v242, v242, v232\n"
    "v_add_f32 v243, v243, v236\n"
    "v_mfma_f32_16x16x32_bf16 a[64:67], a[192:195], v[208:211], a[64:67]\n"
    "s_and_b32 s22, s19, 1\n"
    "s_lshl_b32 s22, s22, 13\n"
    "v_mfma_f32_16x16x32_bf16 a[64:67], a[224:227], v[212:215], a[64:67]\n"
    "v_and_b32 v200, 15, v1\n"
    "v_lshrrev_b32 v201, 1, v200\n"
    "v_mfma_f32_16x16x32_bf16 a[96:99], a[192:195], v[216:219], a[96:99]\n"
    "buffer_load_dwordx4 v2, s[44:47], s71 offen offset:2048 sc1 lds\n"
    "v_lshrrev_b32 v202, 4, v1\n"
    "v_xor_b32 v201, v201, v202\n"
    "v_mfma_f32_16x16x32_bf16 a[96:99], a[224:227], v[220:223], a[96:99]\n"
    "v_lshlrev_b32 v201, 4, v201\n"
    "v_lshlrev_b32 v200, 7, v200\n"
    "v_mfma_f32_16x16x32_bf16 a[68:71], a[196:199], v[208:211], a[68:71]\n"
    "v_add_u32 v200, v200, v201\n"
    "v_add_u32 v196, s22, v200\n"
    "v_mfma_f32_16x16x32_bf16 a[68:71], a[228:231], v[212:215], a[68:71]\n"
    "v_xor_b32 v197, 64, v196\n"
    "s_lshr_b32 s22, s19, 1\n"
    "v_mfma_f32_16x16x32_bf16 a[100:103], a[196:199], v[216:219], a[100:103]\n"
    "buffer_load_dwordx4 v2, s[44:47], s71 offen offset:3072 sc1 lds\n"
    "s_lshl_b32 s22, s22, 12\n"
    "s_add_u32 s22, s22, 0x4000\n"
    "v_mfma_f32_16x16x32_bf16 a[100:103], a[228:231], v[220:223], a[100:103]\n"
    "v_lshlrev_b32 v200, 3, v1\n"
    "v_add_u32 v198, s22, v200\n"
    "v_mfma_f32_16x16x32_bf16 a[72:75], a[200:203], v[208:211], a[72:75]\n"
    "v_mfma_f32_16x16x32_bf16 a[72:75], a[232:235], v[212:215], a[72:75]\n"
    "v_mfma_f32_16x16x32_bf16 a[104:107], a[200:203], v[216:219], a[104:107]\n"
    "s_add_u32 s71, s71, 0x1c000\n"
    "s_add_u32 m0, s16, 0\n"
    "s_nop 0\n"
    "buffer_load_dwordx4 v192, s[64:67], s13 offen lds\n"
    "v_mfma_f32_16x16x32_bf16 a[104:107], a[232:235], v[220:223], a[104:107]\n"
    "v_mfma_f32_16x16x32_bf16 a[76:79], a[204:207], v[208:211], a[76:79]\n"
    "v_mfma_f32_16x16x32_bf16 a[76:79], a[236:239], v[212:215], a[76:79]\n"
    "v_mfma_f32_16x16x32_bf16 a[108:111], a[204:207], v[216:219], a[108:111]\n"
    "buffer_load_dwordx4 v193, s[64:67], s13 offen offset:1024 lds\n"
    "v_mfma_f32_16x16x32_bf16 a[108:111], a[236:239], v[220:223], a[108:111]\n"
    "v_mfma_f32_16x16x32_bf16 a[80:83], a[208:211], v[208:211], a[80:83]\n"
    "v_mfma_f32_16x16x32_bf16 a[80:83], a[240:243], v[212:215], a[80:83]\n"
    "v_mfma_f32_16x16x32_bf16 a[112:115], a[208:211], v[216:219], a[112:115]\n"
    "buffer_load_dwordx4 v194, s[64:67], s13 offen offset:2048 lds\n"
    "v_mfma_f32_16x16x32_bf16 a[112:115], a[240:243], v[220:223], a[112:115]\n"
    "v_mfma_f32_16x16x32_bf16 a[84:87], a[212:215], v[208:211], a[84:87]\n"
    "v_mfma_f32_16x16x32_bf16 a[84:87], a[244:247], v[212:215], a[84:87]\n"
    "v_mfma_f32_16x16x32_bf16 a[116:119], a[212:215], v[216:219], a[116:119]\n"
    "buffer_load_dwordx4 v195, s[64:67], s13 offen offset:3072 lds\n"
    "v_mfma_f32_16x16x32_bf16 a[116:119], a[244:247], v[220:223], a[116:119]\n"
    "v_mfma_f32_16x16x32_bf16 a[88:91], a[216:219], v[208:211], a[88:91]\n"
    "v_mfma_f32_16x16x32_bf16 a[88:91], a[248:251], v[212:215], a[88:91]\n"
    "v_mfma_f32_16x16x32_bf16 a[120:123], a[216:219], v[216:219], a[120:123]\n"
    "v_mfma_f32_16x16x32_bf16 a[120:123], a[248:251], v[220:223], a[120:123]\n"
    "v_mfma_f32_16x16x32_bf16 a[92:95], a[220:223], v[208:211], a[92:95]\n"
    "v_mfma_f32_16x16x32_bf16 a[92:95], a[252:255], v[212:215], a[92:95]\n"
    "v_mfma_f32_16x16x32_bf16 a[124:127], a[220:223], v[216:219], a[124:127]\n"
    "v_mfma_f32_16x16x32_bf16 a[124:127], a[252:255], v[220:223], a[124:127]\n"
    "v_mov_b32 v11, v32\n"
    "v_mov_b32 v12, v44\n"
    "v_mov_b32 v13, v56\n"
    "v_mov_b32 v10, v20\n"
    "v_mov_b32 v9, v10\n"
    "s_nop 1\n"
    "v_permlane32_swap_b32 v10, v9\n"
    "v_max_f32 v10, v10, v9\n"
    "v_mov_b32 v9, v10\n"
    "s_nop 1\n"
    "v_permlane16_swap_b32 v10, v9\n"
    "v_max_f32 v10, v10, v9\n"
    "MERGE_%=:\n"
    "s_mul_i32 s48, s69, 0xb8\n"
    "s_lshl_b32 s22, s20, 2\n"
    "s_add_u32 s48, s48, s22\n"
    "s_lshl_b32 s48, s48, 12\n"
    "s_add_u32 s48, s48, 0x2100000\n"
    "s_lshr_b32 s49, s69, 1\n"
    "s_lshl_b32 s49, s49, 2\n"
    "s_lshr_b32 s22, s20, 3\n"
    "s_add_u32 s49, s49, s22\n"
    "s_lshl_b32 s49, s49, 1\n"
    "s_and_b32 s22, s69, 1\n"
    "s_add_u32 s49, s49, s22\n"
    "s_lshl_b32 s49, s49, 3\n"
    "s_and_b32 s22, s20, 7\n"
    "s_add_u32 s49, s49, s22\n"
    "s_lshl_b32 s49, s49, 4\n"
    "s_add_u32 s49, s49, 0x4000\n"
    "s_lshr_b32 s22, s19, 1\n"
    "s_lshl_b32 s22, s22, 3\n"
    "s_add_u32 s49, s49, s22\n"
    "s_mul_i32 s22, s19, 0x4400\n"
    "s_add_u32 s22, s22, 0x17000\n"
    "v_add_u32 v4, s22, v2\n"
    "s_xor_b32 s22, s19, 2\n"
    "s_mul_i32 s22, s22, 0x4400\n"
    "s_add_u32 s22, s22, 0x17000\n"
    "v_add_u32 v5, s22, v2\n"
    "s_cmp_ge_u32 s19, 2\n"
    "s_cbranch_scc1 MGB_%=\n"
    "v_mov_b32 v224, v12\n"
    "v_mov_b32 v225, v13\n"
    "v_mov_b32 v226, v242\n"
    "v_mov_b32 v227, v243\n"
    "ds_write_b128 v4, v[224:227] offset:16384\n"
    "s_add_u32 s13, s13, 0x80\n"
    "s_add_u32 m0, s16, 0x8000\n"
    "s_nop 0\n"
    "buffer_load_dwordx4 v192, s[64:67], s13 offen lds\n"
    "ds_write_b128 v4, a[64:67]\n"
    "ds_write_b128 v4, a[68:71] offset:1024\n"
    "buffer_load_dwordx4 v193, s[64:67], s13 offen offset:1024 lds\n"
    "ds_write_b128 v4, a[72:75] offset:2048\n"
    "ds_write_b128 v4, a[76:79] offset:3072\n"
    "buffer_load_dwordx4 v194, s[64:67], s13 offen offset:2048 lds\n"
    "ds_write_b128 v4, a[80:83] offset:4096\n"
    "ds_write_b128 v4, a[84:87] offset:5120\n"
    "buffer_load_dwordx4 v195, s[64:67], s13 offen offset:3072 lds\n"
    "ds_write_b128 v4, a[88:91] offset:6144\n"
    "ds_write_b128 v4, a[92:95] offset:7168\n"
    "s_add_u32 s13, s13, 0x80\n"
    "s_waitcnt lgkmcnt(4)\n"
    "ds_write_b128 v4, a[96:99] offset:8192\n"
    "ds_write_b128 v4, a[100:103] offset:9216\n"
    "ds_write_b128 v4, a[104:107] offset:10240\n"
    "ds_write_b128 v4, a[108:111] offset:11264\n"
    "ds_write_b128 v4, a[112:115] offset:12288\n"
    "ds_write_b128 v4, a[116:119] offset:13312\n"
    "ds_write_b128 v4, a[120:123] offset:14336\n"
    "ds_write_b128 v4, a[124:127] offset:15360\n"
    "s_waitcnt lgkmcnt(0)\n"
    "s_barrier\n"
    "ds_read_b128 v[228:231], v5 offset:16384\n"
    "ds_read_b128 v[64:67], v5\n"
    "ds_read_b128 v[68:71], v5 offset:1024\n"
    "ds_read_b128 v[72:75], v5 offset:2048\n"
    "ds_read_b128 v[76:79], v5 offset:3072\n"
    "ds_read_b128 v[80:83], v5 offset:4096\n"
    "ds_read_b128 v[84:87], v5 offset:5120\n"
    "ds_read_b128 v[88:91], v5 offset:6144\n"
    "ds_read_b128 v[92:95], v5 offset:7168\n"
    "ds_read_b128 v[96:99], v5 offset:8192\n"
    "ds_read_b128 v[100:103], v5 offset:9216\n"
    "ds_read_b128 v[104:107], v5 offset:10240\n"
    "ds_read_b128 v[108:111], v5 offset:11264\n"
    "s_waitcnt vmcnt(16) lgkmcnt(12)\n"
    "v_min_f32 v6, v10, v228\n"
    "v_sub_f32 v232, v6, v10\n"
    "v_sub_f32 v234, v6, v228\n"
    "v_exp_f32 v232, v232\n"
    "v_exp_f32 v234, v234\n"
    "s_nop 0\n"
    "v_mul_f32 v7, v240, v232\n"
    "v_fma_f32 v7, v230, v234, v7\n"
    "v_rcp_f32 v7, v7\n"
    "s_nop 0\n"
    "v_mul_f32 v7, v7, v14\n"
    "v_mul_f32 v232, v232, v7\n"
    "v_mul_f32 v234, v234, v7\n"
    "v_mov_b32 v233, v232\n"
    "v_mov_b32 v235, v234\n"
    "v_min_f32 v6, v11, v229\n"
    "v_sub_f32 v236, v6, v11\n"
    "v_sub_f32 v238, v6, v229\n"
    "v_exp_f32 v236, v236\n"
    "v_exp_f32 v238, v238\n"
    "s_nop 0\n"
    "v_mul_f32 v7, v241, v236\n"
    "v_fma_f32 v7, v231, v238, v7\n"
    "v_rcp_f32 v7, v7\n"
    "s_nop 0\n"
    "v_mul_f32 v7, v7, v15\n"
    "v_mul_f32 v236, v236, v7\n"
    "v_mul_f32 v238, v238, v7\n"
    "v_mov_b32 v237, v236\n"
    "v_mov_b32 v239, v238\n"
    "s_waitcnt lgkmcnt(4)\n"
    "ds_read_b128 v[112:115], v5 offset:12288\n"
    "ds_read_b128 v[116:119], v5 offset:13312\n"
    "ds_read_b128 v[120:123], v5 offset:14336\n"
    "ds_read_b128 v[124:127], v5 offset:15360\n"
    "v_accvgpr_read_b32 v128, a0\n"
    "v_accvgpr_read_b32 v129, a1\n"
    "v_accvgpr_read_b32 v130, a2\n"
    "v_accvgpr_read_b32 v131, a3\n"
    "v_accvgpr_read_b32 v132, a4\n"
    "v_accvgpr_read_b32 v133, a5\n"
    "v_accvgpr_read_b32 v134, a6\n"
    "v_accvgpr_read_b32 v135, a7\n"
    "v_pk_mul_f32 v[128:129], v[128:129], v[232:233]\n"
    "v_pk_mul_f32 v[130:131], v[130:131], v[232:233]\n"
    "v_pk_mul_f32 v[132:133], v[132:133], v[232:233]\n"
    "v_pk_mul_f32 v[134:135], v[134:135], v[232:233]\n"
    "v_pk_fma_f32 v[128:129], v[64:65], v[234:235], v[128:129]\n"
    "v_pk_fma_f32 v[130:131], v[66:67], v[234:235], v[130:131]\n"
    "v_pk_fma_f32 v[132:133], v[68:69], v[234:235], v[132:133]\n"
    "v_pk_fma_f32 v[134:135], v[70:71], v[234:235], v[134:135]\n"
    "v_cvt_pk_bf16_f32 v136, v128, v129\n"
    "v_cvt_pk_bf16_f32 v137, v130, v131\n"
    "v_cvt_pk_bf16_f32 v138, v132, v133\n"
    "v_cvt_pk_bf16_f32 v139, v134, v135\n"
    "s_add_u32 s22, s48, 0\n"
    "buffer_store_dwordx4 v[136:139], v2, s[44:47], s22 offen sc1\n"
    "v_accvgpr_read_b32 v140, a8\n"
    "v_accvgpr_read_b32 v141, a9\n"
    "v_accvgpr_read_b32 v142, a10\n"
    "v_accvgpr_read_b32 v143, a11\n"
    "v_accvgpr_read_b32 v144, a12\n"
    "v_accvgpr_read_b32 v145, a13\n"
    "v_accvgpr_read_b32 v146, a14\n"
    "v_accvgpr_read_b32 v147, a15\n"
    "v_pk_mul_f32 v[140:141], v[140:141], v[232:233]\n"
    "v_pk_mul_f32 v[142:143], v[142:143], v[232:233]\n"
    "v_pk_mul_f32 v[144:145], v[144:145], v[232:233]\n"
    "v_pk_mul_f32 v[146:147], v[146:147], v[232:233]\n"
    "v_pk_fma_f32 v[140:141], v[72:73], v[234:235], v[140:141]\n"
    "v_pk_fma_f32 v[142:143], v[74:75], v[234:235], v[142:143]\n"
    "v_pk_fma_f32 v[144:145], v[76:77], v[234:235], v[144:145]\n"
    "v_pk_fma_f32 v[146:147], v[78:79], v[234:235], v[146:147]\n"
    "v_cvt_pk_bf16_f32 v148, v140, v141\n"
    "v_cvt_pk_bf16_f32 v149, v142, v143\n"
    "v_cvt_pk_bf16_f32 v150, v144, v145\n"
    "v_cvt_pk_bf16_f32 v151, v146, v147\n"
    "s_add_u32 s22, s48, 0x1000\n"
    "buffer_store_dwordx4 v[148:151], v2, s[44:47], s22 offen sc1\n"
    "v_accvgpr_read_b32 v128, a16\n"
    "v_accvgpr_read_b32 v129, a17\n"
    "v_accvgpr_read_b32 v130, a18\n"
    "v_accvgpr_read_b32 v131, a19\n"
    "v_accvgpr_read_b32 v132, a20\n"
    "v_accvgpr_read_b32 v133, a21\n"
    "v_accvgpr_read_b32 v134, a22\n"
    "v_accvgpr_read_b32 v135, a23\n"
    "v_pk_mul_f32 v[128:129], v[128:129], v[232:233]\n"
    "v_pk_mul_f32 v[130:131], v[130:131], v[232:233]\n"
    "v_pk_mul_f32 v[132:133], v[132:133], v[232:233]\n"
    "v_pk_mul_f32 v[134:135], v[134:135], v[232:233]\n"
    "v_pk_fma_f32 v[128:129], v[80:81], v[234:235], v[128:129]\n"
    "v_pk_fma_f32 v[130:131], v[82:83], v[234:235], v[130:131]\n"
    "v_pk_fma_f32 v[132:133], v[84:85], v[234:235], v[132:133]\n"
    "v_pk_fma_f32 v[134:135], v[86:87], v[234:235], v[134:135]\n"
    "v_cvt_pk_bf16_f32 v136, v128, v129\n"
    "v_cvt_pk_bf16_f32 v137, v130, v131\n"
    "v_cvt_pk_bf16_f32 v138, v132, v133\n"
    "v_cvt_pk_bf16_f32 v139, v134, v135\n"
    "s_add_u32 s22, s48, 0x2000\n"
    "buffer_store_dwordx4 v[136:139], v2, s[44:47], s22 offen sc1\n"
    "v_accvgpr_read_b32 v140, a24\n"
    "v_accvgpr_read_b32 v141, a25\n"
    "v_accvgpr_read_b32 v142, a26\n"
    "v_accvgpr_read_b32 v143, a27\n"
    "v_accvgpr_read_b32 v144, a28\n"
    "v_accvgpr_read_b32 v145, a29\n"
    "v_accvgpr_read_b32 v146, a30\n"
    "v_accvgpr_read_b32 v147, a31\n"
    "v_pk_mul_f32 v[140:141], v[140:141], v[232:233]\n"
    "v_pk_mul_f32 v[142:143], v[142:143], v[232:233]\n"
    "v_pk_mul_f32 v[144:145], v[144:145], v[232:233]\n"
    "v_pk_mul_f32 v[146:147], v[146:147], v[232:233]\n"
    "v_pk_fma_f32 v[140:141], v[88:89], v[234:235], v[140:141]\n"
    "v_pk_fma_f32 v[142:143], v[90:91], v[234:235], v[142:143]\n"
    "v_pk_fma_f32 v[144:145], v[92:93], v[234:235], v[144:145]\n"
    "v_pk_fma_f32 v[146:147], v[94:95], v[234:235], v[146:147]\n"
    "v_cvt_pk_bf16_f32 v148, v140, v141\n"
    "v_cvt_pk_bf16_f32 v149, v142, v143\n"
    "v_cvt_pk_bf16_f32 v150, v144, v145\n"
    "v_cvt_pk_bf16_f32 v151, v146, v147\n"
    "s_add_u32 s22, s48, 0x3000\n"
    "buffer_store_dwordx4 v[148:151], v2, s[44:47], s22 offen sc1\n"
    "s_waitcnt lgkmcnt(0)\n"
    "v_accvgpr_read_b32 v160, a32\n"
    "v_accvgpr_read_b32 v161, a33\n"
    "v_accvgpr_read_b32 v162, a34\n"
    "v_accvgpr_read_b32 v163, a35\n"
    "v_accvgpr_read_b32 v164, a36\n"
    "v_accvgpr_read_b32 v165, a37\n"
    "v_accvgpr_read_b32 v166, a38\n"
    "v_accvgpr_read_b32 v167, a39\n"
    "v_pk_mul_f32 v[160:161], v[160:161], v[236:237]\n"
    "v_pk_mul_f32 v[162:163], v[162:163], v[236:237]\n"
    "v_pk_mul_f32 v[164:165], v[164:165], v[236:237]\n"
    "v_pk_mul_f32 v[166:167], v[166:167], v[236:237]\n"
    "v_pk_fma_f32 v[160:161], v[96:97], v[238:239], v[160:161]\n"
    "v_pk_fma_f32 v[162:163], v[98:99], v[238:239], v[162:163]\n"
    "v_pk_fma_f32 v[164:165], v[100:101], v[238:239], v[164:165]\n"
    "v_pk_fma_f32 v[166:167], v[102:103], v[238:239], v[166:167]\n"
    "v_cvt_pk_bf16_f32 v168, v160, v161\n"
    "v_cvt_pk_bf16_f32 v169, v162, v163\n"
    "v_cvt_pk_bf16_f32 v170, v164, v165\n"
    "v_cvt_pk_bf16_f32 v171, v166, v167\n"
    "s_add_u32 s22, s48, 0\n"
    "buffer_store_dwordx4 v[168:171], v2, s[44:47], s22 offen offset:1024 sc1\n"
    "v_accvgpr_read_b32 v172, a40\n"
    "v_accvgpr_read_b32 v173, a41\n"
    "v_accvgpr_read_b32 v174, a42\n"
    "v_accvgpr_read_b32 v175, a43\n"
    "v_accvgpr_read_b32 v176, a44\n"
    "v_accvgpr_read_b32 v177, a45\n"
    "v_accvgpr_read_b32 v178, a46\n"
    "v_accvgpr_read_b32 v179, a47\n"
    "v_pk_mul_f32 v[172:173], v[172:173], v[236:237]\n"
    "v_pk_mul_f32 v[174:175], v[174:175], v[236:237]\n"
    "v_pk_mul_f32 v[176:177], v[176:177], v[236:237]\n"
    "v_pk_mul_f32 v[178:179], v[178:179], v[236:237]\n"
    "v_pk_fma_f32 v[172:173], v[104:105], v[238:239], v[172:173]\n"
    "v_pk_fma_f32 v[174:175], v[106:107], v[238:239], v[174:175]\n"
    "v_pk_fma_f32 v[176:177], v[108:109], v[238:239], v[176:177]\n"
    "v_pk_fma_f32 v[178:179], v[110:111], v[238:239], v[178:179]\n"
    "v_cvt_pk_bf16_f32 v180, v172, v173\n"
    "v_cvt_pk_bf16_f32 v181, v174, v175\n"
    "v_cvt_pk_bf16_f32 v182, v176, v177\n"
    "v_cvt_pk_bf16_f32 v183, v178, v179\n"
    "s_add_u32 s22, s48, 0x1000\n"
    "buffer_store_dwordx4 v[180:183], v2, s[44:47], s22 offen offset:1024 sc1\n"
    "v_accvgpr_read_b32 v160, a48\n"
    "v_accvgpr_read_b32 v161, a49\n"
    "v_accvgpr_read_b32 v162, a50\n"
    "v_accvgpr_read_b32 v163, a51\n"
    "v_accvgpr_read_b32 v164, a52\n"
    "v_accvgpr_read_b32 v165, a53\n"
    "v_accvgpr_read_b32 v166, a54\n"
    "v_accvgpr_read_b32 v167, a55\n"
    "v_pk_mul_f32 v[160:161], v[160:161], v[236:237]\n"
    "v_pk_mul_f32 v[162:163], v[162:163], v[236:237]\n"
    "v_pk_mul_f32 v[164:165], v[164:165], v[236:237]\n"
    "v_pk_mul_f32 v[166:167], v[166:167], v[236:237]\n"
    "v_pk_fma_f32 v[160:161], v[112:113], v[238:239], v[160:161]\n"
    "v_pk_fma_f32 v[162:163], v[114:115], v[238:239], v[162:163]\n"
    "v_pk_fma_f32 v[164:165], v[116:117], v[238:239], v[164:165]\n"
    "v_pk_fma_f32 v[166:167], v[118:119], v[238:239], v[166:167]\n"
    "v_cvt_pk_bf16_f32 v168, v160, v161\n"
    "v_cvt_pk_bf16_f32 v169, v162, v163\n"
    "v_cvt_pk_bf16_f32 v170, v164, v165\n"
    "v_cvt_pk_bf16_f32 v171, v166, v167\n"
    "s_add_u32 s22, s48, 0x2000\n"
    "buffer_store_dwordx4 v[168:171], v2, s[44:47], s22 offen offset:1024 sc1\n"
    "v_accvgpr_read_b32 v172, a56\n"
    "v_accvgpr_read_b32 v173, a57\n"
    "v_accvgpr_read_b32 v174, a58\n"
    "v_accvgpr_read_b32 v175, a59\n"
    "v_accvgpr_read_b32 v176, a60\n"
    "v_accvgpr_read_b32 v177, a61\n"
    "v_accvgpr_read_b32 v178, a62\n"
    "v_accvgpr_read_b32 v179, a63\n"
    "v_pk_mul_f32 v[172:173], v[172:173], v[236:237]\n"
    "v_pk_mul_f32 v[174:175], v[174:175], v[236:237]\n"
    "v_pk_mul_f32 v[176:177], v[176:177], v[236:237]\n"
    "v_pk_mul_f32 v[178:179], v[178:179], v[236:237]\n"
    "v_pk_fma_f32 v[172:173], v[120:121], v[238:239], v[172:173]\n"
    "v_pk_fma_f32 v[174:175], v[122:123], v[238:239], v[174:175]\n"
    "v_pk_fma_f32 v[176:177], v[124:125], v[238:239], v[176:177]\n"
    "v_pk_fma_f32 v[178:179], v[126:127], v[238:239], v[178:179]\n"
    "v_cvt_pk_bf16_f32 v180, v172, v173\n"
    "v_cvt_pk_bf16_f32 v181, v174, v175\n"
    "v_cvt_pk_bf16_f32 v182, v176, v177\n"
    "v_cvt_pk_bf16_f32 v183, v178, v179\n"
    "s_add_u32 s22, s48, 0x3000\n"
    "buffer_store_dwordx4 v[180:183], v2, s[44:47], s22 offen offset:1024 sc1\n"
    "s_branch P4S_%=\n"
    "MGB_%=:\n"
    "v_mov_b32 v224, v10\n"
    "v_mov_b32 v225, v11\n"
    "v_mov_b32 v226, v240\n"
    "v_mov_b32 v227, v241\n"
    "ds_write_b128 v4, v[224:227] offset:16384\n"
    "s_add_u32 s13, s13, 0x80\n"
    "s_add_u32 m0, s16, 0x8000\n"
    "s_nop 0\n"
    "buffer_load_dwordx4 v192, s[64:67], s13 offen lds\n"
    "ds_write_b128 v4, a[0:3]\n"
    "ds_write_b128 v4, a[4:7] offset:1024\n"
    "buffer_load_dwordx4 v193, s[64:67], s13 offen offset:1024 lds\n"
    "ds_write_b128 v4, a[8:11] offset:2048\n"
    "ds_write_b128 v4, a[12:15] offset:3072\n"
    "buffer_load_dwordx4 v194, s[64:67], s13 offen offset:2048 lds\n"
    "ds_write_b128 v4, a[16:19] offset:4096\n"
    "ds_write_b128 v4, a[20:23] offset:5120\n"
    "buffer_load_dwordx4 v195, s[64:67], s13 offen offset:3072 lds\n"
    "ds_write_b128 v4, a[24:27] offset:6144\n"
    "ds_write_b128 v4, a[28:31] offset:7168\n"
    "s_add_u32 s13, s13, 0x80\n"
    "s_waitcnt lgkmcnt(4)\n"
    "ds_write_b128 v4, a[32:35] offset:8192\n"
    "ds_write_b128 v4, a[36:39] offset:9216\n"
    "ds_write_b128 v4, a[40:43] offset:10240\n"
    "ds_write_b128 v4, a[44:47] offset:11264\n"
    "ds_write_b128 v4, a[48:51] offset:12288\n"
    "ds_write_b128 v4, a[52:55] offset:13312\n"
    "ds_write_b128 v4, a[56:59] offset:14336\n"
    "ds_write_b128 v4, a[60:63] offset:15360\n"
    "s_waitcnt lgkmcnt(0)\n"
    "s_barrier\n"
    "ds_read_b128 v[228:231], v5 offset:16384\n"
    "ds_read_b128 v[64:67], v5\n"
    "ds_read_b128 v[68:71], v5 offset:1024\n"
    "ds_read_b128 v[72:75], v5 offset:2048\n"
    "ds_read_b128 v[76:79], v5 offset:3072\n"
    "ds_read_b128 v[80:83], v5 offset:4096\n"
    "ds_read_b128 v[84:87], v5 offset:5120\n"
    "ds_read_b128 v[88:91], v5 offset:6144\n"
    "ds_read_b128 v[92:95], v5 offset:7168\n"
    "ds_read_b128 v[96:99], v5 offset:8192\n"
    "ds_read_b128 v[100:103], v5 offset:9216\n"
    "ds_read_b128 v[104:107], v5 offset:10240\n"
    "ds_read_b128 v[108:111], v5 offset:11264\n"
    "s_waitcnt vmcnt(16) lgkmcnt(12)\n"
    "v_min_f32 v6, v12, v228\n"
    "v_sub_f32 v232, v6, v12\n"
    "v_sub_f32 v234, v6, v228\n"
    "v_exp_f32 v232, v232\n"
    "v_exp_f32 v234, v234\n"
    "s_nop 0\n"
    "v_mul_f32 v7, v242, v232\n"
    "v_fma_f32 v7, v230, v234, v7\n"
    "v_rcp_f32 v7, v7\n"
    "s_nop 0\n"
    "v_mul_f32 v7, v7, v16\n"
    "v_mul_f32 v232, v232, v7\n"
    "v_mul_f32 v234, v234, v7\n"
    "v_mov_b32 v233, v232\n"
    "v_mov_b32 v235, v234\n"
    "v_min_f32 v6, v13, v229\n"
    "v_sub_f32 v236, v6, v13\n"
    "v_sub_f32 v238, v6, v229\n"
    "v_exp_f32 v236, v236\n"
    "v_exp_f32 v238, v238\n"
    "s_nop 0\n"
    "v_mul_f32 v7, v243, v236\n"
    "v_fma_f32 v7, v231, v238, v7\n"
    "v_rcp_f32 v7, v7\n"
    "s_nop 0\n"
    "v_mul_f32 v7, v7, v17\n"
    "v_mul_f32 v236, v236, v7\n"
    "v_mul_f32 v238, v238, v7\n"
    "v_mov_b32 v237, v236\n"
    "v_mov_b32 v239, v238\n"
    "s_waitcnt lgkmcnt(4)\n"
    "ds_read_b128 v[112:115], v5 offset:12288\n"
    "ds_read_b128 v[116:119], v5 offset:13312\n"
    "ds_read_b128 v[120:123], v5 offset:14336\n"
    "ds_read_b128 v[124:127], v5 offset:15360\n"
    "v_accvgpr_read_b32 v128, a64\n"
    "v_accvgpr_read_b32 v129, a65\n"
    "v_accvgpr_read_b32 v130, a66\n"
    "v_accvgpr_read_b32 v131, a67\n"
    "v_accvgpr_read_b32 v132, a68\n"
    "v_accvgpr_read_b32 v133, a69\n"
    "v_accvgpr_read_b32 v134, a70\n"
    "v_accvgpr_read_b32 v135, a71\n"
    "v_pk_mul_f32 v[128:129], v[128:129], v[232:233]\n"
    "v_pk_mul_f32 v[130:131], v[130:131], v[232:233]\n"
    "v_pk_mul_f32 v[132:133], v[132:133], v[232:233]\n"
    "v_pk_mul_f32 v[134:135], v[134:135], v[232:233]\n"
    "v_pk_fma_f32 v[128:129], v[64:65], v[234:235], v[128:129]\n"
    "v_pk_fma_f32 v[130:131], v[66:67], v[234:235], v[130:131]\n"
    "v_pk_fma_f32 v[132:133], v[68:69], v[234:235], v[132:133]\n"
    "v_pk_fma_f32 v[134:135], v[70:71], v[234:235], v[134:135]\n"
    "v_cvt_pk_bf16_f32 v136, v128, v129\n"
    "v_cvt_pk_bf16_f32 v137, v130, v131\n"
    "v_cvt_pk_bf16_f32 v138, v132, v133\n"
    "v_cvt_pk_bf16_f32 v139, v134, v135\n"
    "s_add_u32 s22, s48, 0\n"
    "buffer_store_dwordx4 v[136:139], v2, s[44:47], s22 offen offset:2048 sc1\n"
    "v_accvgpr_read_b32 v140, a72\n"
    "v_accvgpr_read_b32 v141, a73\n"
    "v_accvgpr_read_b32 v142, a74\n"
    "v_accvgpr_read_b32 v143, a75\n"
    "v_accvgpr_read_b32 v144, a76\n"
    "v_accvgpr_read_b32 v145, a77\n"
    "v_accvgpr_read_b32 v146, a78\n"
    "v_accvgpr_read_b32 v147, a79\n"
    "v_pk_mul_f32 v[140:141], v[140:141], v[232:233]\n"
    "v_pk_mul_f32 v[142:143], v[142:143], v[232:233]\n"
    "v_pk_mul_f32 v[144:145], v[144:145], v[232:233]\n"
    "v_pk_mul_f32 v[146:147], v[146:147], v[232:233]\n"
    "v_pk_fma_f32 v[140:141], v[72:73], v[234:235], v[140:141]\n"
    "v_pk_fma_f32 v[142:143], v[74:75], v[234:235], v[142:143]\n"
    "v_pk_fma_f32 v[144:145], v[76:77], v[234:235], v[144:145]\n"
    "v_pk_fma_f32 v[146:147], v[78:79], v[234:235], v[146:147]\n"
    "v_cvt_pk_bf16_f32 v148, v140, v141\n"
    "v_cvt_pk_bf16_f32 v149, v142, v143\n"
    "v_cvt_pk_bf16_f32 v150, v144, v145\n"
    "v_cvt_pk_bf16_f32 v151, v146, v147\n"
    "s_add_u32 s22, s48, 0x1000\n"
    "buffer_store_dwordx4 v[148:151], v2, s[44:47], s22 offen offset:2048 sc1\n"
    "v_accvgpr_read_b32 v128, a80\n"
    "v_accvgpr_read_b32 v129, a81\n"
    "v_accvgpr_read_b32 v130, a82\n"
    "v_accvgpr_read_b32 v131, a83\n"
    "v_accvgpr_read_b32 v132, a84\n"
    "v_accvgpr_read_b32 v133, a85\n"
    "v_accvgpr_read_b32 v134, a86\n"
    "v_accvgpr_read_b32 v135, a87\n"
    "v_pk_mul_f32 v[128:129], v[128:129], v[232:233]\n"
    "v_pk_mul_f32 v[130:131], v[130:131], v[232:233]\n"
    "v_pk_mul_f32 v[132:133], v[132:133], v[232:233]\n"
    "v_pk_mul_f32 v[134:135], v[134:135], v[232:233]\n"
    "v_pk_fma_f32 v[128:129], v[80:81], v[234:235], v[128:129]\n"
    "v_pk_fma_f32 v[130:131], v[82:83], v[234:235], v[130:131]\n"
    "v_pk_fma_f32 v[132:133], v[84:85], v[234:235], v[132:133]\n"
    "v_pk_fma_f32 v[134:135], v[86:87], v[234:235], v[134:135]\n"
    "v_cvt_pk_bf16_f32 v136, v128, v129\n"
    "v_cvt_pk_bf16_f32 v137, v130, v131\n"
    "v_cvt_pk_bf16_f32 v138, v132, v133\n"
    "v_cvt_pk_bf16_f32 v139, v134, v135\n"
    "s_add_u32 s22, s48, 0x2000\n"
    "buffer_store_dwordx4 v[136:139], v2, s[44:47], s22 offen offset:2048 sc1\n"
    "v_accvgpr_read_b32 v140, a88\n"
    "v_accvgpr_read_b32 v141, a89\n"
    "v_accvgpr_read_b32 v142, a90\n"
    "v_accvgpr_read_b32 v143, a91\n"
    "v_accvgpr_read_b32 v144, a92\n"
    "v_accvgpr_read_b32 v145, a93\n"
    "v_accvgpr_read_b32 v146, a94\n"
    "v_accvgpr_read_b32 v147, a95\n"
    "v_pk_mul_f32 v[140:141], v[140:141], v[232:233]\n"
    "v_pk_mul_f32 v[142:143], v[142:143], v[232:233]\n"
    "v_pk_mul_f32 v[144:145], v[144:145], v[232:233]\n"
    "v_pk_mul_f32 v[146:147], v[146:147], v[232:233]\n"
    "v_pk_fma_f32 v[140:141], v[88:89], v[234:235], v[140:141]\n"
    "v_pk_fma_f32 v[142:143], v[90:91], v[234:235], v[142:143]\n"
    "v_pk_fma_f32 v[144:145], v[92:93], v[234:235], v[144:145]\n"
    "v_pk_fma_f32 v[146:147], v[94:95], v[234:235], v[146:147]\n"
    "v_cvt_pk_bf16_f32 v148, v140, v141\n"
    "v_cvt_pk_bf16_f32 v149, v142, v143\n"
    "v_cvt_pk_bf16_f32 v150, v144, v145\n"
    "v_cvt_pk_bf16_f32 v151, v146, v147\n"
    "s_add_u32 s22, s48, 0x3000\n"
    "buffer_store_dwordx4 v[148:151], v2, s[44:47], s22 offen offset:2048 sc1\n"
    "s_waitcnt lgkmcnt(0)\n"
    "v_accvgpr_read_b32 v160, a96\n"
    "v_accvgpr_read_b32 v161, a97\n"
    "v_accvgpr_read_b32 v162, a98\n"
    "v_accvgpr_read_b32 v163, a99\n"
    "v_accvgpr_read_b32 v164, a100\n"
    "v_accvgpr_read_b32 v165, a101\n"
    "v_accvgpr_read_b32 v166, a102\n"
    "v_accvgpr_read_b32 v167, a103\n"
    "v_pk_mul_f32 v[160:161], v[160:161], v[236:237]\n"
    "v_pk_mul_f32 v[162:163], v[162:163], v[236:237]\n"
    "v_pk_mul_f32 v[164:165], v[164:165], v[236:237]\n"
    "v_pk_mul_f32 v[166:167], v[166:167], v[236:237]\n"
    "v_pk_fma_f32 v[160:161], v[96:97], v[238:239], v[160:161]\n"
    "v_pk_fma_f32 v[162:163], v[98:99], v[238:239], v[162:163]\n"
    "v_pk_fma_f32 v[164:165], v[100:101], v[238:239], v[164:165]\n"
    "v_pk_fma_f32 v[166:167], v[102:103], v[238:239], v[166:167]\n"
    "v_cvt_pk_bf16_f32 v168, v160, v161\n"
    "v_cvt_pk_bf16_f32 v169, v162, v163\n"
    "v_cvt_pk_bf16_f32 v170, v164, v165\n"
    "v_cvt_pk_bf16_f32 v171, v166, v167\n"
    "s_add_u32 s22, s48, 0\n"
    "buffer_store_dwordx4 v[168:171], v2, s[44:47], s22 offen offset:3072 sc1\n"
    "v_accvgpr_read_b32 v172, a104\n"
    "v_accvgpr_read_b32 v173, a105\n"
    "v_accvgpr_read_b32 v174, a106\n"
    "v_accvgpr_read_b32 v175, a107\n"
    "v_accvgpr_read_b32 v176, a108\n"
    "v_accvgpr_read_b32 v177, a109\n"
    "v_accvgpr_read_b32 v178, a110\n"
    "v_accvgpr_read_b32 v179, a111\n"
    "v_pk_mul_f32 v[172:173], v[172:173], v[236:237]\n"
    "v_pk_mul_f32 v[174:175], v[174:175], v[236:237]\n"
    "v_pk_mul_f32 v[176:177], v[176:177], v[236:237]\n"
    "v_pk_mul_f32 v[178:179], v[178:179], v[236:237]\n"
    "v_pk_fma_f32 v[172:173], v[104:105], v[238:239], v[172:173]\n"
    "v_pk_fma_f32 v[174:175], v[106:107], v[238:239], v[174:175]\n"
    "v_pk_fma_f32 v[176:177], v[108:109], v[238:239], v[176:177]\n"
    "v_pk_fma_f32 v[178:179], v[110:111], v[238:239], v[178:179]\n"
    "v_cvt_pk_bf16_f32 v180, v172, v173\n"
    "v_cvt_pk_bf16_f32 v181, v174, v175\n"
    "v_cvt_pk_bf16_f32 v182, v176, v177\n"
    "v_cvt_pk_bf16_f32 v183, v178, v179\n"
    "s_add_u32 s22, s48, 0x1000\n"
    "buffer_store_dwordx4 v[180:183], v2, s[44:47], s22 offen offset:3072 sc1\n"
    "v_accvgpr_read_b32 v160, a112\n"
    "v_accvgpr_read_b32 v161, a113\n"
    "v_accvgpr_read_b32 v162, a114\n"
    "v_accvgpr_read_b32 v163, a115\n"
    "v_accvgpr_read_b32 v164, a116\n"
    "v_accvgpr_read_b32 v165, a117\n"
    "v_accvgpr_read_b32 v166, a118\n"
    "v_accvgpr_read_b32 v167, a119\n"
    "v_pk_mul_f32 v[160:161], v[160:161], v[236:237]\n"
    "v_pk_mul_f32 v[162:163], v[162:163], v[236:237]\n"
    "v_pk_mul_f32 v[164:165], v[164:165], v[236:237]\n"
    "v_pk_mul_f32 v[166:167], v[166:167], v[236:237]\n"
    "v_pk_fma_f32 v[160:161], v[112:113], v[238:239], v[160:161]\n"
    "v_pk_fma_f32 v[162:163], v[114:115], v[238:239], v[162:163]\n"
    "v_pk_fma_f32 v[164:165], v[116:117], v[238:239], v[164:165]\n"
    "v_pk_fma_f32 v[166:167], v[118:119], v[238:239], v[166:167]\n"
    "v_cvt_pk_bf16_f32 v168, v160, v161\n"
    "v_cvt_pk_bf16_f32 v169, v162, v163\n"
    "v_cvt_pk_bf16_f32 v170, v164, v165\n"
    "v_cvt_pk_bf16_f32 v171, v166, v167\n"
    "s_add_u32 s22, s48, 0x2000\n"
    "buffer_store_dwordx4 v[168:171], v2, s[44:47], s22 offen offset:3072 sc1\n"
    "v_accvgpr_read_b32 v172, a120\n"
    "v_accvgpr_read_b32 v173, a121\n"
    "v_accvgpr_read_b32 v174, a122\n"
    "v_accvgpr_read_b32 v175, a123\n"
    "v_accvgpr_read_b32 v176, a124\n"
    "v_accvgpr_read_b32 v177, a125\n"
    "v_accvgpr_read_b32 v178, a126\n"
    "v_accvgpr_read_b32 v179, a127\n"
    "v_pk_mul_f32 v[172:173], v[172:173], v[236:237]\n"
    "v_pk_mul_f32 v[174:175], v[174:175], v[236:237]\n"
    "v_pk_mul_f32 v[176:177], v[176:177], v[236:237]\n"
    "v_pk_mul_f32 v[178:179], v[178:179], v[236:237]\n"
    "v_pk_fma_f32 v[172:173], v[120:121], v[238:239], v[172:173]\n"
    "v_pk_fma_f32 v[174:175], v[122:123], v[238:239], v[174:175]\n"
    "v_pk_fma_f32 v[176:177], v[124:125], v[238:239], v[176:177]\n"
    "v_pk_fma_f32 v[178:179], v[126:127], v[238:239], v[178:179]\n"
    "v_cvt_pk_bf16_f32 v180, v172, v173\n"
    "v_cvt_pk_bf16_f32 v181, v174, v175\n"
    "v_cvt_pk_bf16_f32 v182, v176, v177\n"
    "v_cvt_pk_bf16_f32 v183, v178, v179\n"
    "s_add_u32 s22, s48, 0x3000\n"
    "buffer_store_dwordx4 v[180:183], v2, s[44:47], s22 offen offset:3072 sc1\n"
    "P4S_%=:\n"
    "s_barrier\n"
    "s_cmp_eq_u32 s59, 1\n"
    "s_cbranch_scc1 IDLE_%=\n"
    "s_cmp_eq_u32 s58, 1\n"
    "s_cbranch_scc1 ODD_%=\n"
    "s_add_u32 m0, s16, 0x14000\n"
    "s_nop 0\n"
    "buffer_load_dwordx4 v2, s[44:47], s71 offen sc1 lds\n"
    "buffer_load_dwordx4 v2, s[44:47], s71 offen offset:1024 sc1 lds\n"
    "buffer_load_dwordx4 v2, s[44:47], s71 offen offset:2048 sc1 lds\n"
    "buffer_load_dwordx4 v2, s[44:47], s71 offen offset:3072 sc1 lds\n"
    "s_add_u32 s71, s71, 0x1c000\n"
    "s_add_u32 m0, s16, 0x10000\n"
    "s_nop 0\n"
    "buffer_load_dwordx4 v192, s[64:67], s13 offen lds\n"
    "buffer_load_dwordx4 v193, s[64:67], s13 offen offset:1024 lds\n"
    "buffer_load_dwordx4 v194, s[64:67], s13 offen offset:2048 lds\n"
    "buffer_load_dwordx4 v195, s[64:67], s13 offen offset:3072 lds\n"
    "s_add_u32 s13, s13, 0x80\n"
    "s_waitcnt vmcnt(16)\n"
    "s_barrier\n"
    "ds_read_b128 a[16:19], v196\n"
    "ds_read_b128 a[20:23], v196 offset:2048\n"
    "ds_read_b128 a[24:27], v196 offset:4096\n"
    "ds_read_b128 a[28:31], v196 offset:6144\n"
    "ds_read_b64_tr_b16 a[0:1], v198\n"
    "ds_read_b64_tr_b16 a[2:3], v198 offset:512\n"
    "ds_read_b64_tr_b16 a[4:5], v198 offset:1024\n"
    "ds_read_b64_tr_b16 a[6:7], v198 offset:1536\n"
    "ds_read_b64_tr_b16 a[8:9], v198 offset:2048\n"
    "ds_read_b64_tr_b16 a[10:11], v198 offset:2560\n"
    "ds_read_b64_tr_b16 a[12:13], v198 offset:3072\n"
    "ds_read_b64_tr_b16 a[14:15], v198 offset:3584\n"
    "s_waitcnt lgkmcnt(4)\n"
    "v_mfma_f32_16x16x32_bf16 v[128:131], a[0:3], a[16:19], 0\n"
    "ds_read_b128 a[48:51], v197\n"
    "v_mfma_f32_16x16x32_bf16 v[132:135], a[0:3], a[20:23], 0\n"
    "ds_read_b128 a[52:55], v197 offset:2048\n"
    "v_mfma_f32_16x16x32_bf16 v[136:139], a[0:3], a[24:27], 0\n"
    "ds_read_b128 a[56:59], v197 offset:4096\n"
    "v_mfma_f32_16x16x32_bf16 v[140:143], a[0:3], a[28:31], 0\n"
    "v_mfma_f32_16x16x32_bf16 v[144:147], a[4:7], a[16:19], 0\n"
    "ds_read_b128 a[60:63], v197 offset:6144\n"
    "v_mfma_f32_16x16x32_bf16 v[148:151], a[4:7], a[20:23], 0\n"
    "ds_read_b64_tr_b16 a[32:33], v198 offset:8192\n"
    "v_mfma_f32_16x16x32_bf16 v[152:155], a[4:7], a[24:27], 0\n"
    "ds_read_b64_tr_b16 a[34:35], v198 offset:8704\n"
    "v_mfma_f32_16x16x32_bf16 v[156:159], a[4:7], a[28:31], 0\n"
    "s_waitcnt lgkmcnt(6)\n"
    "v_mfma_f32_16x16x32_bf16 v[160:163], a[8:11], a[16:19], 0\n"
    "ds_read_b64_tr_b16 a[36:37], v198 offset:9216\n"
    "v_mfma_f32_16x16x32_bf16 v[164:167], a[8:11], a[20:23], 0\n"
    "ds_read_b64_tr_b16 a[38:39], v198 offset:9728\n"
    "v_mfma_f32_16x16x32_bf16 v[168:171], a[8:11], a[24:27], 0\n"
    "ds_read_b64_tr_b16 a[40:41], v198 offset:10240\n"
    "v_mfma_f32_16x16x32_bf16 v[172:175], a[8:11], a[28:31], 0\n"
    "v_mfma_f32_16x16x32_bf16 v[176:179], a[12:15], a[16:19], 0\n"
    "ds_read_b64_tr_b16 a[42:43], v198 offset:10752\n"
    "v_mfma_f32_16x16x32_bf16 v[180:183], a[12:15], a[20:23], 0\n"
    "ds_read_b64_tr_b16 a[44:45], v198 offset:11264\n"
    "v_mfma_f32_16x16x32_bf16 v[184:187], a[12:15], a[24:27], 0\n"
    "ds_read_b64_tr_b16 a[46:47], v198 offset:11776\n"
    "v_mfma_f32_16x16x32_bf16 v[188:191], a[12:15], a[28:31], 0\n"
    "s_barrier\n"
    "s_waitcnt lgkmcnt(4)\n"
    "v_mfma_f32_16x16x32_bf16 v[128:131], a[32:35], a[48:51], v[128:131]\n"
    "ds_read_b128 a[16:19], v196 offset:32768\n"
    "v_mfma_f32_16x16x32_bf16 v[132:135], a[32:35], a[52:55], v[132:135]\n"
    "ds_read_b128 a[20:23], v196 offset:34816\n"
    "v_mfma_f32_16x16x32_bf16 v[136:139], a[32:35], a[56:59], v[136:139]\n"
    "ds_read_b128 a[24:27], v196 offset:36864\n"
    "s_add_u32 m0, s16, 0x1c000\n"
    "v_mfma_f32_16x16x32_bf16 v[140:143], a[32:35], a[60:63], v[140:143]\n"
    "buffer_load_dwordx4 v2, s[44:47], s71 offen sc1 lds\n"
    "v_mfma_f32_16x16x32_bf16 v[144:147], a[36:39], a[48:51], v[144:147]\n"
    "ds_read_b128 a[28:31], v196 offset:38912\n"
    "v_mfma_f32_16x16x32_bf16 v[148:151], a[36:39], a[52:55], v[148:151]\n"
    "ds_read_b64_tr_b16 a[0:1], v198 offset:32768\n"
    "v_mfma_f32_16x16x32_bf16 v[152:155], a[36:39], a[56:59], v[152:155]\n"
    "ds_read_b64_tr_b16 a[2:3], v198 offset:33280\n"
    "v_mfma_f32_16x16x32_bf16 v[156:159], a[36:39], a[60:63], v[156:159]\n"
    "buffer_load_dwordx4 v2, s[44:47], s71 offen offset:1024 sc1 lds\n"
    "s_waitcnt lgkmcnt(6)\n"
    "v_mfma_f32_16x16x32_bf16 v[160:163], a[40:43], a[48:51], v[160:163]\n"
    "ds_read_b64_tr_b16 a[4:5], v198 offset:33792\n"
    "v_mfma_f32_16x16x32_bf16 v[164:167], a[40:43], a[52:55], v[164:167]\n"
    "ds_read_b64_tr_b16 a[6:7], v198 offset:34304\n"
    "v_mfma_f32_16x16x32_bf16 v[168:171], a[40:43], a[56:59], v[168:171]\n"
    "ds_read_b64_tr_b16 a[8:9], v198 offset:34816\n"
    "v_mfma_f32_16x16x32_bf16 v[172:175], a[40:43], a[60:63], v[172:175]\n"
    "buffer_load_dwordx4 v2, s[44:47], s71 offen offset:2048 sc1 lds\n"
    "v_mfma_f32_16x16x32_bf16 v[176:179], a[44:47], a[48:51], v[176:179]\n"
    "ds_read_b64_tr_b16 a[10:11], v198 offset:35328\n"
    "v_mfma_f32_16x16x32_bf16 v[180:183], a[44:47], a[52:55], v[180:183]\n"
    "ds_read_b64_tr_b16 a[12:13], v198 offset:35840\n"
    "v_mfma_f32_16x16x32_bf16 v[184:187], a[44:47], a[56:59], v[184:187]\n"
    "ds_read_b64_tr_b16 a[14:15], v198 offset:36352\n"
    "v_mfma_f32_16x16x32_bf16 v[188:191], a[44:47], a[60:63], v[188:191]\n"
    "buffer_load_dwordx4 v2, s[44:47], s71 offen offset:3072 sc1 lds\n"
    "s_add_u32 s71, s71, 0x1c000\n"
    "s_waitcnt lgkmcnt(4)\n"
    "v_mfma_f32_16x16x32_bf16 v[128:131], a[0:3], a[16:19], v[128:131]\n"
    "ds_read_b128 a[48:51], v197 offset:32768\n"
    "v_mfma_f32_16x16x32_bf16 v[132:135], a[0:3], a[20:23], v[132:135]\n"
    "ds_read_b128 a[52:55], v197 offset:34816\n"
    "v_mfma_f32_16x16x32_bf16 v[136:139], a[0:3], a[24:27], v[136:139]\n"
    "ds_read_b128 a[56:59], v197 offset:36864\n"
    "s_add_u32 m0, s16, 0x18000\n"
    "v_mfma_f32_16x16x32_bf16 v[140:143], a[0:3], a[28:31], v[140:143]\n"
    "buffer_load_dwordx4 v192, s[64:67], s13 offen lds\n"
    "v_mfma_f32_16x16x32_bf16 v[144:147], a[4:7], a[16:19], v[144:147]\n"
    "ds_read_b128 a[60:63], v197 offset:38912\n"
    "v_mfma_f32_16x16x32_bf16 v[148:151], a[4:7], a[20:23], v[148:151]\n"
    "ds_read_b64_tr_b16 a[32:33], v198 offset:40960\n"
    "v_mfma_f32_16x16x32_bf16 v[152:155], a[4:7], a[24:27], v[152:155]\n"
    "ds_read_b64_tr_b16 a[34:35], v198 offset:41472\n"
    "v_mfma_f32_16x16x32_bf16 v[156:159], a[4:7], a[28:31], v[156:159]\n"
    "buffer_load_dwordx4 v193, s[64:67], s13 offen offset:1024 lds\n"
    "s_waitcnt lgkmcnt(6)\n"
    "v_mfma_f32_16x16x32_bf16 v[160:163], a[8:11], a[16:19], v[160:163]\n"
    "ds_read_b64_tr_b16 a[36:37], v198 offset:41984\n"
    "v_mfma_f32_16x16x32_bf16 v[164:167], a[8:11], a[20:23], v[164:167]\n"
    "ds_read_b64_tr_b16 a[38:39], v198 offset:42496\n"
    "v_mfma_f32_16x16x32_bf16 v[168:171], a[8:11], a[24:27], v[168:171]\n"
    "ds_read_b64_tr_b16 a[40:41], v198 offset:43008\n"
    "v_mfma_f32_16x16x32_bf16 v[172:175], a[8:11], a[28:31], v[172:175]\n"
    "buffer_load_dwordx4 v194, s[64:67], s13 offen offset:2048 lds\n"
    "v_mfma_f32_16x16x32_bf16 v[176:179], a[12:15], a[16:19], v[176:179]\n"
    "ds_read_b64_tr_b16 a[42:43], v198 offset:43520\n"
    "v_mfma_f32_16x16x32_bf16 v[180:183], a[12:15], a[20:23], v[180:183]\n"
    "ds_read_b64_tr_b16 a[44:45], v198 offset:44032\n"
    "v_mfma_f32_16x16x32_bf16 v[184:187], a[12:15], a[24:27], v[184:187]\n"
    "ds_read_b64_tr_b16 a[46:47], v198 offset:44544\n"
    "v_xor_b32 v196, 0x10000, v196\n"
    "v_xor_b32 v197, 0x10000, v197\n"
    "v_xor_b32 v198, 0x10000, v198\n"
    "v_mfma_f32_16x16x32_bf16 v[188:191], a[12:15], a[28:31], v[188:191]\n"
    "buffer_load_dwordx4 v195, s[64:67], s13 offen offset:3072 lds\n"
    "s_add_u32 s13, s13, 0x80\n"
    "s_waitcnt vmcnt(8)\n"
    "s_barrier\n"
    "s_waitcnt lgkmcnt(4)\n"
    "v_mfma_f32_16x16x32_bf16 v[128:131], a[32:35], a[48:51], v[128:131]\n"
    "ds_read_b128 a[16:19], v196\n"
    "v_mfma_f32_16x16x32_bf16 v[132:135], a[32:35], a[52:55], v[132:135]\n"
    "ds_read_b128 a[20:23], v196 offset:2048\n"
    "v_mfma_f32_16x16x32_bf16 v[136:139], a[32:35], a[56:59], v[136:139]\n"
    "ds_read_b128 a[24:27], v196 offset:4096\n"
    "s_add_u32 m0, s16, 0x4000\n"
    "v_mfma_f32_16x16x32_bf16 v[140:143], a[32:35], a[60:63], v[140:143]\n"
    "buffer_load_dwordx4 v2, s[44:47], s71 offen sc1 lds\n"
    "v_mfma_f32_16x16x32_bf16 v[144:147], a[36:39], a[48:51], v[144:147]\n"
    "ds_read_b128 a[28:31], v196 offset:6144\n"
    "v_mfma_f32_16x16x32_bf16 v[148:151], a[36:39], a[52:55], v[148:151]\n"
    "ds_read_b64_tr_b16 a[0:1], v198\n"
    "v_mfma_f32_16x16x32_bf16 v[152:155], a[36:39], a[56:59], v[152:155]\n"
    "ds_read_b64_tr_b16 a[2:3], v198 offset:512\n"
    "v_mfma_f32_16x16x32_bf16 v[156:159], a[36:39], a[60:63], v[156:159]\n"
    "buffer_load_dwordx4 v2, s[44:47], s71 offen offset:1024 sc1 lds\n"
    "s_waitcnt lgkmcnt(6)\n"
    "v_mfma_f32_16x16x32_bf16 v[160:163], a[40:43], a[48:51], v[160:163]\n"
    "ds_read_b64_tr_b16 a[4:5], v198 offset:1024\n"
    "v_mfma_f32_16x16x32_bf16 v[164:167], a[40:43], a[52:55], v[164:167]\n"
    "ds_read_b64_tr_b16 a[6:7], v198 offset:1536\n"
    "v_mfma_f32_16x16x32_bf16 v[168:171], a[40:43], a[56:59], v[168:171]\n"
    "ds_read_b64_tr_b16 a[8:9], v198 offset:2048\n"
    "v_mfma_f32_16x16x32_bf16 v[172:175], a[40:43], a[60:63], v[172:175]\n"
    "buffer_load_dwordx4 v2, s[44:47], s71 offen offset:2048 sc1 lds\n"
    "v_mfma_f32_16x16x32_bf16 v[176:179], a[44:47], a[48:51], v[176:179]\n"
    "ds_read_b64_tr_b16 a[10:11], v198 offset:2560\n"
    "v_mfma_f32_16x16x32_bf16 v[180:183], a[44:47], a[52:55], v[180:183]\n"
    "ds_read_b64_tr_b16 a[12:13], v198 offset:3072\n"
    "v_mfma_f32_16x16x32_bf16 v[184:187], a[44:47], a[56:59], v[184:187]\n"
    "ds_read_b64_tr_b16 a[14:15], v198 offset:3584\n"
    "v_mfma_f32_16x16x32_bf16 v[188:191], a[44:47], a[60:63], v[188:191]\n"
    "buffer_load_dwordx4 v2, s[44:47], s71 offen offset:3072 sc1 lds\n"
    "s_add_u32 s71, s71, 0x1c000\n"
    "s_waitcnt lgkmcnt(4)\n"
    "v_mfma_f32_16x16x32_bf16 v[128:131], a[0:3], a[16:19], v[128:131]\n"
    "ds_read_b128 a[48:51], v197\n"
    "v_mfma_f32_16x16x32_bf16 v[132:135], a[0:3], a[20:23], v[132:135]\n"
    "ds_read_b128 a[52:55], v197 offset:2048\n"
    "v_mfma_f32_16x16x32_bf16 v[136:139], a[0:3], a[24:27], v[136:139]\n"
    "ds_read_b128 a[56:59], v197 offset:4096\n"
    "s_add_u32 m0, s16, 0\n"
    "v_mfma_f32_16x16x32_bf16 v[140:143], a[0:3], a[28:31], v[140:143]\n"
    "buffer_load_dwordx4 v192, s[64:67], s13 offen lds\n"
    "v_mfma_f32_16x16x32_bf16 v[144:147], a[4:7], a[16:19], v[144:147]\n"
    "ds_read_b128 a[60:63], v197 offset:6144\n"
    "v_mfma_f32_16x16x32_bf16 v[148:151], a[4:7], a[20:23], v[148:151]\n"
    "ds_read_b64_tr_b16 a[32:33], v198 offset:8192\n"
    "v_mfma_f32_16x16x32_bf16 v[152:155], a[4:7], a[24:27], v[152:155]\n"
    "ds_read_b64_tr_b16 a[34:35], v198 offset:8704\n"
    "v_mfma_f32_16x16x32_bf16 v[156:159], a[4:7], a[28:31], v[156:159]\n"
    "buffer_load_dwordx4 v193, s[64:67], s13 offen offset:1024 lds\n"
    "s_waitcnt lgkmcnt(6)\n"
    "v_mfma_f32_16x16x32_bf16 v[160:163], a[8:11], a[16:19], v[160:163]\n"
    "ds_read_b64_tr_b16 a[36:37], v198 offset:9216\n"
    "v_mfma_f32_16x16x32_bf16 v[164:167], a[8:11], a[20:23], v[164:167]\n"
    "ds_read_b64_tr_b16 a[38:39], v198 offset:9728\n"
    "v_mfma_f32_16x16x32_bf16 v[168:171], a[8:11], a[24:27], v[168:171]\n"
    "ds_read_b64_tr_b16 a[40:41], v198 offset:10240\n"
    "v_mfma_f32_16x16x32_bf16 v[172:175], a[8:11], a[28:31], v[172:175]\n"
    "buffer_load_dwordx4 v194, s[64:67], s13 offen offset:2048 lds\n"
    "v_mfma_f32_16x16x32_bf16 v[176:179], a[12:15], a[16:19], v[176:179]\n"
    "ds_read_b64_tr_b16 a[42:43], v198 offset:10752\n"
    "v_mfma_f32_16x16x32_bf16 v[180:183], a[12:15], a[20:23], v[180:183]\n"
    "ds_read_b64_tr_b16 a[44:45], v198 offset:11264\n"
    "v_mfma_f32_16x16x32_bf16 v[184:187], a[12:15], a[24:27], v[184:187]\n"
    "ds_read_b64_tr_b16 a[46:47], v198 offset:11776\n"
    "v_mfma_f32_16x16x32_bf16 v[188:191], a[12:15], a[28:31], v[188:191]\n"
    "buffer_load_dwordx4 v195, s[64:67], s13 offen offset:3072 lds\n"
    "s_add_u32 s13, s13, 0x80\n"
    "s_waitcnt vmcnt(8)\n"
    "s_barrier\n"
    "s_waitcnt lgkmcnt(4)\n"
    "v_mfma_f32_16x16x32_bf16 v[128:131], a[32:35], a[48:51], v[128:131]\n"
    "ds_read_b128 a[16:19], v196 offset:32768\n"
    "v_mfma_f32_16x16x32_bf16 v[132:135], a[32:35], a[52:55], v[132:135]\n"
    "ds_read_b128 a[20:23], v196 offset:34816\n"
    "v_mfma_f32_16x16x32_bf16 v[136:139], a[32:35], a[56:59], v[136:139]\n"
    "ds_read_b128 a[24:27], v196 offset:36864\n"
    "s_add_u32 m0, s16, 0xc000\n"
    "v_mfma_f32_16x16x32_bf16 v[140:143], a[32:35], a[60:63], v[140:143]\n"
    "buffer_load_dwordx4 v2, s[44:47], s71 offen sc1 lds\n"
    "v_mfma_f32_16x16x32_bf16 v[144:147], a[36:39], a[48:51], v[144:147]\n"
    "ds_read_b128 a[28:31], v196 offset:38912\n"
    "v_mfma_f32_16x16x32_bf16 v[148:151], a[36:39], a[52:55], v[148:151]\n"
    "ds_read_b64_tr_b16 a[0:1], v198 offset:32768\n"
    "v_mfma_f32_16x16x32_bf16 v[152:155], a[36:39], a[56:59], v[152:155]\n"
    "ds_read_b64_tr_b16 a[2:3], v198 offset:33280\n"
    "v_mfma_f32_16x16x32_bf16 v[156:159], a[36:39], a[60:63], v[156:159]\n"
    "buffer_load_dwordx4 v2, s[44:47], s71 offen offset:1024 sc1 lds\n"
    "s_waitcnt lgkmcnt(6)\n"
    "v_mfma_f32_16x16x32_bf16 v[160:163], a[40:43], a[48:51], v[160:163]\n"
    "ds_read_b64_tr_b16 a[4:5], v198 offset:33792\n"
    "v_mfma_f32_16x16x32_bf16 v[164:167], a[40:43], a[52:55], v[164:167]\n"
    "ds_read_b64_tr_b16 a[6:7], v198 offset:34304\n"
    "v_mfma_f32_16x16x32_bf16 v[168:171], a[40:43], a[56:59], v[168:171]\n"
    "ds_read_b64_tr_b16 a[8:9], v198 offset:34816\n"
    "v_mfma_f32_16x16x32_bf16 v[172:175], a[40:43], a[60:63], v[172:175]\n"
    "buffer_load_dwordx4 v2, s[44:47], s71 offen offset:2048 sc1 lds\n"
    "v_mfma_f32_16x16x32_bf16 v[176:179], a[44:47], a[48:51], v[176:179]\n"
    "ds_read_b64_tr_b16 a[10:11], v198 offset:35328\n"
    "v_mfma_f32_16x16x32_bf16 v[180:183], a[44:47], a[52:55], v[180:183]\n"
    "ds_read_b64_tr_b16 a[12:13], v198 offset:35840\n"
    "v_mfma_f32_16x16x32_bf16 v[184:187], a[44:47], a[56:59], v[184:187]\n"
    "ds_read_b64_tr_b16 a[14:15], v198 offset:36352\n"
    "v_mfma_f32_16x16x32_bf16 v[188:191], a[44:47], a[60:63], v[188:191]\n"
    "buffer_load_dwordx4 v2, s[44:47], s71 offen offset:3072 sc1 lds\n"
    "s_add_u32 s71, s71, 0x1c000\n"
    "s_waitcnt lgkmcnt(4)\n"
    "v_mfma_f32_16x16x32_bf16 v[128:131], a[0:3], a[16:19], v[128:131]\n"
    "ds_read_b128 a[48:51], v197 offset:32768\n"
    "v_mfma_f32_16x16x32_bf16 v[132:135], a[0:3], a[20:23], v[132:135]\n"
    "ds_read_b128 a[52:55], v197 offset:34816\n"
    "v_mfma_f32_16x16x32_bf16 v[136:139], a[0:3], a[24:27], v[136:139]\n"
    "ds_read_b128 a[56:59], v197 offset:36864\n"
    "s_add_u32 m0, s16, 0x8000\n"
    "v_mfma_f32_16x16x32_bf16 v[140:143], a[0:3], a[28:31], v[140:143]\n"
    "buffer_load_dwordx4 v192, s[64:67], s13 offen lds\n"
    "v_mfma_f32_16x16x32_bf16 v[144:147], a[4:7], a[16:19], v[144:147]\n"
    "ds_read_b128 a[60:63], v197 offset:38912\n"
    "v_mfma_f32_16x16x32_bf16 v[148:151], a[4:7], a[20:23], v[148:151]\n"
    "ds_read_b64_tr_b16 a[32:33], v198 offset:40960\n"
    "v_mfma_f32_16x16x32_bf16 v[152:155], a[4:7], a[24:27], v[152:155]\n"
    "ds_read_b64_tr_b16 a[34:35], v198 offset:41472\n"
    "v_mfma_f32_16x16x32_bf16 v[156:159], a[4:7], a[28:31], v[156:159]\n"
    "buffer_load_dwordx4 v193, s[64:67], s13 offen offset:1024 lds\n"
    "s_waitcnt lgkmcnt(6)\n"
    "v_mfma_f32_16x16x32_bf16 v[160:163], a[8:11], a[16:19], v[160:163]\n"
    "ds_read_b64_tr_b16 a[36:37], v198 offset:41984\n"
    "v_mfma_f32_16x16x32_bf16 v[164:167], a[8:11], a[20:23], v[164:167]\n"
    "ds_read_b64_tr_b16 a[38:39], v198 offset:42496\n"
    "v_mfma_f32_16x16x32_bf16 v[168:171], a[8:11], a[24:27], v[168:171]\n"
    "ds_read_b64_tr_b16 a[40:41], v198 offset:43008\n"
    "v_mfma_f32_16x16x32_bf16 v[172:175], a[8:11], a[28:31], v[172:175]\n"
    "buffer_load_dwordx4 v194, s[64:67], s13 offen offset:2048 lds\n"
    "v_mfma_f32_16x16x32_bf16 v[176:179], a[12:15], a[16:19], v[176:179]\n"
    "ds_read_b64_tr_b16 a[42:43], v198 offset:43520\n"
    "v_mfma_f32_16x16x32_bf16 v[180:183], a[12:15], a[20:23], v[180:183]\n"
    "ds_read_b64_tr_b16 a[44:45], v198 offset:44032\n"
    "v_mfma_f32_16x16x32_bf16 v[184:187], a[12:15], a[24:27], v[184:187]\n"
    "ds_read_b64_tr_b16 a[46:47], v198 offset:44544\n"
    "v_xor_b32 v196, 0x10000, v196\n"
    "v_xor_b32 v197, 0x10000, v197\n"
    "v_xor_b32 v198, 0x10000, v198\n"
    "v_mfma_f32_16x16x32_bf16 v[188:191], a[12:15], a[28:31], v[188:191]\n"
    "buffer_load_dwordx4 v195, s[64:67], s13 offen offset:3072 lds\n"
    "s_add_u32 s13, s13, 0x80\n"
    "s_waitcnt vmcnt(8)\n"
    "s_barrier\n"
    "s_waitcnt lgkmcnt(4)\n"
    "v_mfma_f32_16x16x32_bf16 v[128:131], a[32:35], a[48:51], v[128:131]\n"
    "ds_read_b128 a[16:19], v196\n"
    "v_mfma_f32_16x16x32_bf16 v[132:135], a[32:35], a[52:55], v[132:135]\n"
    "ds_read_b128 a[20:23], v196 offset:2048\n"
    "v_mfma_f32_16x16x32_bf16 v[136:139], a[32:35], a[56:59], v[136:139]\n"
    "ds_read_b128 a[24:27], v196 offset:4096\n"
    "s_add_u32 m0, s16, 0x14000\n"
    "v_mfma_f32_16x16x32_bf16 v[140:143], a[32:35], a[60:63], v[140:143]\n"
    "buffer_load_dwordx4 v2, s[44:47], s71 offen sc1 lds\n"
    "v_mfma_f32_16x16x32_bf16 v[144:147], a[36:39], a[48:51], v[144:147]\n"
    "ds_read_b128 a[28:31], v196 offset:6144\n"
    "v_mfma_f32_16x16x32_bf16 v[148:151], a[36:39], a[52:55], v[148:151]\n"
    "ds_read_b64_tr_b16 a[0:1], v198\n"
    "v_mfma_f32_16x16x32_bf16 v[152:155], a[36:39], a[56:59], v[152:155]\n"
    "ds_read_b64_tr_b16 a[2:3], v198 offset:512\n"
    "v_mfma_f32_16x16x32_bf16 v[156:159], a[36:39], a[60:63], v[156:159]\n"
    "buffer_load_dwordx4 v2, s[44:47], s71 offen offset:1024 sc1 lds\n"
    "s_waitcnt lgkmcnt(6)\n"
    "v_mfma_f32_16x16x32_bf16 v[160:163], a[40:43], a[48:51], v[160:163]\n"
    "ds_read_b64_tr_b16 a[4:5], v198 offset:1024\n"
    "v_mfma_f32_16x16x32_bf16 v[164:167], a[40:43], a[52:55], v[164:167]\n"
    "ds_read_b64_tr_b16 a[6:7], v198 offset:1536\n"
    "v_mfma_f32_16x16x32_bf16 v[168:171], a[40:43], a[56:59], v[168:171]\n"
    "ds_read_b64_tr_b16 a[8:9], v198 offset:2048\n"
    "v_mfma_f32_16x16x32_bf16 v[172:175], a[40:43], a[60:63], v[172:175]\n"
    "buffer_load_dwordx4 v2, s[44:47], s71 offen offset:2048 sc1 lds\n"
    "v_mfma_f32_16x16x32_bf16 v[176:179], a[44:47], a[48:51], v[176:179]\n"
    "ds_read_b64_tr_b16 a[10:11], v198 offset:2560\n"
    "v_mfma_f32_16x16x32_bf16 v[180:183], a[44:47], a[52:55], v[180:183]\n"
    "ds_read_b64_tr_b16 a[12:13], v198 offset:3072\n"
    "v_mfma_f32_16x16x32_bf16 v[184:187], a[44:47], a[56:59], v[184:187]\n"
    "ds_read_b64_tr_b16 a[14:15], v198 offset:3584\n"
    "v_mfma_f32_16x16x32_bf16 v[188:191], a[44:47], a[60:63], v[188:191]\n"
    "buffer_load_dwordx4 v2, s[44:47], s71 offen offset:3072 sc1 lds\n"
    "s_add_u32 s71, s71, 0x1c000\n"
    "s_waitcnt lgkmcnt(4)\n"
    "v_mfma_f32_16x16x32_bf16 v[128:131], a[0:3], a[16:19], v[128:131]\n"
    "ds_read_b128 a[48:51], v197\n"
    "v_mfma_f32_16x16x32_bf16 v[132:135], a[0:3], a[20:23], v[132:135]\n"
    "ds_read_b128 a[52:55], v197 offset:2048\n"
    "v_mfma_f32_16x16x32_bf16 v[136:139], a[0:3], a[24:27], v[136:139]\n"
    "ds_read_b128 a[56:59], v197 offset:4096\n"
    "s_add_u32 m0, s16, 0x10000\n"
    "v_mfma_f32_16x16x32_bf16 v[140:143], a[0:3], a[28:31], v[140:143]\n"
    "buffer_load_dwordx4 v192, s[64:67], s13 offen lds\n"
    "v_mfma_f32_16x16x32_bf16 v[144:147], a[4:7], a[16:19], v[144:147]\n"
    "ds_read_b128 a[60:63], v197 offset:6144\n"
    "v_mfma_f32_16x16x32_bf16 v[148:151], a[4:7], a[20:23], v[148:151]\n"
    "ds_read_b64_tr_b16 a[32:33], v198 offset:8192\n"
    "v_mfma_f32_16x16x32_bf16 v[152:155], a[4:7], a[24:27], v[152:155]\n"
    "ds_read_b64_tr_b16 a[34:35], v198 offset:8704\n"
    "v_mfma_f32_16x16x32_bf16 v[156:159], a[4:7], a[28:31], v[156:159]\n"
    "buffer_load_dwordx4 v193, s[64:67], s13 offen offset:1024 lds\n"
    "s_waitcnt lgkmcnt(6)\n"
    "v_mfma_f32_16x16x32_bf16 v[160:163], a[8:11], a[16:19], v[160:163]\n"
    "ds_read_b64_tr_b16 a[36:37], v198 offset:9216\n"
    "v_mfma_f32_16x16x32_bf16 v[164:167], a[8:11], a[20:23], v[164:167]\n"
    "ds_read_b64_tr_b16 a[38:39], v198 offset:9728\n"
    "v_mfma_f32_16x16x32_bf16 v[168:171], a[8:11], a[24:27], v[168:171]\n"
    "ds_read_b64_tr_b16 a[40:41], v198 offset:10240\n"
    "v_mfma_f32_16x16x32_bf16 v[172:175], a[8:11], a[28:31], v[172:175]\n"
    "buffer_load_dwordx4 v194, s[64:67], s13 offen offset:2048 lds\n"
    "v_mfma_f32_16x16x32_bf16 v[176:179], a[12:15], a[16:19], v[176:179]\n"
    "ds_read_b64_tr_b16 a[42:43], v198 offset:10752\n"
    "v_mfma_f32_16x16x32_bf16 v[180:183], a[12:15], a[20:23], v[180:183]\n"
    "ds_read_b64_tr_b16 a[44:45], v198 offset:11264\n"
    "v_mfma_f32_16x16x32_bf16 v[184:187], a[12:15], a[24:27], v[184:187]\n"
    "ds_read_b64_tr_b16 a[46:47], v198 offset:11776\n"
    "v_mfma_f32_16x16x32_bf16 v[188:191], a[12:15], a[28:31], v[188:191]\n"
    "buffer_load_dwordx4 v195, s[64:67], s13 offen offset:3072 lds\n"
    "s_add_u32 s13, s13, 0x80\n"
    "s_waitcnt vmcnt(8)\n"
    "s_barrier\n"
    "s_waitcnt lgkmcnt(4)\n"
    "v_mfma_f32_16x16x32_bf16 v[128:131], a[32:35], a[48:51], v[128:131]\n"
    "ds_read_b128 a[16:19], v196 offset:32768\n"
    "v_mfma_f32_16x16x32_bf16 v[132:135], a[32:35], a[52:55], v[132:135]\n"
    "ds_read_b128 a[20:23], v196 offset:34816\n"
    "v_mfma_f32_16x16x32_bf16 v[136:139], a[32:35], a[56:59], v[136:139]\n"
    "ds_read_b128 a[24:27], v196 offset:36864\n"
    "s_add_u32 m0, s16, 0x1c000\n"
    "v_mfma_f32_16x16x32_bf16 v[140:143], a[32:35], a[60:63], v[140:143]\n"
    "buffer_load_dwordx4 v2, s[44:47], s71 offen sc1 lds\n"
    "v_mfma_f32_16x16x32_bf16 v[144:147], a[36:39], a[48:51], v[144:147]\n"
    "ds_read_b128 a[28:31], v196 offset:38912\n"
    "v_mfma_f32_16x16x32_bf16 v[148:151], a[36:39], a[52:55], v[148:151]\n"
    "ds_read_b64_tr_b16 a[0:1], v198 offset:32768\n"
    "v_mfma_f32_16x16x32_bf16 v[152:155], a[36:39], a[56:59], v[152:155]\n"
    "ds_read_b64_tr_b16 a[2:3], v198 offset:33280\n"
    "v_mfma_f32_16x16x32_bf16 v[156:159], a[36:39], a[60:63], v[156:159]\n"
    "buffer_load_dwordx4 v2, s[44:47], s71 offen offset:1024 sc1 lds\n"
    "s_waitcnt lgkmcnt(6)\n"
    "v_mfma_f32_16x16x32_bf16 v[160:163], a[40:43], a[48:51], v[160:163]\n"
    "ds_read_b64_tr_b16 a[4:5], v198 offset:33792\n"
    "v_mfma_f32_16x16x32_bf16 v[164:167], a[40:43], a[52:55], v[164:167]\n"
    "ds_read_b64_tr_b16 a[6:7], v198 offset:34304\n"
    "v_mfma_f32_16x16x32_bf16 v[168:171], a[40:43], a[56:59], v[168:171]\n"
    "ds_read_b64_tr_b16 a[8:9], v198 offset:34816\n"
    "v_mfma_f32_16x16x32_bf16 v[172:175], a[40:43], a[60:63], v[172:175]\n"
    "buffer_load_dwordx4 v2, s[44:47], s71 offen offset:2048 sc1 lds\n"
    "v_mfma_f32_16x16x32_bf16 v[176:179], a[44:47], a[48:51], v[176:179]\n"
    "ds_read_b64_tr_b16 a[10:11], v198 offset:35328\n"
    "v_mfma_f32_16x16x32_bf16 v[180:183], a[44:47], a[52:55], v[180:183]\n"
    "ds_read_b64_tr_b16 a[12:13], v198 offset:35840\n"
    "v_mfma_f32_16x16x32_bf16 v[184:187], a[44:47], a[56:59], v[184:187]\n"
    "ds_read_b64_tr_b16 a[14:15], v198 offset:36352\n"
    "v_mfma_f32_16x16x32_bf16 v[188:191], a[44:47], a[60:63], v[188:191]\n"
    "buffer_load_dwordx4 v2, s[44:47], s71 offen offset:3072 sc1 lds\n"
    "s_add_u32 s71, s71, 0x1c000\n"
    "s_waitcnt lgkmcnt(4)\n"
    "v_mfma_f32_16x16x32_bf16 v[128:131], a[0:3], a[16:19], v[128:131]\n"
    "ds_read_b128 a[48:51], v197 offset:32768\n"
    "v_mfma_f32_16x16x32_bf16 v[132:135], a[0:3], a[20:23], v[132:135]\n"
    "ds_read_b128 a[52:55], v197 offset:34816\n"
    "v_mfma_f32_16x16x32_bf16 v[136:139], a[0:3], a[24:27], v[136:139]\n"
    "ds_read_b128 a[56:59], v197 offset:36864\n"
    "s_add_u32 m0, s16, 0x18000\n"
    "v_mfma_f32_16x16x32_bf16 v[140:143], a[0:3], a[28:31], v[140:143]\n"
    "buffer_load_dwordx4 v192, s[64:67], s13 offen lds\n"
    "v_mfma_f32_16x16x32_bf16 v[144:147], a[4:7], a[16:19], v[144:147]\n"
    "ds_read_b128 a[60:63], v197 offset:38912\n"
    "v_mfma_f32_16x16x32_bf16 v[148:151], a[4:7], a[20:23], v[148:151]\n"
    "ds_read_b64_tr_b16 a[32:33], v198 offset:40960\n"
    "v_mfma_f32_16x16x32_bf16 v[152:155], a[4:7], a[24:27], v[152:155]\n"
    "ds_read_b64_tr_b16 a[34:35], v198 offset:41472\n"
    "v_mfma_f32_16x16x32_bf16 v[156:159], a[4:7], a[28:31], v[156:159]\n"
    "buffer_load_dwordx4 v193, s[64:67], s13 offen offset:1024 lds\n"
    "s_waitcnt lgkmcnt(6)\n"
    "v_mfma_f32_16x16x32_bf16 v[160:163], a[8:11], a[16:19], v[160:163]\n"
    "ds_read_b64_tr_b16 a[36:37], v198 offset:41984\n"
    "v_mfma_f32_16x16x32_bf16 v[164:167], a[8:11], a[20:23], v[164:167]\n"
    "ds_read_b64_tr_b16 a[38:39], v198 offset:42496\n"
    "v_mfma_f32_16x16x32_bf16 v[168:171], a[8:11], a[24:27], v[168:171]\n"
    "ds_read_b64_tr_b16 a[40:41], v198 offset:43008\n"
    "v_mfma_f32_16x16x32_bf16 v[172:175], a[8:11], a[28:31], v[172:175]\n"
    "buffer_load_dwordx4 v194, s[64:67], s13 offen offset:2048 lds\n"
    "v_mfma_f32_16x16x32_bf16 v[176:179], a[12:15], a[16:19], v[176:179]\n"
    "ds_read_b64_tr_b16 a[42:43], v198 offset:43520\n"
    "v_mfma_f32_16x16x32_bf16 v[180:183], a[12:15], a[20:23], v[180:183]\n"
    "ds_read_b64_tr_b16 a[44:45], v198 offset:44032\n"
    "v_mfma_f32_16x16x32_bf16 v[184:187], a[12:15], a[24:27], v[184:187]\n"
    "ds_read_b64_tr_b16 a[46:47], v198 offset:44544\n"
    "v_xor_b32 v196, 0x10000, v196\n"
    "v_xor_b32 v197, 0x10000, v197\n"
    "v_xor_b32 v198, 0x10000, v198\n"
    "v_mfma_f32_16x16x32_bf16 v[188:191], a[12:15], a[28:31], v[188:191]\n"
    "buffer_load_dwordx4 v195, s[64:67], s13 offen offset:3072 lds\n"
    "s_add_u32 s13, s13, 0x80\n"
    "s_waitcnt vmcnt(8)\n"
    "s_barrier\n"
    "s_waitcnt lgkmcnt(4)\n"
    "v_mfma_f32_16x16x32_bf16 v[128:131], a[32:35], a[48:51], v[128:131]\n"
    "ds_read_b128 a[16:19], v196\n"
    "v_mfma_f32_16x16x32_bf16 v[132:135], a[32:35], a[52:55], v[132:135]\n"
    "ds_read_b128 a[20:23], v196 offset:2048\n"
    "v_mfma_f32_16x16x32_bf16 v[136:139], a[32:35], a[56:59], v[136:139]\n"
    "ds_read_b128 a[24:27], v196 offset:4096\n"
    "s_add_u32 m0, s16, 0x4000\n"
    "v_mfma_f32_16x16x32_bf16 v[140:143], a[32:35], a[60:63], v[140:143]\n"
    "buffer_load_dwordx4 v2, s[44:47], s71 offen sc1 lds\n"
    "v_mfma_f32_16x16x32_bf16 v[144:147], a[36:39], a[48:51], v[144:147]\n"
    "ds_read_b128 a[28:31], v196 offset:6144\n"
    "v_mfma_f32_16x16x32_bf16 v[148:151], a[36:39], a[52:55], v[148:151]\n"
    "ds_read_b64_tr_b16 a[0:1], v198\n"
    "v_mfma_f32_16x16x32_bf16 v[152:155], a[36:39], a[56:59], v[152:155]\n"
    "ds_read_b64_tr_b16 a[2:3], v198 offset:512\n"
    "v_mfma_f32_16x16x32_bf16 v[156:159], a[36:39], a[60:63], v[156:159]\n"
    "buffer_load_dwordx4 v2, s[44:47], s71 offen offset:1024 sc1 lds\n"
    "s_waitcnt lgkmcnt(6)\n"
    "v_mfma_f32_16x16x32_bf16 v[160:163], a[40:43], a[48:51], v[160:163]\n"
    "ds_read_b64_tr_b16 a[4:5], v198 offset:1024\n"
    "v_mfma_f32_16x16x32_bf16 v[164:167], a[40:43], a[52:55], v[164:167]\n"
    "ds_read_b64_tr_b16 a[6:7], v198 offset:1536\n"
    "v_mfma_f32_16x16x32_bf16 v[168:171], a[40:43], a[56:59], v[168:171]\n"
    "ds_read_b64_tr_b16 a[8:9], v198 offset:2048\n"
    "v_mfma_f32_16x16x32_bf16 v[172:175], a[40:43], a[60:63], v[172:175]\n"
    "buffer_load_dwordx4 v2, s[44:47], s71 offen offset:2048 sc1 lds\n"
    "v_mfma_f32_16x16x32_bf16 v[176:179], a[44:47], a[48:51], v[176:179]\n"
    "ds_read_b64_tr_b16 a[10:11], v198 offset:2560\n"
    "v_mfma_f32_16x16x32_bf16 v[180:183], a[44:47], a[52:55], v[180:183]\n"
    "ds_read_b64_tr_b16 a[12:13], v198 offset:3072\n"
    "v_mfma_f32_16x16x32_bf16 v[184:187], a[44:47], a[56:59], v[184:187]\n"
    "ds_read_b64_tr_b16 a[14:15], v198 offset:3584\n"
    "v_mfma_f32_16x16x32_bf16 v[188:191], a[44:47], a[60:63], v[188:191]\n"
    "buffer_load_dwordx4 v2, s[44:47], s71 offen offset:3072 sc1 lds\n"
    "s_add_u32 s71, s71, 0x1c000\n"
    "s_waitcnt lgkmcnt(4)\n"
    "v_mfma_f32_16x16x32_bf16 v[128:131], a[0:3], a[16:19], v[128:131]\n"
    "ds_read_b128 a[48:51], v197\n"
    "v_mfma_f32_16x16x32_bf16 v[132:135], a[0:3], a[20:23], v[132:135]\n"
    "ds_read_b128 a[52:55], v197 offset:2048\n"
    "v_mfma_f32_16x16x32_bf16 v[136:139], a[0:3], a[24:27], v[136:139]\n"
    "ds_read_b128 a[56:59], v197 offset:4096\n"
    "s_add_u32 m0, s16, 0\n"
    "v_mfma_f32_16x16x32_bf16 v[140:143], a[0:3], a[28:31], v[140:143]\n"
    "buffer_load_dwordx4 v192, s[64:67], s13 offen lds\n"
    "v_mfma_f32_16x16x32_bf16 v[144:147], a[4:7], a[16:19], v[144:147]\n"
    "ds_read_b128 a[60:63], v197 offset:6144\n"
    "v_mfma_f32_16x16x32_bf16 v[148:151], a[4:7], a[20:23], v[148:151]\n"
    "ds_read_b64_tr_b16 a[32:33], v198 offset:8192\n"
    "v_mfma_f32_16x16x32_bf16 v[152:155], a[4:7], a[24:27], v[152:155]\n"
    "ds_read_b64_tr_b16 a[34:35], v198 offset:8704\n"
    "v_mfma_f32_16x16x32_bf16 v[156:159], a[4:7], a[28:31], v[156:159]\n"
    "buffer_load_dwordx4 v193, s[64:67], s13 offen offset:1024 lds\n"
    "s_waitcnt lgkmcnt(6)\n"
    "v_mfma_f32_16x16x32_bf16 v[160:163], a[8:11], a[16:19], v[160:163]\n"
    "ds_read_b64_tr_b16 a[36:37], v198 offset:9216\n"
    "v_mfma_f32_16x16x32_bf16 v[164:167], a[8:11], a[20:23], v[164:167]\n"
    "ds_read_b64_tr_b16 a[38:39], v198 offset:9728\n"
    "v_mfma_f32_16x16x32_bf16 v[168:171], a[8:11], a[24:27], v[168:171]\n"
    "ds_read_b64_tr_b16 a[40:41], v198 offset:10240\n"
    "v_mfma_f32_16x16x32_bf16 v[172:175], a[8:11], a[28:31], v[172:175]\n"
    "buffer_load_dwordx4 v194, s[64:67], s13 offen offset:2048 lds\n"
    "v_mfma_f32_16x16x32_bf16 v[176:179], a[12:15], a[16:19], v[176:179]\n"
    "ds_read_b64_tr_b16 a[42:43], v198 offset:10752\n"
    "v_mfma_f32_16x16x32_bf16 v[180:183], a[12:15], a[20:23], v[180:183]\n"
    "ds_read_b64_tr_b16 a[44:45], v198 offset:11264\n"
    "v_mfma_f32_16x16x32_bf16 v[184:187], a[12:15], a[24:27], v[184:187]\n"
    "ds_read_b64_tr_b16 a[46:47], v198 offset:11776\n"
    "v_mfma_f32_16x16x32_bf16 v[188:191], a[12:15], a[28:31], v[188:191]\n"
    "buffer_load_dwordx4 v195, s[64:67], s13 offen offset:3072 lds\n"
    "s_add_u32 s13, s13, 0x80\n"
    "s_waitcnt vmcnt(8)\n"
    "s_barrier\n"
    "s_waitcnt lgkmcnt(4)\n"
    "v_mfma_f32_16x16x32_bf16 v[128:131], a[32:35], a[48:51], v[128:131]\n"
    "ds_read_b128 a[16:19], v196 offset:32768\n"
    "v_mfma_f32_16x16x32_bf16 v[132:135], a[32:35], a[52:55], v[132:135]\n"
    "ds_read_b128 a[20:23], v196 offset:34816\n"
    "v_mfma_f32_16x16x32_bf16 v[136:139], a[32:35], a[56:59], v[136:139]\n"
    "ds_read_b128 a[24:27], v196 offset:36864\n"
    "s_add_u32 m0, s16, 0xc000\n"
    "v_mfma_f32_16x16x32_bf16 v[140:143], a[32:35], a[60:63], v[140:143]\n"
    "buffer_load_dwordx4 v2, s[44:47], s71 offen sc1 lds\n"
    "v_mfma_f32_16x16x32_bf16 v[144:147], a[36:39], a[48:51], v[144:147]\n"
    "ds_read_b128 a[28:31], v196 offset:38912\n"
    "v_mfma_f32_16x16x32_bf16 v[148:151], a[36:39], a[52:55], v[148:151]\n"
    "ds_read_b64_tr_b16 a[0:1], v198 offset:32768\n"
    "v_mfma_f32_16x16x32_bf16 v[152:155], a[36:39], a[56:59], v[152:155]\n"
    "ds_read_b64_tr_b16 a[2:3], v198 offset:33280\n"
    "v_mfma_f32_16x16x32_bf16 v[156:159], a[36:39], a[60:63], v[156:159]\n"
    "buffer_load_dwordx4 v2, s[44:47], s71 offen offset:1024 sc1 lds\n"
    "s_waitcnt lgkmcnt(6)\n"
    "v_mfma_f32_16x16x32_bf16 v[160:163], a[40:43], a[48:51], v[160:163]\n"
    "ds_read_b64_tr_b16 a[4:5], v198 offset:33792\n"
    "v_mfma_f32_16x16x32_bf16 v[164:167], a[40:43], a[52:55], v[164:167]\n"
    "ds_read_b64_tr_b16 a[6:7], v198 offset:34304\n"
    "v_mfma_f32_16x16x32_bf16 v[168:171], a[40:43], a[56:59], v[168:171]\n"
    "ds_read_b64_tr_b16 a[8:9], v198 offset:34816\n"
    "v_mfma_f32_16x16x32_bf16 v[172:175], a[40:43], a[60:63], v[172:175]\n"
    "buffer_load_dwordx4 v2, s[44:47], s71 offen offset:2048 sc1 lds\n"
    "v_mfma_f32_16x16x32_bf16 v[176:179], a[44:47], a[48:51], v[176:179]\n"
    "ds_read_b64_tr_b16 a[10:11], v198 offset:35328\n"
    "v_mfma_f32_16x16x32_bf16 v[180:183], a[44:47], a[52:55], v[180:183]\n"
    "ds_read_b64_tr_b16 a[12:13], v198 offset:35840\n"
    "v_mfma_f32_16x16x32_bf16 v[184:187], a[44:47], a[56:59], v[184:187]\n"
    "ds_read_b64_tr_b16 a[14:15], v198 offset:36352\n"
    "v_mfma_f32_16x16x32_bf16 v[188:191], a[44:47], a[60:63], v[188:191]\n"
    "buffer_load_dwordx4 v2, s[44:47], s71 offen offset:3072 sc1 lds\n"
    "s_add_u32 s71, s71, 0x1c000\n"
    "s_waitcnt lgkmcnt(4)\n"
    "v_mfma_f32_16x16x32_bf16 v[128:131], a[0:3], a[16:19], v[128:131]\n"
    "ds_read_b128 a[48:51], v197 offset:32768\n"
    "v_mfma_f32_16x16x32_bf16 v[132:135], a[0:3], a[20:23], v[132:135]\n"
    "ds_read_b128 a[52:55], v197 offset:34816\n"
    "v_mfma_f32_16x16x32_bf16 v[136:139], a[0:3], a[24:27], v[136:139]\n"
    "ds_read_b128 a[56:59], v197 offset:36864\n"
    "s_add_u32 m0, s16, 0x8000\n"
    "v_mfma_f32_16x16x32_bf16 v[140:143], a[0:3], a[28:31], v[140:143]\n"
    "buffer_load_dwordx4 v192, s[64:67], s13 offen lds\n"
    "v_mfma_f32_16x16x32_bf16 v[144:147], a[4:7], a[16:19], v[144:147]\n"
    "ds_read_b128 a[60:63], v197 offset:38912\n"
    "v_mfma_f32_16x16x32_bf16 v[148:151], a[4:7], a[20:23], v[148:151]\n"
    "ds_read_b64_tr_b16 a[32:33], v198 offset:40960\n"
    "v_mfma_f32_16x16x32_bf16 v[152:155], a[4:7], a[24:27], v[152:155]\n"
    "ds_read_b64_tr_b16 a[34:35], v198 offset:41472\n"
    "v_mfma_f32_16x16x32_bf16 v[156:159], a[4:7], a[28:31], v[156:159]\n"
    "buffer_load_dwordx4 v193, s[64:67], s13 offen offset:1024 lds\n"
    "s_waitcnt lgkmcnt(6)\n"
    "v_mfma_f32_16x16x32_bf16 v[160:163], a[8:11], a[16:19], v[160:163]\n"
    "ds_read_b64_tr_b16 a[36:37], v198 offset:41984\n"
    "v_mfma_f32_16x16x32_bf16 v[164:167], a[8:11], a[20:23], v[164:167]\n"
    "ds_read_b64_tr_b16 a[38:39], v198 offset:42496\n"
    "v_mfma_f32_16x16x32_bf16 v[168:171], a[8:11], a[24:27], v[168:171]\n"
    "ds_read_b64_tr_b16 a[40:41], v198 offset:43008\n"
    "v_mfma_f32_16x16x32_bf16 v[172:175], a[8:11], a[28:31], v[172:175]\n"
    "buffer_load_dwordx4 v194, s[64:67], s13 offen offset:2048 lds\n"
    "v_mfma_f32_16x16x32_bf16 v[176:179], a[12:15], a[16:19], v[176:179]\n"
    "ds_read_b64_tr_b16 a[42:43], v198 offset:43520\n"
    "v_mfma_f32_16x16x32_bf16 v[180:183], a[12:15], a[20:23], v[180:183]\n"
    "ds_read_b64_tr_b16 a[44:45], v198 offset:44032\n"
    "v_mfma_f32_16x16x32_bf16 v[184:187], a[12:15], a[24:27], v[184:187]\n"
    "ds_read_b64_tr_b16 a[46:47], v198 offset:44544\n"
    "v_xor_b32 v196, 0x10000, v196\n"
    "v_xor_b32 v197, 0x10000, v197\n"
    "v_xor_b32 v198, 0x10000, v198\n"
    "v_mfma_f32_16x16x32_bf16 v[188:191], a[12:15], a[28:31], v[188:191]\n"
    "buffer_load_dwordx4 v195, s[64:67], s13 offen offset:3072 lds\n"
    "s_add_u32 s13, s13, 0x80\n"
    "s_waitcnt vmcnt(8)\n"
    "s_barrier\n"
    "s_waitcnt lgkmcnt(4)\n"
    "v_mfma_f32_16x16x32_bf16 v[128:131], a[32:35], a[48:51], v[128:131]\n"
    "ds_read_b128 a[16:19], v196\n"
    "v_mfma_f32_16x16x32_bf16 v[132:135], a[32:35], a[52:55], v[132:135]\n"
    "ds_read_b128 a[20:23], v196 offset:2048\n"
    "v_mfma_f32_16x16x32_bf16 v[136:139], a[32:35], a[56:59], v[136:139]\n"
    "ds_read_b128 a[24:27], v196 offset:4096\n"
    "s_add_u32 m0, s16, 0x14000\n"
    "v_mfma_f32_16x16x32_bf16 v[140:143], a[32:35], a[60:63], v[140:143]\n"
    "buffer_load_dwordx4 v2, s[44:47], s71 offen sc1 lds\n"
    "v_mfma_f32_16x16x32_bf16 v[144:147], a[36:39], a[48:51], v[144:147]\n"
    "ds_read_b128 a[28:31], v196 offset:6144\n"
    "v_mfma_f32_16x16x32_bf16 v[148:151], a[36:39], a[52:55], v[148:151]\n"
    "ds_read_b64_tr_b16 a[0:1], v198\n"
    "v_mfma_f32_16x16x32_bf16 v[152:155], a[36:39], a[56:59], v[152:155]\n"
    "ds_read_b64_tr_b16 a[2:3], v198 offset:512\n"
    "v_mfma_f32_16x16x32_bf16 v[156:159], a[36:39], a[60:63], v[156:159]\n"
    "buffer_load_dwordx4 v2, s[44:47], s71 offen offset:1024 sc1 lds\n"
    "s_waitcnt lgkmcnt(6)\n"
    "v_mfma_f32_16x16x32_bf16 v[160:163], a[40:43], a[48:51], v[160:163]\n"
    "ds_read_b64_tr_b16 a[4:5], v198 offset:1024\n"
    "v_mfma_f32_16x16x32_bf16 v[164:167], a[40:43], a[52:55], v[164:167]\n"
    "ds_read_b64_tr_b16 a[6:7], v198 offset:1536\n"
    "v_mfma_f32_16x16x32_bf16 v[168:171], a[40:43], a[56:59], v[168:171]\n"
    "ds_read_b64_tr_b16 a[8:9], v198 offset:2048\n"
    "v_mfma_f32_16x16x32_bf16 v[172:175], a[40:43], a[60:63], v[172:175]\n"
    "buffer_load_dwordx4 v2, s[44:47], s71 offen offset:2048 sc1 lds\n"
    "v_mfma_f32_16x16x32_bf16 v[176:179], a[44:47], a[48:51], v[176:179]\n"
    "ds_read_b64_tr_b16 a[10:11], v198 offset:2560\n"
    "v_mfma_f32_16x16x32_bf16 v[180:183], a[44:47], a[52:55], v[180:183]\n"
    "ds_read_b64_tr_b16 a[12:13], v198 offset:3072\n"
    "v_mfma_f32_16x16x32_bf16 v[184:187], a[44:47], a[56:59], v[184:187]\n"
    "ds_read_b64_tr_b16 a[14:15], v198 offset:3584\n"
    "v_mfma_f32_16x16x32_bf16 v[188:191], a[44:47], a[60:63], v[188:191]\n"
    "buffer_load_dwordx4 v2, s[44:47], s71 offen offset:3072 sc1 lds\n"
    "s_add_u32 s71, s71, 0x1c000\n"
    "P4O_%=:\n"
    "s_waitcnt lgkmcnt(4)\n"
    "v_mfma_f32_16x16x32_bf16 v[128:131], a[0:3], a[16:19], v[128:131]\n"
    "ds_read_b128 a[48:51], v197\n"
    "s_mov_b64 s[28:29], s[88:89]\n"
    "v_mfma_f32_16x16x32_bf16 v[132:135], a[0:3], a[20:23], v[132:135]\n"
    "ds_read_b128 a[52:55], v197 offset:2048\n"
    "s_mov_b32 s30, 0x1000\n"
    "v_mfma_f32_16x16x32_bf16 v[136:139], a[0:3], a[24:27], v[136:139]\n"
    "ds_read_b128 a[56:59], v197 offset:4096\n"
    "s_add_u32 m0, s16, 0x10000\n"
    "v_mfma_f32_16x16x32_bf16 v[140:143], a[0:3], a[28:31], v[140:143]\n"
    "buffer_load_dwordx4 v192, s[64:67], s13 offen lds\n"
    "v_mfma_f32_16x16x32_bf16 v[144:147], a[4:7], a[16:19], v[144:147]\n"
    "ds_read_b128 a[60:63], v197 offset:6144\n"
    "s_mov_b32 s31, 0x20000\n"
    "v_mfma_f32_16x16x32_bf16 v[148:151], a[4:7], a[20:23], v[148:151]\n"
    "ds_read_b64_tr_b16 a[32:33], v198 offset:8192\n"
    "s_lshl_b32 s60, s56, 3\n"
    "v_mfma_f32_16x16x32_bf16 v[152:155], a[4:7], a[24:27], v[152:155]\n"
    "ds_read_b64_tr_b16 a[34:35], v198 offset:8704\n"
    "s_and_b32 s22, s19, 1\n"
    "v_mfma_f32_16x16x32_bf16 v[156:159], a[4:7], a[28:31], v[156:159]\n"
    "buffer_load_dwordx4 v193, s[64:67], s13 offen offset:1024 lds\n"
    "s_waitcnt lgkmcnt(6)\n"
    "v_mfma_f32_16x16x32_bf16 v[160:163], a[8:11], a[16:19], v[160:163]\n"
    "ds_read_b64_tr_b16 a[36:37], v198 offset:9216\n"
    "s_lshl_b32 s22, s22, 2\n"
    "v_mfma_f32_16x16x32_bf16 v[164:167], a[8:11], a[20:23], v[164:167]\n"
    "ds_read_b64_tr_b16 a[38:39], v198 offset:9728\n"
    "s_add_u32 s60, s60, s22\n"
    "v_mfma_f32_16x16x32_bf16 v[168:171], a[8:11], a[24:27], v[168:171]\n"
    "ds_read_b64_tr_b16 a[40:41], v198 offset:10240\n"
    "s_mul_i32 s50, s60, 56\n"
    "v_mfma_f32_16x16x32_bf16 v[172:175], a[8:11], a[28:31], v[172:175]\n"
    "buffer_load_dwordx4 v194, s[64:67], s13 offen offset:2048 lds\n"
    "v_mfma_f32_16x16x32_bf16 v[176:179], a[12:15], a[16:19], v[176:179]\n"
    "ds_read_b64_tr_b16 a[42:43], v198 offset:10752\n"
    "s_lshl_b32 s22, s57, 3\n"
    "v_mfma_f32_16x16x32_bf16 v[180:183], a[12:15], a[20:23], v[180:183]\n"
    "ds_read_b64_tr_b16 a[44:45], v198 offset:11264\n"
    "s_add_u32 s50, s50, s22\n"
    "v_mfma_f32_16x16x32_bf16 v[184:187], a[12:15], a[24:27], v[184:187]\n"
    "ds_read_b64_tr_b16 a[46:47], v198 offset:11776\n"
    "s_lshr_b32 s22, s19, 1\n"
    "v_mfma_f32_16x16x32_bf16 v[188:191], a[12:15], a[28:31], v[188:191]\n"
    "buffer_load_dwordx4 v195, s[64:67], s13 offen offset:3072 lds\n"
    "s_add_u32 s13, s13, 0x80\n"
    "s_waitcnt vmcnt(8)\n"
    "s_barrier\n"
    "s_waitcnt lgkmcnt(4)\n"
    "v_mfma_f32_16x16x32_bf16 v[128:131], a[32:35], a[48:51], v[128:131]\n"
    "ds_read_b128 a[16:19], v196 offset:32768\n"
    "s_lshl_b32 s22, s22, 2\n"
    "v_mfma_f32_16x16x32_bf16 v[132:135], a[32:35], a[52:55], v[132:135]\n"
    "ds_read_b128 a[20:23], v196 offset:34816\n"
    "s_add_u32 s50, s50, s22\n"
    "v_mfma_f32_16x16x32_bf16 v[136:139], a[32:35], a[56:59], v[136:139]\n"
    "ds_read_b128 a[24:27], v196 offset:36864\n"
    "s_add_u32 m0, s16, 0x1c000\n"
    "v_mfma_f32_16x16x32_bf16 v[140:143], a[32:35], a[60:63], v[140:143]\n"
    "buffer_load_dwordx4 v2, s[44:47], s71 offen sc1 lds\n"
    "v_mfma_f32_16x16x32_bf16 v[144:147], a[36:39], a[48:51], v[144:147]\n"
    "ds_read_b128 a[28:31], v196 offset:38912\n"
    "s_lshl_b32 s50, s50, 9\n"
    "v_mfma_f32_16x16x32_bf16 v[148:151], a[36:39], a[52:55], v[148:151]\n"
    "ds_read_b64_tr_b16 a[0:1], v198 offset:32768\n"
    "s_add_u32 s50, s50, 0x4000000\n"
    "v_mfma_f32_16x16x32_bf16 v[152:155], a[36:39], a[56:59], v[152:155]\n"
    "ds_read_b64_tr_b16 a[2:3], v198 offset:33280\n"
    "s_lshr_b32 s51, s60, 2\n"
    "v_mfma_f32_16x16x32_bf16 v[156:159], a[36:39], a[60:63], v[156:159]\n"
    "buffer_load_dwordx4 v2, s[44:47], s71 offen offset:1024 sc1 lds\n"
    "s_waitcnt lgkmcnt(6)\n"
    "v_mfma_f32_16x16x32_bf16 v[160:163], a[40:43], a[48:51], v[160:163]\n"
    "ds_read_b64_tr_b16 a[4:5], v198 offset:33792\n"
    "s_mul_i32 s51, s51, 0xb8\n"
    "v_mfma_f32_16x16x32_bf16 v[164:167], a[40:43], a[52:55], v[164:167]\n"
    "ds_read_b64_tr_b16 a[6:7], v198 offset:34304\n"
    "s_lshl_b32 s22, s57, 2\n"
    "v_mfma_f32_16x16x32_bf16 v[168:171], a[40:43], a[56:59], v[168:171]\n"
    "ds_read_b64_tr_b16 a[8:9], v198 offset:34816\n"
    "s_add_u32 s51, s51, s22\n"
    "v_mfma_f32_16x16x32_bf16 v[172:175], a[40:43], a[60:63], v[172:175]\n"
    "buffer_load_dwordx4 v2, s[44:47], s71 offen offset:2048 sc1 lds\n"
    "v_mfma_f32_16x16x32_bf16 v[176:179], a[44:47], a[48:51], v[176:179]\n"
    "ds_read_b64_tr_b16 a[10:11], v198 offset:35328\n"
    "s_and_b32 s22, s19, 2\n"
    "v_mfma_f32_16x16x32_bf16 v[180:183], a[44:47], a[52:55], v[180:183]\n"
    "ds_read_b64_tr_b16 a[12:13], v198 offset:35840\n"
    "s_add_u32 s51, s51, s22\n"
    "v_mfma_f32_16x16x32_bf16 v[184:187], a[44:47], a[56:59], v[184:187]\n"
    "ds_read_b64_tr_b16 a[14:15], v198 offset:36352\n"
    "s_mul_i32 s22, s58, 28\n"
    "v_mfma_f32_16x16x32_bf16 v[188:191], a[44:47], a[60:63], v[188:191]\n"
    "buffer_load_dwordx4 v2, s[44:47], s71 offen offset:3072 sc1 lds\n"
    "s_add_u32 s71, s71, 0x1c000\n"
    "s_waitcnt lgkmcnt(4)\n"
    "v_mfma_f32_16x16x32_bf16 v[128:131], a[0:3], a[16:19], v[128:131]\n"
    "ds_read_b128 a[48:51], v197 offset:32768\n"
    "s_add_u32 s51, s51, s22\n"
    "v_mfma_f32_16x16x32_bf16 v[132:135], a[0:3], a[20:23], v[132:135]\n"
    "ds_read_b128 a[52:55], v197 offset:34816\n"
    "s_lshl_b32 s51, s51, 12\n"
    "v_mfma_f32_16x16x32_bf16 v[136:139], a[0:3], a[24:27], v[136:139]\n"
    "ds_read_b128 a[56:59], v197 offset:36864\n"
    "s_add_u32 m0, s16, 0x18000\n"
    "v_mfma_f32_16x16x32_bf16 v[140:143], a[0:3], a[28:31], v[140:143]\n"
    "buffer_load_dwordx4 v192, s[64:67], s13 offen lds\n"
    "v_mfma_f32_16x16x32_bf16 v[144:147], a[4:7], a[16:19], v[144:147]\n"
    "ds_read_b128 a[60:63], v197 offset:38912\n"
    "s_add_u32 s51, s51, 0x2180000\n"
    "v_mfma_f32_16x16x32_bf16 v[148:151], a[4:7], a[20:23], v[148:151]\n"
    "ds_read_b64_tr_b16 a[32:33], v198 offset:40960\n"
    "s_lshl_b32 s54, s56, 1\n"
    "v_mfma_f32_16x16x32_bf16 v[152:155], a[4:7], a[24:27], v[152:155]\n"
    "ds_read_b64_tr_b16 a[34:35], v198 offset:41472\n"
    "s_add_u32 s54, s54, s58\n"
    "v_mfma_f32_16x16x32_bf16 v[156:159], a[4:7], a[28:31], v[156:159]\n"
    "buffer_load_dwordx4 v193, s[64:67], s13 offen offset:1024 lds\n"
    "s_waitcnt lgkmcnt(6)\n"
    "v_mfma_f32_16x16x32_bf16 v[160:163], a[8:11], a[16:19], v[160:163]\n"
    "ds_read_b64_tr_b16 a[36:37], v198 offset:41984\n"
    "s_lshl_b32 s54, s54, 3\n"
    "v_mfma_f32_16x16x32_bf16 v[164:167], a[8:11], a[20:23], v[164:167]\n"
    "ds_read_b64_tr_b16 a[38:39], v198 offset:42496\n"
    "s_add_u32 s54, s54, s57\n"
    "v_mfma_f32_16x16x32_bf16 v[168:171], a[8:11], a[24:27], v[168:171]\n"
    "ds_read_b64_tr_b16 a[40:41], v198 offset:43008\n"
    "s_lshl_b32 s54, s54, 2\n"
    "v_mfma_f32_16x16x32_bf16 v[172:175], a[8:11], a[28:31], v[172:175]\n"
    "buffer_load_dwordx4 v194, s[64:67], s13 offen offset:2048 lds\n"
    "v_mfma_f32_16x16x32_bf16 v[176:179], a[12:15], a[16:19], v[176:179]\n"
    "ds_read_b64_tr_b16 a[42:43], v198 offset:43520\n"
    "s_add_u32 s54, s54, s19\n"
    "v_mfma_f32_16x16x32_bf16 v[180:183], a[12:15], a[20:23], v[180:183]\n"
    "ds_read_b64_tr_b16 a[44:45], v198 offset:44032\n"
    "s_lshl_b32 s54, s54, 3\n"
    "v_mfma_f32_16x16x32_bf16 v[184:187], a[12:15], a[24:27], v[184:187]\n"
    "ds_read_b64_tr_b16 a[46:47], v198 offset:44544\n"
    "v_xor_b32 v196, 0x10000, v196\n"
    "v_xor_b32 v197, 0x10000, v197\n"
    "v_xor_b32 v198, 0x10000, v198\n"
    "v_mfma_f32_16x16x32_bf16 v[188:191], a[12:15], a[28:31], v[188:191]\n"
    "buffer_load_dwordx4 v195, s[64:67], s13 offen offset:3072 lds\n"
    "s_add_u32 s13, s13, 0x80\n"
    "s_waitcnt vmcnt(8)\n"
    "s_barrier\n"
    "v_mov_b32 v24, s4\n"
    "v_mov_b32 v25, s5\n"
    "s_mov_b64 exec, 1\n"
    "buffer_store_dwordx2 v[24:25], v2, s[44:47], s49 offen sc1\n"
    "s_mov_b64 exec, -1\n"
    "s_nop 0\n"
    "s_waitcnt lgkmcnt(4)\n"
    "v_mfma_f32_16x16x32_bf16 v[128:131], a[32:35], a[48:51], v[128:131]\n"
    "ds_read_b128 a[16:19], v196\n"
    "s_add_u32 s54, s54, 0x8000\n"
    "v_mfma_f32_16x16x32_bf16 v[132:135], a[32:35], a[52:55], v[132:135]\n"
    "ds_read_b128 a[20:23], v196 offset:2048\n"
    "s_cmp_eq_u32 s58, 1\n"
    "s_cselect_b32 s30, 0, s30\n"
    "v_mfma_f32_16x16x32_bf16 v[136:139], a[32:35], a[56:59], v[136:139]\n"
    "ds_read_b128 a[24:27], v196 offset:4096\n"
    "s_add_u32 m0, s16, 0x4000\n"
    "v_mfma_f32_16x16x32_bf16 v[140:143], a[32:35], a[60:63], v[140:143]\n"
    "buffer_load_dwordx4 v2, s[44:47], s71 offen sc1 lds\n"
    "v_mfma_f32_16x16x32_bf16 v[144:147], a[36:39], a[48:51], v[144:147]\n"
    "ds_read_b128 a[28:31], v196 offset:6144\n"
    "v_and_b32 v27, 15, v1\n"
    "v_mfma_f32_16x16x32_bf16 v[148:151], a[36:39], a[52:55], v[148:151]\n"
    "ds_read_b64_tr_b16 a[0:1], v198\n"
    "v_lshlrev_b32 v26, 5, v27\n"
    "v_mfma_f32_16x16x32_bf16 v[152:155], a[36:39], a[56:59], v[152:155]\n"
    "ds_read_b64_tr_b16 a[2:3], v198 offset:512\n"
    "v_bfe_u32 v28, v1, 4, 1\n"
    "v_mfma_f32_16x16x32_bf16 v[156:159], a[36:39], a[60:63], v[156:159]\n"
    "buffer_load_dwordx4 v2, s[44:47], s71 offen offset:1024 sc1 lds\n"
    "s_waitcnt lgkmcnt(6)\n"
    "v_mfma_f32_16x16x32_bf16 v[160:163], a[40:43], a[48:51], v[160:163]\n"
    "ds_read_b64_tr_b16 a[4:5], v198 offset:1024\n"
    "v_lshl_add_u32 v26, v28, 9, v26\n"
    "v_mfma_f32_16x16x32_bf16 v[164:167], a[40:43], a[52:55], v[164:167]\n"
    "ds_read_b64_tr_b16 a[6:7], v198 offset:1536\n"
    "v_lshrrev_b32 v28, 5, v1\n"
    "v_mfma_f32_16x16x32_bf16 v[168:171], a[40:43], a[56:59], v[168:171]\n"
    "ds_read_b64_tr_b16 a[8:9], v198 offset:2048\n"
    "v_lshl_add_u32 v26, v28, 4, v26\n"
    "v_mfma_f32_16x16x32_bf16 v[172:175], a[40:43], a[60:63], v[172:175]\n"
    "buffer_load_dwordx4 v2, s[44:47], s71 offen offset:2048 sc1 lds\n"
    "v_mfma_f32_16x16x32_bf16 v[176:179], a[44:47], a[48:51], v[176:179]\n"
    "ds_read_b64_tr_b16 a[10:11], v198 offset:2560\n"
    "v_lshlrev_b32 v28, 1, v27\n"
    "v_mfma_f32_16x16x32_bf16 v[180:183], a[44:47], a[52:55], v[180:183]\n"
    "ds_read_b64_tr_b16 a[12:13], v198 offset:3072\n"
    "v_lshlrev_b32 v27, 2, v27\n"
    "v_mfma_f32_16x16x32_bf16 v[184:187], a[44:47], a[56:59], v[184:187]\n"
    "ds_read_b64_tr_b16 a[14:15], v198 offset:3584\n"
    "v_lshlrev_b32 v27, 2, v1\n"
    "v_mfma_f32_16x16x32_bf16 v[188:191], a[44:47], a[60:63], v[188:191]\n"
    "buffer_load_dwordx4 v2, s[44:47], s71 offen offset:3072 sc1 lds\n"
    "s_add_u32 s71, s71, 0x1c000\n"
    "s_waitcnt lgkmcnt(4)\n"
    "v_mfma_f32_16x16x32_bf16 v[128:131], a[0:3], a[16:19], v[128:131]\n"
    "ds_read_b128 a[48:51], v197\n"
    "v_lshlrev_b32 v28, 1, v1\n"
    "v_mfma_f32_16x16x32_bf16 v[132:135], a[0:3], a[20:23], v[132:135]\n"
    "ds_read_b128 a[52:55], v197 offset:2048\n"
    "s_lshl_b32 s55, s60, 6\n"
    "v_mfma_f32_16x16x32_bf16 v[136:139], a[0:3], a[24:27], v[136:139]\n"
    "ds_read_b128 a[56:59], v197 offset:4096\n"
    "s_add_u32 m0, s16, 0\n"
    "v_mfma_f32_16x16x32_bf16 v[140:143], a[0:3], a[28:31], v[140:143]\n"
    "buffer_load_dwordx4 v192, s[64:67], s13 offen lds\n"
    "v_mfma_f32_16x16x32_bf16 v[144:147], a[4:7], a[16:19], v[144:147]\n"
    "ds_read_b128 a[60:63], v197 offset:6144\n"
    "s_add_u32 s55, s55, 0x3c00000\n"
    "v_mfma_f32_16x16x32_bf16 v[148:151], a[4:7], a[20:23], v[148:151]\n"
    "ds_read_b64_tr_b16 a[32:33], v198 offset:8192\n"
    "s_lshl_b32 s62, s60, 5\n"
    "v_mfma_f32_16x16x32_bf16 v[152:155], a[4:7], a[24:27], v[152:155]\n"
    "ds_read_b64_tr_b16 a[34:35], v198 offset:8704\n"
    "s_add_u32 s48, s51, 0x1000\n"
    "v_mfma_f32_16x16x32_bf16 v[156:159], a[4:7], a[28:31], v[156:159]\n"
    "buffer_load_dwordx4 v193, s[64:67], s13 offen offset:1024 lds\n"
    "s_waitcnt lgkmcnt(6)\n"
    "v_mfma_f32_16x16x32_bf16 v[160:163], a[8:11], a[16:19], v[160:163]\n"
    "ds_read_b64_tr_b16 a[36:37], v198 offset:9216\n"
    "s_mov_b64 s[36:37], s[82:83]\n"
    "v_mfma_f32_16x16x32_bf16 v[164:167], a[8:11], a[20:23], v[164:167]\n"
    "ds_read_b64_tr_b16 a[38:39], v198 offset:9728\n"
    "s_mov_b32 s38, 0x400000\n"
    "v_mfma_f32_16x16x32_bf16 v[168:171], a[8:11], a[24:27], v[168:171]\n"
    "ds_read_b64_tr_b16 a[40:41], v198 offset:10240\n"
    "s_mov_b32 s39, 0x20000\n"
    "v_mfma_f32_16x16x32_bf16 v[172:175], a[8:11], a[28:31], v[172:175]\n"
    "buffer_load_dwordx4 v194, s[64:67], s13 offen offset:2048 lds\n"
    "v_mfma_f32_16x16x32_bf16 v[176:179], a[12:15], a[16:19], v[176:179]\n"
    "ds_read_b64_tr_b16 a[42:43], v198 offset:10752\n"
    "s_mov_b64 s[40:41], s[90:91]\n"
    "v_mfma_f32_16x16x32_bf16 v[180:183], a[12:15], a[20:23], v[180:183]\n"
    "ds_read_b64_tr_b16 a[44:45], v198 offset:11264\n"
    "s_mov_b32 s42, 0xe0000\n"
    "v_mfma_f32_16x16x32_bf16 v[184:187], a[12:15], a[24:27], v[184:187]\n"
    "ds_read_b64_tr_b16 a[46:47], v198 offset:11776\n"
    "s_mov_b32 s43, 0x20000\n"
    "v_mfma_f32_16x16x32_bf16 v[188:191], a[12:15], a[28:31], v[188:191]\n"
    "buffer_load_dwordx4 v195, s[64:67], s13 offen offset:3072 lds\n"
    "s_add_u32 s13, s13, 0x80\n"
    "s_waitcnt vmcnt(9)\n"
    "s_barrier\n"
    "s_waitcnt lgkmcnt(4)\n"
    "v_mfma_f32_16x16x32_bf16 v[128:131], a[32:35], a[48:51], v[128:131]\n"
    "ds_read_b128 a[16:19], v196 offset:32768\n"
    "s_mov_b64 s[24:25], s[94:95]\n"
    "v_mfma_f32_16x16x32_bf16 v[132:135], a[32:35], a[52:55], v[132:135]\n"
    "ds_read_b128 a[20:23], v196 offset:34816\n"
    "s_mov_b32 s26, 0x200000\n"
    "v_mfma_f32_16x16x32_bf16 v[136:139], a[32:35], a[56:59], v[136:139]\n"
    "ds_read_b128 a[24:27], v196 offset:36864\n"
    "s_add_u32 m0, s16, 0xc000\n"
    "v_mfma_f32_16x16x32_bf16 v[140:143], a[32:35], a[60:63], v[140:143]\n"
    "buffer_load_dwordx4 v2, s[44:47], s71 offen sc1 lds\n"
    "v_mfma_f32_16x16x32_bf16 v[144:147], a[36:39], a[48:51], v[144:147]\n"
    "ds_read_b128 a[28:31], v196 offset:38912\n"
    "s_mov_b32 s27, 0x20000\n"
    "v_mfma_f32_16x16x32_bf16 v[148:151], a[36:39], a[52:55], v[148:151]\n"
    "ds_read_b64_tr_b16 a[0:1], v198 offset:32768\n"
    "s_and_b32 s22, s18, 7\n"
    "v_mfma_f32_16x16x32_bf16 v[152:155], a[36:39], a[56:59], v[152:155]\n"
    "ds_read_b64_tr_b16 a[2:3], v198 offset:33280\n"
    "s_lshr_b32 s23, s18, 3\n"
    "v_mfma_f32_16x16x32_bf16 v[156:159], a[36:39], a[60:63], v[156:159]\n"
    "buffer_load_dwordx4 v2, s[44:47], s71 offen offset:1024 sc1 lds\n"
    "s_waitcnt lgkmcnt(6)\n"
    "v_mfma_f32_16x16x32_bf16 v[160:163], a[40:43], a[48:51], v[160:163]\n"
    "ds_read_b64_tr_b16 a[4:5], v198 offset:33792\n"
    "s_lshr_b32 s8, s22, 1\n"
    "v_mfma_f32_16x16x32_bf16 v[164:167], a[40:43], a[52:55], v[164:167]\n"
    "ds_read_b64_tr_b16 a[6:7], v198 offset:34304\n"
    "s_and_b32 s9, s22, 1\n"
    "v_mfma_f32_16x16x32_bf16 v[168:171], a[40:43], a[56:59], v[168:171]\n"
    "ds_read_b64_tr_b16 a[8:9], v198 offset:34816\n"
    "s_lshl_b32 s9, s9, 3\n"
    "v_mfma_f32_16x16x32_bf16 v[172:175], a[40:43], a[60:63], v[172:175]\n"
    "buffer_load_dwordx4 v2, s[44:47], s71 offen offset:2048 sc1 lds\n"
    "v_mfma_f32_16x16x32_bf16 v[176:179], a[44:47], a[48:51], v[176:179]\n"
    "ds_read_b64_tr_b16 a[10:11], v198 offset:35328\n"
    "s_lshr_b32 s34, s23, 2\n"
    "v_mfma_f32_16x16x32_bf16 v[180:183], a[44:47], a[52:55], v[180:183]\n"
    "ds_read_b64_tr_b16 a[12:13], v198 offset:35840\n"
    "s_add_u32 s9, s9, s34\n"
    "v_mfma_f32_16x16x32_bf16 v[184:187], a[44:47], a[56:59], v[184:187]\n"
    "ds_read_b64_tr_b16 a[14:15], v198 offset:36352\n"
    "s_and_b32 s10, s23, 3\n"
    "v_mfma_f32_16x16x32_bf16 v[188:191], a[44:47], a[60:63], v[188:191]\n"
    "buffer_load_dwordx4 v2, s[44:47], s71 offen offset:3072 sc1 lds\n"
    "s_add_u32 s71, s71, 0x1c000\n"
    "buffer_load_dword v32, v27, s[44:47], s55 offen sc1\n"
    "buffer_load_ushort v36, v28, s[28:31], s62 offen\n"
    "s_waitcnt lgkmcnt(4)\n"
    "v_mfma_f32_16x16x32_bf16 v[128:131], a[0:3], a[16:19], v[128:131]\n"
    "ds_read_b128 a[48:51], v197 offset:32768\n"
    "s_lshl_b32 s53, s9, 1\n"
    "v_mfma_f32_16x16x32_bf16 v[132:135], a[0:3], a[20:23], v[132:135]\n"
    "ds_read_b128 a[52:55], v197 offset:34816\n"
    "s_and_b32 s22, s19, 1\n"
    "v_mfma_f32_16x16x32_bf16 v[136:139], a[0:3], a[24:27], v[136:139]\n"
    "ds_read_b128 a[56:59], v197 offset:36864\n"
    "s_add_u32 m0, s16, 0x8000\n"
    "v_mfma_f32_16x16x32_bf16 v[140:143], a[0:3], a[28:31], v[140:143]\n"
    "buffer_load_dwordx4 v192, s[64:67], s13 offen lds\n"
    "v_mfma_f32_16x16x32_bf16 v[144:147], a[4:7], a[16:19], v[144:147]\n"
    "ds_read_b128 a[60:63], v197 offset:38912\n"
    "s_add_u32 s53, s53, s22\n"
    "v_mfma_f32_16x16x32_bf16 v[148:151], a[4:7], a[20:23], v[148:151]\n"
    "ds_read_b64_tr_b16 a[32:33], v198 offset:40960\n"
    "s_mul_i32 s53, s53, 0xb8\n"
    "v_mfma_f32_16x16x32_bf16 v[152:155], a[4:7], a[24:27], v[152:155]\n"
    "ds_read_b64_tr_b16 a[34:35], v198 offset:41472\n"
    "s_lshr_b32 s22, s19, 1\n"
    "v_mfma_f32_16x16x32_bf16 v[156:159], a[4:7], a[28:31], v[156:159]\n"
    "buffer_load_dwordx4 v193, s[64:67], s13 offen offset:1024 lds\n"
    "s_waitcnt lgkmcnt(6)\n"
    "v_mfma_f32_16x16x32_bf16 v[160:163], a[8:11], a[16:19], v[160:163]\n"
    "ds_read_b64_tr_b16 a[36:37], v198 offset:41984\n"
    "s_add_u32 s53, s53, s22\n"
    "v_mfma_f32_16x16x32_bf16 v[164:167], a[8:11], a[20:23], v[164:167]\n"
    "ds_read_b64_tr_b16 a[38:39], v198 offset:42496\n"
    "s_mul_i32 s68, s8, 14\n"
    "v_mfma_f32_16x16x32_bf16 v[168:171], a[8:11], a[24:27], v[168:171]\n"
    "ds_read_b64_tr_b16 a[40:41], v198 offset:43008\n"
    "s_add_u32 s68, s68, s53\n"
    "v_mfma_f32_16x16x32_bf16 v[172:175], a[8:11], a[28:31], v[172:175]\n"
    "buffer_load_dwordx4 v194, s[64:67], s13 offen offset:2048 lds\n"
    "v_mfma_f32_16x16x32_bf16 v[176:179], a[12:15], a[16:19], v[176:179]\n"
    "ds_read_b64_tr_b16 a[42:43], v198 offset:43520\n"
    "s_lshl_b32 s68, s68, 12\n"
    "v_mfma_f32_16x16x32_bf16 v[180:183], a[12:15], a[20:23], v[180:183]\n"
    "ds_read_b64_tr_b16 a[44:45], v198 offset:44032\n"
    "s_add_u32 s68, s68, 0x217f000\n"
    "v_mfma_f32_16x16x32_bf16 v[184:187], a[12:15], a[24:27], v[184:187]\n"
    "ds_read_b64_tr_b16 a[46:47], v198 offset:44544\n"
    "v_xor_b32 v196, 0x10000, v196\n"
    "v_xor_b32 v197, 0x10000, v197\n"
    "v_xor_b32 v198, 0x10000, v198\n"
    "v_mfma_f32_16x16x32_bf16 v[188:191], a[12:15], a[28:31], v[188:191]\n"
    "buffer_load_dwordx4 v195, s[64:67], s13 offen offset:3072 lds\n"
    "s_add_u32 s13, s13, 0x80\n"
    "buffer_load_dwordx4 v[68:71], v26, s[44:47], s50 offen sc1\n"
    "s_waitcnt vmcnt(11)\n"
    "s_barrier\n"
    "s_waitcnt lgkmcnt(4)\n"
    "v_mfma_f32_16x16x32_bf16 v[128:131], a[32:35], a[48:51], v[128:131]\n"
    "ds_read_b128 a[16:19], v196\n"
    "s_lshl_b32 s22, s8, 5\n"
    "v_mfma_f32_16x16x32_bf16 v[132:135], a[32:35], a[52:55], v[132:135]\n"
    "ds_read_b128 a[20:23], v196 offset:2048\n"
    "s_add_u32 s53, s53, s22\n"
    "v_mfma_f32_16x16x32_bf16 v[136:139], a[32:35], a[56:59], v[136:139]\n"
    "ds_read_b128 a[24:27], v196 offset:4096\n"
    "s_add_u32 m0, s16, 0x14000\n"
    "v_mfma_f32_16x16x32_bf16 v[140:143], a[32:35], a[60:63], v[140:143]\n"
    "buffer_load_dwordx4 v2, s[44:47], s71 offen sc1 lds\n"
    "v_mfma_f32_16x16x32_bf16 v[144:147], a[36:39], a[48:51], v[144:147]\n"
    "ds_read_b128 a[28:31], v196 offset:6144\n"
    "s_lshl_b32 s53, s53, 12\n"
    "v_mfma_f32_16x16x32_bf16 v[148:151], a[36:39], a[52:55], v[148:151]\n"
    "ds_read_b64_tr_b16 a[0:1], v198\n"
    "s_add_u32 s53, s53, 0x20ff000\n"
    "v_mfma_f32_16x16x32_bf16 v[152:155], a[36:39], a[56:59], v[152:155]\n"
    "ds_read_b64_tr_b16 a[2:3], v198 offset:512\n"
    "s_lshl_b32 s61, s19, 14\n"
    "v_mfma_f32_16x16x32_bf16 v[156:159], a[36:39], a[60:63], v[156:159]\n"
    "buffer_load_dwordx4 v2, s[44:47], s71 offen offset:1024 sc1 lds\n"
    "s_waitcnt lgkmcnt(6)\n"
    "v_mfma_f32_16x16x32_bf16 v[160:163], a[40:43], a[48:51], v[160:163]\n"
    "ds_read_b64_tr_b16 a[4:5], v198 offset:1024\n"
    "s_lshl_b32 s22, s10, 8\n"
    "v_mfma_f32_16x16x32_bf16 v[164:167], a[40:43], a[52:55], v[164:167]\n"
    "ds_read_b64_tr_b16 a[6:7], v198 offset:1536\n"
    "s_add_u32 s61, s61, s22\n"
    "v_mfma_f32_16x16x32_bf16 v[168:171], a[40:43], a[56:59], v[168:171]\n"
    "ds_read_b64_tr_b16 a[8:9], v198 offset:2048\n"
    "s_lshl_b32 s63, s8, 20\n"
    "v_mfma_f32_16x16x32_bf16 v[172:175], a[40:43], a[60:63], v[172:175]\n"
    "buffer_load_dwordx4 v2, s[44:47], s71 offen offset:2048 sc1 lds\n"
    "v_mfma_f32_16x16x32_bf16 v[176:179], a[44:47], a[48:51], v[176:179]\n"
    "ds_read_b64_tr_b16 a[10:11], v198 offset:2560\n"
    "s_add_u32 s63, s63, s61\n"
    "v_mfma_f32_16x16x32_bf16 v[180:183], a[44:47], a[52:55], v[180:183]\n"
    "ds_read_b64_tr_b16 a[12:13], v198 offset:3072\n"
    "s_and_b32 s22, s8, 1\n"
    "v_mfma_f32_16x16x32_bf16 v[184:187], a[44:47], a[56:59], v[184:187]\n"
    "ds_read_b64_tr_b16 a[14:15], v198 offset:3584\n"
    "s_mul_i32 s22, s22, 0x70000\n"
    "v_mfma_f32_16x16x32_bf16 v[188:191], a[44:47], a[60:63], v[188:191]\n"
    "buffer_load_dwordx4 v2, s[44:47], s71 offen offset:3072 sc1 lds\n"
    "s_add_u32 s71, s71, 0x1c000\n"
    "buffer_load_dwordx4 v[76:79], v26, s[44:47], s50 offen offset:1024 sc1\n"
    "s_add_u32 s50, s50, 0x7000\n"
    "s_waitcnt lgkmcnt(4)\n"
    "v_mfma_f32_16x16x32_bf16 v[128:131], a[0:3], a[16:19], v[128:131]\n"
    "ds_read_b128 a[48:51], v197\n"
    "s_add_u32 s61, s61, s22\n"
    "v_mfma_f32_16x16x32_bf16 v[132:135], a[0:3], a[20:23], v[132:135]\n"
    "ds_read_b128 a[52:55], v197 offset:2048\n"
    "v_bfe_u32 v20, v1, 2, 2\n"
    "v_mfma_f32_16x16x32_bf16 v[136:139], a[0:3], a[24:27], v[136:139]\n"
    "ds_read_b128 a[56:59], v197 offset:4096\n"
    "s_add_u32 m0, s16, 0x10000\n"
    "v_mfma_f32_16x16x32_bf16 v[140:143], a[0:3], a[28:31], v[140:143]\n"
    "buffer_load_dwordx4 v192, s[64:67], s13 offen lds\n"
    "v_mfma_f32_16x16x32_bf16 v[144:147], a[4:7], a[16:19], v[144:147]\n"
    "ds_read_b128 a[60:63], v197 offset:6144\n"
    "v_mul_u32_u24 v20, 0x400, v20\n"
    "v_mfma_f32_16x16x32_bf16 v[148:151], a[4:7], a[20:23], v[148:151]\n"
    "ds_read_b64_tr_b16 a[32:33], v198 offset:8192\n"
    "v_lshrrev_b32 v21, 4, v1\n"
    "v_mfma_f32_16x16x32_bf16 v[152:155], a[4:7], a[24:27], v[152:155]\n"
    "ds_read_b64_tr_b16 a[34:35], v198 offset:8704\n"
    "v_lshl_add_u32 v20, v21, 6, v20\n"
    "v_mfma_f32_16x16x32_bf16 v[156:159], a[4:7], a[28:31], v[156:159]\n"
    "buffer_load_dwordx4 v193, s[64:67], s13 offen offset:1024 lds\n"
    "s_waitcnt lgkmcnt(6)\n"
    "v_mfma_f32_16x16x32_bf16 v[160:163], a[8:11], a[16:19], v[160:163]\n"
    "ds_read_b64_tr_b16 a[36:37], v198 offset:9216\n"
    "v_and_b32 v21, 3, v1\n"
    "v_mfma_f32_16x16x32_bf16 v[164:167], a[8:11], a[20:23], v[164:167]\n"
    "ds_read_b64_tr_b16 a[38:39], v198 offset:9728\n"
    "v_lshlrev_b32 v21, 4, v21\n"
    "v_mfma_f32_16x16x32_bf16 v[168:171], a[8:11], a[24:27], v[168:171]\n"
    "ds_read_b64_tr_b16 a[40:41], v198 offset:10240\n"
    "v_xor_b32 v22, 0, v21\n"
    "v_mfma_f32_16x16x32_bf16 v[172:175], a[8:11], a[28:31], v[172:175]\n"
    "buffer_load_dwordx4 v194, s[64:67], s13 offen offset:2048 lds\n"
    "v_mfma_f32_16x16x32_bf16 v[176:179], a[12:15], a[16:19], v[176:179]\n"
    "ds_read_b64_tr_b16 a[42:43], v198 offset:10752\n"
    "v_add_u32 v22, v22, v20\n"
    "v_mfma_f32_16x16x32_bf16 v[180:183], a[12:15], a[20:23], v[180:183]\n"
    "ds_read_b64_tr_b16 a[44:45], v198 offset:11264\n"
    "v_add_u32 v12, 0, v22\n"
    "v_mfma_f32_16x16x32_bf16 v[184:187], a[12:15], a[24:27], v[184:187]\n"
    "ds_read_b64_tr_b16 a[46:47], v198 offset:11776\n"
    "v_xor_b32 v22, 32, v21\n"
    "v_mfma_f32_16x16x32_bf16 v[188:191], a[12:15], a[28:31], v[188:191]\n"
    "buffer_load_dwordx4 v195, s[64:67], s13 offen offset:3072 lds\n"
    "s_add_u32 s13, s13, 0x80\n"
    "buffer_load_dwordx4 v[84:87], v26, s[44:47], s50 offen sc1\n"
    "s_waitcnt vmcnt(11)\n"
    "s_barrier\n"
    "s_waitcnt lgkmcnt(4)\n"
    "v_mfma_f32_16x16x32_bf16 v[128:131], a[32:35], a[48:51], v[128:131]\n"
    "ds_read_b128 a[16:19], v196 offset:32768\n"
    "v_add_u32 v22, v22, v20\n"
    "v_mfma_f32_16x16x32_bf16 v[132:135], a[32:35], a[52:55], v[132:135]\n"
    "ds_read_b128 a[20:23], v196 offset:34816\n"
    "v_add_u32 v13, 0xc00, v22\n"
    "v_mfma_f32_16x16x32_bf16 v[136:139], a[32:35], a[56:59], v[136:139]\n"
    "ds_read_b128 a[24:27], v196 offset:36864\n"
    "s_add_u32 m0, s16, 0x1c000\n"
    "v_mfma_f32_16x16x32_bf16 v[140:143], a[32:35], a[60:63], v[140:143]\n"
    "buffer_load_dwordx4 v2, s[44:47], s71 offen sc1 lds\n"
    "v_mfma_f32_16x16x32_bf16 v[144:147], a[36:39], a[48:51], v[144:147]\n"
    "ds_read_b128 a[28:31], v196 offset:38912\n"
    "v_xor_b32 v22, 0, v21\n"
    "v_mfma_f32_16x16x32_bf16 v[148:151], a[36:39], a[52:55], v[148:151]\n"
    "ds_read_b64_tr_b16 a[0:1], v198 offset:32768\n"
    "v_add_u32 v22, v22, v20\n"
    "v_mfma_f32_16x16x32_bf16 v[152:155], a[36:39], a[56:59], v[152:155]\n"
    "ds_read_b64_tr_b16 a[2:3], v198 offset:33280\n"
    "v_add_u32 v14, 0x1800, v22\n"
    "v_mfma_f32_16x16x32_bf16 v[156:159], a[36:39], a[60:63], v[156:159]\n"
    "buffer_load_dwordx4 v2, s[44:47], s71 offen offset:1024 sc1 lds\n"
    "s_waitcnt lgkmcnt(6)\n"
    "v_mfma_f32_16x16x32_bf16 v[160:163], a[40:43], a[48:51], v[160:163]\n"
    "ds_read_b64_tr_b16 a[4:5], v198 offset:33792\n"
    "v_xor_b32 v22, 32, v21\n"
    "v_mfma_f32_16x16x32_bf16 v[164:167], a[40:43], a[52:55], v[164:167]\n"
    "ds_read_b64_tr_b16 a[6:7], v198 offset:34304\n"
    "v_add_u32 v22, v22, v20\n"
    "v_mfma_f32_16x16x32_bf16 v[168:171], a[40:43], a[56:59], v[168:171]\n"
    "ds_read_b64_tr_b16 a[8:9], v198 offset:34816\n"
    "v_add_u32 v15, 0x2400, v22\n"
    "v_mfma_f32_16x16x32_bf16 v[172:175], a[40:43], a[60:63], v[172:175]\n"
    "buffer_load_dwordx4 v2, s[44:47], s71 offen offset:2048 sc1 lds\n"
    "v_mfma_f32_16x16x32_bf16 v[176:179], a[44:47], a[48:51], v[176:179]\n"
    "ds_read_b64_tr_b16 a[10:11], v198 offset:35328\n"
    "s_xor_b32 s22, s8, 0\n"
    "v_mfma_f32_16x16x32_bf16 v[180:183], a[44:47], a[52:55], v[180:183]\n"
    "ds_read_b64_tr_b16 a[12:13], v198 offset:35840\n"
    "s_lshl_b32 s22, s22, 10\n"
    "v_mfma_f32_16x16x32_bf16 v[184:187], a[44:47], a[56:59], v[184:187]\n"
    "ds_read_b64_tr_b16 a[14:15], v198 offset:36352\n"
    "s_add_u32 s22, s22, 0x1000\n"
    "v_mfma_f32_16x16x32_bf16 v[188:191], a[44:47], a[60:63], v[188:191]\n"
    "buffer_load_dwordx4 v2, s[44:47], s71 offen offset:3072 sc1 lds\n"
    "s_add_u32 s71, s71, 0x1c000\n"
    "buffer_load_dwordx4 v[92:95], v26, s[44:47], s50 offen offset:1024 sc1\n"
    "s_add_u32 s50, s50, 0x7000\n"
    "s_waitcnt lgkmcnt(4)\n"
    "v_mfma_f32_16x16x32_bf16 v[128:131], a[0:3], a[16:19], v[128:131]\n"
    "ds_read_b128 a[48:51], v197 offset:32768\n"
    "v_add_u32 v4, s22, v2\n"
    "v_mfma_f32_16x16x32_bf16 v[132:135], a[0:3], a[20:23], v[132:135]\n"
    "ds_read_b128 a[52:55], v197 offset:34816\n"
    "s_xor_b32 s22, s8, 1\n"
    "v_mfma_f32_16x16x32_bf16 v[136:139], a[0:3], a[24:27], v[136:139]\n"
    "ds_read_b128 a[56:59], v197 offset:36864\n"
    "s_add_u32 m0, s16, 0x18000\n"
    "v_mfma_f32_16x16x32_bf16 v[140:143], a[0:3], a[28:31], v[140:143]\n"
    "buffer_load_dwordx4 v192, s[64:67], s13 offen lds\n"
    "v_mfma_f32_16x16x32_bf16 v[144:147], a[4:7], a[16:19], v[144:147]\n"
    "ds_read_b128 a[60:63], v197 offset:38912\n"
    "s_lshl_b32 s22, s22, 10\n"
    "v_mfma_f32_16x16x32_bf16 v[148:151], a[4:7], a[20:23], v[148:151]\n"
    "ds_read_b64_tr_b16 a[32:33], v198 offset:40960\n"
    "s_add_u32 s22, s22, 0xc00\n"
    "v_mfma_f32_16x16x32_bf16 v[152:155], a[4:7], a[24:27], v[152:155]\n"
    "ds_read_b64_tr_b16 a[34:35], v198 offset:41472\n"
    "v_add_u32 v5, s22, v2\n"
    "v_mfma_f32_16x16x32_bf16 v[156:159], a[4:7], a[28:31], v[156:159]\n"
    "buffer_load_dwordx4 v193, s[64:67], s13 offen offset:1024 lds\n"
    "s_waitcnt lgkmcnt(6)\n"
    "v_mfma_f32_16x16x32_bf16 v[160:163], a[8:11], a[16:19], v[160:163]\n"
    "ds_read_b64_tr_b16 a[36:37], v198 offset:41984\n"
    "s_xor_b32 s22, s8, 2\n"
    "v_mfma_f32_16x16x32_bf16 v[164:167], a[8:11], a[20:23], v[164:167]\n"
    "ds_read_b64_tr_b16 a[38:39], v198 offset:42496\n"
    "s_lshl_b32 s22, s22, 10\n"
    "v_mfma_f32_16x16x32_bf16 v[168:171], a[8:11], a[24:27], v[168:171]\n"
    "ds_read_b64_tr_b16 a[40:41], v198 offset:43008\n"
    "s_add_u32 s22, s22, 0x800\n"
    "v_mfma_f32_16x16x32_bf16 v[172:175], a[8:11], a[28:31], v[172:175]\n"
    "buffer_load_dwordx4 v194, s[64:67], s13 offen offset:2048 lds\n"
    "v_mfma_f32_16x16x32_bf16 v[176:179], a[12:15], a[16:19], v[176:179]\n"
    "ds_read_b64_tr_b16 a[42:43], v198 offset:43520\n"
    "v_add_u32 v6, s22, v2\n"
    "v_mfma_f32_16x16x32_bf16 v[180:183], a[12:15], a[20:23], v[180:183]\n"
    "ds_read_b64_tr_b16 a[44:45], v198 offset:44032\n"
    "s_xor_b32 s22, s8, 3\n"
    "v_mfma_f32_16x16x32_bf16 v[184:187], a[12:15], a[24:27], v[184:187]\n"
    "ds_read_b64_tr_b16 a[46:47], v198 offset:44544\n"
    "v_xor_b32 v196, 0x10000, v196\n"
    "v_xor_b32 v197, 0x10000, v197\n"
    "v_xor_b32 v198, 0x10000, v198\n"
    "v_mfma_f32_16x16x32_bf16 v[188:191], a[12:15], a[28:31], v[188:191]\n"
    "buffer_load_dwordx4 v195, s[64:67], s13 offen offset:3072 lds\n"
    "s_add_u32 s13, s13, 0x80\n"
    "buffer_load_dwordx4 v[100:103], v26, s[44:47], s50 offen sc1\n"
    "s_waitcnt vmcnt(11)\n"
    "s_barrier\n"
    "s_waitcnt lgkmcnt(4)\n"
    "v_mfma_f32_16x16x32_bf16 v[128:131], a[32:35], a[48:51], v[128:131]\n"
    "ds_read_b128 a[16:19], v196\n"
    "s_lshl_b32 s22, s22, 10\n"
    "v_mfma_f32_16x16x32_bf16 v[132:135], a[32:35], a[52:55], v[132:135]\n"
    "ds_read_b128 a[20:23], v196 offset:2048\n"
    "s_add_u32 s22, s22, 0x400\n"
    "v_mfma_f32_16x16x32_bf16 v[136:139], a[32:35], a[56:59], v[136:139]\n"
    "ds_read_b128 a[24:27], v196 offset:4096\n"
    "s_add_u32 m0, s16, 0x4000\n"
    "v_mfma_f32_16x16x32_bf16 v[140:143], a[32:35], a[60:63], v[140:143]\n"
    "buffer_load_dwordx4 v2, s[44:47], s71 offen sc1 lds\n"
    "v_mfma_f32_16x16x32_bf16 v[144:147], a[36:39], a[48:51], v[144:147]\n"
    "ds_read_b128 a[28:31], v196 offset:6144\n"
    "v_add_u32 v7, s22, v2\n"
    "v_mfma_f32_16x16x32_bf16 v[148:151], a[36:39], a[52:55], v[148:151]\n"
    "ds_read_b64_tr_b16 a[0:1], v198\n"
    "s_and_b32 s22, s19, 1\n"
    "v_mfma_f32_16x16x32_bf16 v[152:155], a[36:39], a[56:59], v[152:155]\n"
    "ds_read_b64_tr_b16 a[2:3], v198 offset:512\n"
    "s_lshl_b32 s22, s22, 12\n"
    "v_mfma_f32_16x16x32_bf16 v[156:159], a[36:39], a[60:63], v[156:159]\n"
    "buffer_load_dwordx4 v2, s[44:47], s71 offen offset:1024 sc1 lds\n"
    "s_waitcnt lgkmcnt(6)\n"
    "v_mfma_f32_16x16x32_bf16 v[160:163], a[40:43], a[48:51], v[160:163]\n"
    "ds_read_b64_tr_b16 a[4:5], v198 offset:1024\n"
    "s_add_u32 s22, s22, 0\n"
    "v_mfma_f32_16x16x32_bf16 v[164:167], a[40:43], a[52:55], v[164:167]\n"
    "ds_read_b64_tr_b16 a[6:7], v198 offset:1536\n"
    "v_add_u32 v16, s22, v2\n"
    "v_mfma_f32_16x16x32_bf16 v[168:171], a[40:43], a[56:59], v[168:171]\n"
    "ds_read_b64_tr_b16 a[8:9], v198 offset:2048\n"
    "s_lshr_b32 s22, s19, 1\n"
    "v_mfma_f32_16x16x32_bf16 v[172:175], a[40:43], a[60:63], v[172:175]\n"
    "buffer_load_dwordx4 v2, s[44:47], s71 offen offset:2048 sc1 lds\n"
    "v_mfma_f32_16x16x32_bf16 v[176:179], a[44:47], a[48:51], v[176:179]\n"
    "ds_read_b64_tr_b16 a[10:11], v198 offset:2560\n"
    "s_lshl_b32 s22, s22, 9\n"
    "v_mfma_f32_16x16x32_bf16 v[180:183], a[44:47], a[52:55], v[180:183]\n"
    "ds_read_b64_tr_b16 a[12:13], v198 offset:3072\n"
    "s_add_u32 s22, s22, 0x4000\n"
    "v_mfma_f32_16x16x32_bf16 v[184:187], a[44:47], a[56:59], v[184:187]\n"
    "ds_read_b64_tr_b16 a[14:15], v198 offset:3584\n"
    "v_lshrrev_b32 v20, 4, v1\n"
    "v_mfma_f32_16x16x32_bf16 v[188:191], a[44:47], a[60:63], v[188:191]\n"
    "buffer_load_dwordx4 v2, s[44:47], s71 offen offset:3072 sc1 lds\n"
    "s_add_u32 s71, s71, 0x1c000\n"
    "buffer_load_dwordx4 v[108:111], v26, s[44:47], s50 offen offset:1024 sc1\n"
    "s_add_u32 s50, s50, 0x7000\n"
    "s_waitcnt lgkmcnt(4)\n"
    "v_mfma_f32_16x16x32_bf16 v[128:131], a[0:3], a[16:19], v[128:131]\n"
    "ds_read_b128 a[48:51], v197\n"
    "v_lshlrev_b32 v21, 10, v20\n"
    "v_mfma_f32_16x16x32_bf16 v[132:135], a[0:3], a[20:23], v[132:135]\n"
    "ds_read_b128 a[52:55], v197 offset:2048\n"
    "v_and_b32 v20, 1, v20\n"
    "v_mfma_f32_16x16x32_bf16 v[136:139], a[0:3], a[24:27], v[136:139]\n"
    "ds_read_b128 a[56:59], v197 offset:4096\n"
    "s_add_u32 m0, s16, 0\n"
    "v_mfma_f32_16x16x32_bf16 v[140:143], a[0:3], a[28:31], v[140:143]\n"
    "buffer_load_dwordx4 v192, s[64:67], s13 offen lds\n"
    "v_mfma_f32_16x16x32_bf16 v[144:147], a[4:7], a[16:19], v[144:147]\n"
    "ds_read_b128 a[60:63], v197 offset:6144\n"
    "v_lshl_add_u32 v21, v20, 5, v21\n"
    "v_mfma_f32_16x16x32_bf16 v[148:151], a[4:7], a[20:23], v[148:151]\n"
    "ds_read_b64_tr_b16 a[32:33], v198 offset:8192\n"
    "v_bfe_u32 v20, v1, 2, 2\n"
    "v_mfma_f32_16x16x32_bf16 v[152:155], a[4:7], a[24:27], v[152:155]\n"
    "ds_read_b64_tr_b16 a[34:35], v198 offset:8704\n"
    "v_lshl_add_u32 v21, v20, 6, v21\n"
    "v_mfma_f32_16x16x32_bf16 v[156:159], a[4:7], a[28:31], v[156:159]\n"
    "buffer_load_dwordx4 v193, s[64:67], s13 offen offset:1024 lds\n"
    "s_waitcnt lgkmcnt(6)\n"
    "v_mfma_f32_16x16x32_bf16 v[160:163], a[8:11], a[16:19], v[160:163]\n"
    "ds_read_b64_tr_b16 a[36:37], v198 offset:9216\n"
    "v_and_b32 v20, 3, v1\n"
    "v_mfma_f32_16x16x32_bf16 v[164:167], a[8:11], a[20:23], v[164:167]\n"
    "ds_read_b64_tr_b16 a[38:39], v198 offset:9728\n"
    "v_lshl_add_u32 v21, v20, 3, v21\n"
    "v_mfma_f32_16x16x32_bf16 v[168:171], a[8:11], a[24:27], v[168:171]\n"
    "ds_read_b64_tr_b16 a[40:41], v198 offset:10240\n"
    "v_add_u32 v18, s22, v21\n"
    "v_mfma_f32_16x16x32_bf16 v[172:175], a[8:11], a[28:31], v[172:175]\n"
    "buffer_load_dwordx4 v194, s[64:67], s13 offen offset:2048 lds\n"
    "v_mfma_f32_16x16x32_bf16 v[176:179], a[12:15], a[16:19], v[176:179]\n"
    "ds_read_b64_tr_b16 a[42:43], v198 offset:10752\n"
    "v_xor_b32 v19, 32, v18\n"
    "v_mfma_f32_16x16x32_bf16 v[180:183], a[12:15], a[20:23], v[180:183]\n"
    "ds_read_b64_tr_b16 a[44:45], v198 offset:11264\n"
    "s_lshl_b32 s69, s9, 2\n"
    "v_mfma_f32_16x16x32_bf16 v[184:187], a[12:15], a[24:27], v[184:187]\n"
    "ds_read_b64_tr_b16 a[46:47], v198 offset:11776\n"
    "s_add_u32 s69, s69, s8\n"
    "v_mfma_f32_16x16x32_bf16 v[188:191], a[12:15], a[28:31], v[188:191]\n"
    "buffer_load_dwordx4 v195, s[64:67], s13 offen offset:3072 lds\n"
    "s_add_u32 s13, s13, 0x80\n"
    "buffer_load_dwordx4 v[116:119], v26, s[44:47], s50 offen sc1\n"
    "s_waitcnt vmcnt(11)\n"
    "s_barrier\n"
    "s_waitcnt lgkmcnt(4)\n"
    "v_mfma_f32_16x16x32_bf16 v[128:131], a[32:35], a[48:51], v[128:131]\n"
    "ds_read_b128 a[16:19], v196 offset:32768\n"
    "s_lshl_b32 s69, s69, 8\n"
    "v_mfma_f32_16x16x32_bf16 v[132:135], a[32:35], a[52:55], v[132:135]\n"
    "ds_read_b128 a[20:23], v196 offset:34816\n"
    "s_add_u32 s69, s69, 0x4000\n"
    "v_mfma_f32_16x16x32_bf16 v[136:139], a[32:35], a[56:59], v[136:139]\n"
    "ds_read_b128 a[24:27], v196 offset:36864\n"
    "s_add_u32 m0, s16, 0xc000\n"
    "v_mfma_f32_16x16x32_bf16 v[140:143], a[32:35], a[60:63], v[140:143]\n"
    "buffer_load_dwordx4 v2, s[44:47], s71 offen sc1 lds\n"
    "v_mfma_f32_16x16x32_bf16 v[144:147], a[36:39], a[48:51], v[144:147]\n"
    "ds_read_b128 a[28:31], v196 offset:38912\n"
    "s_lshl_b32 s11, s9, 1\n"
    "v_mfma_f32_16x16x32_bf16 v[148:151], a[36:39], a[52:55], v[148:151]\n"
    "ds_read_b64_tr_b16 a[0:1], v198 offset:32768\n"
    "s_lshr_b32 s22, s8, 1\n"
    "v_mfma_f32_16x16x32_bf16 v[152:155], a[36:39], a[56:59], v[152:155]\n"
    "ds_read_b64_tr_b16 a[2:3], v198 offset:33280\n"
    "s_add_u32 s11, s11, s22\n"
    "v_mfma_f32_16x16x32_bf16 v[156:159], a[36:39], a[60:63], v[156:159]\n"
    "buffer_load_dwordx4 v2, s[44:47], s71 offen offset:1024 sc1 lds\n"
    "s_waitcnt lgkmcnt(6)\n"
    "v_mfma_f32_16x16x32_bf16 v[160:163], a[40:43], a[48:51], v[160:163]\n"
    "ds_read_b64_tr_b16 a[4:5], v198 offset:33792\n"
    "s_lshl_b32 s11, s11, 8\n"
    "v_mfma_f32_16x16x32_bf16 v[164:167], a[40:43], a[52:55], v[164:167]\n"
    "ds_read_b64_tr_b16 a[6:7], v198 offset:34304\n"
    "s_add_u32 s11, s11, 0x8000\n"
    "v_mfma_f32_16x16x32_bf16 v[168:171], a[40:43], a[56:59], v[168:171]\n"
    "ds_read_b64_tr_b16 a[8:9], v198 offset:34816\n"
    "v_mfma_f32_16x16x32_bf16 v[172:175], a[40:43], a[60:63], v[172:175]\n"
    "buffer_load_dwordx4 v2, s[44:47], s71 offen offset:2048 sc1 lds\n"
    "v_mfma_f32_16x16x32_bf16 v[176:179], a[44:47], a[48:51], v[176:179]\n"
    "ds_read_b64_tr_b16 a[10:11], v198 offset:35328\n"
    "v_mfma_f32_16x16x32_bf16 v[180:183], a[44:47], a[52:55], v[180:183]\n"
    "ds_read_b64_tr_b16 a[12:13], v198 offset:35840\n"
    "v_mfma_f32_16x16x32_bf16 v[184:187], a[44:47], a[56:59], v[184:187]\n"
    "ds_read_b64_tr_b16 a[14:15], v198 offset:36352\n"
    "v_mfma_f32_16x16x32_bf16 v[188:191], a[44:47], a[60:63], v[188:191]\n"
    "buffer_load_dwordx4 v2, s[44:47], s71 offen offset:3072 sc1 lds\n"
    "s_add_u32 s71, s71, 0x1c000\n"
    "buffer_load_dwordx4 v[124:127], v26, s[44:47], s50 offen offset:1024 sc1\n"
    "s_add_u32 s50, s50, 0x7000\n"
    "s_waitcnt lgkmcnt(4)\n"
    "v_mfma_f32_16x16x32_bf16 v[128:131], a[0:3], a[16:19], v[128:131]\n"
    "ds_read_b128 a[48:51], v197 offset:32768\n"
    "v_mfma_f32_16x16x32_bf16 v[132:135], a[0:3], a[20:23], v[132:135]\n"
    "ds_read_b128 a[52:55], v197 offset:34816\n"
    "v_mfma_f32_16x16x32_bf16 v[136:139], a[0:3], a[24:27], v[136:139]\n"
    "ds_read_b128 a[56:59], v197 offset:36864\n"
    "s_add_u32 m0, s16, 0x8000\n"
    "v_mfma_f32_16x16x32_bf16 v[140:143], a[0:3], a[28:31], v[140:143]\n"
    "buffer_load_dwordx4 v192, s[64:67], s13 offen lds\n"
    "v_mfma_f32_16x16x32_bf16 v[144:147], a[4:7], a[16:19], v[144:147]\n"
    "ds_read_b128 a[60:63], v197 offset:38912\n"
    "v_mfma_f32_16x16x32_bf16 v[148:151], a[4:7], a[20:23], v[148:151]\n"
    "ds_read_b64_tr_b16 a[32:33], v198 offset:40960\n"
    "v_mfma_f32_16x16x32_bf16 v[152:155], a[4:7], a[24:27], v[152:155]\n"
    "ds_read_b64_tr_b16 a[34:35], v198 offset:41472\n"
    "v_mfma_f32_16x16x32_bf16 v[156:159], a[4:7], a[28:31], v[156:159]\n"
    "buffer_load_dwordx4 v193, s[64:67], s13 offen offset:1024 lds\n"
    "s_waitcnt lgkmcnt(6)\n"
    "v_mfma_f32_16x16x32_bf16 v[160:163], a[8:11], a[16:19], v[160:163]\n"
    "ds_read_b64_tr_b16 a[36:37], v198 offset:41984\n"
    "v_mfma_f32_16x16x32_bf16 v[164:167], a[8:11], a[20:23], v[164:167]\n"
    "ds_read_b64_tr_b16 a[38:39], v198 offset:42496\n"
    "v_mfma_f32_16x16x32_bf16 v[168:171], a[8:11], a[24:27], v[168:171]\n"
    "ds_read_b64_tr_b16 a[40:41], v198 offset:43008\n"
    "v_mfma_f32_16x16x32_bf16 v[172:175], a[8:11], a[28:31], v[172:175]\n"
    "buffer_load_dwordx4 v194, s[64:67], s13 offen offset:2048 lds\n"
    "v_mfma_f32_16x16x32_bf16 v[176:179], a[12:15], a[16:19], v[176:179]\n"
    "ds_read_b64_tr_b16 a[42:43], v198 offset:43520\n"
    "v_mfma_f32_16x16x32_bf16 v[180:183], a[12:15], a[20:23], v[180:183]\n"
    "ds_read_b64_tr_b16 a[44:45], v198 offset:44032\n"
    "v_mfma_f32_16x16x32_bf16 v[184:187], a[12:15], a[24:27], v[184:187]\n"
    "ds_read_b64_tr_b16 a[46:47], v198 offset:44544\n"
    "v_xor_b32 v196, 0x10000, v196\n"
    "v_xor_b32 v197, 0x10000, v197\n"
    "v_xor_b32 v198, 0x10000, v198\n"
    "v_mfma_f32_16x16x32_bf16 v[188:191], a[12:15], a[28:31], v[188:191]\n"
    "buffer_load_dwordx4 v195, s[64:67], s13 offen offset:3072 lds\n"
    "s_add_u32 s13, s13, 0x80\n"
    "s_waitcnt vmcnt(10)\n"
    "s_barrier\n"
    "v_min_u32 v10, 31, v1\n"
    "v_mul_u32_u24 v10, 8, v10\n"
    "buffer_load_dwordx2 v[8:9], v10, s[44:47], s69 offen sc1\n"
    "s_waitcnt lgkmcnt(4)\n"
    "v_mfma_f32_16x16x32_bf16 v[128:131], a[32:35], a[48:51], v[128:131]\n"
    "ds_read_b128 a[16:19], v196\n"
    "v_mfma_f32_16x16x32_bf16 v[132:135], a[32:35], a[52:55], v[132:135]\n"
    "ds_read_b128 a[20:23], v196 offset:2048\n"
    "v_mfma_f32_16x16x32_bf16 v[136:139], a[32:35], a[56:59], v[136:139]\n"
    "ds_read_b128 a[24:27], v196 offset:4096\n"
    "s_add_u32 m0, s16, 0x14000\n"
    "v_mfma_f32_16x16x32_bf16 v[140:143], a[32:35], a[60:63], v[140:143]\n"
    "buffer_load_dwordx4 v2, s[44:47], s71 offen sc1 lds\n"
    "v_mfma_f32_16x16x32_bf16 v[144:147], a[36:39], a[48:51], v[144:147]\n"
    "ds_read_b128 a[28:31], v196 offset:6144\n"
    "v_mfma_f32_16x16x32_bf16 v[148:151], a[36:39], a[52:55], v[148:151]\n"
    "ds_read_b64_tr_b16 a[0:1], v198\n"
    "v_mfma_f32_16x16x32_bf16 v[152:155], a[36:39], a[56:59], v[152:155]\n"
    "ds_read_b64_tr_b16 a[2:3], v198 offset:512\n"
    "v_mfma_f32_16x16x32_bf16 v[156:159], a[36:39], a[60:63], v[156:159]\n"
    "buffer_load_dwordx4 v2, s[44:47], s71 offen offset:1024 sc1 lds\n"
    "s_waitcnt lgkmcnt(6)\n"
    "v_mfma_f32_16x16x32_bf16 v[160:163], a[40:43], a[48:51], v[160:163]\n"
    "ds_read_b64_tr_b16 a[4:5], v198 offset:1024\n"
    "v_mfma_f32_16x16x32_bf16 v[164:167], a[40:43], a[52:55], v[164:167]\n"
    "ds_read_b64_tr_b16 a[6:7], v198 offset:1536\n"
    "v_mfma_f32_16x16x32_bf16 v[168:171], a[40:43], a[56:59], v[168:171]\n"
    "ds_read_b64_tr_b16 a[8:9], v198 offset:2048\n"
    "v_mfma_f32_16x16x32_bf16 v[172:175], a[40:43], a[60:63], v[172:175]\n"
    "buffer_load_dwordx4 v2, s[44:47], s71 offen offset:2048 sc1 lds\n"
    "v_mfma_f32_16x16x32_bf16 v[176:179], a[44:47], a[48:51], v[176:179]\n"
    "ds_read_b64_tr_b16 a[10:11], v198 offset:2560\n"
    "v_mfma_f32_16x16x32_bf16 v[180:183], a[44:47], a[52:55], v[180:183]\n"
    "ds_read_b64_tr_b16 a[12:13], v198 offset:3072\n"
    "v_mfma_f32_16x16x32_bf16 v[184:187], a[44:47], a[56:59], v[184:187]\n"
    "ds_read_b64_tr_b16 a[14:15], v198 offset:3584\n"
    "v_mfma_f32_16x16x32_bf16 v[188:191], a[44:47], a[60:63], v[188:191]\n"
    "buffer_load_dwordx4 v2, s[44:47], s71 offen offset:3072 sc1 lds\n"
    "s_add_u32 s71, s71, 0x1c000\n"
    "s_waitcnt lgkmcnt(4)\n"
    "v_mfma_f32_16x16x32_bf16 v[128:131], a[0:3], a[16:19], v[128:131]\n"
    "ds_read_b128 a[48:51], v197\n"
    "v_mfma_f32_16x16x32_bf16 v[132:135], a[0:3], a[20:23], v[132:135]\n"
    "ds_read_b128 a[52:55], v197 offset:2048\n"
    "v_mfma_f32_16x16x32_bf16 v[136:139], a[0:3], a[24:27], v[136:139]\n"
    "ds_read_b128 a[56:59], v197 offset:4096\n"
    "s_add_u32 m0, s16, 0x10000\n"
    "v_mfma_f32_16x16x32_bf16 v[140:143], a[0:3], a[28:31], v[140:143]\n"
    "buffer_load_dwordx4 v192, s[64:67], s13 offen lds\n"
    "v_mfma_f32_16x16x32_bf16 v[144:147], a[4:7], a[16:19], v[144:147]\n"
    "ds_read_b128 a[60:63], v197 offset:6144\n"
    "v_mfma_f32_16x16x32_bf16 v[148:151], a[4:7], a[20:23], v[148:151]\n"
    "ds_read_b64_tr_b16 a[32:33], v198 offset:8192\n"
    "v_mfma_f32_16x16x32_bf16 v[152:155], a[4:7], a[24:27], v[152:155]\n"
    "ds_read_b64_tr_b16 a[34:35], v198 offset:8704\n"
    "v_mfma_f32_16x16x32_bf16 v[156:159], a[4:7], a[28:31], v[156:159]\n"
    "buffer_load_dwordx4 v193, s[64:67], s13 offen offset:1024 lds\n"
    "s_waitcnt lgkmcnt(6)\n"
    "v_mfma_f32_16x16x32_bf16 v[160:163], a[8:11], a[16:19], v[160:163]\n"
    "ds_read_b64_tr_b16 a[36:37], v198 offset:9216\n"
    "v_mfma_f32_16x16x32_bf16 v[164:167], a[8:11], a[20:23], v[164:167]\n"
    "ds_read_b64_tr_b16 a[38:39], v198 offset:9728\n"
    "v_mfma_f32_16x16x32_bf16 v[168:171], a[8:11], a[24:27], v[168:171]\n"
    "ds_read_b64_tr_b16 a[40:41], v198 offset:10240\n"
    "v_mfma_f32_16x16x32_bf16 v[172:175], a[8:11], a[28:31], v[172:175]\n"
    "buffer_load_dwordx4 v194, s[64:67], s13 offen offset:2048 lds\n"
    "v_mfma_f32_16x16x32_bf16 v[176:179], a[12:15], a[16:19], v[176:179]\n"
    "ds_read_b64_tr_b16 a[42:43], v198 offset:10752\n"
    "v_mfma_f32_16x16x32_bf16 v[180:183], a[12:15], a[20:23], v[180:183]\n"
    "ds_read_b64_tr_b16 a[44:45], v198 offset:11264\n"
    "v_mfma_f32_16x16x32_bf16 v[184:187], a[12:15], a[24:27], v[184:187]\n"
    "ds_read_b64_tr_b16 a[46:47], v198 offset:11776\n"
    "v_mfma_f32_16x16x32_bf16 v[188:191], a[12:15], a[28:31], v[188:191]\n"
    "buffer_load_dwordx4 v195, s[64:67], s13 offen offset:3072 lds\n"
    "s_add_u32 s13, s13, 0x80\n"
    "s_waitcnt vmcnt(13)\n"
    "s_waitcnt vmcnt(9)\n"
    "s_barrier\n"
    "s_waitcnt lgkmcnt(4)\n"
    "v_mfma_f32_16x16x32_bf16 v[128:131], a[32:35], a[48:51], v[128:131]\n"
    "ds_read_b128 a[16:19], v196 offset:32768\n"
    "v_sub_f32 v32, 1.0, v32\n"
    "v_mfma_f32_16x16x32_bf16 v[132:135], a[32:35], a[52:55], v[132:135]\n"
    "ds_read_b128 a[20:23], v196 offset:34816\n"
    "v_lshlrev_b32 v36, 16, v36\n"
    "v_mfma_f32_16x16x32_bf16 v[136:139], a[32:35], a[56:59], v[136:139]\n"
    "ds_read_b128 a[24:27], v196 offset:36864\n"
    "s_add_u32 m0, s16, 0x1c000\n"
    "v_mfma_f32_16x16x32_bf16 v[140:143], a[32:35], a[60:63], v[140:143]\n"
    "buffer_load_dwordx4 v2, s[44:47], s71 offen sc1 lds\n"
    "v_mfma_f32_16x16x32_bf16 v[144:147], a[36:39], a[48:51], v[144:147]\n"
    "ds_read_b128 a[28:31], v196 offset:38912\n"
    "v_mul_f32 v36, v36, v32\n"
    "v_mfma_f32_16x16x32_bf16 v[148:151], a[36:39], a[52:55], v[148:151]\n"
    "ds_read_b64_tr_b16 a[0:1], v198 offset:32768\n"
    "v_mov_b32 v34, v32\n"
    "v_mfma_f32_16x16x32_bf16 v[152:155], a[36:39], a[56:59], v[152:155]\n"
    "ds_read_b64_tr_b16 a[2:3], v198 offset:33280\n"
    "v_mov_b32 v38, v36\n"
    "v_mfma_f32_16x16x32_bf16 v[156:159], a[36:39], a[60:63], v[156:159]\n"
    "buffer_load_dwordx4 v2, s[44:47], s71 offen offset:1024 sc1 lds\n"
    "s_waitcnt lgkmcnt(6)\n"
    "v_mfma_f32_16x16x32_bf16 v[160:163], a[40:43], a[48:51], v[160:163]\n"
    "ds_read_b64_tr_b16 a[4:5], v198 offset:33792\n"
    "s_nop 0\n"
    "v_mfma_f32_16x16x32_bf16 v[164:167], a[40:43], a[52:55], v[164:167]\n"
    "ds_read_b64_tr_b16 a[6:7], v198 offset:34304\n"
    "v_permlane32_swap_b32 v32, v34\n"
    "v_mfma_f32_16x16x32_bf16 v[168:171], a[40:43], a[56:59], v[168:171]\n"
    "ds_read_b64_tr_b16 a[8:9], v198 offset:34816\n"
    "v_permlane32_swap_b32 v36, v38\n"
    "v_mfma_f32_16x16x32_bf16 v[172:175], a[40:43], a[60:63], v[172:175]\n"
    "buffer_load_dwordx4 v2, s[44:47], s71 offen offset:2048 sc1 lds\n"
    "v_mfma_f32_16x16x32_bf16 v[176:179], a[44:47], a[48:51], v[176:179]\n"
    "ds_read_b64_tr_b16 a[10:11], v198 offset:35328\n"
    "v_mov_b32 v33, v32\n"
    "v_mfma_f32_16x16x32_bf16 v[180:183], a[44:47], a[52:55], v[180:183]\n"
    "ds_read_b64_tr_b16 a[12:13], v198 offset:35840\n"
    "v_mov_b32 v37, v36\n"
    "v_mfma_f32_16x16x32_bf16 v[184:187], a[44:47], a[56:59], v[184:187]\n"
    "ds_read_b64_tr_b16 a[14:15], v198 offset:36352\n"
    "v_mov_b32 v35, v34\n"
    "v_mfma_f32_16x16x32_bf16 v[188:191], a[44:47], a[60:63], v[188:191]\n"
    "buffer_load_dwordx4 v2, s[44:47], s71 offen offset:3072 sc1 lds\n"
    "s_add_u32 s71, s71, 0x1c000\n"
    "s_waitcnt lgkmcnt(4)\n"
    "v_mfma_f32_16x16x32_bf16 v[128:131], a[0:3], a[16:19], v[128:131]\n"
    "ds_read_b128 a[48:51], v197 offset:32768\n"
    "v_mov_b32 v39, v38\n"
    "v_mfma_f32_16x16x32_bf16 v[132:135], a[0:3], a[20:23], v[132:135]\n"
    "ds_read_b128 a[52:55], v197 offset:34816\n"
    "s_nop 0\n"
    "v_mfma_f32_16x16x32_bf16 v[136:139], a[0:3], a[24:27], v[136:139]\n"
    "ds_read_b128 a[56:59], v197 offset:36864\n"
    "s_add_u32 m0, s16, 0x18000\n"
    "v_mfma_f32_16x16x32_bf16 v[140:143], a[0:3], a[28:31], v[140:143]\n"
    "buffer_load_dwordx4 v192, s[64:67], s13 offen lds\n"
    "v_mfma_f32_16x16x32_bf16 v[144:147], a[4:7], a[16:19], v[144:147]\n"
    "ds_read_b128 a[60:63], v197 offset:38912\n"
    "v_permlane16_swap_b32 v32, v33\n"
    "v_mfma_f32_16x16x32_bf16 v[148:151], a[4:7], a[20:23], v[148:151]\n"
    "ds_read_b64_tr_b16 a[32:33], v198 offset:40960\n"
    "v_permlane16_swap_b32 v36, v37\n"
    "v_mfma_f32_16x16x32_bf16 v[152:155], a[4:7], a[24:27], v[152:155]\n"
    "ds_read_b64_tr_b16 a[34:35], v198 offset:41472\n"
    "v_permlane16_swap_b32 v34, v35\n"
    "v_mfma_f32_16x16x32_bf16 v[156:159], a[4:7], a[28:31], v[156:159]\n"
    "buffer_load_dwordx4 v193, s[64:67], s13 offen offset:1024 lds\n"
    "s_waitcnt lgkmcnt(6)\n"
    "v_mfma_f32_16x16x32_bf16 v[160:163], a[8:11], a[16:19], v[160:163]\n"
    "ds_read_b64_tr_b16 a[36:37], v198 offset:41984\n"
    "v_permlane16_swap_b32 v38, v39\n"
    "v_mfma_f32_16x16x32_bf16 v[164:167], a[8:11], a[20:23], v[164:167]\n"
    "ds_read_b64_tr_b16 a[38:39], v198 offset:42496\n"
    "v_permlane16_swap_b32 v68, v70\n"
    "v_mfma_f32_16x16x32_bf16 v[168:171], a[8:11], a[24:27], v[168:171]\n"
    "ds_read_b64_tr_b16 a[40:41], v198 offset:43008\n"
    "v_permlane16_swap_b32 v69, v71\n"
    "v_mfma_f32_16x16x32_bf16 v[172:175], a[8:11], a[28:31], v[172:175]\n"
    "buffer_load_dwordx4 v194, s[64:67], s13 offen offset:2048 lds\n"
    "v_mfma_f32_16x16x32_bf16 v[176:179], a[12:15], a[16:19], v[176:179]\n"
    "ds_read_b64_tr_b16 a[42:43], v198 offset:43520\n"
    "v_lshlrev_b32 v64, 16, v68\n"
    "v_mfma_f32_16x16x32_bf16 v[180:183], a[12:15], a[20:23], v[180:183]\n"
    "ds_read_b64_tr_b16 a[44:45], v198 offset:44032\n"
    "v_and_b32 v65, 0xffff0000, v68\n"
    "v_mfma_f32_16x16x32_bf16 v[184:187], a[12:15], a[24:27], v[184:187]\n"
    "ds_read_b64_tr_b16 a[46:47], v198 offset:44544\n"
    "v_xor_b32 v196, 0x10000, v196\n"
    "v_xor_b32 v197, 0x10000, v197\n"
    "v_xor_b32 v198, 0x10000, v198\n"
    "v_mfma_f32_16x16x32_bf16 v[188:191], a[12:15], a[28:31], v[188:191]\n"
    "buffer_load_dwordx4 v195, s[64:67], s13 offen offset:3072 lds\n"
    "s_add_u32 s13, s13, 0x80\n"
    "s_waitcnt vmcnt(8)\n"
    "s_barrier\n"
    "v_cmp_ne_u64 vcc, s[4:5], v[8:9]\n"
    "s_nop 0\n"
    "s_cbranch_vccnz PS6_%=\n"
    "PR6_%=:\n"
    "s_waitcnt lgkmcnt(4)\n"
    "v_mfma_f32_16x16x32_bf16 v[128:131], a[32:35], a[48:51], v[128:131]\n"
    "ds_read_b128 a[16:19], v196\n"
    "v_lshlrev_b32 v66, 16, v69\n"
    "v_mfma_f32_16x16x32_bf16 v[132:135], a[32:35], a[52:55], v[132:135]\n"
    "ds_read_b128 a[20:23], v196 offset:2048\n"
    "v_and_b32 v67, 0xffff0000, v69\n"
    "v_mfma_f32_16x16x32_bf16 v[136:139], a[32:35], a[56:59], v[136:139]\n"
    "ds_read_b128 a[24:27], v196 offset:4096\n"
    "s_add_u32 m0, s16, 0x4000\n"
    "v_mfma_f32_16x16x32_bf16 v[140:143], a[32:35], a[60:63], v[140:143]\n"
    "buffer_load_dwordx4 v12, s[36:39], s63 offen lds\n"
    "v_mfma_f32_16x16x32_bf16 v[144:147], a[36:39], a[48:51], v[144:147]\n"
    "ds_read_b128 a[28:31], v196 offset:6144\n"
    "v_lshlrev_b32 v68, 16, v70\n"
    "v_mfma_f32_16x16x32_bf16 v[148:151], a[36:39], a[52:55], v[148:151]\n"
    "ds_read_b64_tr_b16 a[0:1], v198\n"
    "v_and_b32 v69, 0xffff0000, v70\n"
    "v_mfma_f32_16x16x32_bf16 v[152:155], a[36:39], a[56:59], v[152:155]\n"
    "ds_read_b64_tr_b16 a[2:3], v198 offset:512\n"
    "v_lshlrev_b32 v70, 16, v71\n"
    "v_mfma_f32_16x16x32_bf16 v[156:159], a[36:39], a[60:63], v[156:159]\n"
    "buffer_load_dwordx4 v13, s[36:39], s63 offen offset:1024 lds\n"
    "s_waitcnt lgkmcnt(6)\n"
    "v_mfma_f32_16x16x32_bf16 v[160:163], a[40:43], a[48:51], v[160:163]\n"
    "ds_read_b64_tr_b16 a[4:5], v198 offset:1024\n"
    "v_and_b32 v71, 0xffff0000, v71\n"
    "v_mfma_f32_16x16x32_bf16 v[164:167], a[40:43], a[52:55], v[164:167]\n"
    "ds_read_b64_tr_b16 a[6:7], v198 offset:1536\n"
    "v_permlane16_swap_b32 v84, v86\n"
    "v_mfma_f32_16x16x32_bf16 v[168:171], a[40:43], a[56:59], v[168:171]\n"
    "ds_read_b64_tr_b16 a[8:9], v198 offset:2048\n"
    "v_permlane16_swap_b32 v85, v87\n"
    "v_mfma_f32_16x16x32_bf16 v[172:175], a[40:43], a[60:63], v[172:175]\n"
    "buffer_load_dwordx4 v14, s[36:39], s63 offen offset:2048 lds\n"
    "v_mfma_f32_16x16x32_bf16 v[176:179], a[44:47], a[48:51], v[176:179]\n"
    "ds_read_b64_tr_b16 a[10:11], v198 offset:2560\n"
    "v_lshlrev_b32 v80, 16, v84\n"
    "v_mfma_f32_16x16x32_bf16 v[180:183], a[44:47], a[52:55], v[180:183]\n"
    "ds_read_b64_tr_b16 a[12:13], v198 offset:3072\n"
    "v_and_b32 v81, 0xffff0000, v84\n"
    "v_mfma_f32_16x16x32_bf16 v[184:187], a[44:47], a[56:59], v[184:187]\n"
    "ds_read_b64_tr_b16 a[14:15], v198 offset:3584\n"
    "v_lshlrev_b32 v82, 16, v85\n"
    "v_mfma_f32_16x16x32_bf16 v[188:191], a[44:47], a[60:63], v[188:191]\n"
    "buffer_load_dwordx4 v15, s[36:39], s63 offen offset:3072 lds\n"
    "s_add_u32 s63, s63, 0x10000\n"
    "s_waitcnt lgkmcnt(4)\n"
    "v_mfma_f32_16x16x32_bf16 v[128:131], a[0:3], a[16:19], v[128:131]\n"
    "ds_read_b128 a[48:51], v197\n"
    "v_and_b32 v83, 0xffff0000, v85\n"
    "v_mfma_f32_16x16x32_bf16 v[132:135], a[0:3], a[20:23], v[132:135]\n"
    "ds_read_b128 a[52:55], v197 offset:2048\n"
    "v_lshlrev_b32 v84, 16, v86\n"
    "v_mfma_f32_16x16x32_bf16 v[136:139], a[0:3], a[24:27], v[136:139]\n"
    "ds_read_b128 a[56:59], v197 offset:4096\n"
    "s_add_u32 m0, s16, 0\n"
    "v_mfma_f32_16x16x32_bf16 v[140:143], a[0:3], a[28:31], v[140:143]\n"
    "buffer_load_dwordx4 v4, s[44:47], s53 offen sc1 lds\n"
    "v_mfma_f32_16x16x32_bf16 v[144:147], a[4:7], a[16:19], v[144:147]\n"
    "ds_read_b128 a[60:63], v197 offset:6144\n"
    "v_and_b32 v85, 0xffff0000, v86\n"
    "v_mfma_f32_16x16x32_bf16 v[148:151], a[4:7], a[20:23], v[148:151]\n"
    "ds_read_b64_tr_b16 a[32:33], v198 offset:8192\n"
    "v_lshlrev_b32 v86, 16, v87\n"
    "v_mfma_f32_16x16x32_bf16 v[152:155], a[4:7], a[24:27], v[152:155]\n"
    "ds_read_b64_tr_b16 a[34:35], v198 offset:8704\n"
    "v_and_b32 v87, 0xffff0000, v87\n"
    "v_mfma_f32_16x16x32_bf16 v[156:159], a[4:7], a[28:31], v[156:159]\n"
    "buffer_load_dwordx4 v5, s[44:47], s53 offen offset:1024 sc1 lds\n"
    "s_waitcnt lgkmcnt(6)\n"
    "v_mfma_f32_16x16x32_bf16 v[160:163], a[8:11], a[16:19], v[160:163]\n"
    "ds_read_b64_tr_b16 a[36:37], v198 offset:9216\n"
    "v_permlane16_swap_b32 v100, v102\n"
    "v_mfma_f32_16x16x32_bf16 v[164:167], a[8:11], a[20:23], v[164:167]\n"
    "ds_read_b64_tr_b16 a[38:39], v198 offset:9728\n"
    "v_permlane16_swap_b32 v101, v103\n"
    "v_mfma_f32_16x16x32_bf16 v[168:171], a[8:11], a[24:27], v[168:171]\n"
    "ds_read_b64_tr_b16 a[40:41], v198 offset:10240\n"
    "v_lshlrev_b32 v96, 16, v100\n"
    "v_mfma_f32_16x16x32_bf16 v[172:175], a[8:11], a[28:31], v[172:175]\n"
    "buffer_load_dwordx4 v6, s[44:47], s53 offen offset:2048 sc1 lds\n"
    "v_mfma_f32_16x16x32_bf16 v[176:179], a[12:15], a[16:19], v[176:179]\n"
    "ds_read_b64_tr_b16 a[42:43], v198 offset:10752\n"
    "v_and_b32 v97, 0xffff0000, v100\n"
    "v_mfma_f32_16x16x32_bf16 v[180:183], a[12:15], a[20:23], v[180:183]\n"
    "ds_read_b64_tr_b16 a[44:45], v198 offset:11264\n"
    "v_lshlrev_b32 v98, 16, v101\n"
    "v_mfma_f32_16x16x32_bf16 v[184:187], a[12:15], a[24:27], v[184:187]\n"
    "ds_read_b64_tr_b16 a[46:47], v198 offset:11776\n"
    "v_and_b32 v99, 0xffff0000, v101\n"
    "v_mfma_f32_16x16x32_bf16 v[188:191], a[12:15], a[28:31], v[188:191]\n"
    "buffer_load_dwordx4 v7, s[44:47], s53 offen offset:3072 sc1 lds\n"
    "s_add_u32 s53, s53, 0x2000\n"
    "s_waitcnt vmcnt(8)\n"
    "s_barrier\n"
    "s_waitcnt lgkmcnt(4)\n"
    "v_mfma_f32_16x16x32_bf16 v[128:131], a[32:35], a[48:51], v[128:131]\n"
    "ds_read_b128 a[16:19], v196 offset:32768\n"
    "v_lshlrev_b32 v100, 16, v102\n"
    "v_mfma_f32_16x16x32_bf16 v[132:135], a[32:35], a[52:55], v[132:135]\n"
    "ds_read_b128 a[20:23], v196 offset:34816\n"
    "v_and_b32 v101, 0xffff0000, v102\n"
    "v_mfma_f32_16x16x32_bf16 v[136:139], a[32:35], a[56:59], v[136:139]\n"
    "ds_read_b128 a[24:27], v196 offset:36864\n"
    "s_add_u32 m0, s16, 0xc000\n"
    "v_mfma_f32_16x16x32_bf16 v[140:143], a[32:35], a[60:63], v[140:143]\n"
    "buffer_load_dwordx4 v12, s[36:39], s63 offen lds\n"
    "v_mfma_f32_16x16x32_bf16 v[144:147], a[36:39], a[48:51], v[144:147]\n"
    "ds_read_b128 a[28:31], v196 offset:38912\n"
    "v_lshlrev_b32 v102, 16, v103\n"
    "v_mfma_f32_16x16x32_bf16 v[148:151], a[36:39], a[52:55], v[148:151]\n"
    "ds_read_b64_tr_b16 a[0:1], v198 offset:32768\n"
    "v_and_b32 v103, 0xffff0000, v103\n"
    "v_mfma_f32_16x16x32_bf16 v[152:155], a[36:39], a[56:59], v[152:155]\n"
    "ds_read_b64_tr_b16 a[2:3], v198 offset:33280\n"
    "v_permlane16_swap_b32 v116, v118\n"
    "v_mfma_f32_16x16x32_bf16 v[156:159], a[36:39], a[60:63], v[156:159]\n"
    "buffer_load_dwordx4 v13, s[36:39], s63 offen offset:1024 lds\n"
    "s_waitcnt lgkmcnt(6)\n"
    "v_mfma_f32_16x16x32_bf16 v[160:163], a[40:43], a[48:51], v[160:163]\n"
    "ds_read_b64_tr_b16 a[4:5], v198 offset:33792\n"
    "v_permlane16_swap_b32 v117, v119\n"
    "v_mfma_f32_16x16x32_bf16 v[164:167], a[40:43], a[52:55], v[164:167]\n"
    "ds_read_b64_tr_b16 a[6:7], v198 offset:34304\n"
    "v_lshlrev_b32 v112, 16, v116\n"
    "v_mfma_f32_16x16x32_bf16 v[168:171], a[40:43], a[56:59], v[168:171]\n"
    "ds_read_b64_tr_b16 a[8:9], v198 offset:34816\n"
    "v_and_b32 v113, 0xffff0000, v116\n"
    "v_mfma_f32_16x16x32_bf16 v[172:175], a[40:43], a[60:63], v[172:175]\n"
    "buffer_load_dwordx4 v14, s[36:39], s63 offen offset:2048 lds\n"
    "v_mfma_f32_16x16x32_bf16 v[176:179], a[44:47], a[48:51], v[176:179]\n"
    "ds_read_b64_tr_b16 a[10:11], v198 offset:35328\n"
    "v_lshlrev_b32 v114, 16, v117\n"
    "v_mfma_f32_16x16x32_bf16 v[180:183], a[44:47], a[52:55], v[180:183]\n"
    "ds_read_b64_tr_b16 a[12:13], v198 offset:35840\n"
    "v_and_b32 v115, 0xffff0000, v117\n"
    "v_mfma_f32_16x16x32_bf16 v[184:187], a[44:47], a[56:59], v[184:187]\n"
    "ds_read_b64_tr_b16 a[14:15], v198 offset:36352\n"
    "v_lshlrev_b32 v116, 16, v118\n"
    "v_mfma_f32_16x16x32_bf16 v[188:191], a[44:47], a[60:63], v[188:191]\n"
    "buffer_load_dwordx4 v15, s[36:39], s63 offen offset:3072 lds\n"
    "s_add_u32 s63, s63, 0x10000\n"
    "s_waitcnt lgkmcnt(4)\n"
    "v_mfma_f32_16x16x32_bf16 v[128:131], a[0:3], a[16:19], v[128:131]\n"
    "ds_read_b128 a[48:51], v197 offset:32768\n"
    "v_and_b32 v117, 0xffff0000, v118\n"
    "v_mfma_f32_16x16x32_bf16 v[132:135], a[0:3], a[20:23], v[132:135]\n"
    "ds_read_b128 a[52:55], v197 offset:34816\n"
    "v_lshlrev_b32 v118, 16, v119\n"
    "v_mfma_f32_16x16x32_bf16 v[136:139], a[0:3], a[24:27], v[136:139]\n"
    "ds_read_b128 a[56:59], v197 offset:36864\n"
    "s_add_u32 m0, s16, 0x8000\n"
    "v_mfma_f32_16x16x32_bf16 v[140:143], a[0:3], a[28:31], v[140:143]\n"
    "buffer_load_dwordx4 v4, s[44:47], s53 offen sc1 lds\n"
    "v_mfma_f32_16x16x32_bf16 v[144:147], a[4:7], a[16:19], v[144:147]\n"
    "ds_read_b128 a[60:63], v197 offset:38912\n"
    "v_and_b32 v119, 0xffff0000, v119\n"
    "v_mfma_f32_16x16x32_bf16 v[148:151], a[4:7], a[20:23], v[148:151]\n"
    "ds_read_b64_tr_b16 a[32:33], v198 offset:40960\n"
    "v_permlane16_swap_b32 v76, v78\n"
    "v_mfma_f32_16x16x32_bf16 v[152:155], a[4:7], a[24:27], v[152:155]\n"
    "ds_read_b64_tr_b16 a[34:35], v198 offset:41472\n"
    "v_permlane16_swap_b32 v77, v79\n"
    "v_mfma_f32_16x16x32_bf16 v[156:159], a[4:7], a[28:31], v[156:159]\n"
    "buffer_load_dwordx4 v5, s[44:47], s53 offen offset:1024 sc1 lds\n"
    "s_waitcnt lgkmcnt(6)\n"
    "v_mfma_f32_16x16x32_bf16 v[160:163], a[8:11], a[16:19], v[160:163]\n"
    "ds_read_b64_tr_b16 a[36:37], v198 offset:41984\n"
    "v_lshlrev_b32 v72, 16, v76\n"
    "v_mfma_f32_16x16x32_bf16 v[164:167], a[8:11], a[20:23], v[164:167]\n"
    "ds_read_b64_tr_b16 a[38:39], v198 offset:42496\n"
    "v_and_b32 v73, 0xffff0000, v76\n"
    "v_mfma_f32_16x16x32_bf16 v[168:171], a[8:11], a[24:27], v[168:171]\n"
    "ds_read_b64_tr_b16 a[40:41], v198 offset:43008\n"
    "v_lshlrev_b32 v74, 16, v77\n"
    "v_mfma_f32_16x16x32_bf16 v[172:175], a[8:11], a[28:31], v[172:175]\n"
    "buffer_load_dwordx4 v6, s[44:47], s53 offen offset:2048 sc1 lds\n"
    "v_mfma_f32_16x16x32_bf16 v[176:179], a[12:15], a[16:19], v[176:179]\n"
    "ds_read_b64_tr_b16 a[42:43], v198 offset:43520\n"
    "v_and_b32 v75, 0xffff0000, v77\n"
    "v_mfma_f32_16x16x32_bf16 v[180:183], a[12:15], a[20:23], v[180:183]\n"
    "ds_read_b64_tr_b16 a[44:45], v198 offset:44032\n"
    "v_lshlrev_b32 v76, 16, v78\n"
    "v_mfma_f32_16x16x32_bf16 v[184:187], a[12:15], a[24:27], v[184:187]\n"
    "ds_read_b64_tr_b16 a[46:47], v198 offset:44544\n"
    "v_xor_b32 v196, 0x10000, v196\n"
    "v_xor_b32 v197, 0x10000, v197\n"
    "v_xor_b32 v198, 0x10000, v198\n"
    "v_mfma_f32_16x16x32_bf16 v[188:191], a[12:15], a[28:31], v[188:191]\n"
    "buffer_load_dwordx4 v7, s[44:47], s53 offen offset:3072 sc1 lds\n"
    "s_add_u32 s53, s53, 0x2000\n"
    "s_waitcnt vmcnt(8)\n"
    "s_barrier\n"
    "s_waitcnt lgkmcnt(4)\n"
    "v_mfma_f32_16x16x32_bf16 v[128:131], a[32:35], a[48:51], v[128:131]\n"
    "ds_read_b128 a[16:19], v16\n"
    "v_and_b32 v77, 0xffff0000, v78\n"
    "v_mfma_f32_16x16x32_bf16 v[132:135], a[32:35], a[52:55], v[132:135]\n"
    "ds_read_b128 a[20:23], v16 offset:1024\n"
    "v_lshlrev_b32 v78, 16, v79\n"
    "v_mfma_f32_16x16x32_bf16 v[136:139], a[32:35], a[56:59], v[136:139]\n"
    "ds_read_b128 a[24:27], v16 offset:2048\n"
    "s_add_u32 m0, s16, 0x14000\n"
    "v_mfma_f32_16x16x32_bf16 v[140:143], a[32:35], a[60:63], v[140:143]\n"
    "buffer_load_dwordx4 v12, s[36:39], s63 offen lds\n"
    "v_mfma_f32_16x16x32_bf16 v[144:147], a[36:39], a[48:51], v[144:147]\n"
    "ds_read_b128 a[28:31], v16 offset:3072\n"
    "v_and_b32 v79, 0xffff0000, v79\n"
    "v_mfma_f32_16x16x32_bf16 v[148:151], a[36:39], a[52:55], v[148:151]\n"
    "ds_read_b64_tr_b16 a[0:1], v18\n"
    "v_permlane16_swap_b32 v92, v94\n"
    "v_mfma_f32_16x16x32_bf16 v[152:155], a[36:39], a[56:59], v[152:155]\n"
    "ds_read_b64_tr_b16 a[2:3], v18 offset:4096\n"
    "v_permlane16_swap_b32 v93, v95\n"
    "v_mfma_f32_16x16x32_bf16 v[156:159], a[36:39], a[60:63], v[156:159]\n"
    "buffer_load_dwordx4 v13, s[36:39], s63 offen offset:1024 lds\n"
    "s_waitcnt lgkmcnt(6)\n"
    "v_mfma_f32_16x16x32_bf16 v[160:163], a[40:43], a[48:51], v[160:163]\n"
    "ds_read_b64_tr_b16 a[4:5], v19\n"
    "v_lshlrev_b32 v88, 16, v92\n"
    "v_mfma_f32_16x16x32_bf16 v[164:167], a[40:43], a[52:55], v[164:167]\n"
    "ds_read_b64_tr_b16 a[6:7], v19 offset:4096\n"
    "v_and_b32 v89, 0xffff0000, v92\n"
    "v_mfma_f32_16x16x32_bf16 v[168:171], a[40:43], a[56:59], v[168:171]\n"
    "ds_read_b64_tr_b16 a[8:9], v18 offset:256\n"
    "v_lshlrev_b32 v90, 16, v93\n"
    "v_mfma_f32_16x16x32_bf16 v[172:175], a[40:43], a[60:63], v[172:175]\n"
    "buffer_load_dwordx4 v14, s[36:39], s63 offen offset:2048 lds\n"
    "v_mfma_f32_16x16x32_bf16 v[176:179], a[44:47], a[48:51], v[176:179]\n"
    "ds_read_b64_tr_b16 a[10:11], v18 offset:4352\n"
    "v_and_b32 v91, 0xffff0000, v93\n"
    "v_mfma_f32_16x16x32_bf16 v[180:183], a[44:47], a[52:55], v[180:183]\n"
    "ds_read_b64_tr_b16 a[12:13], v19 offset:256\n"
    "v_lshlrev_b32 v92, 16, v94\n"
    "v_mfma_f32_16x16x32_bf16 v[184:187], a[44:47], a[56:59], v[184:187]\n"
    "ds_read_b64_tr_b16 a[14:15], v19 offset:4352\n"
    "v_and_b32 v93, 0xffff0000, v94\n"
    "v_mfma_f32_16x16x32_bf16 v[188:191], a[44:47], a[60:63], v[188:191]\n"
    "buffer_load_dwordx4 v15, s[36:39], s63 offen offset:3072 lds\n"
    "s_add_u32 s63, s63, 0x10000\n"
    "P5S_%=:\n"
    "s_waitcnt lgkmcnt(4)\n"
    "v_mfma_f32_16x16x32_bf16 v[192:195], a[0:3], a[16:19], 0\n"
    "ds_read_b128 a[48:51], v16 offset:8192\n"
    "v_lshlrev_b32 v94, 16, v95\n"
    "v_mfma_f32_16x16x32_bf16 v[196:199], a[0:3], a[20:23], 0\n"
    "ds_read_b128 a[52:55], v16 offset:9216\n"
    "v_and_b32 v95, 0xffff0000, v95\n"
    "v_mfma_f32_16x16x32_bf16 v[200:203], a[0:3], a[24:27], 0\n"
    "ds_read_b128 a[56:59], v16 offset:10240\n"
    "s_add_u32 m0, s16, 0x10000\n"
    "v_mfma_f32_16x16x32_bf16 v[204:207], a[0:3], a[28:31], 0\n"
    "buffer_load_dwordx4 v4, s[44:47], s53 offen sc1 lds\n"
    "v_mfma_f32_16x16x32_bf16 v[208:211], a[4:7], a[16:19], 0\n"
    "ds_read_b128 a[60:63], v16 offset:11264\n"
    "v_permlane16_swap_b32 v108, v110\n"
    "v_mfma_f32_16x16x32_bf16 v[212:215], a[4:7], a[20:23], 0\n"
    "ds_read_b64_tr_b16 a[32:33], v18 offset:8192\n"
    "v_permlane16_swap_b32 v109, v111\n"
    "v_mfma_f32_16x16x32_bf16 v[216:219], a[4:7], a[24:27], 0\n"
    "ds_read_b64_tr_b16 a[34:35], v18 offset:12288\n"
    "v_lshlrev_b32 v104, 16, v108\n"
    "v_mfma_f32_16x16x32_bf16 v[220:223], a[4:7], a[28:31], 0\n"
    "buffer_load_dwordx4 v5, s[44:47], s53 offen offset:1024 sc1 lds\n"
    "s_waitcnt lgkmcnt(6)\n"
    "v_mfma_f32_16x16x32_bf16 v[224:227], a[8:11], a[16:19], 0\n"
    "ds_read_b64_tr_b16 a[36:37], v19 offset:8192\n"
    "v_and_b32 v105, 0xffff0000, v108\n"
    "v_mfma_f32_16x16x32_bf16 v[228:231], a[8:11], a[20:23], 0\n"
    "ds_read_b64_tr_b16 a[38:39], v19 offset:12288\n"
    "v_lshlrev_b32 v106, 16, v109\n"
    "v_mfma_f32_16x16x32_bf16 v[232:235], a[8:11], a[24:27], 0\n"
    "ds_read_b64_tr_b16 a[40:41], v18 offset:8448\n"
    "v_and_b32 v107, 0xffff0000, v109\n"
    "v_mfma_f32_16x16x32_bf16 v[236:239], a[8:11], a[28:31], 0\n"
    "buffer_load_dwordx4 v6, s[44:47], s53 offen offset:2048 sc1 lds\n"
    "v_mfma_f32_16x16x32_bf16 v[240:243], a[12:15], a[16:19], 0\n"
    "ds_read_b64_tr_b16 a[42:43], v18 offset:12544\n"
    "v_lshlrev_b32 v108, 16, v110\n"
    "v_mfma_f32_16x16x32_bf16 v[244:247], a[12:15], a[20:23], 0\n"
    "ds_read_b64_tr_b16 a[44:45], v19 offset:8448\n"
    "v_and_b32 v109, 0xffff0000, v110\n"
    "v_mfma_f32_16x16x32_bf16 v[248:251], a[12:15], a[24:27], 0\n"
    "ds_read_b64_tr_b16 a[46:47], v19 offset:12544\n"
    "v_lshlrev_b32 v110, 16, v111\n"
    "v_mfma_f32_16x16x32_bf16 v[252:255], a[12:15], a[28:31], 0\n"
    "buffer_load_dwordx4 v7, s[44:47], s53 offen offset:3072 sc1 lds\n"
    "s_add_u32 s53, s53, 0x2000\n"
    "s_waitcnt vmcnt(8)\n"
    "s_barrier\n"
    "s_waitcnt lgkmcnt(4)\n"
    "v_mfma_f32_16x16x32_bf16 v[192:195], a[32:35], a[48:51], v[192:195]\n"
    "ds_read_b128 a[16:19], v16 offset:32768\n"
    "v_and_b32 v111, 0xffff0000, v111\n"
    "v_mfma_f32_16x16x32_bf16 v[196:199], a[32:35], a[52:55], v[196:199]\n"
    "ds_read_b128 a[20:23], v16 offset:33792\n"
    "v_permlane16_swap_b32 v124, v126\n"
    "v_mfma_f32_16x16x32_bf16 v[200:203], a[32:35], a[56:59], v[200:203]\n"
    "ds_read_b128 a[24:27], v16 offset:34816\n"
    "s_add_u32 m0, s16, 0x1c000\n"
    "v_mfma_f32_16x16x32_bf16 v[204:207], a[32:35], a[60:63], v[204:207]\n"
    "buffer_load_dwordx4 v12, s[36:39], s63 offen lds\n"
    "v_mfma_f32_16x16x32_bf16 v[208:211], a[36:39], a[48:51], v[208:211]\n"
    "ds_read_b128 a[28:31], v16 offset:35840\n"
    "v_permlane16_swap_b32 v125, v127\n"
    "v_mfma_f32_16x16x32_bf16 v[212:215], a[36:39], a[52:55], v[212:215]\n"
    "ds_read_b64_tr_b16 a[0:1], v18 offset:32768\n"
    "v_lshlrev_b32 v120, 16, v124\n"
    "v_mfma_f32_16x16x32_bf16 v[216:219], a[36:39], a[56:59], v[216:219]\n"
    "ds_read_b64_tr_b16 a[2:3], v18 offset:36864\n"
    "v_and_b32 v121, 0xffff0000, v124\n"
    "v_mfma_f32_16x16x32_bf16 v[220:223], a[36:39], a[60:63], v[220:223]\n"
    "buffer_load_dwordx4 v13, s[36:39], s63 offen offset:1024 lds\n"
    "s_waitcnt lgkmcnt(6)\n"
    "v_mfma_f32_16x16x32_bf16 v[224:227], a[40:43], a[48:51], v[224:227]\n"
    "ds_read_b64_tr_b16 a[4:5], v19 offset:32768\n"
    "v_lshlrev_b32 v122, 16, v125\n"
    "v_mfma_f32_16x16x32_bf16 v[228:231], a[40:43], a[52:55], v[228:231]\n"
    "ds_read_b64_tr_b16 a[6:7], v19 offset:36864\n"
    "v_and_b32 v123, 0xffff0000, v125\n"
    "v_mfma_f32_16x16x32_bf16 v[232:235], a[40:43], a[56:59], v[232:235]\n"
    "ds_read_b64_tr_b16 a[8:9], v18 offset:33024\n"
    "v_lshlrev_b32 v124, 16, v126\n"
    "v_mfma_f32_16x16x32_bf16 v[236:239], a[40:43], a[60:63], v[236:239]\n"
    "buffer_load_dwordx4 v14, s[36:39], s63 offen offset:2048 lds\n"
    "v_mfma_f32_16x16x32_bf16 v[240:243], a[44:47], a[48:51], v[240:243]\n"
    "ds_read_b64_tr_b16 a[10:11], v18 offset:37120\n"
    "v_and_b32 v125, 0xffff0000, v126\n"
    "v_mfma_f32_16x16x32_bf16 v[244:247], a[44:47], a[52:55], v[244:247]\n"
    "ds_read_b64_tr_b16 a[12:13], v19 offset:33024\n"
    "v_lshlrev_b32 v126, 16, v127\n"
    "v_mfma_f32_16x16x32_bf16 v[248:251], a[44:47], a[56:59], v[248:251]\n"
    "ds_read_b64_tr_b16 a[14:15], v19 offset:37120\n"
    "v_and_b32 v127, 0xffff0000, v127\n"
    "v_mfma_f32_16x16x32_bf16 v[252:255], a[44:47], a[60:63], v[252:255]\n"
    "buffer_load_dwordx4 v15, s[36:39], s63 offen offset:3072 lds\n"
    "s_add_u32 s63, s63, 0x10000\n"
    "s_waitcnt lgkmcnt(4)\n"
    "v_mfma_f32_16x16x32_bf16 v[192:195], a[0:3], a[16:19], v[192:195]\n"
    "ds_read_b128 a[48:51], v16 offset:40960\n"
    "v_fma_f32 v128, v128, v32, v36\n"
    "v_mfma_f32_16x16x32_bf16 v[196:199], a[0:3], a[20:23], v[196:199]\n"
    "ds_read_b128 a[52:55], v16 offset:41984\n"
    "v_mul_f32 v64, v64, v128\n"
    "v_mfma_f32_16x16x32_bf16 v[200:203], a[0:3], a[24:27], v[200:203]\n"
    "ds_read_b128 a[56:59], v16 offset:43008\n"
    "s_add_u32 m0, s16, 0x18000\n"
    "v_mfma_f32_16x16x32_bf16 v[204:207], a[0:3], a[28:31], v[204:207]\n"
    "buffer_load_dwordx4 v4, s[44:47], s53 offen sc1 lds\n"
    "v_mfma_f32_16x16x32_bf16 v[208:211], a[4:7], a[16:19], v[208:211]\n"
    "ds_read_b128 a[60:63], v16 offset:44032\n"
    "v_fma_f32 v129, v129, v32, v36\n"
    "v_mfma_f32_16x16x32_bf16 v[212:215], a[4:7], a[20:23], v[212:215]\n"
    "ds_read_b64_tr_b16 a[32:33], v18 offset:40960\n"
    "v_mul_f32 v65, v65, v129\n"
    "v_mfma_f32_16x16x32_bf16 v[216:219], a[4:7], a[24:27], v[216:219]\n"
    "ds_read_b64_tr_b16 a[34:35], v18 offset:45056\n"
    "v_fma_f32 v130, v130, v32, v36\n"
    "v_mfma_f32_16x16x32_bf16 v[220:223], a[4:7], a[28:31], v[220:223]\n"
    "buffer_load_dwordx4 v5, s[44:47], s53 offen offset:1024 sc1 lds\n"
    "s_waitcnt lgkmcnt(6)\n"
    "v_mfma_f32_16x16x32_bf16 v[224:227], a[8:11], a[16:19], v[224:227]\n"
    "ds_read_b64_tr_b16 a[36:37], v19 offset:40960\n"
    "v_mul_f32 v66, v66, v130\n"
    "v_mfma_f32_16x16x32_bf16 v[228:231], a[8:11], a[20:23], v[228:231]\n"
    "ds_read_b64_tr_b16 a[38:39], v19 offset:45056\n"
    "v_fma_f32 v131, v131, v32, v36\n"
    "v_mfma_f32_16x16x32_bf16 v[232:235], a[8:11], a[24:27], v[232:235]\n"
    "ds_read_b64_tr_b16 a[40:41], v18 offset:41216\n"
    "v_mul_f32 v67, v67, v131\n"
    "v_mfma_f32_16x16x32_bf16 v[236:239], a[8:11], a[28:31], v[236:239]\n"
    "buffer_load_dwordx4 v6, s[44:47], s53 offen offset:2048 sc1 lds\n"
    "v_mfma_f32_16x16x32_bf16 v[240:243], a[12:15], a[16:19], v[240:243]\n"
    "ds_read_b64_tr_b16 a[42:43], v18 offset:45312\n"
    "v_fma_f32 v144, v144, v32, v36\n"
    "v_mfma_f32_16x16x32_bf16 v[244:247], a[12:15], a[20:23], v[244:247]\n"
    "ds_read_b64_tr_b16 a[44:45], v19 offset:41216\n"
    "v_mul_f32 v68, v68, v144\n"
    "v_mfma_f32_16x16x32_bf16 v[248:251], a[12:15], a[24:27], v[248:251]\n"
    "ds_read_b64_tr_b16 a[46:47], v19 offset:45312\n"
    "v_xor_b32 v16, 0x10000, v16\n"
    "v_xor_b32 v18, 0x10000, v18\n"
    "v_xor_b32 v19, 0x10000, v19\n"
    "v_mfma_f32_16x16x32_bf16 v[252:255], a[12:15], a[28:31], v[252:255]\n"
    "buffer_load_dwordx4 v7, s[44:47], s53 offen offset:3072 sc1 lds\n"
    "s_add_u32 s53, s53, 0x2000\n"
    "s_waitcnt vmcnt(8)\n"
    "s_barrier\n"
    "s_waitcnt lgkmcnt(4)\n"
    "v_mfma_f32_16x16x32_bf16 v[192:195], a[32:35], a[48:51], v[192:195]\n"
    "ds_read_b128 a[16:19], v16\n"
    "v_fma_f32 v145, v145, v32, v36\n"
    "v_mfma_f32_16x16x32_bf16 v[196:199], a[32:35], a[52:55], v[196:199]\n"
    "ds_read_b128 a[20:23], v16 offset:1024\n"
    "v_mul_f32 v69, v69, v145\n"
    "v_mfma_f32_16x16x32_bf16 v[200:203], a[32:35], a[56:59], v[200:203]\n"
    "ds_read_b128 a[24:27], v16 offset:2048\n"
    "s_add_u32 m0, s16, 0x4000\n"
    "v_mfma_f32_16x16x32_bf16 v[204:207], a[32:35], a[60:63], v[204:207]\n"
    "buffer_load_dwordx4 v12, s[36:39], s63 offen lds\n"
    "v_mfma_f32_16x16x32_bf16 v[208:211], a[36:39], a[48:51], v[208:211]\n"
    "ds_read_b128 a[28:31], v16 offset:3072\n"
    "v_fma_f32 v146, v146, v32, v36\n"
    "v_mfma_f32_16x16x32_bf16 v[212:215], a[36:39], a[52:55], v[212:215]\n"
    "ds_read_b64_tr_b16 a[0:1], v18\n"
    "v_mul_f32 v70, v70, v146\n"
    "v_mfma_f32_16x16x32_bf16 v[216:219], a[36:39], a[56:59], v[216:219]\n"
    "ds_read_b64_tr_b16 a[2:3], v18 offset:4096\n"
    "v_fma_f32 v147, v147, v32, v36\n"
    "v_mfma_f32_16x16x32_bf16 v[220:223], a[36:39], a[60:63], v[220:223]\n"
    "buffer_load_dwordx4 v13, s[36:39], s63 offen offset:1024 lds\n"
    "s_waitcnt lgkmcnt(6)\n"
    "v_mfma_f32_16x16x32_bf16 v[224:227], a[40:43], a[48:51], v[224:227]\n"
    "ds_read_b64_tr_b16 a[4:5], v19\n"
    "v_mul_f32 v71, v71, v147\n"
    "v_mfma_f32_16x16x32_bf16 v[228:231], a[40:43], a[52:55], v[228:231]\n"
    "ds_read_b64_tr_b16 a[6:7], v19 offset:4096\n"
    "v_cvt_pk_bf16_f32 v64, v64, v65\n"
    "v_mfma_f32_16x16x32_bf16 v[232:235], a[40:43], a[56:59], v[232:235]\n"
    "ds_read_b64_tr_b16 a[8:9], v18 offset:256\n"
    "v_cvt_pk_bf16_f32 v65, v66, v67\n"
    "v_mfma_f32_16x16x32_bf16 v[236:239], a[40:43], a[60:63], v[236:239]\n"
    "buffer_load_dwordx4 v14, s[36:39], s63 offen offset:2048 lds\n"
    "v_mfma_f32_16x16x32_bf16 v[240:243], a[44:47], a[48:51], v[240:243]\n"
    "ds_read_b64_tr_b16 a[10:11], v18 offset:4352\n"
    "v_cvt_pk_bf16_f32 v66, v68, v69\n"
    "v_mfma_f32_16x16x32_bf16 v[244:247], a[44:47], a[52:55], v[244:247]\n"
    "ds_read_b64_tr_b16 a[12:13], v19 offset:256\n"
    "v_cvt_pk_bf16_f32 v67, v70, v71\n"
    "v_mfma_f32_16x16x32_bf16 v[248:251], a[44:47], a[56:59], v[248:251]\n"
    "ds_read_b64_tr_b16 a[14:15], v19 offset:4352\n"
    "v_fma_f32 v132, v132, v33, v37\n"
    "v_mfma_f32_16x16x32_bf16 v[252:255], a[44:47], a[60:63], v[252:255]\n"
    "buffer_load_dwordx4 v15, s[36:39], s63 offen offset:3072 lds\n"
    "s_add_u32 s63, s63, 0x10000\n"
    "buffer_store_dwordx4 v[64:67], v2, s[44:47], s51 offen sc1\n"
    "s_waitcnt lgkmcnt(4)\n"
    "v_mfma_f32_16x16x32_bf16 v[192:195], a[0:3], a[16:19], v[192:195]\n"
    "ds_read_b128 a[48:51], v16 offset:8192\n"
    "v_mul_f32 v80, v80, v132\n"
    "v_mfma_f32_16x16x32_bf16 v[196:199], a[0:3], a[20:23], v[196:199]\n"
    "ds_read_b128 a[52:55], v16 offset:9216\n"
    "v_fma_f32 v133, v133, v33, v37\n"
    "v_mfma_f32_16x16x32_bf16 v[200:203], a[0:3], a[24:27], v[200:203]\n"
    "ds_read_b128 a[56:59], v16 offset:10240\n"
    "s_add_u32 m0, s16, 0\n"
    "v_mfma_f32_16x16x32_bf16 v[204:207], a[0:3], a[28:31], v[204:207]\n"
    "buffer_load_dwordx4 v4, s[44:47], s53 offen sc1 lds\n"
    "v_mfma_f32_16x16x32_bf16 v[208:211], a[4:7], a[16:19], v[208:211]\n"
    "ds_read_b128 a[60:63], v16 offset:11264\n"
    "v_mul_f32 v81, v81, v133\n"
    "v_mfma_f32_16x16x32_bf16 v[212:215], a[4:7], a[20:23], v[212:215]\n"
    "ds_read_b64_tr_b16 a[32:33], v18 offset:8192\n"
    "v_fma_f32 v134, v134, v33, v37\n"
    "v_mfma_f32_16x16x32_bf16 v[216:219], a[4:7], a[24:27], v[216:219]\n"
    "ds_read_b64_tr_b16 a[34:35], v18 offset:12288\n"
    "v_mul_f32 v82, v82, v134\n"
    "v_mfma_f32_16x16x32_bf16 v[220:223], a[4:7], a[28:31], v[220:223]\n"
    "buffer_load_dwordx4 v5, s[44:47], s53 offen offset:1024 sc1 lds\n"
    "s_waitcnt lgkmcnt(6)\n"
    "v_mfma_f32_16x16x32_bf16 v[224:227], a[8:11], a[16:19], v[224:227]\n"
    "ds_read_b64_tr_b16 a[36:37], v19 offset:8192\n"
    "v_fma_f32 v135, v135, v33, v37\n"
    "v_mfma_f32_16x16x32_bf16 v[228:231], a[8:11], a[20:23], v[228:231]\n"
    "ds_read_b64_tr_b16 a[38:39], v19 offset:12288\n"
    "v_mul_f32 v83, v83, v135\n"
    "v_mfma_f32_16x16x32_bf16 v[232:235], a[8:11], a[24:27], v[232:235]\n"
    "ds_read_b64_tr_b16 a[40:41], v18 offset:8448\n"
    "v_fma_f32 v148, v148, v33, v37\n"
    "v_mfma_f32_16x16x32_bf16 v[236:239], a[8:11], a[28:31], v[236:239]\n"
    "buffer_load_dwordx4 v6, s[44:47], s53 offen offset:2048 sc1 lds\n"
    "v_mfma_f32_16x16x32_bf16 v[240:243], a[12:15], a[16:19], v[240:243]\n"
    "ds_read_b64_tr_b16 a[42:43], v18 offset:12544\n"
    "v_mul_f32 v84, v84, v148\n"
    "v_mfma_f32_16x16x32_bf16 v[244:247], a[12:15], a[20:23], v[244:247]\n"
    "ds_read_b64_tr_b16 a[44:45], v19 offset:8448\n"
    "v_fma_f32 v149, v149, v33, v37\n"
    "v_mfma_f32_16x16x32_bf16 v[248:251], a[12:15], a[24:27], v[248:251]\n"
    "ds_read_b64_tr_b16 a[46:47], v19 offset:12544\n"
    "v_mul_f32 v85, v85, v149\n"
    "v_mfma_f32_16x16x32_bf16 v[252:255], a[12:15], a[28:31], v[252:255]\n"
    "buffer_load_dwordx4 v7, s[44:47], s53 offen offset:3072 sc1 lds\n"
    "s_add_u32 s53, s53, 0x2000\n"
    "s_waitcnt vmcnt(9)\n"
    "s_barrier\n"
    "s_waitcnt lgkmcnt(4)\n"
    "v_mfma_f32_16x16x32_bf16 v[192:195], a[32:35], a[48:51], v[192:195]\n"
    "ds_read_b128 a[16:19], v16 offset:32768\n"
    "v_fma_f32 v150, v150, v33, v37\n"
    "v_mfma_f32_16x16x32_bf16 v[196:199], a[32:35], a[52:55], v[196:199]\n"
    "ds_read_b128 a[20:23], v16 offset:33792\n"
    "v_mul_f32 v86, v86, v150\n"
    "v_mfma_f32_16x16x32_bf16 v[200:203], a[32:35], a[56:59], v[200:203]\n"
    "ds_read_b128 a[24:27], v16 offset:34816\n"
    "s_add_u32 m0, s16, 0xc000\n"
    "v_mfma_f32_16x16x32_bf16 v[204:207], a[32:35], a[60:63], v[204:207]\n"
    "buffer_load_dwordx4 v12, s[36:39], s63 offen lds\n"
    "v_mfma_f32_16x16x32_bf16 v[208:211], a[36:39], a[48:51], v[208:211]\n"
    "ds_read_b128 a[28:31], v16 offset:35840\n"
    "v_fma_f32 v151, v151, v33, v37\n"
    "v_mfma_f32_16x16x32_bf16 v[212:215], a[36:39], a[52:55], v[212:215]\n"
    "ds_read_b64_tr_b16 a[0:1], v18 offset:32768\n"
    "v_mul_f32 v87, v87, v151\n"
    "v_mfma_f32_16x16x32_bf16 v[216:219], a[36:39], a[56:59], v[216:219]\n"
    "ds_read_b64_tr_b16 a[2:3], v18 offset:36864\n"
    "v_cvt_pk_bf16_f32 v80, v80, v81\n"
    "v_mfma_f32_16x16x32_bf16 v[220:223], a[36:39], a[60:63], v[220:223]\n"
    "buffer_load_dwordx4 v13, s[36:39], s63 offen offset:1024 lds\n"
    "s_waitcnt lgkmcnt(6)\n"
    "v_mfma_f32_16x16x32_bf16 v[224:227], a[40:43], a[48:51], v[224:227]\n"
    "ds_read_b64_tr_b16 a[4:5], v19 offset:32768\n"
    "v_cvt_pk_bf16_f32 v81, v82, v83\n"
    "v_mfma_f32_16x16x32_bf16 v[228:231], a[40:43], a[52:55], v[228:231]\n"
    "ds_read_b64_tr_b16 a[6:7], v19 offset:36864\n"
    "v_cvt_pk_bf16_f32 v82, v84, v85\n"
    "v_mfma_f32_16x16x32_bf16 v[232:235], a[40:43], a[56:59], v[232:235]\n"
    "ds_read_b64_tr_b16 a[8:9], v18 offset:33024\n"
    "v_cvt_pk_bf16_f32 v83, v86, v87\n"
    "v_mfma_f32_16x16x32_bf16 v[236:239], a[40:43], a[60:63], v[236:239]\n"
    "buffer_load_dwordx4 v14, s[36:39], s63 offen offset:2048 lds\n"
    "v_mfma_f32_16x16x32_bf16 v[240:243], a[44:47], a[48:51], v[240:243]\n"
    "ds_read_b64_tr_b16 a[10:11], v18 offset:37120\n"
    "v_fma_f32 v136, v136, v34, v38\n"
    "v_mfma_f32_16x16x32_bf16 v[244:247], a[44:47], a[52:55], v[244:247]\n"
    "ds_read_b64_tr_b16 a[12:13], v19 offset:33024\n"
    "v_mul_f32 v96, v96, v136\n"
    "v_mfma_f32_16x16x32_bf16 v[248:251], a[44:47], a[56:59], v[248:251]\n"
    "ds_read_b64_tr_b16 a[14:15], v19 offset:37120\n"
    "v_fma_f32 v137, v137, v34, v38\n"
    "v_mfma_f32_16x16x32_bf16 v[252:255], a[44:47], a[60:63], v[252:255]\n"
    "buffer_load_dwordx4 v15, s[36:39], s63 offen offset:3072 lds\n"
    "s_add_u32 s63, s63, 0x10000\n"
    "buffer_store_dwordx4 v[80:83], v2, s[44:47], s51 offen offset:1024 sc1\n"
    "s_waitcnt lgkmcnt(4)\n"
    "v_mfma_f32_16x16x32_bf16 v[192:195], a[0:3], a[16:19], v[192:195]\n"
    "ds_read_b128 a[48:51], v16 offset:40960\n"
    "v_mul_f32 v97, v97, v137\n"
    "v_mfma_f32_16x16x32_bf16 v[196:199], a[0:3], a[20:23], v[196:199]\n"
    "ds_read_b128 a[52:55], v16 offset:41984\n"
    "v_fma_f32 v138, v138, v34, v38\n"
    "v_mfma_f32_16x16x32_bf16 v[200:203], a[0:3], a[24:27], v[200:203]\n"
    "ds_read_b128 a[56:59], v16 offset:43008\n"
    "s_add_u32 m0, s16, 0x8000\n"
    "v_mfma_f32_16x16x32_bf16 v[204:207], a[0:3], a[28:31], v[204:207]\n"
    "buffer_load_dwordx4 v4, s[44:47], s53 offen sc1 lds\n"
    "v_mfma_f32_16x16x32_bf16 v[208:211], a[4:7], a[16:19], v[208:211]\n"
    "ds_read_b128 a[60:63], v16 offset:44032\n"
    "v_mul_f32 v98, v98, v138\n"
    "v_mfma_f32_16x16x32_bf16 v[212:215], a[4:7], a[20:23], v[212:215]\n"
    "ds_read_b64_tr_b16 a[32:33], v18 offset:40960\n"
    "v_fma_f32 v139, v139, v34, v38\n"
    "v_mfma_f32_16x16x32_bf16 v[216:219], a[4:7], a[24:27], v[216:219]\n"
    "ds_read_b64_tr_b16 a[34:35], v18 offset:45056\n"
    "v_mul_f32 v99, v99, v139\n"
    "v_mfma_f32_16x16x32_bf16 v[220:223], a[4:7], a[28:31], v[220:223]\n"
    "buffer_load_dwordx4 v5, s[44:47], s53 offen offset:1024 sc1 lds\n"
    "s_waitcnt lgkmcnt(6)\n"
    "v_mfma_f32_16x16x32_bf16 v[224:227], a[8:11], a[16:19], v[224:227]\n"
    "ds_read_b64_tr_b16 a[36:37], v19 offset:40960\n"
    "v_fma_f32 v152, v152, v34, v38\n"
    "v_mfma_f32_16x16x32_bf16 v[228:231], a[8:11], a[20:23], v[228:231]\n"
    "ds_read_b64_tr_b16 a[38:39], v19 offset:45056\n"
    "v_mul_f32 v100, v100, v152\n"
    "v_mfma_f32_16x16x32_bf16 v[232:235], a[8:11], a[24:27], v[232:235]\n"
    "ds_read_b64_tr_b16 a[40:41], v18 offset:41216\n"
    "v_fma_f32 v153, v153, v34, v38\n"
    "v_mfma_f32_16x16x32_bf16 v[236:239], a[8:11], a[28:31], v[236:239]\n"
    "buffer_load_dwordx4 v6, s[44:47], s53 offen offset:2048 sc1 lds\n"
    "v_mfma_f32_16x16x32_bf16 v[240:243], a[12:15], a[16:19], v[240:243]\n"
    "ds_read_b64_tr_b16 a[42:43], v18 offset:45312\n"
    "v_mul_f32 v101, v101, v153\n"
    "v_mfma_f32_16x16x32_bf16 v[244:247], a[12:15], a[20:23], v[244:247]\n"
    "ds_read_b64_tr_b16 a[44:45], v19 offset:41216\n"
    "v_fma_f32 v154, v154, v34, v38\n"
    "v_mfma_f32_16x16x32_bf16 v[248:251], a[12:15], a[24:27], v[248:251]\n"
    "ds_read_b64_tr_b16 a[46:47], v19 offset:45312\n"
    "v_xor_b32 v16, 0x10000, v16\n"
    "v_xor_b32 v18, 0x10000, v18\n"
    "v_xor_b32 v19, 0x10000, v19\n"
    "v_mfma_f32_16x16x32_bf16 v[252:255], a[12:15], a[28:31], v[252:255]\n"
    "buffer_load_dwordx4 v7, s[44:47], s53 offen offset:3072 sc1 lds\n"
    "s_add_u32 s53, s53, 0x2000\n"
    "s_waitcnt vmcnt(9)\n"
    "s_barrier\n"
    "s_waitcnt lgkmcnt(4)\n"
    "v_mfma_f32_16x16x32_bf16 v[192:195], a[32:35], a[48:51], v[192:195]\n"
    "ds_read_b128 a[16:19], v16\n"
    "v_mul_f32 v102, v102, v154\n"
    "v_mfma_f32_16x16x32_bf16 v[196:199], a[32:35], a[52:55], v[196:199]\n"
    "ds_read_b128 a[20:23], v16 offset:1024\n"
    "v_fma_f32 v155, v155, v34, v38\n"
    "v_mfma_f32_16x16x32_bf16 v[200:203], a[32:35], a[56:59], v[200:203]\n"
    "ds_read_b128 a[24:27], v16 offset:2048\n"
    "s_add_u32 m0, s16, 0x14000\n"
    "v_mfma_f32_16x16x32_bf16 v[204:207], a[32:35], a[60:63], v[204:207]\n"
    "buffer_load_dwordx4 v12, s[36:39], s63 offen lds\n"
    "v_mfma_f32_16x16x32_bf16 v[208:211], a[36:39], a[48:51], v[208:211]\n"
    "ds_read_b128 a[28:31], v16 offset:3072\n"
    "v_mul_f32 v103, v103, v155\n"
    "v_mfma_f32_16x16x32_bf16 v[212:215], a[36:39], a[52:55], v[212:215]\n"
    "ds_read_b64_tr_b16 a[0:1], v18\n"
    "v_cvt_pk_bf16_f32 v96, v96, v97\n"
    "v_mfma_f32_16x16x32_bf16 v[216:219], a[36:39], a[56:59], v[216:219]\n"
    "ds_read_b64_tr_b16 a[2:3], v18 offset:4096\n"
    "v_cvt_pk_bf16_f32 v97, v98, v99\n"
    "v_mfma_f32_16x16x32_bf16 v[220:223], a[36:39], a[60:63], v[220:223]\n"
    "buffer_load_dwordx4 v13, s[36:39], s63 offen offset:1024 lds\n"
    "s_waitcnt lgkmcnt(6)\n"
    "v_mfma_f32_16x16x32_bf16 v[224:227], a[40:43], a[48:51], v[224:227]\n"
    "ds_read_b64_tr_b16 a[4:5], v19\n"
    "v_cvt_pk_bf16_f32 v98, v100, v101\n"
    "v_mfma_f32_16x16x32_bf16 v[228:231], a[40:43], a[52:55], v[228:231]\n"
    "ds_read_b64_tr_b16 a[6:7], v19 offset:4096\n"
    "v_cvt_pk_bf16_f32 v99, v102, v103\n"
    "v_mfma_f32_16x16x32_bf16 v[232:235], a[40:43], a[56:59], v[232:235]\n"
    "ds_read_b64_tr_b16 a[8:9], v18 offset:256\n"
    "v_fma_f32 v140, v140, v35, v39\n"
    "v_mfma_f32_16x16x32_bf16 v[236:239], a[40:43], a[60:63], v[236:239]\n"
    "buffer_load_dwordx4 v14, s[36:39], s63 offen offset:2048 lds\n"
    "v_mfma_f32_16x16x32_bf16 v[240:243], a[44:47], a[48:51], v[240:243]\n"
    "ds_read_b64_tr_b16 a[10:11], v18 offset:4352\n"
    "v_mul_f32 v112, v112, v140\n"
    "v_mfma_f32_16x16x32_bf16 v[244:247], a[44:47], a[52:55], v[244:247]\n"
    "ds_read_b64_tr_b16 a[12:13], v19 offset:256\n"
    "v_fma_f32 v141, v141, v35, v39\n"
    "v_mfma_f32_16x16x32_bf16 v[248:251], a[44:47], a[56:59], v[248:251]\n"
    "ds_read_b64_tr_b16 a[14:15], v19 offset:4352\n"
    "v_mul_f32 v113, v113, v141\n"
    "v_mfma_f32_16x16x32_bf16 v[252:255], a[44:47], a[60:63], v[252:255]\n"
    "buffer_load_dwordx4 v15, s[36:39], s63 offen offset:3072 lds\n"
    "s_add_u32 s63, s63, 0x10000\n"
    "buffer_store_dwordx4 v[96:99], v2, s[44:47], s51 offen offset:2048 sc1\n"
    "s_waitcnt lgkmcnt(4)\n"
    "v_mfma_f32_16x16x32_bf16 v[192:195], a[0:3], a[16:19], v[192:195]\n"
    "ds_read_b128 a[48:51], v16 offset:8192\n"
    "v_fma_f32 v142, v142, v35, v39\n"
    "v_mfma_f32_16x16x32_bf16 v[196:199], a[0:3], a[20:23], v[196:199]\n"
    "ds_read_b128 a[52:55], v16 offset:9216\n"
    "v_mul_f32 v114, v114, v142\n"
    "v_mfma_f32_16x16x32_bf16 v[200:203], a[0:3], a[24:27], v[200:203]\n"
    "ds_read_b128 a[56:59], v16 offset:10240\n"
    "s_add_u32 m0, s16, 0x10000\n"
    "v_mfma_f32_16x16x32_bf16 v[204:207], a[0:3], a[28:31], v[204:207]\n"
    "buffer_load_dwordx4 v4, s[44:47], s53 offen sc1 lds\n"
    "v_mfma_f32_16x16x32_bf16 v[208:211], a[4:7], a[16:19], v[208:211]\n"
    "ds_read_b128 a[60:63], v16 offset:11264\n"
    "v_fma_f32 v143, v143, v35, v39\n"
    "v_mfma_f32_16x16x32_bf16 v[212:215], a[4:7], a[20:23], v[212:215]\n"
    "ds_read_b64_tr_b16 a[32:33], v18 offset:8192\n"
    "v_mul_f32 v115, v115, v143\n"
    "v_mfma_f32_16x16x32_bf16 v[216:219], a[4:7], a[24:27], v[216:219]\n"
    "ds_read_b64_tr_b16 a[34:35], v18 offset:12288\n"
    "v_fma_f32 v156, v156, v35, v39\n"
    "v_mfma_f32_16x16x32_bf16 v[220:223], a[4:7], a[28:31], v[220:223]\n"
    "buffer_load_dwordx4 v5, s[44:47], s53 offen offset:1024 sc1 lds\n"
    "s_waitcnt lgkmcnt(6)\n"
    "v_mfma_f32_16x16x32_bf16 v[224:227], a[8:11], a[16:19], v[224:227]\n"
    "ds_read_b64_tr_b16 a[36:37], v19 offset:8192\n"
    "v_mul_f32 v116, v116, v156\n"
    "v_mfma_f32_16x16x32_bf16 v[228:231], a[8:11], a[20:23], v[228:231]\n"
    "ds_read_b64_tr_b16 a[38:39], v19 offset:12288\n"
    "v_fma_f32 v157, v157, v35, v39\n"
    "v_mfma_f32_16x16x32_bf16 v[232:235], a[8:11], a[24:27], v[232:235]\n"
    "ds_read_b64_tr_b16 a[40:41], v18 offset:8448\n"
    "v_mul_f32 v117, v117, v157\n"
    "v_mfma_f32_16x16x32_bf16 v[236:239], a[8:11], a[28:31], v[236:239]\n"
    "buffer_load_dwordx4 v6, s[44:47], s53 offen offset:2048 sc1 lds\n"
    "v_mfma_f32_16x16x32_bf16 v[240:243], a[12:15], a[16:19], v[240:243]\n"
    "ds_read_b64_tr_b16 a[42:43], v18 offset:12544\n"
    "v_fma_f32 v158, v158, v35, v39\n"
    "v_mfma_f32_16x16x32_bf16 v[244:247], a[12:15], a[20:23], v[244:247]\n"
    "ds_read_b64_tr_b16 a[44:45], v19 offset:8448\n"
    "v_mul_f32 v118, v118, v158\n"
    "v_mfma_f32_16x16x32_bf16 v[248:251], a[12:15], a[24:27], v[248:251]\n"
    "ds_read_b64_tr_b16 a[46:47], v19 offset:12544\n"
    "v_fma_f32 v159, v159, v35, v39\n"
    "v_mfma_f32_16x16x32_bf16 v[252:255], a[12:15], a[28:31], v[252:255]\n"
    "buffer_load_dwordx4 v7, s[44:47], s53 offen offset:3072 sc1 lds\n"
    "s_add_u32 s53, s53, 0x2000\n"
    "s_waitcnt vmcnt(9)\n"
    "s_barrier\n"
    "s_waitcnt lgkmcnt(4)\n"
    "v_mfma_f32_16x16x32_bf16 v[192:195], a[32:35], a[48:51], v[192:195]\n"
    "ds_read_b128 a[16:19], v16 offset:32768\n"
    "v_mul_f32 v119, v119, v159\n"
    "v_mfma_f32_16x16x32_bf16 v[196:199], a[32:35], a[52:55], v[196:199]\n"
    "ds_read_b128 a[20:23], v16 offset:33792\n"
    "v_cvt_pk_bf16_f32 v112, v112, v113\n"
    "v_mfma_f32_16x16x32_bf16 v[200:203], a[32:35], a[56:59], v[200:203]\n"
    "ds_read_b128 a[24:27], v16 offset:34816\n"
    "s_add_u32 m0, s16, 0x1c000\n"
    "v_mfma_f32_16x16x32_bf16 v[204:207], a[32:35], a[60:63], v[204:207]\n"
    "buffer_load_dwordx4 v12, s[36:39], s63 offen lds\n"
    "v_mfma_f32_16x16x32_bf16 v[208:211], a[36:39], a[48:51], v[208:211]\n"
    "ds_read_b128 a[28:31], v16 offset:35840\n"
    "v_cvt_pk_bf16_f32 v113, v114, v115\n"
    "v_mfma_f32_16x16x32_bf16 v[212:215], a[36:39], a[52:55], v[212:215]\n"
    "ds_read_b64_tr_b16 a[0:1], v18 offset:32768\n"
    "v_cvt_pk_bf16_f32 v114, v116, v117\n"
    "v_mfma_f32_16x16x32_bf16 v[216:219], a[36:39], a[56:59], v[216:219]\n"
    "ds_read_b64_tr_b16 a[2:3], v18 offset:36864\n"
    "v_cvt_pk_bf16_f32 v115, v118, v119\n"
    "v_mfma_f32_16x16x32_bf16 v[220:223], a[36:39], a[60:63], v[220:223]\n"
    "buffer_load_dwordx4 v13, s[36:39], s63 offen offset:1024 lds\n"
    "s_waitcnt lgkmcnt(6)\n"
    "v_mfma_f32_16x16x32_bf16 v[224:227], a[40:43], a[48:51], v[224:227]\n"
    "ds_read_b64_tr_b16 a[4:5], v19 offset:32768\n"
    "v_fma_f32 v160, v160, v32, v36\n"
    "v_mfma_f32_16x16x32_bf16 v[228:231], a[40:43], a[52:55], v[228:231]\n"
    "ds_read_b64_tr_b16 a[6:7], v19 offset:36864\n"
    "v_mul_f32 v72, v72, v160\n"
    "v_mfma_f32_16x16x32_bf16 v[232:235], a[40:43], a[56:59], v[232:235]\n"
    "ds_read_b64_tr_b16 a[8:9], v18 offset:33024\n"
    "v_fma_f32 v161, v161, v32, v36\n"
    "v_mfma_f32_16x16x32_bf16 v[236:239], a[40:43], a[60:63], v[236:239]\n"
    "buffer_load_dwordx4 v14, s[36:39], s63 offen offset:2048 lds\n"
    "v_mfma_f32_16x16x32_bf16 v[240:243], a[44:47], a[48:51], v[240:243]\n"
    "ds_read_b64_tr_b16 a[10:11], v18 offset:37120\n"
    "v_mul_f32 v73, v73, v161\n"
    "v_mfma_f32_16x16x32_bf16 v[244:247], a[44:47], a[52:55], v[244:247]\n"
    "ds_read_b64_tr_b16 a[12:13], v19 offset:33024\n"
    "v_fma_f32 v162, v162, v32, v36\n"
    "v_mfma_f32_16x16x32_bf16 v[248:251], a[44:47], a[56:59], v[248:251]\n"
    "ds_read_b64_tr_b16 a[14:15], v19 offset:37120\n"
    "v_mul_f32 v74, v74, v162\n"
    "v_mfma_f32_16x16x32_bf16 v[252:255], a[44:47], a[60:63], v[252:255]\n"
    "buffer_load_dwordx4 v15, s[36:39], s63 offen offset:3072 lds\n"
    "s_add_u32 s63, s63, 0x10000\n"
    "buffer_store_dwordx4 v[112:115], v2, s[44:47], s51 offen offset:3072 sc1\n"
    "s_waitcnt lgkmcnt(4)\n"
    "v_mfma_f32_16x16x32_bf16 v[192:195], a[0:3], a[16:19], v[192:195]\n"
    "ds_read_b128 a[48:51], v16 offset:40960\n"
    "v_fma_f32 v163, v163, v32, v36\n"
    "v_mfma_f32_16x16x32_bf16 v[196:199], a[0:3], a[20:23], v[196:199]\n"
    "ds_read_b128 a[52:55], v16 offset:41984\n"
    "v_mul_f32 v75, v75, v163\n"
    "v_mfma_f32_16x16x32_bf16 v[200:203], a[0:3], a[24:27], v[200:203]\n"
    "ds_read_b128 a[56:59], v16 offset:43008\n"
    "s_add_u32 m0, s16, 0x18000\n"
    "v_mfma_f32_16x16x32_bf16 v[204:207], a[0:3], a[28:31], v[204:207]\n"
    "buffer_load_dwordx4 v4, s[44:47], s53 offen sc1 lds\n"
    "v_mfma_f32_16x16x32_bf16 v[208:211], a[4:7], a[16:19], v[208:211]\n"
    "ds_read_b128 a[60:63], v16 offset:44032\n"
    "v_fma_f32 v176, v176, v32, v36\n"
    "v_mfma_f32_16x16x32_bf16 v[212:215], a[4:7], a[20:23], v[212:215]\n"
    "ds_read_b64_tr_b16 a[32:33], v18 offset:40960\n"
    "v_mul_f32 v76, v76, v176\n"
    "v_mfma_f32_16x16x32_bf16 v[216:219], a[4:7], a[24:27], v[216:219]\n"
    "ds_read_b64_tr_b16 a[34:35], v18 offset:45056\n"
    "v_fma_f32 v177, v177, v32, v36\n"
    "v_mfma_f32_16x16x32_bf16 v[220:223], a[4:7], a[28:31], v[220:223]\n"
    "buffer_load_dwordx4 v5, s[44:47], s53 offen offset:1024 sc1 lds\n"
    "s_waitcnt lgkmcnt(6)\n"
    "v_mfma_f32_16x16x32_bf16 v[224:227], a[8:11], a[16:19], v[224:227]\n"
    "ds_read_b64_tr_b16 a[36:37], v19 offset:40960\n"
    "v_mul_f32 v77, v77, v177\n"
    "v_mfma_f32_16x16x32_bf16 v[228:231], a[8:11], a[20:23], v[228:231]\n"
    "ds_read_b64_tr_b16 a[38:39], v19 offset:45056\n"
    "v_fma_f32 v178, v178, v32, v36\n"
    "v_mfma_f32_16x16x32_bf16 v[232:235], a[8:11], a[24:27], v[232:235]\n"
    "ds_read_b64_tr_b16 a[40:41], v18 offset:41216\n"
    "v_mul_f32 v78, v78, v178\n"
    "v_mfma_f32_16x16x32_bf16 v[236:239], a[8:11], a[28:31], v[236:239]\n"
    "buffer_load_dwordx4 v6, s[44:47], s53 offen offset:2048 sc1 lds\n"
    "v_mfma_f32_16x16x32_bf16 v[240:243], a[12:15], a[16:19], v[240:243]\n"
    "ds_read_b64_tr_b16 a[42:43], v18 offset:45312\n"
    "v_fma_f32 v179, v179, v32, v36\n"
    "v_mfma_f32_16x16x32_bf16 v[244:247], a[12:15], a[20:23], v[244:247]\n"
    "ds_read_b64_tr_b16 a[44:45], v19 offset:41216\n"
    "v_mul_f32 v79, v79, v179\n"
    "v_mfma_f32_16x16x32_bf16 v[248:251], a[12:15], a[24:27], v[248:251]\n"
    "ds_read_b64_tr_b16 a[46:47], v19 offset:45312\n"
    "v_xor_b32 v16, 0x10000, v16\n"
    "v_xor_b32 v18, 0x10000, v18\n"
    "v_xor_b32 v19, 0x10000, v19\n"
    "v_mfma_f32_16x16x32_bf16 v[252:255], a[12:15], a[28:31], v[252:255]\n"
    "buffer_load_dwordx4 v7, s[44:47], s53 offen offset:3072 sc1 lds\n"
    "s_add_u32 s53, s53, 0x2000\n"
    "s_waitcnt vmcnt(9)\n"
    "s_barrier\n"
    "s_waitcnt lgkmcnt(4)\n"
    "v_mfma_f32_16x16x32_bf16 v[192:195], a[32:35], a[48:51], v[192:195]\n"
    "ds_read_b128 a[16:19], v16\n"
    "v_cvt_pk_bf16_f32 v72, v72, v73\n"
    "v_mfma_f32_16x16x32_bf16 v[196:199], a[32:35], a[52:55], v[196:199]\n"
    "ds_read_b128 a[20:23], v16 offset:1024\n"
    "v_cvt_pk_bf16_f32 v73, v74, v75\n"
    "v_mfma_f32_16x16x32_bf16 v[200:203], a[32:35], a[56:59], v[200:203]\n"
    "ds_read_b128 a[24:27], v16 offset:2048\n"
    "s_add_u32 m0, s16, 0x4000\n"
    "v_mfma_f32_16x16x32_bf16 v[204:207], a[32:35], a[60:63], v[204:207]\n"
    "buffer_load_dwordx4 v12, s[36:39], s63 offen lds\n"
    "v_mfma_f32_16x16x32_bf16 v[208:211], a[36:39], a[48:51], v[208:211]\n"
    "ds_read_b128 a[28:31], v16 offset:3072\n"
    "v_cvt_pk_bf16_f32 v74, v76, v77\n"
    "v_mfma_f32_16x16x32_bf16 v[212:215], a[36:39], a[52:55], v[212:215]\n"
    "ds_read_b64_tr_b16 a[0:1], v18\n"
    "v_cvt_pk_bf16_f32 v75, v78, v79\n"
    "v_mfma_f32_16x16x32_bf16 v[216:219], a[36:39], a[56:59], v[216:219]\n"
    "ds_read_b64_tr_b16 a[2:3], v18 offset:4096\n"
    "v_fma_f32 v164, v164, v33, v37\n"
    "v_mfma_f32_16x16x32_bf16 v[220:223], a[36:39], a[60:63], v[220:223]\n"
    "buffer_load_dwordx4 v13, s[36:39], s63 offen offset:1024 lds\n"
    "s_waitcnt lgkmcnt(6)\n"
    "v_mfma_f32_16x16x32_bf16 v[224:227], a[40:43], a[48:51], v[224:227]\n"
    "ds_read_b64_tr_b16 a[4:5], v19\n"
    "v_mul_f32 v88, v88, v164\n"
    "v_mfma_f32_16x16x32_bf16 v[228:231], a[40:43], a[52:55], v[228:231]\n"
    "ds_read_b64_tr_b16 a[6:7], v19 offset:4096\n"
    "v_fma_f32 v165, v165, v33, v37\n"
    "v_mfma_f32_16x16x32_bf16 v[232:235], a[40:43], a[56:59], v[232:235]\n"
    "ds_read_b64_tr_b16 a[8:9], v18 offset:256\n"
    "v_mul_f32 v89, v89, v165\n"
    "v_mfma_f32_16x16x32_bf16 v[236:239], a[40:43], a[60:63], v[236:239]\n"
    "buffer_load_dwordx4 v14, s[36:39], s63 offen offset:2048 lds\n"
    "v_mfma_f32_16x16x32_bf16 v[240:243], a[44:47], a[48:51], v[240:243]\n"
    "ds_read_b64_tr_b16 a[10:11], v18 offset:4352\n"
    "v_fma_f32 v166, v166, v33, v37\n"
    "v_mfma_f32_16x16x32_bf16 v[244:247], a[44:47], a[52:55], v[244:247]\n"
    "ds_read_b64_tr_b16 a[12:13], v19 offset:256\n"
    "v_mul_f32 v90, v90, v166\n"
    "v_mfma_f32_16x16x32_bf16 v[248:251], a[44:47], a[56:59], v[248:251]\n"
    "ds_read_b64_tr_b16 a[14:15], v19 offset:4352\n"
    "v_fma_f32 v167, v167, v33, v37\n"
    "v_mfma_f32_16x16x32_bf16 v[252:255], a[44:47], a[60:63], v[252:255]\n"
    "buffer_load_dwordx4 v15, s[36:39], s63 offen offset:3072 lds\n"
    "s_add_u32 s63, s63, 0x10000\n"
    "buffer_store_dwordx4 v[72:75], v2, s[44:47], s48 offen sc1\n"
    "s_waitcnt lgkmcnt(4)\n"
    "v_mfma_f32_16x16x32_bf16 v[192:195], a[0:3], a[16:19], v[192:195]\n"
    "ds_read_b128 a[48:51], v16 offset:8192\n"
    "v_mul_f32 v91, v91, v167\n"
    "v_mfma_f32_16x16x32_bf16 v[196:199], a[0:3], a[20:23], v[196:199]\n"
    "ds_read_b128 a[52:55], v16 offset:9216\n"
    "v_fma_f32 v180, v180, v33, v37\n"
    "v_mfma_f32_16x16x32_bf16 v[200:203], a[0:3], a[24:27], v[200:203]\n"
    "ds_read_b128 a[56:59], v16 offset:10240\n"
    "s_add_u32 m0, s16, 0\n"
    "v_mfma_f32_16x16x32_bf16 v[204:207], a[0:3], a[28:31], v[204:207]\n"
    "buffer_load_dwordx4 v4, s[44:47], s53 offen sc1 lds\n"
    "v_mfma_f32_16x16x32_bf16 v[208:211], a[4:7], a[16:19], v[208:211]\n"
    "ds_read_b128 a[60:63], v16 offset:11264\n"
    "v_mul_f32 v92, v92, v180\n"
    "v_mfma_f32_16x16x32_bf16 v[212:215], a[4:7], a[20:23], v[212:215]\n"
    "ds_read_b64_tr_b16 a[32:33], v18 offset:8192\n"
    "v_fma_f32 v181, v181, v33, v37\n"
    "v_mfma_f32_16x16x32_bf16 v[216:219], a[4:7], a[24:27], v[216:219]\n"
    "ds_read_b64_tr_b16 a[34:35], v18 offset:12288\n"
    "v_mul_f32 v93, v93, v181\n"
    "v_mfma_f32_16x16x32_bf16 v[220:223], a[4:7], a[28:31], v[220:223]\n"
    "buffer_load_dwordx4 v5, s[44:47], s53 offen offset:1024 sc1 lds\n"
    "s_waitcnt lgkmcnt(6)\n"
    "v_mfma_f32_16x16x32_bf16 v[224:227], a[8:11], a[16:19], v[224:227]\n"
    "ds_read_b64_tr_b16 a[36:37], v19 offset:8192\n"
    "v_fma_f32 v182, v182, v33, v37\n"
    "v_mfma_f32_16x16x32_bf16 v[228:231], a[8:11], a[20:23], v[228:231]\n"
    "ds_read_b64_tr_b16 a[38:39], v19 offset:12288\n"
    "v_mul_f32 v94, v94, v182\n"
    "v_mfma_f32_16x16x32_bf16 v[232:235], a[8:11], a[24:27], v[232:235]\n"
    "ds_read_b64_tr_b16 a[40:41], v18 offset:8448\n"
    "v_fma_f32 v183, v183, v33, v37\n"
    "v_mfma_f32_16x16x32_bf16 v[236:239], a[8:11], a[28:31], v[236:239]\n"
    "buffer_load_dwordx4 v6, s[44:47], s53 offen offset:2048 sc1 lds\n"
    "v_mfma_f32_16x16x32_bf16 v[240:243], a[12:15], a[16:19], v[240:243]\n"
    "ds_read_b64_tr_b16 a[42:43], v18 offset:12544\n"
    "v_mul_f32 v95, v95, v183\n"
    "v_mfma_f32_16x16x32_bf16 v[244:247], a[12:15], a[20:23], v[244:247]\n"
    "ds_read_b64_tr_b16 a[44:45], v19 offset:8448\n"
    "v_cvt_pk_bf16_f32 v88, v88, v89\n"
    "v_mfma_f32_16x16x32_bf16 v[248:251], a[12:15], a[24:27], v[248:251]\n"
    "ds_read_b64_tr_b16 a[46:47], v19 offset:12544\n"
    "v_cvt_pk_bf16_f32 v89, v90, v91\n"
    "v_mfma_f32_16x16x32_bf16 v[252:255], a[12:15], a[28:31], v[252:255]\n"
    "buffer_load_dwordx4 v7, s[44:47], s53 offen offset:3072 sc1 lds\n"
    "s_add_u32 s53, s53, 0x2000\n"
    "s_waitcnt vmcnt(9)\n"
    "s_barrier\n"
    "s_waitcnt lgkmcnt(4)\n"
    "v_mfma_f32_16x16x32_bf16 v[192:195], a[32:35], a[48:51], v[192:195]\n"
    "ds_read_b128 a[16:19], v16 offset:32768\n"
    "v_cvt_pk_bf16_f32 v90, v92, v93\n"
    "v_mfma_f32_16x16x32_bf16 v[196:199], a[32:35], a[52:55], v[196:199]\n"
    "ds_read_b128 a[20:23], v16 offset:33792\n"
    "v_cvt_pk_bf16_f32 v91, v94, v95\n"
    "v_mfma_f32_16x16x32_bf16 v[200:203], a[32:35], a[56:59], v[200:203]\n"
    "ds_read_b128 a[24:27], v16 offset:34816\n"
    "s_add_u32 m0, s16, 0xc000\n"
    "v_mfma_f32_16x16x32_bf16 v[204:207], a[32:35], a[60:63], v[204:207]\n"
    "buffer_load_dwordx4 v12, s[36:39], s63 offen lds\n"
    "v_mfma_f32_16x16x32_bf16 v[208:211], a[36:39], a[48:51], v[208:211]\n"
    "ds_read_b128 a[28:31], v16 offset:35840\n"
    "v_fma_f32 v168, v168, v34, v38\n"
    "v_mfma_f32_16x16x32_bf16 v[212:215], a[36:39], a[52:55], v[212:215]\n"
    "ds_read_b64_tr_b16 a[0:1], v18 offset:32768\n"
    "v_mul_f32 v104, v104, v168\n"
    "v_mfma_f32_16x16x32_bf16 v[216:219], a[36:39], a[56:59], v[216:219]\n"
    "ds_read_b64_tr_b16 a[2:3], v18 offset:36864\n"
    "v_fma_f32 v169, v169, v34, v38\n"
    "v_mfma_f32_16x16x32_bf16 v[220:223], a[36:39], a[60:63], v[220:223]\n"
    "buffer_load_dwordx4 v13, s[36:39], s63 offen offset:1024 lds\n"
    "s_waitcnt lgkmcnt(6)\n"
    "v_mfma_f32_16x16x32_bf16 v[224:227], a[40:43], a[48:51], v[224:227]\n"
    "ds_read_b64_tr_b16 a[4:5], v19 offset:32768\n"
    "v_mul_f32 v105, v105, v169\n"
    "v_mfma_f32_16x16x32_bf16 v[228:231], a[40:43], a[52:55], v[228:231]\n"
    "ds_read_b64_tr_b16 a[6:7], v19 offset:36864\n"
    "v_fma_f32 v170, v170, v34, v38\n"
    "v_mfma_f32_16x16x32_bf16 v[232:235], a[40:43], a[56:59], v[232:235]\n"
    "ds_read_b64_tr_b16 a[8:9], v18 offset:33024\n"
    "v_mul_f32 v106, v106, v170\n"
    "v_mfma_f32_16x16x32_bf16 v[236:239], a[40:43], a[60:63], v[236:239]\n"
    "buffer_load_dwordx4 v14, s[36:39], s63 offen offset:2048 lds\n"
    "v_mfma_f32_16x16x32_bf16 v[240:243], a[44:47], a[48:51], v[240:243]\n"
    "ds_read_b64_tr_b16 a[10:11], v18 offset:37120\n"
    "v_fma_f32 v171, v171, v34, v38\n"
    "v_mfma_f32_16x16x32_bf16 v[244:247], a[44:47], a[52:55], v[244:247]\n"
    "ds_read_b64_tr_b16 a[12:13], v19 offset:33024\n"
    "v_mul_f32 v107, v107, v171\n"
    "v_mfma_f32_16x16x32_bf16 v[248:251], a[44:47], a[56:59], v[248:251]\n"
    "ds_read_b64_tr_b16 a[14:15], v19 offset:37120\n"
    "v_fma_f32 v184, v184, v34, v38\n"
    "v_mfma_f32_16x16x32_bf16 v[252:255], a[44:47], a[60:63], v[252:255]\n"
    "buffer_load_dwordx4 v15, s[36:39], s63 offen offset:3072 lds\n"
    "s_add_u32 s63, s63, 0x10000\n"
    "buffer_store_dwordx4 v[88:91], v2, s[44:47], s48 offen offset:1024 sc1\n"
    "s_waitcnt lgkmcnt(4)\n"
    "v_mfma_f32_16x16x32_bf16 v[192:195], a[0:3], a[16:19], v[192:195]\n"
    "ds_read_b128 a[48:51], v16 offset:40960\n"
    "v_mul_f32 v108, v108, v184\n"
    "v_mfma_f32_16x16x32_bf16 v[196:199], a[0:3], a[20:23], v[196:199]\n"
    "ds_read_b128 a[52:55], v16 offset:41984\n"
    "v_fma_f32 v185, v185, v34, v38\n"
    "v_mfma_f32_16x16x32_bf16 v[200:203], a[0:3], a[24:27], v[200:203]\n"
    "ds_read_b128 a[56:59], v16 offset:43008\n"
    "s_add_u32 m0, s16, 0x8000\n"
    "v_mfma_f32_16x16x32_bf16 v[204:207], a[0:3], a[28:31], v[204:207]\n"
    "buffer_load_dwordx4 v4, s[44:47], s53 offen sc1 lds\n"
    "v_mfma_f32_16x16x32_bf16 v[208:211], a[4:7], a[16:19], v[208:211]\n"
    "ds_read_b128 a[60:63], v16 offset:44032\n"
    "v_mul_f32 v109, v109, v185\n"
    "v_mfma_f32_16x16x32_bf16 v[212:215], a[4:7], a[20:23], v[212:215]\n"
    "ds_read_b64_tr_b16 a[32:33], v18 offset:40960\n"
    "v_fma_f32 v186, v186, v34, v38\n"
    "v_mfma_f32_16x16x32_bf16 v[216:219], a[4:7], a[24:27], v[216:219]\n"
    "ds_read_b64_tr_b16 a[34:35], v18 offset:45056\n"
    "v_mul_f32 v110, v110, v186\n"
    "v_mfma_f32_16x16x32_bf16 v[220:223], a[4:7], a[28:31], v[220:223]\n"
    "buffer_load_dwordx4 v5, s[44:47], s53 offen offset:1024 sc1 lds\n"
    "s_waitcnt lgkmcnt(6)\n"
    "v_mfma_f32_16x16x32_bf16 v[224:227], a[8:11], a[16:19], v[224:227]\n"
    "ds_read_b64_tr_b16 a[36:37], v19 offset:40960\n"
    "v_fma_f32 v187, v187, v34, v38\n"
    "v_mfma_f32_16x16x32_bf16 v[228:231], a[8:11], a[20:23], v[228:231]\n"
    "ds_read_b64_tr_b16 a[38:39], v19 offset:45056\n"
    "v_mul_f32 v111, v111, v187\n"
    "v_mfma_f32_16x16x32_bf16 v[232:235], a[8:11], a[24:27], v[232:235]\n"
    "ds_read_b64_tr_b16 a[40:41], v18 offset:41216\n"
    "v_cvt_pk_bf16_f32 v104, v104, v105\n"
    "v_mfma_f32_16x16x32_bf16 v[236:239], a[8:11], a[28:31], v[236:239]\n"
    "buffer_load_dwordx4 v6, s[44:47], s53 offen offset:2048 sc1 lds\n"
    "v_mfma_f32_16x16x32_bf16 v[240:243], a[12:15], a[16:19], v[240:243]\n"
    "ds_read_b64_tr_b16 a[42:43], v18 offset:45312\n"
    "v_cvt_pk_bf16_f32 v105, v106, v107\n"
    "v_mfma_f32_16x16x32_bf16 v[244:247], a[12:15], a[20:23], v[244:247]\n"
    "ds_read_b64_tr_b16 a[44:45], v19 offset:41216\n"
    "v_cvt_pk_bf16_f32 v106, v108, v109\n"
    "v_mfma_f32_16x16x32_bf16 v[248:251], a[12:15], a[24:27], v[248:251]\n"
    "ds_read_b64_tr_b16 a[46:47], v19 offset:45312\n"
    "v_xor_b32 v16, 0x10000, v16\n"
    "v_xor_b32 v18, 0x10000, v18\n"
    "v_xor_b32 v19, 0x10000, v19\n"
    "v_mfma_f32_16x16x32_bf16 v[252:255], a[12:15], a[28:31], v[252:255]\n"
    "buffer_load_dwordx4 v7, s[44:47], s53 offen offset:3072 sc1 lds\n"
    "s_add_u32 s53, s53, 0x2000\n"
    "s_waitcnt vmcnt(9)\n"
    "s_barrier\n"
    "s_waitcnt lgkmcnt(4)\n"
    "v_mfma_f32_16x16x32_bf16 v[192:195], a[32:35], a[48:51], v[192:195]\n"
    "ds_read_b128 a[16:19], v16\n"
    "v_cvt_pk_bf16_f32 v107, v110, v111\n"
    "v_mfma_f32_16x16x32_bf16 v[196:199], a[32:35], a[52:55], v[196:199]\n"
    "ds_read_b128 a[20:23], v16 offset:1024\n"
    "v_fma_f32 v172, v172, v35, v39\n"
    "v_mfma_f32_16x16x32_bf16 v[200:203], a[32:35], a[56:59], v[200:203]\n"
    "ds_read_b128 a[24:27], v16 offset:2048\n"
    "s_add_u32 m0, s16, 0x14000\n"
    "v_mfma_f32_16x16x32_bf16 v[204:207], a[32:35], a[60:63], v[204:207]\n"
    "buffer_load_dwordx4 v12, s[36:39], s63 offen lds\n"
    "v_mfma_f32_16x16x32_bf16 v[208:211], a[36:39], a[48:51], v[208:211]\n"
    "ds_read_b128 a[28:31], v16 offset:3072\n"
    "v_mul_f32 v120, v120, v172\n"
    "v_mfma_f32_16x16x32_bf16 v[212:215], a[36:39], a[52:55], v[212:215]\n"
    "ds_read_b64_tr_b16 a[0:1], v18\n"
    "v_fma_f32 v173, v173, v35, v39\n"
    "v_mfma_f32_16x16x32_bf16 v[216:219], a[36:39], a[56:59], v[216:219]\n"
    "ds_read_b64_tr_b16 a[2:3], v18 offset:4096\n"
    "v_mul_f32 v121, v121, v173\n"
    "v_mfma_f32_16x16x32_bf16 v[220:223], a[36:39], a[60:63], v[220:223]\n"
    "buffer_load_dwordx4 v13, s[36:39], s63 offen offset:1024 lds\n"
    "s_waitcnt lgkmcnt(6)\n"
    "v_mfma_f32_16x16x32_bf16 v[224:227], a[40:43], a[48:51], v[224:227]\n"
    "ds_read_b64_tr_b16 a[4:5], v19\n"
    "v_fma_f32 v174, v174, v35, v39\n"
    "v_mfma_f32_16x16x32_bf16 v[228:231], a[40:43], a[52:55], v[228:231]\n"
    "ds_read_b64_tr_b16 a[6:7], v19 offset:4096\n"
    "v_mul_f32 v122, v122, v174\n"
    "v_mfma_f32_16x16x32_bf16 v[232:235], a[40:43], a[56:59], v[232:235]\n"
    "ds_read_b64_tr_b16 a[8:9], v18 offset:256\n"
    "v_fma_f32 v175, v175, v35, v39\n"
    "v_mfma_f32_16x16x32_bf16 v[236:239], a[40:43], a[60:63], v[236:239]\n"
    "buffer_load_dwordx4 v14, s[36:39], s63 offen offset:2048 lds\n"
    "v_mfma_f32_16x16x32_bf16 v[240:243], a[44:47], a[48:51], v[240:243]\n"
    "ds_read_b64_tr_b16 a[10:11], v18 offset:4352\n"
    "v_mul_f32 v123, v123, v175\n"
    "v_mfma_f32_16x16x32_bf16 v[244:247], a[44:47], a[52:55], v[244:247]\n"
    "ds_read_b64_tr_b16 a[12:13], v19 offset:256\n"
    "v_fma_f32 v188, v188, v35, v39\n"
    "v_mfma_f32_16x16x32_bf16 v[248:251], a[44:47], a[56:59], v[248:251]\n"
    "ds_read_b64_tr_b16 a[14:15], v19 offset:4352\n"
    "v_mul_f32 v124, v124, v188\n"
    "v_mfma_f32_16x16x32_bf16 v[252:255], a[44:47], a[60:63], v[252:255]\n"
    "buffer_load_dwordx4 v15, s[36:39], s63 offen offset:3072 lds\n"
    "s_add_u32 s63, s63, 0x10000\n"
    "buffer_store_dwordx4 v[104:107], v2, s[44:47], s48 offen offset:2048 sc1\n"
    "s_waitcnt lgkmcnt(4)\n"
    "v_mfma_f32_16x16x32_bf16 v[192:195], a[0:3], a[16:19], v[192:195]\n"
    "ds_read_b128 a[48:51], v16 offset:8192\n"
    "v_fma_f32 v189, v189, v35, v39\n"
    "v_mfma_f32_16x16x32_bf16 v[196:199], a[0:3], a[20:23], v[196:199]\n"
    "ds_read_b128 a[52:55], v16 offset:9216\n"
    "v_mul_f32 v125, v125, v189\n"
    "v_mfma_f32_16x16x32_bf16 v[200:203], a[0:3], a[24:27], v[200:203]\n"
    "ds_read_b128 a[56:59], v16 offset:10240\n"
    "s_add_u32 m0, s16, 0x10000\n"
    "v_mfma_f32_16x16x32_bf16 v[204:207], a[0:3], a[28:31], v[204:207]\n"
    "buffer_load_dwordx4 v4, s[44:47], s53 offen sc1 lds\n"
    "v_mfma_f32_16x16x32_bf16 v[208:211], a[4:7], a[16:19], v[208:211]\n"
    "ds_read_b128 a[60:63], v16 offset:11264\n"
    "v_fma_f32 v190, v190, v35, v39\n"
    "v_mfma_f32_16x16x32_bf16 v[212:215], a[4:7], a[20:23], v[212:215]\n"
    "ds_read_b64_tr_b16 a[32:33], v18 offset:8192\n"
    "v_mul_f32 v126, v126, v190\n"
    "v_mfma_f32_16x16x32_bf16 v[216:219], a[4:7], a[24:27], v[216:219]\n"
    "ds_read_b64_tr_b16 a[34:35], v18 offset:12288\n"
    "v_fma_f32 v191, v191, v35, v39\n"
    "v_mfma_f32_16x16x32_bf16 v[220:223], a[4:7], a[28:31], v[220:223]\n"
    "buffer_load_dwordx4 v5, s[44:47], s53 offen offset:1024 sc1 lds\n"
    "s_waitcnt lgkmcnt(6)\n"
    "v_mfma_f32_16x16x32_bf16 v[224:227], a[8:11], a[16:19], v[224:227]\n"
    "ds_read_b64_tr_b16 a[36:37], v19 offset:8192\n"
    "v_mul_f32 v127, v127, v191\n"
    "v_mfma_f32_16x16x32_bf16 v[228:231], a[8:11], a[20:23], v[228:231]\n"
    "ds_read_b64_tr_b16 a[38:39], v19 offset:12288\n"
    "v_cvt_pk_bf16_f32 v120, v120, v121\n"
    "v_mfma_f32_16x16x32_bf16 v[232:235], a[8:11], a[24:27], v[232:235]\n"
    "ds_read_b64_tr_b16 a[40:41], v18 offset:8448\n"
    "v_cvt_pk_bf16_f32 v121, v122, v123\n"
    "v_mfma_f32_16x16x32_bf16 v[236:239], a[8:11], a[28:31], v[236:239]\n"
    "buffer_load_dwordx4 v6, s[44:47], s53 offen offset:2048 sc1 lds\n"
    "v_mfma_f32_16x16x32_bf16 v[240:243], a[12:15], a[16:19], v[240:243]\n"
    "ds_read_b64_tr_b16 a[42:43], v18 offset:12544\n"
    "v_cvt_pk_bf16_f32 v122, v124, v125\n"
    "v_mfma_f32_16x16x32_bf16 v[244:247], a[12:15], a[20:23], v[244:247]\n"
    "ds_read_b64_tr_b16 a[44:45], v19 offset:8448\n"
    "v_cvt_pk_bf16_f32 v123, v126, v127\n"
    "v_mfma_f32_16x16x32_bf16 v[248:251], a[12:15], a[24:27], v[248:251]\n"
    "ds_read_b64_tr_b16 a[46:47], v19 offset:12544\n"
    "v_mfma_f32_16x16x32_bf16 v[252:255], a[12:15], a[28:31], v[252:255]\n"
    "buffer_load_dwordx4 v7, s[44:47], s53 offen offset:3072 sc1 lds\n"
    "s_add_u32 s53, s53, 0x2000\n"
    "buffer_store_dwordx4 v[120:123], v2, s[44:47], s48 offen offset:3072 sc1\n"
    "s_waitcnt vmcnt(10)\n"
    "s_barrier\n"
    "s_waitcnt lgkmcnt(4)\n"
    "v_mfma_f32_16x16x32_bf16 v[192:195], a[32:35], a[48:51], v[192:195]\n"
    "ds_read_b128 a[16:19], v16 offset:32768\n"
    "v_mfma_f32_16x16x32_bf16 v[196:199], a[32:35], a[52:55], v[196:199]\n"
    "ds_read_b128 a[20:23], v16 offset:33792\n"
    "v_mfma_f32_16x16x32_bf16 v[200:203], a[32:35], a[56:59], v[200:203]\n"
    "ds_read_b128 a[24:27], v16 offset:34816\n"
    "s_add_u32 m0, s16, 0x1c000\n"
    "v_mfma_f32_16x16x32_bf16 v[204:207], a[32:35], a[60:63], v[204:207]\n"
    "buffer_load_dwordx4 v12, s[36:39], s63 offen lds\n"
    "v_mfma_f32_16x16x32_bf16 v[208:211], a[36:39], a[48:51], v[208:211]\n"
    "ds_read_b128 a[28:31], v16 offset:35840\n"
    "v_mfma_f32_16x16x32_bf16 v[212:215], a[36:39], a[52:55], v[212:215]\n"
    "ds_read_b64_tr_b16 a[0:1], v18 offset:32768\n"
    "v_mfma_f32_16x16x32_bf16 v[216:219], a[36:39], a[56:59], v[216:219]\n"
    "ds_read_b64_tr_b16 a[2:3], v18 offset:36864\n"
    "v_mfma_f32_16x16x32_bf16 v[220:223], a[36:39], a[60:63], v[220:223]\n"
    "buffer_load_dwordx4 v13, s[36:39], s63 offen offset:1024 lds\n"
    "s_waitcnt lgkmcnt(6)\n"
    "v_mfma_f32_16x16x32_bf16 v[224:227], a[40:43], a[48:51], v[224:227]\n"
    "ds_read_b64_tr_b16 a[4:5], v19 offset:32768\n"
    "v_mfma_f32_16x16x32_bf16 v[228:231], a[40:43], a[52:55], v[228:231]\n"
    "ds_read_b64_tr_b16 a[6:7], v19 offset:36864\n"
    "v_mfma_f32_16x16x32_bf16 v[232:235], a[40:43], a[56:59], v[232:235]\n"
    "ds_read_b64_tr_b16 a[8:9], v18 offset:33024\n"
    "v_mfma_f32_16x16x32_bf16 v[236:239], a[40:43], a[60:63], v[236:239]\n"
    "buffer_load_dwordx4 v14, s[36:39], s63 offen offset:2048 lds\n"
    "v_mfma_f32_16x16x32_bf16 v[240:243], a[44:47], a[48:51], v[240:243]\n"
    "ds_read_b64_tr_b16 a[10:11], v18 offset:37120\n"
    "v_mfma_f32_16x16x32_bf16 v[244:247], a[44:47], a[52:55], v[244:247]\n"
    "ds_read_b64_tr_b16 a[12:13], v19 offset:33024\n"
    "v_mfma_f32_16x16x32_bf16 v[248:251], a[44:47], a[56:59], v[248:251]\n"
    "ds_read_b64_tr_b16 a[14:15], v19 offset:37120\n"
    "v_mfma_f32_16x16x32_bf16 v[252:255], a[44:47], a[60:63], v[252:255]\n"
    "buffer_load_dwordx4 v15, s[36:39], s63 offen offset:3072 lds\n"
    "s_add_u32 s63, s63, 0x10000\n"
    "s_waitcnt lgkmcnt(4)\n"
    "v_mfma_f32_16x16x32_bf16 v[192:195], a[0:3], a[16:19], v[192:195]\n"
    "ds_read_b128 a[48:51], v16 offset:40960\n"
    "v_mfma_f32_16x16x32_bf16 v[196:199], a[0:3], a[20:23], v[196:199]\n"
    "ds_read_b128 a[52:55], v16 offset:41984\n"
    "v_mfma_f32_16x16x32_bf16 v[200:203], a[0:3], a[24:27], v[200:203]\n"
    "ds_read_b128 a[56:59], v16 offset:43008\n"
    "s_add_u32 m0, s16, 0x18000\n"
    "v_mfma_f32_16x16x32_bf16 v[204:207], a[0:3], a[28:31], v[204:207]\n"
    "buffer_load_dwordx4 v4, s[44:47], s53 offen sc1 lds\n"
    "v_mfma_f32_16x16x32_bf16 v[208:211], a[4:7], a[16:19], v[208:211]\n"
    "ds_read_b128 a[60:63], v16 offset:44032\n"
    "v_mfma_f32_16x16x32_bf16 v[212:215], a[4:7], a[20:23], v[212:215]\n"
    "ds_read_b64_tr_b16 a[32:33], v18 offset:40960\n"
    "v_mfma_f32_16x16x32_bf16 v[216:219], a[4:7], a[24:27], v[216:219]\n"
    "ds_read_b64_tr_b16 a[34:35], v18 offset:45056\n"
    "v_mfma_f32_16x16x32_bf16 v[220:223], a[4:7], a[28:31], v[220:223]\n"
    "buffer_load_dwordx4 v5, s[44:47], s53 offen offset:1024 sc1 lds\n"
    "s_waitcnt lgkmcnt(6)\n"
    "v_mfma_f32_16x16x32_bf16 v[224:227], a[8:11], a[16:19], v[224:227]\n"
    "ds_read_b64_tr_b16 a[36:37], v19 offset:40960\n"
    "v_mfma_f32_16x16x32_bf16 v[228:231], a[8:11], a[20:23], v[228:231]\n"
    "ds_read_b64_tr_b16 a[38:39], v19 offset:45056\n"
    "v_mfma_f32_16x16x32_bf16 v[232:235], a[8:11], a[24:27], v[232:235]\n"
    "ds_read_b64_tr_b16 a[40:41], v18 offset:41216\n"
    "v_mfma_f32_16x16x32_bf16 v[236:239], a[8:11], a[28:31], v[236:239]\n"
    "buffer_load_dwordx4 v6, s[44:47], s53 offen offset:2048 sc1 lds\n"
    "v_mfma_f32_16x16x32_bf16 v[240:243], a[12:15], a[16:19], v[240:243]\n"
    "ds_read_b64_tr_b16 a[42:43], v18 offset:45312\n"
    "v_mfma_f32_16x16x32_bf16 v[244:247], a[12:15], a[20:23], v[244:247]\n"
    "ds_read_b64_tr_b16 a[44:45], v19 offset:41216\n"
    "v_mfma_f32_16x16x32_bf16 v[248:251], a[12:15], a[24:27], v[248:251]\n"
    "ds_read_b64_tr_b16 a[46:47], v19 offset:45312\n"
    "v_xor_b32 v16, 0x10000, v16\n"
    "v_xor_b32 v18, 0x10000, v18\n"
    "v_xor_b32 v19, 0x10000, v19\n"
    "v_mfma_f32_16x16x32_bf16 v[252:255], a[12:15], a[28:31], v[252:255]\n"
    "buffer_load_dwordx4 v7, s[44:47], s53 offen offset:3072 sc1 lds\n"
    "s_add_u32 s53, s53, 0x2000\n"
    "s_waitcnt vmcnt(9)\n"
    "s_barrier\n"
    "s_waitcnt vmcnt(8)\n"
    "v_mov_b32 v24, s4\n"
    "v_mov_b32 v25, s5\n"
    "s_mov_b64 exec, 1\n"
    "buffer_store_dwordx2 v[24:25], v2, s[44:47], s54 offen sc1\n"
    "s_mov_b64 exec, -1\n"
    "s_waitcnt lgkmcnt(4)\n"
    "v_mfma_f32_16x16x32_bf16 v[192:195], a[32:35], a[48:51], v[192:195]\n"
    "ds_read_b128 a[16:19], v16\n"
    "v_mfma_f32_16x16x32_bf16 v[196:199], a[32:35], a[52:55], v[196:199]\n"
    "ds_read_b128 a[20:23], v16 offset:1024\n"
    "v_mfma_f32_16x16x32_bf16 v[200:203], a[32:35], a[56:59], v[200:203]\n"
    "ds_read_b128 a[24:27], v16 offset:2048\n"
    "s_add_u32 m0, s16, 0x4000\n"
    "v_mfma_f32_16x16x32_bf16 v[204:207], a[32:35], a[60:63], v[204:207]\n"
    "buffer_load_dwordx4 v12, s[36:39], s63 offen lds\n"
    "v_mfma_f32_16x16x32_bf16 v[208:211], a[36:39], a[48:51], v[208:211]\n"
    "ds_read_b128 a[28:31], v16 offset:3072\n"
    "v_mfma_f32_16x16x32_bf16 v[212:215], a[36:39], a[52:55], v[212:215]\n"
    "ds_read_b64_tr_b16 a[0:1], v18\n"
    "v_mfma_f32_16x16x32_bf16 v[216:219], a[36:39], a[56:59], v[216:219]\n"
    "ds_read_b64_tr_b16 a[2:3], v18 offset:4096\n"
    "v_mfma_f32_16x16x32_bf16 v[220:223], a[36:39], a[60:63], v[220:223]\n"
    "buffer_load_dwordx4 v13, s[36:39], s63 offen offset:1024 lds\n"
    "s_waitcnt lgkmcnt(6)\n"
    "v_mfma_f32_16x16x32_bf16 v[224:227], a[40:43], a[48:51], v[224:227]\n"
    "ds_read_b64_tr_b16 a[4:5], v19\n"
    "v_mfma_f32_16x16x32_bf16 v[228:231], a[40:43], a[52:55], v[228:231]\n"
    "ds_read_b64_tr_b16 a[6:7], v19 offset:4096\n"
    "v_mfma_f32_16x16x32_bf16 v[232:235], a[40:43], a[56:59], v[232:235]\n"
    "ds_read_b64_tr_b16 a[8:9], v18 offset:256\n"
    "v_mfma_f32_16x16x32_bf16 v[236:239], a[40:43], a[60:63], v[236:239]\n"
    "buffer_load_dwordx4 v14, s[36:39], s63 offen offset:2048 lds\n"
    "v_mfma_f32_16x16x32_bf16 v[240:243], a[44:47], a[48:51], v[240:243]\n"
    "ds_read_b64_tr_b16 a[10:11], v18 offset:4352\n"
    "v_mfma_f32_16x16x32_bf16 v[244:247], a[44:47], a[52:55], v[244:247]\n"
    "ds_read_b64_tr_b16 a[12:13], v19 offset:256\n"
    "v_mfma_f32_16x16x32_bf16 v[248:251], a[44:47], a[56:59], v[248:251]\n"
    "ds_read_b64_tr_b16 a[14:15], v19 offset:4352\n"
    "v_mfma_f32_16x16x32_bf16 v[252:255], a[44:47], a[60:63], v[252:255]\n"
    "buffer_load_dwordx4 v15, s[36:39], s63 offen offset:3072 lds\n"
    "s_add_u32 s63, s63, 0x10000\n"
    "s_waitcnt lgkmcnt(4)\n"
    "v_mfma_f32_16x16x32_bf16 v[192:195], a[0:3], a[16:19], v[192:195]\n"
    "ds_read_b128 a[48:51], v16 offset:8192\n"
    "v_mfma_f32_16x16x32_bf16 v[196:199], a[0:3], a[20:23], v[196:199]\n"
    "ds_read_b128 a[52:55], v16 offset:9216\n"
    "v_mfma_f32_16x16x32_bf16 v[200:203], a[0:3], a[24:27], v[200:203]\n"
    "ds_read_b128 a[56:59], v16 offset:10240\n"
    "s_add_u32 m0, s16, 0\n"
    "v_mfma_f32_16x16x32_bf16 v[204:207], a[0:3], a[28:31], v[204:207]\n"
    "buffer_load_dwordx4 v4, s[44:47], s53 offen sc1 lds\n"
    "v_mfma_f32_16x16x32_bf16 v[208:211], a[4:7], a[16:19], v[208:211]\n"
    "ds_read_b128 a[60:63], v16 offset:11264\n"
    "v_mfma_f32_16x16x32_bf16 v[212:215], a[4:7], a[20:23], v[212:215]\n"
    "ds_read_b64_tr_b16 a[32:33], v18 offset:8192\n"
    "v_mfma_f32_16x16x32_bf16 v[216:219], a[4:7], a[24:27], v[216:219]\n"
    "ds_read_b64_tr_b16 a[34:35], v18 offset:12288\n"
    "v_mfma_f32_16x16x32_bf16 v[220:223], a[4:7], a[28:31], v[220:223]\n"
    "buffer_load_dwordx4 v5, s[44:47], s53 offen offset:1024 sc1 lds\n"
    "s_waitcnt lgkmcnt(6)\n"
    "v_mfma_f32_16x16x32_bf16 v[224:227], a[8:11], a[16:19], v[224:227]\n"
    "ds_read_b64_tr_b16 a[36:37], v19 offset:8192\n"
    "v_mfma_f32_16x16x32_bf16 v[228:231], a[8:11], a[20:23], v[228:231]\n"
    "ds_read_b64_tr_b16 a[38:39], v19 offset:12288\n"
    "v_mfma_f32_16x16x32_bf16 v[232:235], a[8:11], a[24:27], v[232:235]\n"
    "ds_read_b64_tr_b16 a[40:41], v18 offset:8448\n"
    "v_mfma_f32_16x16x32_bf16 v[236:239], a[8:11], a[28:31], v[236:239]\n"
    "buffer_load_dwordx4 v6, s[44:47], s53 offen offset:2048 sc1 lds\n"
    "v_mfma_f32_16x16x32_bf16 v[240:243], a[12:15], a[16:19], v[240:243]\n"
    "ds_read_b64_tr_b16 a[42:43], v18 offset:12544\n"
    "v_mfma_f32_16x16x32_bf16 v[244:247], a[12:15], a[20:23], v[244:247]\n"
    "ds_read_b64_tr_b16 a[44:45], v19 offset:8448\n"
    "v_mfma_f32_16x16x32_bf16 v[248:251], a[12:15], a[24:27], v[248:251]\n"
    "ds_read_b64_tr_b16 a[46:47], v19 offset:12544\n"
    "v_mfma_f32_16x16x32_bf16 v[252:255], a[12:15], a[28:31], v[252:255]\n"
    "buffer_load_dwordx4 v7, s[44:47], s53 offen offset:3072 sc1 lds\n"
    "s_add_u32 s53, s53, 0x2000\n"
    "s_waitcnt vmcnt(9)\n"
    "s_barrier\n"
    "s_waitcnt lgkmcnt(4)\n"
    "v_mfma_f32_16x16x32_bf16 v[192:195], a[32:35], a[48:51], v[192:195]\n"
    "ds_read_b128 a[16:19], v16 offset:32768\n"
    "v_mfma_f32_16x16x32_bf16 v[196:199], a[32:35], a[52:55], v[196:199]\n"
    "ds_read_b128 a[20:23], v16 offset:33792\n"
    "v_mfma_f32_16x16x32_bf16 v[200:203], a[32:35], a[56:59], v[200:203]\n"
    "ds_read_b128 a[24:27], v16 offset:34816\n"
    "s_add_u32 m0, s16, 0xc000\n"
    "v_mfma_f32_16x16x32_bf16 v[204:207], a[32:35], a[60:63], v[204:207]\n"
    "buffer_load_dwordx4 v12, s[36:39], s63 offen lds\n"
    "v_mfma_f32_16x16x32_bf16 v[208:211], a[36:39], a[48:51], v[208:211]\n"
    "ds_read_b128 a[28:31], v16 offset:35840\n"
    "v_mfma_f32_16x16x32_bf16 v[212:215], a[36:39], a[52:55], v[212:215]\n"
    "ds_read_b64_tr_b16 a[0:1], v18 offset:32768\n"
    "v_mfma_f32_16x16x32_bf16 v[216:219], a[36:39], a[56:59], v[216:219]\n"
    "ds_read_b64_tr_b16 a[2:3], v18 offset:36864\n"
    "v_mfma_f32_16x16x32_bf16 v[220:223], a[36:39], a[60:63], v[220:223]\n"
    "buffer_load_dwordx4 v13, s[36:39], s63 offen offset:1024 lds\n"
    "s_waitcnt lgkmcnt(6)\n"
    "v_mfma_f32_16x16x32_bf16 v[224:227], a[40:43], a[48:51], v[224:227]\n"
    "ds_read_b64_tr_b16 a[4:5], v19 offset:32768\n"
    "v_mfma_f32_16x16x32_bf16 v[228:231], a[40:43], a[52:55], v[228:231]\n"
    "ds_read_b64_tr_b16 a[6:7], v19 offset:36864\n"
    "v_mfma_f32_16x16x32_bf16 v[232:235], a[40:43], a[56:59], v[232:235]\n"
    "ds_read_b64_tr_b16 a[8:9], v18 offset:33024\n"
    "v_mfma_f32_16x16x32_bf16 v[236:239], a[40:43], a[60:63], v[236:239]\n"
    "buffer_load_dwordx4 v14, s[36:39], s63 offen offset:2048 lds\n"
    "v_mfma_f32_16x16x32_bf16 v[240:243], a[44:47], a[48:51], v[240:243]\n"
    "ds_read_b64_tr_b16 a[10:11], v18 offset:37120\n"
    "v_mfma_f32_16x16x32_bf16 v[244:247], a[44:47], a[52:55], v[244:247]\n"
    "ds_read_b64_tr_b16 a[12:13], v19 offset:33024\n"
    "v_mfma_f32_16x16x32_bf16 v[248:251], a[44:47], a[56:59], v[248:251]\n"
    "ds_read_b64_tr_b16 a[14:15], v19 offset:37120\n"
    "v_mfma_f32_16x16x32_bf16 v[252:255], a[44:47], a[60:63], v[252:255]\n"
    "buffer_load_dwordx4 v15, s[36:39], s63 offen offset:3072 lds\n"
    "s_add_u32 s63, s63, 0x10000\n"
    "s_waitcnt lgkmcnt(4)\n"
    "v_mfma_f32_16x16x32_bf16 v[192:195], a[0:3], a[16:19], v[192:195]\n"
    "ds_read_b128 a[48:51], v16 offset:40960\n"
    "v_mfma_f32_16x16x32_bf16 v[196:199], a[0:3], a[20:23], v[196:199]\n"
    "ds_read_b128 a[52:55], v16 offset:41984\n"
    "v_mfma_f32_16x16x32_bf16 v[200:203], a[0:3], a[24:27], v[200:203]\n"
    "ds_read_b128 a[56:59], v16 offset:43008\n"
    "s_add_u32 m0, s16, 0x8000\n"
    "v_mfma_f32_16x16x32_bf16 v[204:207], a[0:3], a[28:31], v[204:207]\n"
    "buffer_load_dwordx4 v4, s[44:47], s53 offen sc1 lds\n"
    "v_mfma_f32_16x16x32_bf16 v[208:211], a[4:7], a[16:19], v[208:211]\n"
    "ds_read_b128 a[60:63], v16 offset:44032\n"
    "v_mfma_f32_16x16x32_bf16 v[212:215], a[4:7], a[20:23], v[212:215]\n"
    "ds_read_b64_tr_b16 a[32:33], v18 offset:40960\n"
    "v_mfma_f32_16x16x32_bf16 v[216:219], a[4:7], a[24:27], v[216:219]\n"
    "ds_read_b64_tr_b16 a[34:35], v18 offset:45056\n"
    "v_mfma_f32_16x16x32_bf16 v[220:223], a[4:7], a[28:31], v[220:223]\n"
    "buffer_load_dwordx4 v5, s[44:47], s53 offen offset:1024 sc1 lds\n"
    "s_waitcnt lgkmcnt(6)\n"
    "v_mfma_f32_16x16x32_bf16 v[224:227], a[8:11], a[16:19], v[224:227]\n"
    "ds_read_b64_tr_b16 a[36:37], v19 offset:40960\n"
    "v_mfma_f32_16x16x32_bf16 v[228:231], a[8:11], a[20:23], v[228:231]\n"
    "ds_read_b64_tr_b16 a[38:39], v19 offset:45056\n"
    "v_mfma_f32_16x16x32_bf16 v[232:235], a[8:11], a[24:27], v[232:235]\n"
    "ds_read_b64_tr_b16 a[40:41], v18 offset:41216\n"
    "v_mfma_f32_16x16x32_bf16 v[236:239], a[8:11], a[28:31], v[236:239]\n"
    "buffer_load_dwordx4 v6, s[44:47], s53 offen offset:2048 sc1 lds\n"
    "v_mfma_f32_16x16x32_bf16 v[240:243], a[12:15], a[16:19], v[240:243]\n"
    "ds_read_b64_tr_b16 a[42:43], v18 offset:45312\n"
    "v_mfma_f32_16x16x32_bf16 v[244:247], a[12:15], a[20:23], v[244:247]\n"
    "ds_read_b64_tr_b16 a[44:45], v19 offset:41216\n"
    "v_mfma_f32_16x16x32_bf16 v[248:251], a[12:15], a[24:27], v[248:251]\n"
    "ds_read_b64_tr_b16 a[46:47], v19 offset:45312\n"
    "v_xor_b32 v16, 0x10000, v16\n"
    "v_xor_b32 v18, 0x10000, v18\n"
    "v_xor_b32 v19, 0x10000, v19\n"
    "v_mfma_f32_16x16x32_bf16 v[252:255], a[12:15], a[28:31], v[252:255]\n"
    "buffer_load_dwordx4 v7, s[44:47], s53 offen offset:3072 sc1 lds\n"
    "s_add_u32 s53, s53, 0x2000\n"
    "s_waitcnt vmcnt(8)\n"
    "s_barrier\n"
    "s_waitcnt lgkmcnt(4)\n"
    "v_mfma_f32_16x16x32_bf16 v[192:195], a[32:35], a[48:51], v[192:195]\n"
    "ds_read_b128 a[16:19], v16\n"
    "v_mfma_f32_16x16x32_bf16 v[196:199], a[32:35], a[52:55], v[196:199]\n"
    "ds_read_b128 a[20:23], v16 offset:1024\n"
    "v_mfma_f32_16x16x32_bf16 v[200:203], a[32:35], a[56:59], v[200:203]\n"
    "ds_read_b128 a[24:27], v16 offset:2048\n"
    "s_add_u32 m0, s16, 0x14000\n"
    "v_mfma_f32_16x16x32_bf16 v[204:207], a[32:35], a[60:63], v[204:207]\n"
    "buffer_load_dwordx4 v12, s[36:39], s63 offen lds\n"
    "v_mfma_f32_16x16x32_bf16 v[208:211], a[36:39], a[48:51], v[208:211]\n"
    "ds_read_b128 a[28:31], v16 offset:3072\n"
    "v_mfma_f32_16x16x32_bf16 v[212:215], a[36:39], a[52:55], v[212:215]\n"
    "ds_read_b64_tr_b16 a[0:1], v18\n"
    "v_mfma_f32_16x16x32_bf16 v[216:219], a[36:39], a[56:59], v[216:219]\n"
    "ds_read_b64_tr_b16 a[2:3], v18 offset:4096\n"
    "v_mfma_f32_16x16x32_bf16 v[220:223], a[36:39], a[60:63], v[220:223]\n"
    "buffer_load_dwordx4 v13, s[36:39], s63 offen offset:1024 lds\n"
    "s_waitcnt lgkmcnt(6)\n"
    "v_mfma_f32_16x16x32_bf16 v[224:227], a[40:43], a[48:51], v[224:227]\n"
    "ds_read_b64_tr_b16 a[4:5], v19\n"
    "v_mfma_f32_16x16x32_bf16 v[228:231], a[40:43], a[52:55], v[228:231]\n"
    "ds_read_b64_tr_b16 a[6:7], v19 offset:4096\n"
    "v_mfma_f32_16x16x32_bf16 v[232:235], a[40:43], a[56:59], v[232:235]\n"
    "ds_read_b64_tr_b16 a[8:9], v18 offset:256\n"
    "v_mfma_f32_16x16x32_bf16 v[236:239], a[40:43], a[60:63], v[236:239]\n"
    "buffer_load_dwordx4 v14, s[36:39], s63 offen offset:2048 lds\n"
    "v_mfma_f32_16x16x32_bf16 v[240:243], a[44:47], a[48:51], v[240:243]\n"
    "ds_read_b64_tr_b16 a[10:11], v18 offset:4352\n"
    "v_mfma_f32_16x16x32_bf16 v[244:247], a[44:47], a[52:55], v[244:247]\n"
    "ds_read_b64_tr_b16 a[12:13], v19 offset:256\n"
    "v_mfma_f32_16x16x32_bf16 v[248:251], a[44:47], a[56:59], v[248:251]\n"
    "ds_read_b64_tr_b16 a[14:15], v19 offset:4352\n"
    "v_mfma_f32_16x16x32_bf16 v[252:255], a[44:47], a[60:63], v[252:255]\n"
    "buffer_load_dwordx4 v15, s[36:39], s63 offen offset:3072 lds\n"
    "s_add_u32 s63, s63, 0x10000\n"
    "s_waitcnt lgkmcnt(4)\n"
    "v_mfma_f32_16x16x32_bf16 v[192:195], a[0:3], a[16:19], v[192:195]\n"
    "ds_read_b128 a[48:51], v16 offset:8192\n"
    "v_mfma_f32_16x16x32_bf16 v[196:199], a[0:3], a[20:23], v[196:199]\n"
    "ds_read_b128 a[52:55], v16 offset:9216\n"
    "v_mfma_f32_16x16x32_bf16 v[200:203], a[0:3], a[24:27], v[200:203]\n"
    "ds_read_b128 a[56:59], v16 offset:10240\n"
    "s_add_u32 m0, s16, 0x10000\n"
    "v_mfma_f32_16x16x32_bf16 v[204:207], a[0:3], a[28:31], v[204:207]\n"
    "buffer_load_dwordx4 v4, s[44:47], s53 offen sc1 lds\n"
    "v_mfma_f32_16x16x32_bf16 v[208:211], a[4:7], a[16:19], v[208:211]\n"
    "ds_read_b128 a[60:63], v16 offset:11264\n"
    "v_mfma_f32_16x16x32_bf16 v[212:215], a[4:7], a[20:23], v[212:215]\n"
    "ds_read_b64_tr_b16 a[32:33], v18 offset:8192\n"
    "v_mfma_f32_16x16x32_bf16 v[216:219], a[4:7], a[24:27], v[216:219]\n"
    "ds_read_b64_tr_b16 a[34:35], v18 offset:12288\n"
    "v_mfma_f32_16x16x32_bf16 v[220:223], a[4:7], a[28:31], v[220:223]\n"
    "buffer_load_dwordx4 v5, s[44:47], s53 offen offset:1024 sc1 lds\n"
    "s_waitcnt lgkmcnt(6)\n"
    "v_mfma_f32_16x16x32_bf16 v[224:227], a[8:11], a[16:19], v[224:227]\n"
    "ds_read_b64_tr_b16 a[36:37], v19 offset:8192\n"
    "v_mfma_f32_16x16x32_bf16 v[228:231], a[8:11], a[20:23], v[228:231]\n"
    "ds_read_b64_tr_b16 a[38:39], v19 offset:12288\n"
    "v_mfma_f32_16x16x32_bf16 v[232:235], a[8:11], a[24:27], v[232:235]\n"
    "ds_read_b64_tr_b16 a[40:41], v18 offset:8448\n"
    "v_mfma_f32_16x16x32_bf16 v[236:239], a[8:11], a[28:31], v[236:239]\n"
    "buffer_load_dwordx4 v6, s[44:47], s53 offen offset:2048 sc1 lds\n"
    "v_mfma_f32_16x16x32_bf16 v[240:243], a[12:15], a[16:19], v[240:243]\n"
    "ds_read_b64_tr_b16 a[42:43], v18 offset:12544\n"
    "v_mfma_f32_16x16x32_bf16 v[244:247], a[12:15], a[20:23], v[244:247]\n"
    "ds_read_b64_tr_b16 a[44:45], v19 offset:8448\n"
    "v_mfma_f32_16x16x32_bf16 v[248:251], a[12:15], a[24:27], v[248:251]\n"
    "ds_read_b64_tr_b16 a[46:47], v19 offset:12544\n"
    "v_mfma_f32_16x16x32_bf16 v[252:255], a[12:15], a[28:31], v[252:255]\n"
    "buffer_load_dwordx4 v7, s[44:47], s53 offen offset:3072 sc1 lds\n"
    "s_add_u32 s53, s53, 0x2000\n"
    "s_waitcnt vmcnt(8)\n"
    "s_barrier\n"
    "v_min_u32 v10, 27, v1\n"
    "v_mul_u32_u24 v10, 8, v10\n"
    "buffer_load_dwordx2 v[8:9], v10, s[44:47], s11 offen sc1\n"
    "s_waitcnt lgkmcnt(4)\n"
    "v_mfma_f32_16x16x32_bf16 v[192:195], a[32:35], a[48:51], v[192:195]\n"
    "ds_read_b128 a[16:19], v16 offset:32768\n"
    "v_mfma_f32_16x16x32_bf16 v[196:199], a[32:35], a[52:55], v[196:199]\n"
    "ds_read_b128 a[20:23], v16 offset:33792\n"
    "v_mfma_f32_16x16x32_bf16 v[200:203], a[32:35], a[56:59], v[200:203]\n"
    "ds_read_b128 a[24:27], v16 offset:34816\n"
    "s_add_u32 m0, s16, 0x1c000\n"
    "v_mfma_f32_16x16x32_bf16 v[204:207], a[32:35], a[60:63], v[204:207]\n"
    "buffer_load_dwordx4 v12, s[36:39], s63 offen lds\n"
    "v_mfma_f32_16x16x32_bf16 v[208:211], a[36:39], a[48:51], v[208:211]\n"
    "ds_read_b128 a[28:31], v16 offset:35840\n"
    "v_mfma_f32_16x16x32_bf16 v[212:215], a[36:39], a[52:55], v[212:215]\n"
    "ds_read_b64_tr_b16 a[0:1], v18 offset:32768\n"
    "v_mfma_f32_16x16x32_bf16 v[216:219], a[36:39], a[56:59], v[216:219]\n"
    "ds_read_b64_tr_b16 a[2:3], v18 offset:36864\n"
    "v_mfma_f32_16x16x32_bf16 v[220:223], a[36:39], a[60:63], v[220:223]\n"
    "buffer_load_dwordx4 v13, s[36:39], s63 offen offset:1024 lds\n"
    "s_waitcnt lgkmcnt(6)\n"
    "v_mfma_f32_16x16x32_bf16 v[224:227], a[40:43], a[48:51], v[224:227]\n"
    "ds_read_b64_tr_b16 a[4:5], v19 offset:32768\n"
    "v_mfma_f32_16x16x32_bf16 v[228:231], a[40:43], a[52:55], v[228:231]\n"
    "ds_read_b64_tr_b16 a[6:7], v19 offset:36864\n"
    "v_mfma_f32_16x16x32_bf16 v[232:235], a[40:43], a[56:59], v[232:235]\n"
    "ds_read_b64_tr_b16 a[8:9], v18 offset:33024\n"
    "v_mfma_f32_16x16x32_bf16 v[236:239], a[40:43], a[60:63], v[236:239]\n"
    "buffer_load_dwordx4 v14, s[36:39], s63 offen offset:2048 lds\n"
    "v_mfma_f32_16x16x32_bf16 v[240:243], a[44:47], a[48:51], v[240:243]\n"
    "ds_read_b64_tr_b16 a[10:11], v18 offset:37120\n"
    "v_mfma_f32_16x16x32_bf16 v[244:247], a[44:47], a[52:55], v[244:247]\n"
    "ds_read_b64_tr_b16 a[12:13], v19 offset:33024\n"
    "v_mfma_f32_16x16x32_bf16 v[248:251], a[44:47], a[56:59], v[248:251]\n"
    "ds_read_b64_tr_b16 a[14:15], v19 offset:37120\n"
    "v_mfma_f32_16x16x32_bf16 v[252:255], a[44:47], a[60:63], v[252:255]\n"
    "buffer_load_dwordx4 v15, s[36:39], s63 offen offset:3072 lds\n"
    "s_add_u32 s63, s63, 0x10000\n"
    "s_waitcnt lgkmcnt(4)\n"
    "v_mfma_f32_16x16x32_bf16 v[192:195], a[0:3], a[16:19], v[192:195]\n"
    "ds_read_b128 a[48:51], v16 offset:40960\n"
    "v_mfma_f32_16x16x32_bf16 v[196:199], a[0:3], a[20:23], v[196:199]\n"
    "ds_read_b128 a[52:55], v16 offset:41984\n"
    "v_mfma_f32_16x16x32_bf16 v[200:203], a[0:3], a[24:27], v[200:203]\n"
    "ds_read_b128 a[56:59], v16 offset:43008\n"
    "s_add_u32 m0, s16, 0x18000\n"
    "v_mfma_f32_16x16x32_bf16 v[204:207], a[0:3], a[28:31], v[204:207]\n"
    "buffer_load_dwordx4 v4, s[44:47], s53 offen sc1 lds\n"
    "v_mfma_f32_16x16x32_bf16 v[208:211], a[4:7], a[16:19], v[208:211]\n"
    "ds_read_b128 a[60:63], v16 offset:44032\n"
    "v_mfma_f32_16x16x32_bf16 v[212:215], a[4:7], a[20:23], v[212:215]\n"
    "ds_read_b64_tr_b16 a[32:33], v18 offset:40960\n"
    "v_mfma_f32_16x16x32_bf16 v[216:219], a[4:7], a[24:27], v[216:219]\n"
    "ds_read_b64_tr_b16 a[34:35], v18 offset:45056\n"
    "v_mfma_f32_16x16x32_bf16 v[220:223], a[4:7], a[28:31], v[220:223]\n"
    "buffer_load_dwordx4 v5, s[44:47], s53 offen offset:1024 sc1 lds\n"
    "s_waitcnt lgkmcnt(6)\n"
    "v_mfma_f32_16x16x32_bf16 v[224:227], a[8:11], a[16:19], v[224:227]\n"
    "ds_read_b64_tr_b16 a[36:37], v19 offset:40960\n"
    "v_mfma_f32_16x16x32_bf16 v[228:231], a[8:11], a[20:23], v[228:231]\n"
    "ds_read_b64_tr_b16 a[38:39], v19 offset:45056\n"
    "v_mfma_f32_16x16x32_bf16 v[232:235], a[8:11], a[24:27], v[232:235]\n"
    "ds_read_b64_tr_b16 a[40:41], v18 offset:41216\n"
    "v_mfma_f32_16x16x32_bf16 v[236:239], a[8:11], a[28:31], v[236:239]\n"
    "buffer_load_dwordx4 v6, s[44:47], s53 offen offset:2048 sc1 lds\n"
    "v_mfma_f32_16x16x32_bf16 v[240:243], a[12:15], a[16:19], v[240:243]\n"
    "ds_read_b64_tr_b16 a[42:43], v18 offset:45312\n"
    "v_mfma_f32_16x16x32_bf16 v[244:247], a[12:15], a[20:23], v[244:247]\n"
    "ds_read_b64_tr_b16 a[44:45], v19 offset:41216\n"
    "v_mfma_f32_16x16x32_bf16 v[248:251], a[12:15], a[24:27], v[248:251]\n"
    "ds_read_b64_tr_b16 a[46:47], v19 offset:45312\n"
    "v_xor_b32 v16, 0x10000, v16\n"
    "v_xor_b32 v18, 0x10000, v18\n"
    "v_xor_b32 v19, 0x10000, v19\n"
    "v_mfma_f32_16x16x32_bf16 v[252:255], a[12:15], a[28:31], v[252:255]\n"
    "buffer_load_dwordx4 v7, s[44:47], s53 offen offset:3072 sc1 lds\n"
    "s_add_u32 s53, s53, 0x2000\n"
    "s_waitcnt vmcnt(9)\n"
    "s_barrier\n"
    "s_waitcnt vmcnt(8)\n"
    "v_cmp_ne_u64 vcc, s[4:5], v[8:9]\n"
    "s_nop 0\n"
    "s_cbranch_vccnz PS8_%=\n"
    "PR8_%=:\n"
    "s_waitcnt lgkmcnt(4)\n"
    "v_mfma_f32_16x16x32_bf16 v[192:195], a[32:35], a[48:51], v[192:195]\n"
    "ds_read_b128 a[16:19], v16\n"
    "v_mfma_f32_16x16x32_bf16 v[196:199], a[32:35], a[52:55], v[196:199]\n"
    "ds_read_b128 a[20:23], v16 offset:1024\n"
    "v_mfma_f32_16x16x32_bf16 v[200:203], a[32:35], a[56:59], v[200:203]\n"
    "ds_read_b128 a[24:27], v16 offset:2048\n"
    "s_add_u32 m0, s16, 0x4000\n"
    "v_mfma_f32_16x16x32_bf16 v[204:207], a[32:35], a[60:63], v[204:207]\n"
    "buffer_load_dwordx4 v12, s[40:43], s61 offen lds\n"
    "v_mfma_f32_16x16x32_bf16 v[208:211], a[36:39], a[48:51], v[208:211]\n"
    "ds_read_b128 a[28:31], v16 offset:3072\n"
    "v_mfma_f32_16x16x32_bf16 v[212:215], a[36:39], a[52:55], v[212:215]\n"
    "ds_read_b64_tr_b16 a[0:1], v18\n"
    "v_mfma_f32_16x16x32_bf16 v[216:219], a[36:39], a[56:59], v[216:219]\n"
    "ds_read_b64_tr_b16 a[2:3], v18 offset:4096\n"
    "v_mfma_f32_16x16x32_bf16 v[220:223], a[36:39], a[60:63], v[220:223]\n"
    "buffer_load_dwordx4 v13, s[40:43], s61 offen offset:1024 lds\n"
    "s_waitcnt lgkmcnt(6)\n"
    "v_mfma_f32_16x16x32_bf16 v[224:227], a[40:43], a[48:51], v[224:227]\n"
    "ds_read_b64_tr_b16 a[4:5], v19\n"
    "v_mfma_f32_16x16x32_bf16 v[228:231], a[40:43], a[52:55], v[228:231]\n"
    "ds_read_b64_tr_b16 a[6:7], v19 offset:4096\n"
    "v_mfma_f32_16x16x32_bf16 v[232:235], a[40:43], a[56:59], v[232:235]\n"
    "ds_read_b64_tr_b16 a[8:9], v18 offset:256\n"
    "v_mfma_f32_16x16x32_bf16 v[236:239], a[40:43], a[60:63], v[236:239]\n"
    "buffer_load_dwordx4 v14, s[40:43], s61 offen offset:2048 lds\n"
    "v_mfma_f32_16x16x32_bf16 v[240:243], a[44:47], a[48:51], v[240:243]\n"
    "ds_read_b64_tr_b16 a[10:11], v18 offset:4352\n"
    "v_mfma_f32_16x16x32_bf16 v[244:247], a[44:47], a[52:55], v[244:247]\n"
    "ds_read_b64_tr_b16 a[12:13], v19 offset:256\n"
    "v_mfma_f32_16x16x32_bf16 v[248:251], a[44:47], a[56:59], v[248:251]\n"
    "ds_read_b64_tr_b16 a[14:15], v19 offset:4352\n"
    "v_mfma_f32_16x16x32_bf16 v[252:255], a[44:47], a[60:63], v[252:255]\n"
    "buffer_load_dwordx4 v15, s[40:43], s61 offen offset:3072 lds\n"
    "s_add_u32 s61, s61, 0x10000\n"
    "s_waitcnt lgkmcnt(4)\n"
    "v_mfma_f32_16x16x32_bf16 v[192:195], a[0:3], a[16:19], v[192:195]\n"
    "ds_read_b128 a[48:51], v16 offset:8192\n"
    "v_mfma_f32_16x16x32_bf16 v[196:199], a[0:3], a[20:23], v[196:199]\n"
    "ds_read_b128 a[52:55], v16 offset:9216\n"
    "v_mfma_f32_16x16x32_bf16 v[200:203], a[0:3], a[24:27], v[200:203]\n"
    "ds_read_b128 a[56:59], v16 offset:10240\n"
    "s_add_u32 m0, s16, 0\n"
    "v_mfma_f32_16x16x32_bf16 v[204:207], a[0:3], a[28:31], v[204:207]\n"
    "buffer_load_dwordx4 v4, s[44:47], s68 offen sc1 lds\n"
    "v_mfma_f32_16x16x32_bf16 v[208:211], a[4:7], a[16:19], v[208:211]\n"
    "ds_read_b128 a[60:63], v16 offset:11264\n"
    "v_mfma_f32_16x16x32_bf16 v[212:215], a[4:7], a[20:23], v[212:215]\n"
    "ds_read_b64_tr_b16 a[32:33], v18 offset:8192\n"
    "v_mfma_f32_16x16x32_bf16 v[216:219], a[4:7], a[24:27], v[216:219]\n"
    "ds_read_b64_tr_b16 a[34:35], v18 offset:12288\n"
    "v_mfma_f32_16x16x32_bf16 v[220:223], a[4:7], a[28:31], v[220:223]\n"
    "buffer_load_dwordx4 v5, s[44:47], s68 offen offset:1024 sc1 lds\n"
    "s_waitcnt lgkmcnt(6)\n"
    "v_mfma_f32_16x16x32_bf16 v[224:227], a[8:11], a[16:19], v[224:227]\n"
    "ds_read_b64_tr_b16 a[36:37], v19 offset:8192\n"
    "v_mfma_f32_16x16x32_bf16 v[228:231], a[8:11], a[20:23], v[228:231]\n"
    "ds_read_b64_tr_b16 a[38:39], v19 offset:12288\n"
    "v_mfma_f32_16x16x32_bf16 v[232:235], a[8:11], a[24:27], v[232:235]\n"
    "ds_read_b64_tr_b16 a[40:41], v18 offset:8448\n"
    "v_mfma_f32_16x16x32_bf16 v[236:239], a[8:11], a[28:31], v[236:239]\n"
    "buffer_load_dwordx4 v6, s[44:47], s68 offen offset:2048 sc1 lds\n"
    "v_mfma_f32_16x16x32_bf16 v[240:243], a[12:15], a[16:19], v[240:243]\n"
    "ds_read_b64_tr_b16 a[42:43], v18 offset:12544\n"
    "v_mfma_f32_16x16x32_bf16 v[244:247], a[12:15], a[20:23], v[244:247]\n"
    "ds_read_b64_tr_b16 a[44:45], v19 offset:8448\n"
    "v_mfma_f32_16x16x32_bf16 v[248:251], a[12:15], a[24:27], v[248:251]\n"
    "ds_read_b64_tr_b16 a[46:47], v19 offset:12544\n"
    "v_mfma_f32_16x16x32_bf16 v[252:255], a[12:15], a[28:31], v[252:255]\n"
    "buffer_load_dwordx4 v7, s[44:47], s68 offen offset:3072 sc1 lds\n"
    "s_add_u32 s68, s68, 0x2000\n"
    "s_waitcnt vmcnt(8)\n"
    "s_barrier\n"
    "s_waitcnt lgkmcnt(4)\n"
    "v_mfma_f32_16x16x32_bf16 v[192:195], a[32:35], a[48:51], v[192:195]\n"
    "ds_read_b128 a[16:19], v16 offset:32768\n"
    "s_lshl_b32 s69, s9, 2\n"
    "v_mfma_f32_16x16x32_bf16 v[196:199], a[32:35], a[52:55], v[196:199]\n"
    "ds_read_b128 a[20:23], v16 offset:33792\n"
    "s_add_u32 s69, s69, s10\n"
    "v_mfma_f32_16x16x32_bf16 v[200:203], a[32:35], a[56:59], v[200:203]\n"
    "ds_read_b128 a[24:27], v16 offset:34816\n"
    "s_add_u32 m0, s16, 0xc000\n"
    "v_mfma_f32_16x16x32_bf16 v[204:207], a[32:35], a[60:63], v[204:207]\n"
    "buffer_load_dwordx4 v12, s[40:43], s61 offen lds\n"
    "v_mfma_f32_16x16x32_bf16 v[208:211], a[36:39], a[48:51], v[208:211]\n"
    "ds_read_b128 a[28:31], v16 offset:35840\n"
    "s_lshl_b32 s70, s69, 4\n"
    "v_mfma_f32_16x16x32_bf16 v[212:215], a[36:39], a[52:55], v[212:215]\n"
    "ds_read_b64_tr_b16 a[0:1], v18 offset:32768\n"
    "s_add_u32 s70, s70, s19\n"
    "v_mfma_f32_16x16x32_bf16 v[216:219], a[36:39], a[56:59], v[216:219]\n"
    "ds_read_b64_tr_b16 a[2:3], v18 offset:36864\n"
    "s_lshl_b32 s69, s70, 3\n"
    "v_mfma_f32_16x16x32_bf16 v[220:223], a[36:39], a[60:63], v[220:223]\n"
    "buffer_load_dwordx4 v13, s[40:43], s61 offen offset:1024 lds\n"
    "s_waitcnt lgkmcnt(6)\n"
    "v_mfma_f32_16x16x32_bf16 v[224:227], a[40:43], a[48:51], v[224:227]\n"
    "ds_read_b64_tr_b16 a[4:5], v19 offset:32768\n"
    "s_add_u32 s69, s69, 0xc000\n"
    "v_mfma_f32_16x16x32_bf16 v[228:231], a[40:43], a[52:55], v[228:231]\n"
    "ds_read_b64_tr_b16 a[6:7], v19 offset:36864\n"
    "s_lshl_b32 s62, s8, 5\n"
    "v_mfma_f32_16x16x32_bf16 v[232:235], a[40:43], a[56:59], v[232:235]\n"
    "ds_read_b64_tr_b16 a[8:9], v18 offset:33024\n"
    "s_add_u32 s62, s62, s69\n"
    "v_mfma_f32_16x16x32_bf16 v[236:239], a[40:43], a[60:63], v[236:239]\n"
    "buffer_load_dwordx4 v14, s[40:43], s61 offen offset:2048 lds\n"
    "v_mfma_f32_16x16x32_bf16 v[240:243], a[44:47], a[48:51], v[240:243]\n"
    "ds_read_b64_tr_b16 a[10:11], v18 offset:37120\n"
    "s_mul_i32 s70, s70, 0x1800\n"
    "v_mfma_f32_16x16x32_bf16 v[244:247], a[44:47], a[52:55], v[244:247]\n"
    "ds_read_b64_tr_b16 a[12:13], v19 offset:33024\n"
    "s_add_u32 s70, s70, 0x5400000\n"
    "v_mfma_f32_16x16x32_bf16 v[248:251], a[44:47], a[56:59], v[248:251]\n"
    "ds_read_b64_tr_b16 a[14:15], v19 offset:37120\n"
    "s_mul_i32 s60, s8, 0x6000\n"
    "v_mfma_f32_16x16x32_bf16 v[252:255], a[44:47], a[60:63], v[252:255]\n"
    "buffer_load_dwordx4 v15, s[40:43], s61 offen offset:3072 lds\n"
    "s_add_u32 s61, s61, 0x10000\n"
    "s_waitcnt lgkmcnt(4)\n"
    "v_mfma_f32_16x16x32_bf16 v[192:195], a[0:3], a[16:19], v[192:195]\n"
    "ds_read_b128 a[48:51], v16 offset:40960\n"
    "s_add_u32 s60, s60, s70\n"
    "v_mfma_f32_16x16x32_bf16 v[196:199], a[0:3], a[20:23], v[196:199]\n"
    "ds_read_b128 a[52:55], v16 offset:41984\n"
    "v_and_b32 v20, 15, v1\n"
    "v_mfma_f32_16x16x32_bf16 v[200:203], a[0:3], a[24:27], v[200:203]\n"
    "ds_read_b128 a[56:59], v16 offset:43008\n"
    "s_add_u32 m0, s16, 0x8000\n"
    "v_mfma_f32_16x16x32_bf16 v[204:207], a[0:3], a[28:31], v[204:207]\n"
    "buffer_load_dwordx4 v4, s[44:47], s68 offen sc1 lds\n"
    "v_mfma_f32_16x16x32_bf16 v[208:211], a[4:7], a[16:19], v[208:211]\n"
    "ds_read_b128 a[60:63], v16 offset:44032\n"
    "v_lshlrev_b32 v20, 10, v20\n"
    "v_mfma_f32_16x16x32_bf16 v[212:215], a[4:7], a[20:23], v[212:215]\n"
    "ds_read_b64_tr_b16 a[32:33], v18 offset:40960\n"
    "v_bfe_u32 v21, v1, 4, 1\n"
    "v_mfma_f32_16x16x32_bf16 v[216:219], a[4:7], a[24:27], v[216:219]\n"
    "ds_read_b64_tr_b16 a[34:35], v18 offset:45056\n"
    "v_lshl_add_u32 v20, v21, 5, v20\n"
    "v_mfma_f32_16x16x32_bf16 v[220:223], a[4:7], a[28:31], v[220:223]\n"
    "buffer_load_dwordx4 v5, s[44:47], s68 offen offset:1024 sc1 lds\n"
    "s_waitcnt lgkmcnt(6)\n"
    "v_mfma_f32_16x16x32_bf16 v[224:227], a[8:11], a[16:19], v[224:227]\n"
    "ds_read_b64_tr_b16 a[36:37], v19 offset:40960\n"
    "v_lshrrev_b32 v21, 5, v1\n"
    "v_mfma_f32_16x16x32_bf16 v[228:231], a[8:11], a[20:23], v[228:231]\n"
    "ds_read_b64_tr_b16 a[38:39], v19 offset:45056\n"
    "v_lshl_add_u32 v20, v21, 4, v20\n"
    "v_mfma_f32_16x16x32_bf16 v[232:235], a[8:11], a[24:27], v[232:235]\n"
    "ds_read_b64_tr_b16 a[40:41], v18 offset:41216\n"
    "s_lshl_b32 s52, s9, 3\n"
    "v_mfma_f32_16x16x32_bf16 v[236:239], a[8:11], a[28:31], v[236:239]\n"
    "buffer_load_dwordx4 v6, s[44:47], s68 offen offset:2048 sc1 lds\n"
    "v_mfma_f32_16x16x32_bf16 v[240:243], a[12:15], a[16:19], v[240:243]\n"
    "ds_read_b64_tr_b16 a[42:43], v18 offset:45312\n"
    "s_and_b32 s22, s19, 1\n"
    "v_mfma_f32_16x16x32_bf16 v[244:247], a[12:15], a[20:23], v[244:247]\n"
    "ds_read_b64_tr_b16 a[44:45], v19 offset:41216\n"
    "s_lshl_b32 s22, s22, 2\n"
    "v_mfma_f32_16x16x32_bf16 v[248:251], a[12:15], a[24:27], v[248:251]\n"
    "ds_read_b64_tr_b16 a[46:47], v19 offset:45312\n"
    "v_xor_b32 v16, 0x10000, v16\n"
    "v_xor_b32 v18, 0x10000, v18\n"
    "v_xor_b32 v19, 0x10000, v19\n"
    "v_mfma_f32_16x16x32_bf16 v[252:255], a[12:15], a[28:31], v[252:255]\n"
    "buffer_load_dwordx4 v7, s[44:47], s68 offen offset:3072 sc1 lds\n"
    "s_add_u32 s68, s68, 0x2000\n"
    "s_waitcnt vmcnt(8)\n"
    "s_barrier\n"
    "s_waitcnt lgkmcnt(4)\n"
    "v_mfma_f32_16x16x32_bf16 v[192:195], a[32:35], a[48:51], v[192:195]\n"
    "ds_read_b128 a[16:19], v16\n"
    "s_add_u32 s52, s52, s22\n"
    "v_mfma_f32_16x16x32_bf16 v[196:199], a[32:35], a[52:55], v[196:199]\n"
    "ds_read_b128 a[20:23], v16 offset:1024\n"
    "s_add_u32 s52, s52, s8\n"
    "v_mfma_f32_16x16x32_bf16 v[200:203], a[32:35], a[56:59], v[200:203]\n"
    "ds_read_b128 a[24:27], v16 offset:2048\n"
    "s_add_u32 m0, s16, 0x14000\n"
    "v_mfma_f32_16x16x32_bf16 v[204:207], a[32:35], a[60:63], v[204:207]\n"
    "buffer_load_dwordx4 v12, s[40:43], s61 offen lds\n"
    "v_mfma_f32_16x16x32_bf16 v[208:211], a[36:39], a[48:51], v[208:211]\n"
    "ds_read_b128 a[28:31], v16 offset:3072\n"
    "s_lshl_b32 s52, s52, 14\n"
    "v_mfma_f32_16x16x32_bf16 v[212:215], a[36:39], a[52:55], v[212:215]\n"
    "ds_read_b64_tr_b16 a[0:1], v18\n"
    "s_lshl_b32 s22, s10, 8\n"
    "v_mfma_f32_16x16x32_bf16 v[216:219], a[36:39], a[56:59], v[216:219]\n"
    "ds_read_b64_tr_b16 a[2:3], v18 offset:4096\n"
    "s_add_u32 s52, s52, s22\n"
    "v_mfma_f32_16x16x32_bf16 v[220:223], a[36:39], a[60:63], v[220:223]\n"
    "buffer_load_dwordx4 v13, s[40:43], s61 offen offset:1024 lds\n"
    "s_waitcnt lgkmcnt(6)\n"
    "v_mfma_f32_16x16x32_bf16 v[224:227], a[40:43], a[48:51], v[224:227]\n"
    "ds_read_b64_tr_b16 a[4:5], v19\n"
    "s_lshr_b32 s22, s19, 1\n"
    "v_mfma_f32_16x16x32_bf16 v[228:231], a[40:43], a[52:55], v[228:231]\n"
    "ds_read_b64_tr_b16 a[6:7], v19 offset:4096\n"
    "s_lshl_b32 s22, s22, 7\n"
    "v_mfma_f32_16x16x32_bf16 v[232:235], a[40:43], a[56:59], v[232:235]\n"
    "ds_read_b64_tr_b16 a[8:9], v18 offset:256\n"
    "s_add_u32 s52, s52, s22\n"
    "v_mfma_f32_16x16x32_bf16 v[236:239], a[40:43], a[60:63], v[236:239]\n"
    "buffer_load_dwordx4 v14, s[40:43], s61 offen offset:2048 lds\n"
    "v_mfma_f32_16x16x32_bf16 v[240:243], a[44:47], a[48:51], v[240:243]\n"
    "ds_read_b64_tr_b16 a[10:11], v18 offset:4352\n"
    "v_min_u32 v10, 2, v1\n"
    "v_mfma_f32_16x16x32_bf16 v[244:247], a[44:47], a[52:55], v[244:247]\n"
    "ds_read_b64_tr_b16 a[12:13], v19 offset:256\n"
    "v_add_u32 v10, 1, v10\n"
    "v_mfma_f32_16x16x32_bf16 v[248:251], a[44:47], a[56:59], v[248:251]\n"
    "ds_read_b64_tr_b16 a[14:15], v19 offset:4352\n"
    "v_xor_b32 v10, s8, v10\n"
    "v_mfma_f32_16x16x32_bf16 v[252:255], a[44:47], a[60:63], v[252:255]\n"
    "buffer_load_dwordx4 v15, s[40:43], s61 offen offset:3072 lds\n"
    "s_add_u32 s61, s61, 0x10000\n"
    "s_waitcnt lgkmcnt(4)\n"
    "v_mfma_f32_16x16x32_bf16 v[192:195], a[0:3], a[16:19], v[192:195]\n"
    "ds_read_b128 a[48:51], v16 offset:8192\n"
    "v_lshlrev_b32 v10, 5, v10\n"
    "v_mfma_f32_16x16x32_bf16 v[196:199], a[0:3], a[20:23], v[196:199]\n"
    "ds_read_b128 a[52:55], v16 offset:9216\n"
    "s_xor_b32 s56, s8, 1\n"
    "v_mfma_f32_16x16x32_bf16 v[200:203], a[0:3], a[24:27], v[200:203]\n"
    "ds_read_b128 a[56:59], v16 offset:10240\n"
    "s_add_u32 m0, s16, 0x10000\n"
    "v_mfma_f32_16x16x32_bf16 v[204:207], a[0:3], a[28:31], v[204:207]\n"
    "buffer_load_dwordx4 v4, s[44:47], s68 offen sc1 lds\n"
    "v_mfma_f32_16x16x32_bf16 v[208:211], a[4:7], a[16:19], v[208:211]\n"
    "ds_read_b128 a[60:63], v16 offset:11264\n"
    "s_mul_i32 s56, s56, 0x6000\n"
    "v_mfma_f32_16x16x32_bf16 v[212:215], a[4:7], a[20:23], v[212:215]\n"
    "ds_read_b64_tr_b16 a[32:33], v18 offset:8192\n"
    "s_add_u32 s56, s56, s70\n"
    "v_mfma_f32_16x16x32_bf16 v[216:219], a[4:7], a[24:27], v[216:219]\n"
    "ds_read_b64_tr_b16 a[34:35], v18 offset:12288\n"
    "s_add_u32 s56, s56, 0\n"
    "v_mfma_f32_16x16x32_bf16 v[220:223], a[4:7], a[28:31], v[220:223]\n"
    "buffer_load_dwordx4 v5, s[44:47], s68 offen offset:1024 sc1 lds\n"
    "s_waitcnt lgkmcnt(6)\n"
    "v_mfma_f32_16x16x32_bf16 v[224:227], a[8:11], a[16:19], v[224:227]\n"
    "ds_read_b64_tr_b16 a[36:37], v19 offset:8192\n"
    "s_xor_b32 s57, s8, 2\n"
    "v_mfma_f32_16x16x32_bf16 v[228:231], a[8:11], a[20:23], v[228:231]\n"
    "ds_read_b64_tr_b16 a[38:39], v19 offset:12288\n"
    "s_mul_i32 s57, s57, 0x6000\n"
    "v_mfma_f32_16x16x32_bf16 v[232:235], a[8:11], a[24:27], v[232:235]\n"
    "ds_read_b64_tr_b16 a[40:41], v18 offset:8448\n"
    "s_add_u32 s57, s57, s70\n"
    "v_mfma_f32_16x16x32_bf16 v[236:239], a[8:11], a[28:31], v[236:239]\n"
    "buffer_load_dwordx4 v6, s[44:47], s68 offen offset:2048 sc1 lds\n"
    "v_mfma_f32_16x16x32_bf16 v[240:243], a[12:15], a[16:19], v[240:243]\n"
    "ds_read_b64_tr_b16 a[42:43], v18 offset:12544\n"
    "s_add_u32 s57, s57, 0x800\n"
    "v_mfma_f32_16x16x32_bf16 v[244:247], a[12:15], a[20:23], v[244:247]\n"
    "ds_read_b64_tr_b16 a[44:45], v19 offset:8448\n"
    "s_xor_b32 s58, s8, 3\n"
    "v_mfma_f32_16x16x32_bf16 v[248:251], a[12:15], a[24:27], v[248:251]\n"
    "ds_read_b64_tr_b16 a[46:47], v19 offset:12544\n"
    "s_mul_i32 s58, s58, 0x6000\n"
    "v_mfma_f32_16x16x32_bf16 v[252:255], a[12:15], a[28:31], v[252:255]\n"
    "buffer_load_dwordx4 v7, s[44:47], s68 offen offset:3072 sc1 lds\n"
    "s_add_u32 s68, s68, 0x2000\n"
    "s_waitcnt vmcnt(8)\n"
    "s_barrier\n"
    "s_waitcnt lgkmcnt(4)\n"
    "v_mfma_f32_16x16x32_bf16 v[192:195], a[32:35], a[48:51], v[192:195]\n"
    "ds_read_b128 a[16:19], v16 offset:32768\n"
    "s_add_u32 s58, s58, s70\n"
    "v_mfma_f32_16x16x32_bf16 v[196:199], a[32:35], a[52:55], v[196:199]\n"
    "ds_read_b128 a[20:23], v16 offset:33792\n"
    "s_add_u32 s58, s58, 0x1000\n"
    "v_mfma_f32_16x16x32_bf16 v[200:203], a[32:35], a[56:59], v[200:203]\n"
    "ds_read_b128 a[24:27], v16 offset:34816\n"
    "s_add_u32 m0, s16, 0x1c000\n"
    "v_mfma_f32_16x16x32_bf16 v[204:207], a[32:35], a[60:63], v[204:207]\n"
    "buffer_load_dwordx4 v12, s[40:43], s61 offen lds\n"
    "v_mfma_f32_16x16x32_bf16 v[208:211], a[36:39], a[48:51], v[208:211]\n"
    "ds_read_b128 a[28:31], v16 offset:35840\n"
    "v_mfma_f32_16x16x32_bf16 v[212:215], a[36:39], a[52:55], v[212:215]\n"
    "ds_read_b64_tr_b16 a[0:1], v18 offset:32768\n"
    "v_mfma_f32_16x16x32_bf16 v[216:219], a[36:39], a[56:59], v[216:219]\n"
    "ds_read_b64_tr_b16 a[2:3], v18 offset:36864\n"
    "v_mfma_f32_16x16x32_bf16 v[220:223], a[36:39], a[60:63], v[220:223]\n"
    "buffer_load_dwordx4 v13, s[40:43], s61 offen offset:1024 lds\n"
    "s_waitcnt lgkmcnt(6)\n"
    "v_mfma_f32_16x16x32_bf16 v[224:227], a[40:43], a[48:51], v[224:227]\n"
    "ds_read_b64_tr_b16 a[4:5], v19 offset:32768\n"
    "v_mfma_f32_16x16x32_bf16 v[228:231], a[40:43], a[52:55], v[228:231]\n"
    "ds_read_b64_tr_b16 a[6:7], v19 offset:36864\n"
    "v_mfma_f32_16x16x32_bf16 v[232:235], a[40:43], a[56:59], v[232:235]\n"
    "ds_read_b64_tr_b16 a[8:9], v18 offset:33024\n"
    "v_mfma_f32_16x16x32_bf16 v[236:239], a[40:43], a[60:63], v[236:239]\n"
    "buffer_load_dwordx4 v14, s[40:43], s61 offen offset:2048 lds\n"
    "v_mfma_f32_16x16x32_bf16 v[240:243], a[44:47], a[48:51], v[240:243]\n"
    "ds_read_b64_tr_b16 a[10:11], v18 offset:37120\n"
    "v_mfma_f32_16x16x32_bf16 v[244:247], a[44:47], a[52:55], v[244:247]\n"
    "ds_read_b64_tr_b16 a[12:13], v19 offset:33024\n"
    "v_mfma_f32_16x16x32_bf16 v[248:251], a[44:47], a[56:59], v[248:251]\n"
    "ds_read_b64_tr_b16 a[14:15], v19 offset:37120\n"
    "v_mfma_f32_16x16x32_bf16 v[252:255], a[44:47], a[60:63], v[252:255]\n"
    "buffer_load_dwordx4 v15, s[40:43], s61 offen offset:3072 lds\n"
    "s_add_u32 s61, s61, 0x10000\n"
    "s_waitcnt lgkmcnt(4)\n"
    "v_mfma_f32_16x16x32_bf16 v[192:195], a[0:3], a[16:19], v[192:195]\n"
    "ds_read_b128 a[48:51], v16 offset:40960\n"
    "v_mfma_f32_16x16x32_bf16 v[196:199], a[0:3], a[20:23], v[196:199]\n"
    "ds_read_b128 a[52:55], v16 offset:41984\n"
    "v_mfma_f32_16x16x32_bf16 v[200:203], a[0:3], a[24:27], v[200:203]\n"
    "ds_read_b128 a[56:59], v16 offset:43008\n"
    "s_add_u32 m0, s16, 0x18000\n"
    "v_mfma_f32_16x16x32_bf16 v[204:207], a[0:3], a[28:31], v[204:207]\n"
    "buffer_load_dwordx4 v4, s[44:47], s68 offen sc1 lds\n"
    "v_mfma_f32_16x16x32_bf16 v[208:211], a[4:7], a[16:19], v[208:211]\n"
    "ds_read_b128 a[60:63], v16 offset:44032\n"
    "v_mfma_f32_16x16x32_bf16 v[212:215], a[4:7], a[20:23], v[212:215]\n"
    "ds_read_b64_tr_b16 a[32:33], v18 offset:40960\n"
    "v_mfma_f32_16x16x32_bf16 v[216:219], a[4:7], a[24:27], v[216:219]\n"
    "ds_read_b64_tr_b16 a[34:35], v18 offset:45056\n"
    "v_mfma_f32_16x16x32_bf16 v[220:223], a[4:7], a[28:31], v[220:223]\n"
    "buffer_load_dwordx4 v5, s[44:47], s68 offen offset:1024 sc1 lds\n"
    "s_waitcnt lgkmcnt(6)\n"
    "v_mfma_f32_16x16x32_bf16 v[224:227], a[8:11], a[16:19], v[224:227]\n"
    "ds_read_b64_tr_b16 a[36:37], v19 offset:40960\n"
    "v_mfma_f32_16x16x32_bf16 v[228:231], a[8:11], a[20:23], v[228:231]\n"
    "ds_read_b64_tr_b16 a[38:39], v19 offset:45056\n"
    "v_mfma_f32_16x16x32_bf16 v[232:235], a[8:11], a[24:27], v[232:235]\n"
    "ds_read_b64_tr_b16 a[40:41], v18 offset:41216\n"
    "v_mfma_f32_16x16x32_bf16 v[236:239], a[8:11], a[28:31], v[236:239]\n"
    "buffer_load_dwordx4 v6, s[44:47], s68 offen offset:2048 sc1 lds\n"
    "v_mfma_f32_16x16x32_bf16 v[240:243], a[12:15], a[16:19], v[240:243]\n"
    "ds_read_b64_tr_b16 a[42:43], v18 offset:45312\n"
    "v_mfma_f32_16x16x32_bf16 v[244:247], a[12:15], a[20:23], v[244:247]\n"
    "ds_read_b64_tr_b16 a[44:45], v19 offset:41216\n"
    "v_mfma_f32_16x16x32_bf16 v[248:251], a[12:15], a[24:27], v[248:251]\n"
    "ds_read_b64_tr_b16 a[46:47], v19 offset:45312\n"
    "v_xor_b32 v16, 0x10000, v16\n"
    "v_xor_b32 v18, 0x10000, v18\n"
    "v_xor_b32 v19, 0x10000, v19\n"
    "v_mfma_f32_16x16x32_bf16 v[252:255], a[12:15], a[28:31], v[252:255]\n"
    "buffer_load_dwordx4 v7, s[44:47], s68 offen offset:3072 sc1 lds\n"
    "s_add_u32 s68, s68, 0x2000\n"
    "s_waitcnt vmcnt(8)\n"
    "s_barrier\n"
    "s_waitcnt lgkmcnt(4)\n"
    "v_mfma_f32_16x16x32_bf16 v[192:195], a[32:35], a[48:51], v[192:195]\n"
    "ds_read_b128 a[16:19], v16\n"
    "v_mfma_f32_16x16x32_bf16 v[196:199], a[32:35], a[52:55], v[196:199]\n"
    "ds_read_b128 a[20:23], v16 offset:1024\n"
    "v_mfma_f32_16x16x32_bf16 v[200:203], a[32:35], a[56:59], v[200:203]\n"
    "ds_read_b128 a[24:27], v16 offset:2048\n"
    "s_add_u32 m0, s16, 0x4000\n"
    "v_mfma_f32_16x16x32_bf16 v[204:207], a[32:35], a[60:63], v[204:207]\n"
    "buffer_load_dwordx4 v12, s[40:43], s61 offen lds\n"
    "v_mfma_f32_16x16x32_bf16 v[208:211], a[36:39], a[48:51], v[208:211]\n"
    "ds_read_b128 a[28:31], v16 offset:3072\n"
    "v_mfma_f32_16x16x32_bf16 v[212:215], a[36:39], a[52:55], v[212:215]\n"
    "ds_read_b64_tr_b16 a[0:1], v18\n"
    "v_mfma_f32_16x16x32_bf16 v[216:219], a[36:39], a[56:59], v[216:219]\n"
    "ds_read_b64_tr_b16 a[2:3], v18 offset:4096\n"
    "v_mfma_f32_16x16x32_bf16 v[220:223], a[36:39], a[60:63], v[220:223]\n"
    "buffer_load_dwordx4 v13, s[40:43], s61 offen offset:1024 lds\n"
    "s_waitcnt lgkmcnt(6)\n"
    "v_mfma_f32_16x16x32_bf16 v[224:227], a[40:43], a[48:51], v[224:227]\n"
    "ds_read_b64_tr_b16 a[4:5], v19\n"
    "v_mfma_f32_16x16x32_bf16 v[228:231], a[40:43], a[52:55], v[228:231]\n"
    "ds_read_b64_tr_b16 a[6:7], v19 offset:4096\n"
    "v_mfma_f32_16x16x32_bf16 v[232:235], a[40:43], a[56:59], v[232:235]\n"
    "ds_read_b64_tr_b16 a[8:9], v18 offset:256\n"
    "v_mfma_f32_16x16x32_bf16 v[236:239], a[40:43], a[60:63], v[236:239]\n"
    "buffer_load_dwordx4 v14, s[40:43], s61 offen offset:2048 lds\n"
    "v_mfma_f32_16x16x32_bf16 v[240:243], a[44:47], a[48:51], v[240:243]\n"
    "ds_read_b64_tr_b16 a[10:11], v18 offset:4352\n"
    "v_mfma_f32_16x16x32_bf16 v[244:247], a[44:47], a[52:55], v[244:247]\n"
    "ds_read_b64_tr_b16 a[12:13], v19 offset:256\n"
    "v_mfma_f32_16x16x32_bf16 v[248:251], a[44:47], a[56:59], v[248:251]\n"
    "ds_read_b64_tr_b16 a[14:15], v19 offset:4352\n"
    "v_mfma_f32_16x16x32_bf16 v[252:255], a[44:47], a[60:63], v[252:255]\n"
    "buffer_load_dwordx4 v15, s[40:43], s61 offen offset:3072 lds\n"
    "s_add_u32 s61, s61, 0x10000\n"
    "s_waitcnt lgkmcnt(4)\n"
    "v_mfma_f32_16x16x32_bf16 v[192:195], a[0:3], a[16:19], v[192:195]\n"
    "ds_read_b128 a[48:51], v16 offset:8192\n"
    "v_mfma_f32_16x16x32_bf16 v[196:199], a[0:3], a[20:23], v[196:199]\n"
    "ds_read_b128 a[52:55], v16 offset:9216\n"
    "v_mfma_f32_16x16x32_bf16 v[200:203], a[0:3], a[24:27], v[200:203]\n"
    "ds_read_b128 a[56:59], v16 offset:10240\n"
    "s_add_u32 m0, s16, 0\n"
    "v_mfma_f32_16x16x32_bf16 v[204:207], a[0:3], a[28:31], v[204:207]\n"
    "buffer_load_dwordx4 v4, s[44:47], s68 offen sc1 lds\n"
    "v_mfma_f32_16x16x32_bf16 v[208:211], a[4:7], a[16:19], v[208:211]\n"
    "ds_read_b128 a[60:63], v16 offset:11264\n"
    "v_mfma_f32_16x16x32_bf16 v[212:215], a[4:7], a[20:23], v[212:215]\n"
    "ds_read_b64_tr_b16 a[32:33], v18 offset:8192\n"
    "v_mfma_f32_16x16x32_bf16 v[216:219], a[4:7], a[24:27], v[216:219]\n"
    "ds_read_b64_tr_b16 a[34:35], v18 offset:12288\n"
    "v_mfma_f32_16x16x32_bf16 v[220:223], a[4:7], a[28:31], v[220:223]\n"
    "buffer_load_dwordx4 v5, s[44:47], s68 offen offset:1024 sc1 lds\n"
    "s_waitcnt lgkmcnt(6)\n"
    "v_mfma_f32_16x16x32_bf16 v[224:227], a[8:11], a[16:19], v[224:227]\n"
    "ds_read_b64_tr_b16 a[36:37], v19 offset:8192\n"
    "v_mfma_f32_16x16x32_bf16 v[228:231], a[8:11], a[20:23], v[228:231]\n"
    "ds_read_b64_tr_b16 a[38:39], v19 offset:12288\n"
    "v_mfma_f32_16x16x32_bf16 v[232:235], a[8:11], a[24:27], v[232:235]\n"
    "ds_read_b64_tr_b16 a[40:41], v18 offset:8448\n"
    "v_mfma_f32_16x16x32_bf16 v[236:239], a[8:11], a[28:31], v[236:239]\n"
    "buffer_load_dwordx4 v6, s[44:47], s68 offen offset:2048 sc1 lds\n"
    "v_mfma_f32_16x16x32_bf16 v[240:243], a[12:15], a[16:19], v[240:243]\n"
    "ds_read_b64_tr_b16 a[42:43], v18 offset:12544\n"
    "v_mfma_f32_16x16x32_bf16 v[244:247], a[12:15], a[20:23], v[244:247]\n"
    "ds_read_b64_tr_b16 a[44:45], v19 offset:8448\n"
    "v_mfma_f32_16x16x32_bf16 v[248:251], a[12:15], a[24:27], v[248:251]\n"
    "ds_read_b64_tr_b16 a[46:47], v19 offset:12544\n"
    "v_mfma_f32_16x16x32_bf16 v[252:255], a[12:15], a[28:31], v[252:255]\n"
    "buffer_load_dwordx4 v7, s[44:47], s68 offen offset:3072 sc1 lds\n"
    "s_add_u32 s68, s68, 0x2000\n"
    "s_waitcnt vmcnt(8)\n"
    "s_barrier\n"
    "s_waitcnt lgkmcnt(4)\n"
    "v_mfma_f32_16x16x32_bf16 v[192:195], a[32:35], a[48:51], v[192:195]\n"
    "ds_read_b128 a[16:19], v16 offset:32768\n"
    "v_mfma_f32_16x16x32_bf16 v[196:199], a[32:35], a[52:55], v[196:199]\n"
    "ds_read_b128 a[20:23], v16 offset:33792\n"
    "v_mfma_f32_16x16x32_bf16 v[200:203], a[32:35], a[56:59], v[200:203]\n"
    "ds_read_b128 a[24:27], v16 offset:34816\n"
    "s_add_u32 m0, s16, 0xc000\n"
    "v_mfma_f32_16x16x32_bf16 v[204:207], a[32:35], a[60:63], v[204:207]\n"
    "buffer_load_dwordx4 v12, s[40:43], s61 offen lds\n"
    "v_mfma_f32_16x16x32_bf16 v[208:211], a[36:39], a[48:51], v[208:211]\n"
    "ds_read_b128 a[28:31], v16 offset:35840\n"
    "v_mfma_f32_16x16x32_bf16 v[212:215], a[36:39], a[52:55], v[212:215]\n"
    "ds_read_b64_tr_b16 a[0:1], v18 offset:32768\n"
    "v_mfma_f32_16x16x32_bf16 v[216:219], a[36:39], a[56:59], v[216:219]\n"
    "ds_read_b64_tr_b16 a[2:3], v18 offset:36864\n"
    "v_mfma_f32_16x16x32_bf16 v[220:223], a[36:39], a[60:63], v[220:223]\n"
    "buffer_load_dwordx4 v13, s[40:43], s61 offen offset:1024 lds\n"
    "s_waitcnt lgkmcnt(6)\n"
    "v_mfma_f32_16x16x32_bf16 v[224:227], a[40:43], a[48:51], v[224:227]\n"
    "ds_read_b64_tr_b16 a[4:5], v19 offset:32768\n"
    "v_mfma_f32_16x16x32_bf16 v[228:231], a[40:43], a[52:55], v[228:231]\n"
    "ds_read_b64_tr_b16 a[6:7], v19 offset:36864\n"
    "v_mfma_f32_16x16x32_bf16 v[232:235], a[40:43], a[56:59], v[232:235]\n"
    "ds_read_b64_tr_b16 a[8:9], v18 offset:33024\n"
    "v_mfma_f32_16x16x32_bf16 v[236:239], a[40:43], a[60:63], v[236:239]\n"
    "buffer_load_dwordx4 v14, s[40:43], s61 offen offset:2048 lds\n"
    "v_mfma_f32_16x16x32_bf16 v[240:243], a[44:47], a[48:51], v[240:243]\n"
    "ds_read_b64_tr_b16 a[10:11], v18 offset:37120\n"
    "v_mfma_f32_16x16x32_bf16 v[244:247], a[44:47], a[52:55], v[244:247]\n"
    "ds_read_b64_tr_b16 a[12:13], v19 offset:33024\n"
    "v_mfma_f32_16x16x32_bf16 v[248:251], a[44:47], a[56:59], v[248:251]\n"
    "ds_read_b64_tr_b16 a[14:15], v19 offset:37120\n"
    "v_mfma_f32_16x16x32_bf16 v[252:255], a[44:47], a[60:63], v[252:255]\n"
    "buffer_load_dwordx4 v15, s[40:43], s61 offen offset:3072 lds\n"
    "s_add_u32 s61, s61, 0x10000\n"
    "s_waitcnt lgkmcnt(4)\n"
    "v_mfma_f32_16x16x32_bf16 v[192:195], a[0:3], a[16:19], v[192:195]\n"
    "ds_read_b128 a[48:51], v16 offset:40960\n"
    "v_mfma_f32_16x16x32_bf16 v[196:199], a[0:3], a[20:23], v[196:199]\n"
    "ds_read_b128 a[52:55], v16 offset:41984\n"
    "v_mfma_f32_16x16x32_bf16 v[200:203], a[0:3], a[24:27], v[200:203]\n"
    "ds_read_b128 a[56:59], v16 offset:43008\n"
    "s_add_u32 m0, s16, 0x8000\n"
    "v_mfma_f32_16x16x32_bf16 v[204:207], a[0:3], a[28:31], v[204:207]\n"
    "buffer_load_dwordx4 v4, s[44:47], s68 offen sc1 lds\n"
    "v_mfma_f32_16x16x32_bf16 v[208:211], a[4:7], a[16:19], v[208:211]\n"
    "ds_read_b128 a[60:63], v16 offset:44032\n"
    "v_mfma_f32_16x16x32_bf16 v[212:215], a[4:7], a[20:23], v[212:215]\n"
    "ds_read_b64_tr_b16 a[32:33], v18 offset:40960\n"
    "v_mfma_f32_16x16x32_bf16 v[216:219], a[4:7], a[24:27], v[216:219]\n"
    "ds_read_b64_tr_b16 a[34:35], v18 offset:45056\n"
    "v_mfma_f32_16x16x32_bf16 v[220:223], a[4:7], a[28:31], v[220:223]\n"
    "buffer_load_dwordx4 v5, s[44:47], s68 offen offset:1024 sc1 lds\n"
    "s_waitcnt lgkmcnt(6)\n"
    "v_mfma_f32_16x16x32_bf16 v[224:227], a[8:11], a[16:19], v[224:227]\n"
    "ds_read_b64_tr_b16 a[36:37], v19 offset:40960\n"
    "v_mfma_f32_16x16x32_bf16 v[228:231], a[8:11], a[20:23], v[228:231]\n"
    "ds_read_b64_tr_b16 a[38:39], v19 offset:45056\n"
    "v_mfma_f32_16x16x32_bf16 v[232:235], a[8:11], a[24:27], v[232:235]\n"
    "ds_read_b64_tr_b16 a[40:41], v18 offset:41216\n"
    "v_mfma_f32_16x16x32_bf16 v[236:239], a[8:11], a[28:31], v[236:239]\n"
    "buffer_load_dwordx4 v6, s[44:47], s68 offen offset:2048 sc1 lds\n"
    "v_mfma_f32_16x16x32_bf16 v[240:243], a[12:15], a[16:19], v[240:243]\n"
    "ds_read_b64_tr_b16 a[42:43], v18 offset:45312\n"
    "v_mfma_f32_16x16x32_bf16 v[244:247], a[12:15], a[20:23], v[244:247]\n"
    "ds_read_b64_tr_b16 a[44:45], v19 offset:41216\n"
    "v_mfma_f32_16x16x32_bf16 v[248:251], a[12:15], a[24:27], v[248:251]\n"
    "ds_read_b64_tr_b16 a[46:47], v19 offset:45312\n"
    "v_xor_b32 v16, 0x10000, v16\n"
    "v_xor_b32 v18, 0x10000, v18\n"
    "v_xor_b32 v19, 0x10000, v19\n"
    "v_mfma_f32_16x16x32_bf16 v[252:255], a[12:15], a[28:31], v[252:255]\n"
    "buffer_load_dwordx4 v7, s[44:47], s68 offen offset:3072 sc1 lds\n"
    "s_add_u32 s68, s68, 0x2000\n"
    "s_waitcnt vmcnt(8)\n"
    "s_barrier\n"
    "s_waitcnt lgkmcnt(4)\n"
    "v_mfma_f32_16x16x32_bf16 v[192:195], a[32:35], a[48:51], v[192:195]\n"
    "ds_read_b128 a[16:19], v16\n"
    "v_mfma_f32_16x16x32_bf16 v[196:199], a[32:35], a[52:55], v[196:199]\n"
    "ds_read_b128 a[20:23], v16 offset:1024\n"
    "v_mfma_f32_16x16x32_bf16 v[200:203], a[32:35], a[56:59], v[200:203]\n"
    "ds_read_b128 a[24:27], v16 offset:2048\n"
    "s_add_u32 m0, s16, 0x14000\n"
    "v_mfma_f32_16x16x32_bf16 v[204:207], a[32:35], a[60:63], v[204:207]\n"
    "buffer_load_dwordx4 v12, s[40:43], s61 offen lds\n"
    "v_mfma_f32_16x16x32_bf16 v[208:211], a[36:39], a[48:51], v[208:211]\n"
    "ds_read_b128 a[28:31], v16 offset:3072\n"
    "v_mfma_f32_16x16x32_bf16 v[212:215], a[36:39], a[52:55], v[212:215]\n"
    "ds_read_b64_tr_b16 a[0:1], v18\n"
    "v_mfma_f32_16x16x32_bf16 v[216:219], a[36:39], a[56:59], v[216:219]\n"
    "ds_read_b64_tr_b16 a[2:3], v18 offset:4096\n"
    "v_mfma_f32_16x16x32_bf16 v[220:223], a[36:39], a[60:63], v[220:223]\n"
    "buffer_load_dwordx4 v13, s[40:43], s61 offen offset:1024 lds\n"
    "s_waitcnt lgkmcnt(6)\n"
    "v_mfma_f32_16x16x32_bf16 v[224:227], a[40:43], a[48:51], v[224:227]\n"
    "ds_read_b64_tr_b16 a[4:5], v19\n"
    "v_mfma_f32_16x16x32_bf16 v[228:231], a[40:43], a[52:55], v[228:231]\n"
    "ds_read_b64_tr_b16 a[6:7], v19 offset:4096\n"
    "v_mfma_f32_16x16x32_bf16 v[232:235], a[40:43], a[56:59], v[232:235]\n"
    "ds_read_b64_tr_b16 a[8:9], v18 offset:256\n"
    "v_mfma_f32_16x16x32_bf16 v[236:239], a[40:43], a[60:63], v[236:239]\n"
    "buffer_load_dwordx4 v14, s[40:43], s61 offen offset:2048 lds\n"
    "v_mfma_f32_16x16x32_bf16 v[240:243], a[44:47], a[48:51], v[240:243]\n"
    "ds_read_b64_tr_b16 a[10:11], v18 offset:4352\n"
    "v_mfma_f32_16x16x32_bf16 v[244:247], a[44:47], a[52:55], v[244:247]\n"
    "ds_read_b64_tr_b16 a[12:13], v19 offset:256\n"
    "v_mfma_f32_16x16x32_bf16 v[248:251], a[44:47], a[56:59], v[248:251]\n"
    "ds_read_b64_tr_b16 a[14:15], v19 offset:4352\n"
    "v_mfma_f32_16x16x32_bf16 v[252:255], a[44:47], a[60:63], v[252:255]\n"
    "buffer_load_dwordx4 v15, s[40:43], s61 offen offset:3072 lds\n"
    "s_add_u32 s61, s61, 0x10000\n"
    "s_waitcnt lgkmcnt(4)\n"
    "v_mfma_f32_16x16x32_bf16 v[192:195], a[0:3], a[16:19], v[192:195]\n"
    "ds_read_b128 a[48:51], v16 offset:8192\n"
    "v_mfma_f32_16x16x32_bf16 v[196:199], a[0:3], a[20:23], v[196:199]\n"
    "ds_read_b128 a[52:55], v16 offset:9216\n"
    "v_mfma_f32_16x16x32_bf16 v[200:203], a[0:3], a[24:27], v[200:203]\n"
    "ds_read_b128 a[56:59], v16 offset:10240\n"
    "s_add_u32 m0, s16, 0x10000\n"
    "v_mfma_f32_16x16x32_bf16 v[204:207], a[0:3], a[28:31], v[204:207]\n"
    "buffer_load_dwordx4 v4, s[44:47], s68 offen sc1 lds\n"
    "v_mfma_f32_16x16x32_bf16 v[208:211], a[4:7], a[16:19], v[208:211]\n"
    "ds_read_b128 a[60:63], v16 offset:11264\n"
    "v_mfma_f32_16x16x32_bf16 v[212:215], a[4:7], a[20:23], v[212:215]\n"
    "ds_read_b64_tr_b16 a[32:33], v18 offset:8192\n"
    "v_mfma_f32_16x16x32_bf16 v[216:219], a[4:7], a[24:27], v[216:219]\n"
    "ds_read_b64_tr_b16 a[34:35], v18 offset:12288\n"
    "v_mfma_f32_16x16x32_bf16 v[220:223], a[4:7], a[28:31], v[220:223]\n"
    "buffer_load_dwordx4 v5, s[44:47], s68 offen offset:1024 sc1 lds\n"
    "s_waitcnt lgkmcnt(6)\n"
    "v_mfma_f32_16x16x32_bf16 v[224:227], a[8:11], a[16:19], v[224:227]\n"
    "ds_read_b64_tr_b16 a[36:37], v19 offset:8192\n"
    "v_mfma_f32_16x16x32_bf16 v[228:231], a[8:11], a[20:23], v[228:231]\n"
    "ds_read_b64_tr_b16 a[38:39], v19 offset:12288\n"
    "v_mfma_f32_16x16x32_bf16 v[232:235], a[8:11], a[24:27], v[232:235]\n"
    "ds_read_b64_tr_b16 a[40:41], v18 offset:8448\n"
    "v_mfma_f32_16x16x32_bf16 v[236:239], a[8:11], a[28:31], v[236:239]\n"
    "buffer_load_dwordx4 v6, s[44:47], s68 offen offset:2048 sc1 lds\n"
    "v_mfma_f32_16x16x32_bf16 v[240:243], a[12:15], a[16:19], v[240:243]\n"
    "ds_read_b64_tr_b16 a[42:43], v18 offset:12544\n"
    "v_mfma_f32_16x16x32_bf16 v[244:247], a[12:15], a[20:23], v[244:247]\n"
    "ds_read_b64_tr_b16 a[44:45], v19 offset:8448\n"
    "v_mfma_f32_16x16x32_bf16 v[248:251], a[12:15], a[24:27], v[248:251]\n"
    "ds_read_b64_tr_b16 a[46:47], v19 offset:12544\n"
    "v_mfma_f32_16x16x32_bf16 v[252:255], a[12:15], a[28:31], v[252:255]\n"
    "buffer_load_dwordx4 v7, s[44:47], s68 offen offset:3072 sc1 lds\n"
    "s_add_u32 s68, s68, 0x2000\n"
    "s_waitcnt vmcnt(8)\n"
    "s_barrier\n"
    "s_waitcnt lgkmcnt(4)\n"
    "v_mfma_f32_16x16x32_bf16 v[192:195], a[32:35], a[48:51], v[192:195]\n"
    "ds_read_b128 a[16:19], v16 offset:32768\n"
    "v_mfma_f32_16x16x32_bf16 v[196:199], a[32:35], a[52:55], v[196:199]\n"
    "ds_read_b128 a[20:23], v16 offset:33792\n"
    "v_mfma_f32_16x16x32_bf16 v[200:203], a[32:35], a[56:59], v[200:203]\n"
    "ds_read_b128 a[24:27], v16 offset:34816\n"
    "v_mfma_f32_16x16x32_bf16 v[204:207], a[32:35], a[60:63], v[204:207]\n"
    "v_mfma_f32_16x16x32_bf16 v[208:211], a[36:39], a[48:51], v[208:211]\n"
    "ds_read_b128 a[28:31], v16 offset:35840\n"
    "v_mfma_f32_16x16x32_bf16 v[212:215], a[36:39], a[52:55], v[212:215]\n"
    "ds_read_b64_tr_b16 a[0:1], v18 offset:32768\n"
    "v_mfma_f32_16x16x32_bf16 v[216:219], a[36:39], a[56:59], v[216:219]\n"
    "ds_read_b64_tr_b16 a[2:3], v18 offset:36864\n"
    "v_mfma_f32_16x16x32_bf16 v[220:223], a[36:39], a[60:63], v[220:223]\n"
    "s_waitcnt lgkmcnt(6)\n"
    "v_mfma_f32_16x16x32_bf16 v[224:227], a[40:43], a[48:51], v[224:227]\n"
    "ds_read_b64_tr_b16 a[4:5], v19 offset:32768\n"
    "v_mfma_f32_16x16x32_bf16 v[228:231], a[40:43], a[52:55], v[228:231]\n"
    "ds_read_b64_tr_b16 a[6:7], v19 offset:36864\n"
    "v_mfma_f32_16x16x32_bf16 v[232:235], a[40:43], a[56:59], v[232:235]\n"
    "ds_read_b64_tr_b16 a[8:9], v18 offset:33024\n"
    "v_mfma_f32_16x16x32_bf16 v[236:239], a[40:43], a[60:63], v[236:239]\n"
    "v_mfma_f32_16x16x32_bf16 v[240:243], a[44:47], a[48:51], v[240:243]\n"
    "ds_read_b64_tr_b16 a[10:11], v18 offset:37120\n"
    "v_mfma_f32_16x16x32_bf16 v[244:247], a[44:47], a[52:55], v[244:247]\n"
    "ds_read_b64_tr_b16 a[12:13], v19 offset:33024\n"
    "v_mfma_f32_16x16x32_bf16 v[248:251], a[44:47], a[56:59], v[248:251]\n"
    "ds_read_b64_tr_b16 a[14:15], v19 offset:37120\n"
    "v_mfma_f32_16x16x32_bf16 v[252:255], a[44:47], a[60:63], v[252:255]\n"
    "s_waitcnt lgkmcnt(4)\n"
    "v_mfma_f32_16x16x32_bf16 v[192:195], a[0:3], a[16:19], v[192:195]\n"
    "ds_read_b128 a[48:51], v16 offset:40960\n"
    "v_mfma_f32_16x16x32_bf16 v[196:199], a[0:3], a[20:23], v[196:199]\n"
    "ds_read_b128 a[52:55], v16 offset:41984\n"
    "v_mfma_f32_16x16x32_bf16 v[200:203], a[0:3], a[24:27], v[200:203]\n"
    "ds_read_b128 a[56:59], v16 offset:43008\n"
    "v_mfma_f32_16x16x32_bf16 v[204:207], a[0:3], a[28:31], v[204:207]\n"
    "v_mfma_f32_16x16x32_bf16 v[208:211], a[4:7], a[16:19], v[208:211]\n"
    "ds_read_b128 a[60:63], v16 offset:44032\n"
    "v_mfma_f32_16x16x32_bf16 v[212:215], a[4:7], a[20:23], v[212:215]\n"
    "ds_read_b64_tr_b16 a[32:33], v18 offset:40960\n"
    "v_mfma_f32_16x16x32_bf16 v[216:219], a[4:7], a[24:27], v[216:219]\n"
    "ds_read_b64_tr_b16 a[34:35], v18 offset:45056\n"
    "v_mfma_f32_16x16x32_bf16 v[220:223], a[4:7], a[28:31], v[220:223]\n"
    "s_waitcnt lgkmcnt(6)\n"
    "v_mfma_f32_16x16x32_bf16 v[224:227], a[8:11], a[16:19], v[224:227]\n"
    "ds_read_b64_tr_b16 a[36:37], v19 offset:40960\n"
    "v_mfma_f32_16x16x32_bf16 v[228:231], a[8:11], a[20:23], v[228:231]\n"
    "ds_read_b64_tr_b16 a[38:39], v19 offset:45056\n"
    "v_mfma_f32_16x16x32_bf16 v[232:235], a[8:11], a[24:27], v[232:235]\n"
    "ds_read_b64_tr_b16 a[40:41], v18 offset:41216\n"
    "v_mfma_f32_16x16x32_bf16 v[236:239], a[8:11], a[28:31], v[236:239]\n"
    "v_mfma_f32_16x16x32_bf16 v[240:243], a[12:15], a[16:19], v[240:243]\n"
    "ds_read_b64_tr_b16 a[42:43], v18 offset:45312\n"
    "v_mfma_f32_16x16x32_bf16 v[244:247], a[12:15], a[20:23], v[244:247]\n"
    "ds_read_b64_tr_b16 a[44:45], v19 offset:41216\n"
    "v_mfma_f32_16x16x32_bf16 v[248:251], a[12:15], a[24:27], v[248:251]\n"
    "ds_read_b64_tr_b16 a[46:47], v19 offset:45312\n"
    "v_xor_b32 v16, 0x10000, v16\n"
    "v_xor_b32 v18, 0x10000, v18\n"
    "v_xor_b32 v19, 0x10000, v19\n"
    "v_mfma_f32_16x16x32_bf16 v[252:255], a[12:15], a[28:31], v[252:255]\n"
    "s_waitcnt vmcnt(0)\n"
    "s_barrier\n"
    "s_waitcnt lgkmcnt(4)\n"
    "v_mfma_f32_16x16x32_bf16 v[192:195], a[32:35], a[48:51], v[192:195]\n"
    "ds_read_b128 a[16:19], v16\n"
    "v_mfma_f32_16x16x32_bf16 v[196:199], a[32:35], a[52:55], v[196:199]\n"
    "ds_read_b128 a[20:23], v16 offset:1024\n"
    "v_mfma_f32_16x16x32_bf16 v[200:203], a[32:35], a[56:59], v[200:203]\n"
    "ds_read_b128 a[24:27], v16 offset:2048\n"
    "v_mfma_f32_16x16x32_bf16 v[204:207], a[32:35], a[60:63], v[204:207]\n"
    "v_mfma_f32_16x16x32_bf16 v[208:211], a[36:39], a[48:51], v[208:211]\n"
    "ds_read_b128 a[28:31], v16 offset:3072\n"
    "v_mfma_f32_16x16x32_bf16 v[212:215], a[36:39], a[52:55], v[212:215]\n"
    "ds_read_b64_tr_b16 a[0:1], v18\n"
    "v_mfma_f32_16x16x32_bf16 v[216:219], a[36:39], a[56:59], v[216:219]\n"
    "ds_read_b64_tr_b16 a[2:3], v18 offset:4096\n"
    "v_mfma_f32_16x16x32_bf16 v[220:223], a[36:39], a[60:63], v[220:223]\n"
    "s_waitcnt lgkmcnt(6)\n"
    "v_mfma_f32_16x16x32_bf16 v[224:227], a[40:43], a[48:51], v[224:227]\n"
    "ds_read_b64_tr_b16 a[4:5], v19\n"
    "v_mfma_f32_16x16x32_bf16 v[228:231], a[40:43], a[52:55], v[228:231]\n"
    "ds_read_b64_tr_b16 a[6:7], v19 offset:4096\n"
    "v_mfma_f32_16x16x32_bf16 v[232:235], a[40:43], a[56:59], v[232:235]\n"
    "ds_read_b64_tr_b16 a[8:9], v18 offset:256\n"
    "v_mfma_f32_16x16x32_bf16 v[236:239], a[40:43], a[60:63], v[236:239]\n"
    "v_mfma_f32_16x16x32_bf16 v[240:243], a[44:47], a[48:51], v[240:243]\n"
    "ds_read_b64_tr_b16 a[10:11], v18 offset:4352\n"
    "v_mfma_f32_16x16x32_bf16 v[244:247], a[44:47], a[52:55], v[244:247]\n"
    "ds_read_b64_tr_b16 a[12:13], v19 offset:256\n"
    "v_mfma_f32_16x16x32_bf16 v[248:251], a[44:47], a[56:59], v[248:251]\n"
    "ds_read_b64_tr_b16 a[14:15], v19 offset:4352\n"
    "v_mfma_f32_16x16x32_bf16 v[252:255], a[44:47], a[60:63], v[252:255]\n"
    "s_waitcnt lgkmcnt(4)\n"
    "v_mfma_f32_16x16x32_bf16 v[192:195], a[0:3], a[16:19], v[192:195]\n"
    "ds_read_b128 a[48:51], v16 offset:8192\n"
    "v_mfma_f32_16x16x32_bf16 v[196:199], a[0:3], a[20:23], v[196:199]\n"
    "ds_read_b128 a[52:55], v16 offset:9216\n"
    "v_mfma_f32_16x16x32_bf16 v[200:203], a[0:3], a[24:27], v[200:203]\n"
    "ds_read_b128 a[56:59], v16 offset:10240\n"
    "v_mfma_f32_16x16x32_bf16 v[204:207], a[0:3], a[28:31], v[204:207]\n"
    "v_mfma_f32_16x16x32_bf16 v[208:211], a[4:7], a[16:19], v[208:211]\n"
    "ds_read_b128 a[60:63], v16 offset:11264\n"
    "v_mfma_f32_16x16x32_bf16 v[212:215], a[4:7], a[20:23], v[212:215]\n"
    "ds_read_b64_tr_b16 a[32:33], v18 offset:8192\n"
    "v_mfma_f32_16x16x32_bf16 v[216:219], a[4:7], a[24:27], v[216:219]\n"
    "ds_read_b64_tr_b16 a[34:35], v18 offset:12288\n"
    "v_mfma_f32_16x16x32_bf16 v[220:223], a[4:7], a[28:31], v[220:223]\n"
    "s_waitcnt lgkmcnt(6)\n"
    "v_mfma_f32_16x16x32_bf16 v[224:227], a[8:11], a[16:19], v[224:227]\n"
    "ds_read_b64_tr_b16 a[36:37], v19 offset:8192\n"
    "v_mfma_f32_16x16x32_bf16 v[228:231], a[8:11], a[20:23], v[228:231]\n"
    "ds_read_b64_tr_b16 a[38:39], v19 offset:12288\n"
    "v_mfma_f32_16x16x32_bf16 v[232:235], a[8:11], a[24:27], v[232:235]\n"
    "ds_read_b64_tr_b16 a[40:41], v18 offset:8448\n"
    "v_mfma_f32_16x16x32_bf16 v[236:239], a[8:11], a[28:31], v[236:239]\n"
    "v_mfma_f32_16x16x32_bf16 v[240:243], a[12:15], a[16:19], v[240:243]\n"
    "ds_read_b64_tr_b16 a[42:43], v18 offset:12544\n"
    "v_mfma_f32_16x16x32_bf16 v[244:247], a[12:15], a[20:23], v[244:247]\n"
    "ds_read_b64_tr_b16 a[44:45], v19 offset:8448\n"
    "v_mfma_f32_16x16x32_bf16 v[248:251], a[12:15], a[24:27], v[248:251]\n"
    "ds_read_b64_tr_b16 a[46:47], v19 offset:12544\n"
    "v_mfma_f32_16x16x32_bf16 v[252:255], a[12:15], a[28:31], v[252:255]\n"
    "s_barrier\n"
    "s_waitcnt lgkmcnt(4)\n"
    "v_mfma_f32_16x16x32_bf16 v[192:195], a[32:35], a[48:51], v[192:195]\n"
    "v_mfma_f32_16x16x32_bf16 v[196:199], a[32:35], a[52:55], v[196:199]\n"
    "v_mfma_f32_16x16x32_bf16 v[200:203], a[32:35], a[56:59], v[200:203]\n"
    "v_mfma_f32_16x16x32_bf16 v[204:207], a[32:35], a[60:63], v[204:207]\n"
    "v_mfma_f32_16x16x32_bf16 v[208:211], a[36:39], a[48:51], v[208:211]\n"
    "v_mfma_f32_16x16x32_bf16 v[212:215], a[36:39], a[52:55], v[212:215]\n"
    "v_mfma_f32_16x16x32_bf16 v[216:219], a[36:39], a[56:59], v[216:219]\n"
    "v_mfma_f32_16x16x32_bf16 v[220:223], a[36:39], a[60:63], v[220:223]\n"
    "s_waitcnt lgkmcnt(0)\n"
    "v_mfma_f32_16x16x32_bf16 v[224:227], a[40:43], a[48:51], v[224:227]\n"
    "v_mfma_f32_16x16x32_bf16 v[228:231], a[40:43], a[52:55], v[228:231]\n"
    "v_mfma_f32_16x16x32_bf16 v[232:235], a[40:43], a[56:59], v[232:235]\n"
    "v_mfma_f32_16x16x32_bf16 v[236:239], a[40:43], a[60:63], v[236:239]\n"
    "v_mfma_f32_16x16x32_bf16 v[240:243], a[44:47], a[48:51], v[240:243]\n"
    "v_mfma_f32_16x16x32_bf16 v[244:247], a[44:47], a[52:55], v[244:247]\n"
    "v_mfma_f32_16x16x32_bf16 v[248:251], a[44:47], a[56:59], v[248:251]\n"
    "v_mfma_f32_16x16x32_bf16 v[252:255], a[44:47], a[60:63], v[252:255]\n"
    "s_nop 7\n"
    "v_cvt_pk_bf16_f32 v128, v196, v197\n"
    "v_cvt_pk_bf16_f32 v129, v198, v199\n"
    "v_cvt_pk_bf16_f32 v130, v212, v213\n"
    "v_cvt_pk_bf16_f32 v131, v214, v215\n"
    "buffer_store_dwordx4 v[128:131], v2, s[44:47], s60 offen sc1\n"
    "v_cvt_pk_bf16_f32 v132, v228, v229\n"
    "v_cvt_pk_bf16_f32 v133, v230, v231\n"
    "v_cvt_pk_bf16_f32 v134, v244, v245\n"
    "v_cvt_pk_bf16_f32 v135, v246, v247\n"
    "buffer_store_dwordx4 v[132:135], v2, s[44:47], s60 offen offset:1024 sc1\n"
    "s_add_u32 s60, s60, 0x800\n"
    "v_cvt_pk_bf16_f32 v136, v200, v201\n"
    "v_cvt_pk_bf16_f32 v137, v202, v203\n"
    "v_cvt_pk_bf16_f32 v138, v216, v217\n"
    "v_cvt_pk_bf16_f32 v139, v218, v219\n"
    "buffer_store_dwordx4 v[136:139], v2, s[44:47], s60 offen sc1\n"
    "v_cvt_pk_bf16_f32 v140, v232, v233\n"
    "v_cvt_pk_bf16_f32 v141, v234, v235\n"
    "v_cvt_pk_bf16_f32 v142, v248, v249\n"
    "v_cvt_pk_bf16_f32 v143, v250, v251\n"
    "buffer_store_dwordx4 v[140:143], v2, s[44:47], s60 offen offset:1024 sc1\n"
    "s_add_u32 s60, s60, 0x800\n"
    "v_cvt_pk_bf16_f32 v144, v204, v205\n"
    "v_cvt_pk_bf16_f32 v145, v206, v207\n"
    "v_cvt_pk_bf16_f32 v146, v220, v221\n"
    "v_cvt_pk_bf16_f32 v147, v222, v223\n"
    "buffer_store_dwordx4 v[144:147], v2, s[44:47], s60 offen sc1\n"
    "v_cvt_pk_bf16_f32 v148, v236, v237\n"
    "v_cvt_pk_bf16_f32 v149, v238, v239\n"
    "v_cvt_pk_bf16_f32 v150, v252, v253\n"
    "v_cvt_pk_bf16_f32 v151, v254, v255\n"
    "buffer_store_dwordx4 v[148:151], v2, s[44:47], s60 offen offset:1024 sc1\n"
    "s_add_u32 s60, s60, 0x800\n"
    "buffer_load_dwordx2 v[8:9], v10, s[44:47], s69 offen sc1\n"
    "s_waitcnt vmcnt(0)\n"
    "v_mov_b32 v24, s4\n"
    "v_mov_b32 v25, s5\n"
    "s_mov_b64 exec, 1\n"
    "buffer_store_dwordx2 v[24:25], v2, s[44:47], s62 offen sc1\n"
    "s_mov_b64 exec, -1\n"
    "v_cmp_ne_u64 vcc, s[4:5], v[8:9]\n"
    "s_nop 0\n"
    "s_cbranch_vccz XRDY_%=\n"
    "s_mov_b32 s34, 0\n"
    "PL11_%=:\n"
    "buffer_load_dwordx2 v[8:9], v10, s[44:47], s69 offen sc1\n"
    "s_waitcnt vmcnt(0)\n"
    "v_cmp_ne_u64 vcc, s[4:5], v[8:9]\n"
    "s_add_u32 s34, s34, 1\n"
    "s_cbranch_vccz PD11_%=\n"
    "s_sleep 1\n"
    "s_cmp_lt_u32 s34, 0x30d40\n"
    "s_cbranch_scc1 PL11_%=\n"
    "PD11_%=:\n"
    "XRDY_%=:\n"
    "buffer_load_dwordx4 v[64:67], v2, s[44:47], s56 offen sc1\n"
    "buffer_load_dwordx4 v[80:83], v2, s[44:47], s57 offen sc1\n"
    "buffer_load_dwordx4 v[96:99], v2, s[44:47], s58 offen sc1\n"
    "buffer_load_dwordx4 v[68:71], v2, s[44:47], s56 offen offset:1024 sc1\n"
    "buffer_load_dwordx4 v[84:87], v2, s[44:47], s57 offen offset:1024 sc1\n"
    "buffer_load_dwordx4 v[100:103], v2, s[44:47], s58 offen offset:1024 sc1\n"
    "s_waitcnt vmcnt(3)\n"
    "v_lshlrev_b32 v34, 16, v64\n"
    "v_and_b32 v35, 0xffff0000, v64\n"
    "v_lshlrev_b32 v36, 16, v80\n"
    "v_and_b32 v37, 0xffff0000, v80\n"
    "v_lshlrev_b32 v38, 16, v96\n"
    "v_and_b32 v39, 0xffff0000, v96\n"
    "v_pk_add_f32 v[192:193], v[192:193], v[34:35]\n"
    "v_pk_add_f32 v[36:37], v[36:37], v[38:39]\n"
    "v_pk_add_f32 v[192:193], v[192:193], v[36:37]\n"
    "v_cvt_pk_bf16_f32 v48, v192, v193\n"
    "v_lshlrev_b32 v34, 16, v65\n"
    "v_and_b32 v35, 0xffff0000, v65\n"
    "v_lshlrev_b32 v36, 16, v81\n"
    "v_and_b32 v37, 0xffff0000, v81\n"
    "v_lshlrev_b32 v38, 16, v97\n"
    "v_and_b32 v39, 0xffff0000, v97\n"
    "v_pk_add_f32 v[194:195], v[194:195], v[34:35]\n"
    "v_pk_add_f32 v[36:37], v[36:37], v[38:39]\n"
    "v_pk_add_f32 v[194:195], v[194:195], v[36:37]\n"
    "v_cvt_pk_bf16_f32 v49, v194, v195\n"
    "v_lshlrev_b32 v34, 16, v66\n"
    "v_and_b32 v35, 0xffff0000, v66\n"
    "v_lshlrev_b32 v36, 16, v82\n"
    "v_and_b32 v37, 0xffff0000, v82\n"
    "v_lshlrev_b32 v38, 16, v98\n"
    "v_and_b32 v39, 0xffff0000, v98\n"
    "v_pk_add_f32 v[208:209], v[208:209], v[34:35]\n"
    "v_pk_add_f32 v[36:37], v[36:37], v[38:39]\n"
    "v_pk_add_f32 v[208:209], v[208:209], v[36:37]\n"
    "v_cvt_pk_bf16_f32 v50, v208, v209\n"
    "v_lshlrev_b32 v34, 16, v67\n"
    "v_and_b32 v35, 0xffff0000, v67\n"
    "v_lshlrev_b32 v36, 16, v83\n"
    "v_and_b32 v37, 0xffff0000, v83\n"
    "v_lshlrev_b32 v38, 16, v99\n"
    "v_and_b32 v39, 0xffff0000, v99\n"
    "v_pk_add_f32 v[210:211], v[210:211], v[34:35]\n"
    "v_pk_add_f32 v[36:37], v[36:37], v[38:39]\n"
    "v_pk_add_f32 v[210:211], v[210:211], v[36:37]\n"
    "v_cvt_pk_bf16_f32 v51, v210, v211\n"
    "s_nop 1\n"
    "v_permlane16_swap_b32 v48, v50\n"
    "v_permlane16_swap_b32 v49, v51\n"
    "buffer_store_dwordx4 v[48:51], v20, s[24:27], s52 offen\n"
    "s_waitcnt vmcnt(1)\n"
    "v_lshlrev_b32 v34, 16, v68\n"
    "v_and_b32 v35, 0xffff0000, v68\n"
    "v_lshlrev_b32 v36, 16, v84\n"
    "v_and_b32 v37, 0xffff0000, v84\n"
    "v_lshlrev_b32 v38, 16, v100\n"
    "v_and_b32 v39, 0xffff0000, v100\n"
    "v_pk_add_f32 v[224:225], v[224:225], v[34:35]\n"
    "v_pk_add_f32 v[36:37], v[36:37], v[38:39]\n"
    "v_pk_add_f32 v[224:225], v[224:225], v[36:37]\n"
    "v_cvt_pk_bf16_f32 v52, v224, v225\n"
    "v_lshlrev_b32 v34, 16, v69\n"
    "v_and_b32 v35, 0xffff0000, v69\n"
    "v_lshlrev_b32 v36, 16, v85\n"
    "v_and_b32 v37, 0xffff0000, v85\n"
    "v_lshlrev_b32 v38, 16, v101\n"
    "v_and_b32 v39, 0xffff0000, v101\n"
    "v_pk_add_f32 v[226:227], v[226:227], v[34:35]\n"
    "v_pk_add_f32 v[36:37], v[36:37], v[38:39]\n"
    "v_pk_add_f32 v[226:227], v[226:227], v[36:37]\n"
    "v_cvt_pk_bf16_f32 v53, v226, v227\n"
    "v_lshlrev_b32 v34, 16, v70\n"
    "v_and_b32 v35, 0xffff0000, v70\n"
    "v_lshlrev_b32 v36, 16, v86\n"
    "v_and_b32 v37, 0xffff0000, v86\n"
    "v_lshlrev_b32 v38, 16, v102\n"
    "v_and_b32 v39, 0xffff0000, v102\n"
    "v_pk_add_f32 v[240:241], v[240:241], v[34:35]\n"
    "v_pk_add_f32 v[36:37], v[36:37], v[38:39]\n"
    "v_pk_add_f32 v[240:241], v[240:241], v[36:37]\n"
    "v_cvt_pk_bf16_f32 v54, v240, v241\n"
    "v_lshlrev_b32 v34, 16, v71\n"
    "v_and_b32 v35, 0xffff0000, v71\n"
    "v_lshlrev_b32 v36, 16, v87\n"
    "v_and_b32 v37, 0xffff0000, v87\n"
    "v_lshlrev_b32 v38, 16, v103\n"
    "v_and_b32 v39, 0xffff0000, v103\n"
    "v_pk_add_f32 v[242:243], v[242:243], v[34:35]\n"
    "v_pk_add_f32 v[36:37], v[36:37], v[38:39]\n"
    "v_pk_add_f32 v[242:243], v[242:243], v[36:37]\n"
    "v_cvt_pk_bf16_f32 v55, v242, v243\n"
    "s_nop 1\n"
    "v_permlane16_swap_b32 v52, v54\n"
    "v_permlane16_swap_b32 v53, v55\n"
    "buffer_store_dwordx4 v[52:55], v20, s[24:27], s52 offen offset:64\n"
    "s_cmp_eq_u32 s18, 0\n"
    "s_cselect_b32 s22, 0, 0x7ff00000\n"
    "s_cmp_eq_u32 s19, 0\n"
    "s_cselect_b32 s22, s22, 0x7ff00000\n"
    "v_mov_b32 v24, s4\n"
    "v_mov_b32 v25, s5\n"
    "s_mov_b64 exec, 1\n"
    "buffer_store_dwordx2 v[24:25], v2, s[44:47], s22 offen sc1\n"
    "s_mov_b64 exec, -1\n"
    "s_waitcnt vmcnt(0)\n"
    "s_branch END_%=\n"
    "PS0_%=:\n"
    "s_cmp_eq_u32 s58, 0\n"
    "s_cbranch_scc1 PR0_%=\n"
    "s_mov_b32 s34, 0\n"
    "PL1_%=:\n"
    "buffer_load_dwordx2 v[60:61], v62, s[44:47], s55 offen sc1\n"
    "s_waitcnt vmcnt(0)\n"
    "v_cmp_ne_u64 vcc, s[4:5], v[60:61]\n"
    "s_add_u32 s34, s34, 1\n"
    "s_cbranch_vccz PD1_%=\n"
    "s_sleep 1\n"
    "s_cmp_lt_u32 s34, 0x30d40\n"
    "s_cbranch_scc1 PL1_%=\n"
    "PD1_%=:\n"
    "s_branch PR0_%=\n"
    "PS2_%=:\n"
    "s_mov_b32 s34, 0\n"
    "PL3_%=:\n"
    "buffer_load_dwordx2 v[12:13], v14, s[44:47], s12 offen sc1\n"
    "s_waitcnt vmcnt(0)\n"
    "v_cmp_ne_u64 vcc, s[4:5], v[12:13]\n"
    "s_add_u32 s34, s34, 1\n"
    "s_cbranch_vccz PD3_%=\n"
    "s_sleep 1\n"
    "s_cmp_lt_u32 s34, 0x30d40\n"
    "s_cbranch_scc1 PL3_%=\n"
    "PD3_%=:\n"
    "s_branch PR2_%=\n"
    "PS4_%=:\n"
    "s_mov_b32 s34, 0\n"
    "PL5_%=:\n"
    "buffer_load_dwordx2 v[8:9], v10, s[44:47], s11 offen sc1\n"
    "s_waitcnt vmcnt(0)\n"
    "v_cmp_ne_u64 vcc, s[4:5], v[8:9]\n"
    "s_add_u32 s34, s34, 1\n"
    "s_cbranch_vccz PD5_%=\n"
    "s_sleep 1\n"
    "s_cmp_lt_u32 s34, 0x30d40\n"
    "s_cbranch_scc1 PL5_%=\n"
    "PD5_%=:\n"
    "s_branch PR4_%=\n"
    "EM2_%=:\n"
    "v_lshrrev_b32 v200, 3, v1\n"
    "v_lshlrev_b32 v200, 12, v200\n"
    "v_lshrrev_b32 v201, 4, v1\n"
    "v_and_b32 v202, 7, v1\n"
    "v_xor_b32 v201, v201, v202\n"
    "v_lshlrev_b32 v201, 4, v201\n"
    "v_xor_b32 v202, 0, v201\n"
    "v_add_u32 v202, v202, v200\n"
    "v_add_u32 v192, 0, v202\n"
    "v_xor_b32 v202, 64, v201\n"
    "v_add_u32 v202, v202, v200\n"
    "v_add_u32 v193, 0x7c00, v202\n"
    "v_xor_b32 v202, 0, v201\n"
    "v_add_u32 v202, v202, v200\n"
    "v_add_u32 v194, 0xf800, v202\n"
    "v_xor_b32 v202, 64, v201\n"
    "v_add_u32 v202, v202, v200\n"
    "v_add_u32 v195, 0x17400, v202\n"
    "s_and_b32 s22, s19, 1\n"
    "s_lshl_b32 s22, s22, 13\n"
    "v_and_b32 v200, 15, v1\n"
    "v_lshrrev_b32 v201, 1, v200\n"
    "v_lshrrev_b32 v202, 4, v1\n"
    "v_xor_b32 v201, v201, v202\n"
    "v_lshlrev_b32 v201, 4, v201\n"
    "v_lshlrev_b32 v200, 7, v200\n"
    "v_add_u32 v200, v200, v201\n"
    "v_add_u32 v196, s22, v200\n"
    "v_xor_b32 v197, 64, v196\n"
    "s_lshr_b32 s22, s19, 1\n"
    "s_lshl_b32 s22, s22, 12\n"
    "s_add_u32 s22, s22, 0x4000\n"
    "v_lshlrev_b32 v200, 3, v1\n"
    "v_add_u32 v198, s22, v200\n"
    "s_add_u32 m0, s16, 0x4000\n"
    "s_nop 0\n"
    "buffer_load_dwordx4 v2, s[44:47], s71 offen sc1 lds\n"
    "buffer_load_dwordx4 v2, s[44:47], s71 offen offset:1024 sc1 lds\n"
    "buffer_load_dwordx4 v2, s[44:47], s71 offen offset:2048 sc1 lds\n"
    "buffer_load_dwordx4 v2, s[44:47], s71 offen offset:3072 sc1 lds\n"
    "s_add_u32 s71, s71, 0x1c000\n"
    "s_add_u32 m0, s16, 0xc000\n"
    "s_nop 0\n"
    "buffer_load_dwordx4 v2, s[44:47], s71 offen sc1 lds\n"
    "buffer_load_dwordx4 v2, s[44:47], s71 offen offset:1024 sc1 lds\n"
    "buffer_load_dwordx4 v2, s[44:47], s71 offen offset:2048 sc1 lds\n"
    "buffer_load_dwordx4 v2, s[44:47], s71 offen offset:3072 sc1 lds\n"
    "s_add_u32 s71, s71, 0x1c000\n"
    "s_add_u32 m0, s16, 0\n"
    "s_nop 0\n"
    "buffer_load_dwordx4 v192, s[64:67], s13 offen lds\n"
    "buffer_load_dwordx4 v193, s[64:67], s13 offen offset:1024 lds\n"
    "buffer_load_dwordx4 v194, s[64:67], s13 offen offset:2048 lds\n"
    "buffer_load_dwordx4 v195, s[64:67], s13 offen offset:3072 lds\n"
    "s_branch MERGE_%=\n"
    "PS6_%=:\n"
    "s_mov_b32 s34, 0\n"
    "PL7_%=:\n"
    "buffer_load_dwordx2 v[8:9], v10, s[44:47], s69 offen sc1\n"
    "s_waitcnt vmcnt(0)\n"
    "v_cmp_ne_u64 vcc, s[4:5], v[8:9]\n"
    "s_add_u32 s34, s34, 1\n"
    "s_cbranch_vccz PD7_%=\n"
    "s_sleep 1\n"
    "s_cmp_lt_u32 s34, 0x30d40\n"
    "s_cbranch_scc1 PL7_%=\n"
    "PD7_%=:\n"
    "s_branch PR6_%=\n"
    "PS8_%=:\n"
    "s_mov_b32 s34, 0\n"
    "PL9_%=:\n"
    "buffer_load_dwordx2 v[8:9], v10, s[44:47], s11 offen sc1\n"
    "s_waitcnt vmcnt(0)\n"
    "v_cmp_ne_u64 vcc, s[4:5], v[8:9]\n"
    "s_add_u32 s34, s34, 1\n"
    "s_cbranch_vccz PD9_%=\n"
    "s_sleep 1\n"
    "s_cmp_lt_u32 s34, 0x30d40\n"
    "s_cbranch_scc1 PL9_%=\n"
    "PD9_%=:\n"
    "s_branch PR8_%=\n"
    "ODD_%=:\n"
    "s_nop 1\n"
    "s_add_u32 m0, s16, 0x14000\n"
    "s_nop 0\n"
    "buffer_load_dwordx4 v2, s[44:47], s71 offen sc1 lds\n"
    "buffer_load_dwordx4 v2, s[44:47], s71 offen offset:1024 sc1 lds\n"
    "buffer_load_dwordx4 v2, s[44:47], s71 offen offset:2048 sc1 lds\n"
    "buffer_load_dwordx4 v2, s[44:47], s71 offen offset:3072 sc1 lds\n"
    "s_add_u32 s71, s71, 0x1c000\n"
    "v_mov_b32 v128, 0\n"
    "v_mov_b32 v129, 0\n"
    "v_mov_b32 v130, 0\n"
    "v_mov_b32 v131, 0\n"
    "v_mov_b32 v132, 0\n"
    "v_mov_b32 v133, 0\n"
    "v_mov_b32 v134, 0\n"
    "v_mov_b32 v135, 0\n"
    "v_mov_b32 v136, 0\n"
    "v_mov_b32 v137, 0\n"
    "v_mov_b32 v138, 0\n"
    "v_mov_b32 v139, 0\n"
    "v_mov_b32 v140, 0\n"
    "v_mov_b32 v141, 0\n"
    "v_mov_b32 v142, 0\n"
    "v_mov_b32 v143, 0\n"
    "v_mov_b32 v144, 0\n"
    "v_mov_b32 v145, 0\n"
    "v_mov_b32 v146, 0\n"
    "v_mov_b32 v147, 0\n"
    "v_mov_b32 v148, 0\n"
    "v_mov_b32 v149, 0\n"
    "v_mov_b32 v150, 0\n"
    "v_mov_b32 v151, 0\n"
    "v_mov_b32 v152, 0\n"
    "v_mov_b32 v153, 0\n"
    "v_mov_b32 v154, 0\n"
    "v_mov_b32 v155, 0\n"
    "v_mov_b32 v156, 0\n"
    "v_mov_b32 v157, 0\n"
    "v_mov_b32 v158, 0\n"
    "v_mov_b32 v159, 0\n"
    "v_mov_b32 v160, 0\n"
    "v_mov_b32 v161, 0\n"
    "v_mov_b32 v162, 0\n"
    "v_mov_b32 v163, 0\n"
    "v_mov_b32 v164, 0\n"
    "v_mov_b32 v165, 0\n"
    "v_mov_b32 v166, 0\n"
    "v_mov_b32 v167, 0\n"
    "v_mov_b32 v168, 0\n"
    "v_mov_b32 v169, 0\n"
    "v_mov_b32 v170, 0\n"
    "v_mov_b32 v171, 0\n"
    "v_mov_b32 v172, 0\n"
    "v_mov_b32 v173, 0\n"
    "v_mov_b32 v174, 0\n"
    "v_mov_b32 v175, 0\n"
    "v_mov_b32 v176, 0\n"
    "v_mov_b32 v177, 0\n"
    "v_mov_b32 v178, 0\n"
    "v_mov_b32 v179, 0\n"
    "v_mov_b32 v180, 0\n"
    "v_mov_b32 v181, 0\n"
    "v_mov_b32 v182, 0\n"
    "v_mov_b32 v183, 0\n"
    "v_mov_b32 v184, 0\n"
    "v_mov_b32 v185, 0\n"
    "v_mov_b32 v186, 0\n"
    "v_mov_b32 v187, 0\n"
    "v_mov_b32 v188, 0\n"
    "v_mov_b32 v189, 0\n"
    "v_mov_b32 v190, 0\n"
    "v_mov_b32 v191, 0\n"
    "s_waitcnt vmcnt(12)\n"
    "s_barrier\n"
    "ds_read_b128 a[16:19], v196\n"
    "ds_read_b128 a[20:23], v196 offset:2048\n"
    "ds_read_b128 a[24:27], v196 offset:4096\n"
    "ds_read_b128 a[28:31], v196 offset:6144\n"
    "ds_read_b64_tr_b16 a[0:1], v198\n"
    "ds_read_b64_tr_b16 a[2:3], v198 offset:512\n"
    "ds_read_b64_tr_b16 a[4:5], v198 offset:1024\n"
    "ds_read_b64_tr_b16 a[6:7], v198 offset:1536\n"
    "ds_read_b64_tr_b16 a[8:9], v198 offset:2048\n"
    "ds_read_b64_tr_b16 a[10:11], v198 offset:2560\n"
    "ds_read_b64_tr_b16 a[12:13], v198 offset:3072\n"
    "ds_read_b64_tr_b16 a[14:15], v198 offset:3584\n"
    "s_branch P4O_%=\n"
    "IDLE_%=:\n"
    "s_mov_b64 s[36:37], s[82:83]\n"
    "s_mov_b32 s38, 0x400000\n"
    "s_mov_b32 s39, 0x20000\n"
    "s_mov_b64 s[40:41], s[90:91]\n"
    "s_mov_b32 s42, 0xe0000\n"
    "s_mov_b32 s43, 0x20000\n"
    "s_mov_b64 s[24:25], s[94:95]\n"
    "s_mov_b32 s26, 0x200000\n"
    "s_mov_b32 s27, 0x20000\n"
    "s_mov_b64 s[28:29], s[88:89]\n"
    "s_mov_b32 s30, 0x1000\n"
    "s_mov_b32 s31, 0x20000\n"
    "s_and_b32 s22, s18, 7\n"
    "s_lshr_b32 s23, s18, 3\n"
    "s_lshr_b32 s8, s22, 1\n"
    "s_and_b32 s9, s22, 1\n"
    "s_lshl_b32 s9, s9, 3\n"
    "s_lshr_b32 s34, s23, 2\n"
    "s_add_u32 s9, s9, s34\n"
    "s_and_b32 s10, s23, 3\n"
    "s_lshl_b32 s53, s9, 1\n"
    "s_and_b32 s22, s19, 1\n"
    "s_add_u32 s53, s53, s22\n"
    "s_mul_i32 s53, s53, 0xb8\n"
    "s_lshr_b32 s22, s19, 1\n"
    "s_add_u32 s53, s53, s22\n"
    "s_mul_i32 s68, s8, 14\n"
    "s_add_u32 s68, s68, s53\n"
    "s_lshl_b32 s68, s68, 12\n"
    "s_add_u32 s68, s68, 0x217f000\n"
    "s_lshl_b32 s22, s8, 5\n"
    "s_add_u32 s53, s53, s22\n"
    "s_lshl_b32 s53, s53, 12\n"
    "s_add_u32 s53, s53, 0x20ff000\n"
    "s_lshl_b32 s61, s19, 14\n"
    "s_lshl_b32 s22, s10, 8\n"
    "s_add_u32 s61, s61, s22\n"
    "s_lshl_b32 s63, s8, 20\n"
    "s_add_u32 s63, s63, s61\n"
    "s_and_b32 s22, s8, 1\n"
    "s_mul_i32 s22, s22, 0x70000\n"
    "s_add_u32 s61, s61, s22\n"
    "v_bfe_u32 v20, v1, 2, 2\n"
    "v_mul_u32_u24 v20, 0x400, v20\n"
    "v_lshrrev_b32 v21, 4, v1\n"
    "v_lshl_add_u32 v20, v21, 6, v20\n"
    "v_and_b32 v21, 3, v1\n"
    "v_lshlrev_b32 v21, 4, v21\n"
    "v_xor_b32 v22, 0, v21\n"
    "v_add_u32 v22, v22, v20\n"
    "v_add_u32 v12, 0, v22\n"
    "v_xor_b32 v22, 32, v21\n"
    "v_add_u32 v22, v22, v20\n"
    "v_add_u32 v13, 0xc00, v22\n"
    "v_xor_b32 v22, 0, v21\n"
    "v_add_u32 v22, v22, v20\n"
    "v_add_u32 v14, 0x1800, v22\n"
    "v_xor_b32 v22, 32, v21\n"
    "v_add_u32 v22, v22, v20\n"
    "v_add_u32 v15, 0x2400, v22\n"
    "s_xor_b32 s22, s8, 0\n"
    "s_lshl_b32 s22, s22, 10\n"
    "s_add_u32 s22, s22, 0x1000\n"
    "v_add_u32 v4, s22, v2\n"
    "s_xor_b32 s22, s8, 1\n"
    "s_lshl_b32 s22, s22, 10\n"
    "s_add_u32 s22, s22, 0xc00\n"
    "v_add_u32 v5, s22, v2\n"
    "s_xor_b32 s22, s8, 2\n"
    "s_lshl_b32 s22, s22, 10\n"
    "s_add_u32 s22, s22, 0x800\n"
    "v_add_u32 v6, s22, v2\n"
    "s_xor_b32 s22, s8, 3\n"
    "s_lshl_b32 s22, s22, 10\n"
    "s_add_u32 s22, s22, 0x400\n"
    "v_add_u32 v7, s22, v2\n"
    "s_and_b32 s22, s19, 1\n"
    "s_lshl_b32 s22, s22, 12\n"
    "s_add_u32 s22, s22, 0\n"
    "v_add_u32 v16, s22, v2\n"
    "s_lshr_b32 s22, s19, 1\n"
    "s_lshl_b32 s22, s22, 9\n"
    "s_add_u32 s22, s22, 0x4000\n"
    "v_lshrrev_b32 v20, 4, v1\n"
    "v_lshlrev_b32 v21, 10, v20\n"
    "v_and_b32 v20, 1, v20\n"
    "v_lshl_add_u32 v21, v20, 5, v21\n"
    "v_bfe_u32 v20, v1, 2, 2\n"
    "v_lshl_add_u32 v21, v20, 6, v21\n"
    "v_and_b32 v20, 3, v1\n"
    "v_lshl_add_u32 v21, v20, 3, v21\n"
    "v_add_u32 v18, s22, v21\n"
    "v_xor_b32 v19, 32, v18\n"
    "s_lshl_b32 s69, s9, 2\n"
    "s_add_u32 s69, s69, s8\n"
    "s_lshl_b32 s69, s69, 8\n"
    "s_add_u32 s69, s69, 0x4000\n"
    "s_lshl_b32 s11, s9, 1\n"
    "s_lshr_b32 s22, s8, 1\n"
    "s_add_u32 s11, s11, s22\n"
    "s_lshl_b32 s11, s11, 8\n"
    "s_add_u32 s11, s11, 0x8000\n"
    "s_lshl_b32 s60, s56, 3\n"
    "s_and_b32 s22, s19, 1\n"
    "s_lshl_b32 s22, s22, 2\n"
    "s_add_u32 s60, s60, s22\n"
    "s_mul_i32 s50, s60, 56\n"
    "s_lshl_b32 s22, s57, 3\n"
    "s_add_u32 s50, s50, s22\n"
    "s_lshr_b32 s22, s19, 1\n"
    "s_lshl_b32 s22, s22, 2\n"
    "s_add_u32 s50, s50, s22\n"
    "s_lshl_b32 s50, s50, 9\n"
    "s_add_u32 s50, s50, 0x4000000\n"
    "s_lshr_b32 s51, s60, 2\n"
    "s_mul_i32 s51, s51, 0xb8\n"
    "s_lshl_b32 s22, s57, 2\n"
    "s_add_u32 s51, s51, s22\n"
    "s_and_b32 s22, s19, 2\n"
    "s_add_u32 s51, s51, s22\n"
    "s_mul_i32 s22, s58, 28\n"
    "s_add_u32 s51, s51, s22\n"
    "s_lshl_b32 s51, s51, 12\n"
    "s_add_u32 s51, s51, 0x2180000\n"
    "s_lshl_b32 s54, s56, 1\n"
    "s_add_u32 s54, s54, s58\n"
    "s_lshl_b32 s54, s54, 3\n"
    "s_add_u32 s54, s54, s57\n"
    "s_lshl_b32 s54, s54, 2\n"
    "s_add_u32 s54, s54, s19\n"
    "s_lshl_b32 s54, s54, 3\n"
    "s_add_u32 s54, s54, 0x8000\n"
    "s_cmp_eq_u32 s58, 1\n"
    "s_cselect_b32 s30, 0, s30\n"
    "v_and_b32 v27, 15, v1\n"
    "v_lshlrev_b32 v26, 5, v27\n"
    "v_bfe_u32 v28, v1, 4, 1\n"
    "v_lshl_add_u32 v26, v28, 9, v26\n"
    "v_lshrrev_b32 v28, 5, v1\n"
    "v_lshl_add_u32 v26, v28, 4, v26\n"
    "v_lshlrev_b32 v28, 1, v27\n"
    "v_lshlrev_b32 v27, 2, v27\n"
    "s_mov_b32 s51, 0x7ff00000\n"
    "s_mov_b32 s48, 0x7ff00000\n"
    "s_mov_b32 s54, 0x7ff00000\n"
    "s_waitcnt vmcnt(0)\n"
    "v_mov_b32 v24, s4\n"
    "v_mov_b32 v25, s5\n"
    "s_mov_b64 exec, 1\n"
    "buffer_store_dwordx2 v[24:25], v2, s[44:47], s49 offen sc1\n"
    "s_mov_b64 exec, -1\n"
    "s_nop 0\n"
    "v_lshlrev_b32 v26, 7, v1\n"
    "s_lshr_b32 s22, s18, 3\n"
    "s_and_b32 s22, s22, 3\n"
    "s_and_b32 s23, s18, 1\n"
    "s_lshl_b32 s23, s23, 2\n"
    "s_add_u32 s22, s22, s23\n"
    "s_lshl_b32 s22, s22, 2\n"
    "s_add_u32 s22, s22, s19\n"
    "s_lshl_b32 s23, s22, 13\n"
    "s_lshl_b32 s35, s8, 20\n"
    "s_add_u32 s23, s23, s35\n"
    "buffer_load_dword v27, v26, s[36:39], s23 offen\n"
    "s_add_u32 s23, s23, 0x40000\n"
    "buffer_load_dword v27, v26, s[36:39], s23 offen\n"
    "s_add_u32 s23, s23, 0x40000\n"
    "buffer_load_dword v27, v26, s[36:39], s23 offen\n"
    "s_add_u32 s23, s23, 0x40000\n"
    "buffer_load_dword v27, v26, s[36:39], s23 offen\n"
    "s_add_u32 s23, s23, 0x40000\n"
    "s_lshl_b32 s23, s8, 5\n"
    "s_add_u32 s23, s23, s22\n"
    "s_lshl_b32 s23, s23, 13\n"
    "buffer_load_dword v27, v26, s[40:43], s23 offen\n"
    "v_min_u32 v10, 31, v1\n"
    "v_mul_u32_u24 v10, 8, v10\n"
    "s_mov_b32 s34, 0\n"
    "PL10_%=:\n"
    "buffer_load_dwordx2 v[8:9], v10, s[44:47], s69 offen sc1\n"
    "s_waitcnt vmcnt(0)\n"
    "v_cmp_ne_u64 vcc, s[4:5], v[8:9]\n"
    "s_add_u32 s34, s34, 1\n"
    "s_cbranch_vccz PD10_%=\n"
    "s_sleep 1\n"
    "s_cmp_lt_u32 s34, 0x30d40\n"
    "s_cbranch_scc1 PL10_%=\n"
    "PD10_%=:\n"
    "s_add_u32 m0, s16, 0x4000\n"
    "s_nop 0\n"
    "buffer_load_dwordx4 v12, s[36:39], s63 offen lds\n"
    "buffer_load_dwordx4 v13, s[36:39], s63 offen offset:1024 lds\n"
    "buffer_load_dwordx4 v14, s[36:39], s63 offen offset:2048 lds\n"
    "buffer_load_dwordx4 v15, s[36:39], s63 offen offset:3072 lds\n"
    "s_add_u32 s63, s63, 0x10000\n"
    "s_add_u32 m0, s16, 0\n"
    "s_nop 0\n"
    "buffer_load_dwordx4 v4, s[44:47], s53 offen sc1 lds\n"
    "buffer_load_dwordx4 v5, s[44:47], s53 offen offset:1024 sc1 lds\n"
    "buffer_load_dwordx4 v6, s[44:47], s53 offen offset:2048 sc1 lds\n"
    "buffer_load_dwordx4 v7, s[44:47], s53 offen offset:3072 sc1 lds\n"
    "s_add_u32 s53, s53, 0x2000\n"
    "s_add_u32 m0, s16, 0xc000\n"
    "s_nop 0\n"
    "buffer_load_dwordx4 v12, s[36:39], s63 offen lds\n"
    "buffer_load_dwordx4 v13, s[36:39], s63 offen offset:1024 lds\n"
    "buffer_load_dwordx4 v14, s[36:39], s63 offen offset:2048 lds\n"
    "buffer_load_dwordx4 v15, s[36:39], s63 offen offset:3072 lds\n"
    "s_add_u32 s63, s63, 0x10000\n"
    "s_add_u32 m0, s16, 0x8000\n"
    "s_nop 0\n"
    "buffer_load_dwordx4 v4, s[44:47], s53 offen sc1 lds\n"
    "buffer_load_dwordx4 v5, s[44:47], s53 offen offset:1024 sc1 lds\n"
    "buffer_load_dwordx4 v6, s[44:47], s53 offen offset:2048 sc1 lds\n"
    "buffer_load_dwordx4 v7, s[44:47], s53 offen offset:3072 sc1 lds\n"
    "s_add_u32 s53, s53, 0x2000\n"
    "s_add_u32 m0, s16, 0x14000\n"
    "s_nop 0\n"
    "buffer_load_dwordx4 v12, s[36:39], s63 offen lds\n"
    "buffer_load_dwordx4 v13, s[36:39], s63 offen offset:1024 lds\n"
    "buffer_load_dwordx4 v14, s[36:39], s63 offen offset:2048 lds\n"
    "buffer_load_dwordx4 v15, s[36:39], s63 offen offset:3072 lds\n"
    "s_add_u32 s63, s63, 0x10000\n"
    "s_waitcnt vmcnt(12)\n"
    "s_barrier\n"
    "ds_read_b128 a[16:19], v16\n"
    "ds_read_b128 a[20:23], v16 offset:1024\n"
    "ds_read_b128 a[24:27], v16 offset:2048\n"
    "ds_read_b128 a[28:31], v16 offset:3072\n"
    "ds_read_b64_tr_b16 a[0:1], v18\n"
    "ds_read_b64_tr_b16 a[2:3], v18 offset:4096\n"
    "ds_read_b64_tr_b16 a[4:5], v19\n"
    "ds_read_b64_tr_b16 a[6:7], v19 offset:4096\n"
    "ds_read_b64_tr_b16 a[8:9], v18 offset:256\n"
    "ds_read_b64_tr_b16 a[10:11], v18 offset:4352\n"
    "ds_read_b64_tr_b16 a[12:13], v19 offset:256\n"
    "ds_read_b64_tr_b16 a[14:15], v19 offset:4352\n"
    "s_branch P5S_%=\n"
    "END_%=:\n"
    : "+v"(tid) : "s"(__builtin_amdgcn_kernarg_segment_ptr()), "s"(blockIdx.x) : "v1","v2","v3","v4","v5","v6","v7","v8","v9","v10","v11","v12","v13","v14","v15","v16","v17","v18","v19","v20","v21","v22","v23","v24","v25","v26","v27","v28","v29","v30","v31","v32","v33","v34","v35","v36","v37","v38","v39","v40","v41","v42","v43","v44","v45","v46","v47","v48","v49","v50","v51","v52","v53","v54","v55","v56","v57","v58","v59","v60","v61","v62","v63","v64","v65","v66","v67","v68","v69","v70","v71","v72","v73","v74","v75","v76","v77","v78","v79","v80","v81","v82","v83","v84","v85","v86","v87","v88","v89","v90","v91","v92","v93","v94","v95","v96","v97","v98","v99","v100","v101","v102","v103","v104","v105","v106","v107","v108","v109","v110","v111","v112","v113","v114","v115","v116","v117","v118","v119","v120","v121","v122","v123","v124","v125","v126","v127","v128","v129","v130","v131","v132","v133","v134","v135","v136","v137","v138","v139","v140","v141","v142","v143","v144","v145","v146","v147","v148","v149","v150","v151","v152","v153","v154","v155","v156","v157","v158","v159","v160","v161","v162","v163","v164","v165","v166","v167","v168","v169","v170","v171","v172","v173","v174","v175","v176","v177","v178","v179","v180","v181","v182","v183","v184","v185","v186","v187","v188","v189","v190","v191","v192","v193","v194","v195","v196","v197","v198","v199","v200","v201","v202","v203","v204","v205","v206","v207","v208","v209","v210","v211","v212","v213","v214","v215","v216","v217","v218","v219","v220","v221","v222","v223","v224","v225","v226","v227","v228","v229","v230","v231","v232","v233","v234","v235","v236","v237","v238","v239","v240","v241","v242","v243","v244","v245","v246","v247","v248","v249","v250","v251","v252","v253","v254","v255","a0","a1","a2","a3","a4","a5","a6","a7","a8","a9","a10","a11","a12","a13","a14","a15","a16","a17","a18","a19","a20","a21","a22","a23","a24","a25","a26","a27","a28","a29","a30","a31","a32","a33","a34","a35","a36","a37","a38","a39","a40","a41","a42","a43","a44","a45","a46","a47","a48","a49","a50","a51","a52","a53","a54","a55","a56","a57","a58","a59","a60","a61","a62","a63","a64","a65","a66","a67","a68","a69","a70","a71","a72","a73","a74","a75","a76","a77","a78","a79","a80","a81","a82","a83","a84","a85","a86","a87","a88","a89","a90","a91","a92","a93","a94","a95","a96","a97","a98","a99","a100","a101","a102","a103","a104","a105","a106","a107","a108","a109","a110","a111","a112","a113","a114","a115","a116","a117","a118","a119","a120","a121","a122","a123","a124","a125","a126","a127","a128","a129","a130","a131","a132","a133","a134","a135","a136","a137","a138","a139","a140","a141","a142","a143","a144","a145","a146","a147","a148","a149","a150","a151","a152","a153","a154","a155","a156","a157","a158","a159","a160","a161","a162","a163","a164","a165","a166","a167","a168","a169","a170","a171","a172","a173","a174","a175","a176","a177","a178","a179","a180","a181","a182","a183","a184","a185","a186","a187","a188","a189","a190","a191","a192","a193","a194","a195","a196","a197","a198","a199","a200","a201","a202","a203","a204","a205","a206","a207","a208","a209","a210","a211","a212","a213","a214","a215","a216","a217","a218","a219","a220","a221","a222","a223","a224","a225","a226","a227","a228","a229","a230","a231","a232","a233","a234","a235","a236","a237","a238","a239","a240","a241","a242","a243","a244","a245","a246","a247","a248","a249","a250","a251","a252","a253","a254","a255","s4","s5","s6","s7","s8","s9","s10","s11","s12","s13","s14","s15","s16","s17","s18","s19","s20","s21","s22","s23","s24","s25","s26","s27","s28","s29","s30","s31","s34","s35","s36","s37","s38","s39","s40","s41","s42","s43","s44","s45","s46","s47","s48","s49","s50","s51","s52","s53","s54","s55","s56","s57","s58","s59","s60","s61","s62","s63","s64","s65","s66","s67","s68","s69","s70","s71","s72","s73","s74","s75","s76","s77","s78","s79","s80","s81","s82","s83","s84","s85","s86","s87","s88","s89","s90","s91","s92","s93","s94","s95","s96","s97","s98","s99","vcc","scc","memory");
}

extern "C" void kernel_launch(void* const* d_in, const int* in_sizes, int n_in, void* d_out, int out_size, void* d_ws, size_t ws_size, hipStream_t stream){
  (void)hipFuncSetAttribute((const void*)k_fused,hipFuncAttributeMaxDynamicSharedMemorySize,163840);
  k_fused<<<256,256,163840,stream>>>(d_in[0],d_in[1],d_in[2],d_in[3],d_in[4],d_in[5],d_in[7],d_in[8],d_in[9],d_in[10],d_in[11],d_out,d_ws);
}
```
